```python
import torch

def _softmax(x, axis=-1):
    return torch.softmax(x, dim=axis)
def _gelu(x):
    return torch.nn.functional.gelu(x, approximate="tanh")
def _silu(x):
    return torch.nn.functional.silu(x)
def _sigmoid(x):
    return torch.sigmoid(x)
def _layernorm(x, axis=-1, eps=1e-5):
    m = x.mean(dim=axis, keepdim=True)
    v = x.var(dim=axis, keepdim=True, unbiased=False)
    return (x - m) / torch.sqrt(v + eps)
def _scatter_add(tgt, idx, src, axis=0):
    return tgt.clone().index_add_(axis, idx, src)
def _segment_sum(src, seg, n):
    out = torch.zeros((n,) + src.shape[1:], dtype=src.dtype)
    return out.index_add_(0, seg, src)
def _pad(x, pw):
    flat = tuple(v for pair in reversed(pw) for v in pair)
    return torch.nn.functional.pad(x, flat)

kernel_name = 'interleave_layers__ssm_conv_s4d__mixer_channel_mlp__prompt'

def setup_inputs():

    _w = (lambda a: (lambda t: t.to(torch.bfloat16) if t.is_floating_point() else t)(torch.as_tensor(a)))
    def _A_setup():
        Bsz, L, D, N = 1, 2048, 512, 128
        return {
            "u": _w((lambda *s: torch.randn(*s, dtype=torch.bfloat16) * 0.05)(Bsz, L, D)),
            "log_A_real": _w((lambda *s: torch.randn(*s, dtype=torch.bfloat16) * 3.2)(D, N) * 0.5),
            "A_imag": _w((lambda *s: torch.randn(*s, dtype=torch.bfloat16) * 0.05)(D, N) * 3.14),
            "C_real": _w((lambda *s: torch.randn(*s, dtype=torch.bfloat16) * 0.05)(D, N) / N ** 0.5),
            "C_imag": _w((lambda *s: torch.randn(*s, dtype=torch.bfloat16) * 0.05)(D, N) / N ** 0.5),
            "log_dt": _w((lambda *s: torch.randn(*s, dtype=torch.bfloat16) * 0.05)(D) * 0.5 - 4.0),
            "D_skip": _w((lambda *s: torch.randn(*s, dtype=torch.bfloat16) * 0.05)(D)),
        }
    _A_in = _A_setup()

    def _B_setup():
        B, S, D, F = 1, 2048, 512, 1792
        return {
            "x": _w((lambda *s: torch.randn(*s, dtype=torch.bfloat16) * 0.05)(B, S, D)),
            "w1": _w((lambda *s: torch.randn(*s, dtype=torch.bfloat16) * 1.6)(D, F) * (D ** -0.5)),
            "b1": _w((lambda *s: torch.randn(*s, dtype=torch.bfloat16) * 0.4)(F)),
            "w2": _w((lambda *s: torch.randn(*s, dtype=torch.bfloat16) * 0.1)(F, D) * (F ** -0.5)),
            "b2": _w((lambda *s: torch.randn(*s, dtype=torch.bfloat16) * 0.1)(D)),
        }
    _B_in = _B_setup()

    return {
        "A_u": _A_in["u"],
        "A_log_A_real": _A_in["log_A_real"],
        "A_A_imag": _A_in["A_imag"],
        "A_C_real": _A_in["C_real"],
        "A_C_imag": _A_in["C_imag"],
        "A_log_dt": _A_in["log_dt"],
        "A_D_skip": _A_in["D_skip"],
        "B_x": _B_in["x"],
        "B_w1": _B_in["w1"],
        "B_b1": _B_in["b1"],
        "B_w2": _B_in["w2"],
        "B_b2": _B_in["b2"],
    }

def reference(A_u, A_log_A_real, A_A_imag, A_C_real, A_C_imag, A_log_dt, A_D_skip, B_x, B_w1, B_b1, B_w2, B_b2):
    def _A_ref(u, log_A_real, A_imag, C_real, C_imag, log_dt, D_skip):
        L = u.shape[1]
        dt = torch.exp(log_dt)
        A = -torch.exp(log_A_real) + 1j * A_imag
        C = (C_real + 1j * C_imag) * dt[:, None]
        pos = (lambda x, dt: x.to(dt))(torch.arange(L), log_dt.dtype)
        P = torch.exp((A * dt[:, None])[:, :, None] * pos[None, None, :])
        K = 2.0 * torch.einsum("dn,dnl->dl", C, P).real
        L2 = 2 * L
        u_t = (lambda x, *d: x.permute(*d))(u, 0, 2, 1)
        Kf = (lambda x, *a, **k: torch.fft.rfft(x.float(), *a, **k))(K, L2)
        Uf = (lambda x, *a, **k: torch.fft.rfft(x.float(), *a, **k))(u_t, L2)
        y = (lambda x, *a, **k: torch.fft.irfft(x, *a, **k).to(torch.bfloat16))(Uf * Kf[None, :, :], L2)[:, :, :L]
        y = (lambda x, *d: x.permute(*d))(y, 0, 2, 1)
        return y + u * D_skip[None, None, :]

    def _B_ref(x, w1, b1, w2, b2):
        y = _layernorm(x, axis=-1)
        h = _gelu(torch.einsum("bsd,df->bsf", y, w1) + b1)
        return torch.einsum("bsf,fd->bsd", h, w2) + b2

    x = A_u
    x = x + _A_ref(_layernorm(x, axis=-1), A_log_A_real, A_A_imag, A_C_real, A_C_imag, A_log_dt, A_D_skip)
    x = x + _B_ref(_layernorm(x, axis=-1), B_w1, B_b1, B_w2, B_b2)
    x = x + _A_ref(_layernorm(x, axis=-1), A_log_A_real, A_A_imag, A_C_real, A_C_imag, A_log_dt, A_D_skip)
    x = x + _B_ref(_layernorm(x, axis=-1), B_w1, B_b1, B_w2, B_b2)
    return x
```

```cpp
#include <hip/hip_runtime.h>
#include <hip/hip_bf16.h>

typedef unsigned short u16;
typedef unsigned int u32;
typedef long long i64;
typedef __attribute__((ext_vector_type(4))) short s16x4;
typedef __attribute__((ext_vector_type(8))) short s16x8;
typedef __attribute__((ext_vector_type(8))) __bf16 bf16x8;
typedef __attribute__((ext_vector_type(2))) __bf16 bf16x2;
typedef __attribute__((ext_vector_type(8))) _Float16 f16x8;
typedef __attribute__((ext_vector_type(2))) _Float16 f16x2;
typedef __attribute__((ext_vector_type(4))) float f32x4;
typedef __attribute__((ext_vector_type(2))) float f32x2;
typedef __attribute__((ext_vector_type(4))) unsigned u32x4;
typedef __attribute__((ext_vector_type(2))) unsigned u32x2;
typedef __attribute__((ext_vector_type(2))) long long i64x2;
#define LAS __attribute__((address_space(3)))
#define DEVI __device__ __forceinline__

constexpr int SEQ = 2048, DM = 512, NST = 128, FF = 1792;
constexpr float LN_EPS = 1e-5f;
constexpr float FX_SCALE = 16777216.0f;
constexpr float FX_INV = 1.0f / 16777216.0f;

constexpr size_t MB = 1024 * 1024;
constexpr size_t WS_XT0 = 0, WS_XT1 = 2 * MB, WS_XT2 = 4 * MB, WS_XT3 = 6 * MB;
constexpr size_t WS_KT = 8 * MB;
constexpr size_t WS_W1P = 12 * MB;
constexpr size_t WS_W2P = 14 * MB;
constexpr size_t WS_H = 16 * MB;
constexpr size_t WS_ST0 = 24 * MB;
constexpr size_t WS_ST2 = 24 * MB + 65536;
constexpr size_t WS_S1 = 24 * MB + 131072;
constexpr size_t WS_KSC = 24 * MB + 196608;

DEVI float bf2f(u16 v) { return __uint_as_float((u32)v << 16); }
DEVI float lo_bf(u32 w) { return __uint_as_float(w << 16); }
DEVI float hi_bf(u32 w) { return __uint_as_float(w & 0xffff0000u); }
DEVI float rbf(float x) { return (float)(__bf16)x; }
DEVI u32 pack_bf(float a, float b) { f32x2 q = {a, b}; return __builtin_bit_cast(u32, __builtin_convertvector(q, bf16x2)); }
DEVI u32 pack_h(float a, float b) { f32x2 q = {a, b}; return __builtin_bit_cast(u32, __builtin_convertvector(q, f16x2)); }
DEVI float dot2bf(u32 a, u32 b, float c) {
  return __builtin_amdgcn_fdot2_f32_bf16(__builtin_bit_cast(bf16x2, a), __builtin_bit_cast(bf16x2, b), c, false);
}
DEVI f32x4 mfma_bf(u32x4 a, u32x4 b, f32x4 c) {
  return __builtin_amdgcn_mfma_f32_16x16x32_bf16(__builtin_bit_cast(bf16x8, a), __builtin_bit_cast(bf16x8, b), c, 0, 0, 0);
}
DEVI f32x4 mfma_h(u32x4 a, u32x4 b, f32x4 c) {
  return __builtin_amdgcn_mfma_f32_16x16x32_f16(__builtin_bit_cast(f16x8, a), __builtin_bit_cast(f16x8, b), c, 0, 0, 0);
}
DEVI u32x2 tr_read(const char* p) {
  return __builtin_bit_cast(u32x2, __builtin_amdgcn_ds_read_tr16_b64_v4i16((LAS s16x4*)p));
}
DEVI u32x4 cat2(u32x2 a, u32x2 b) { return __builtin_shufflevector(a, b, 0, 1, 2, 3); }
DEVI u32 lds_addr(const void* p) { return (u32)(size_t)(const LAS char*)p; }
DEVI void glds16(const void* gsrc, u32 lds_dst) {
  u32 keep;
  asm volatile("s_mov_b32 %0, m0\n\ts_mov_b32 m0, %2\n\ts_nop 0\n\tglobal_load_lds_dwordx4 %1, off\n\ts_mov_b32 m0, %0"
               : "=&s"(keep) : "v"(gsrc), "s"(lds_dst) : "memory");
}
#define WAIT_VM(n) asm volatile("s_waitcnt vmcnt(" #n ")" ::: "memory")
#define WAIT_VM_BAR(n) asm volatile("s_waitcnt vmcnt(" #n ")\n\ts_barrier" ::: "memory")
#define WAIT_LGKM0() asm volatile("s_waitcnt lgkmcnt(0)" ::: "memory")
#define BAR() __builtin_amdgcn_s_barrier()
DEVI float sum_x16(float v) {
  auto r = __builtin_amdgcn_permlane16_swap(__float_as_uint(v), __float_as_uint(v), false, false);
  return __uint_as_float(r[0]) + __uint_as_float(r[1]);
}
DEVI float sum_x32(float v) {
  auto r = __builtin_amdgcn_permlane32_swap(__float_as_uint(v), __float_as_uint(v), false, false);
  return __uint_as_float(r[0]) + __uint_as_float(r[1]);
}
DEVI float wave_sum(float v) {
  v += __shfl_xor(v, 1); v += __shfl_xor(v, 2); v += __shfl_xor(v, 4); v += __shfl_xor(v, 8);
  v = sum_x16(v); return sum_x32(v);
}
DEVI float fx2f(i64 v) { return ((float)(int)(v >> 32)) * (4294967296.0f * FX_INV) + ((float)(u32)v) * FX_INV; }
DEVI i64 f2fx(float v) { return (i64)(v * FX_SCALE); }

DEVI int hi_pos(int jj) {
  if (jj < 8) return jj;
  if (jj < 16) return 120 + jj;
  if (jj < 24) return 256 + 128 * ((jj - 16) >> 2) + 2 * ((jj - 16) & 3);
  if (jj < 32) return 512 + 128 * ((jj - 24) >> 1) + 4 * ((jj - 24) & 1);
  if (jj < 41) return 1024 + 128 * (jj - 32);
  return 0;
}
DEVI int cidx(int p) { return p < 256 ? p : (p < 512 ? 128 + (p >> 1) : (p < 1024 ? 256 + (p >> 2) : 384 + (p >> 3))); }

DEVI void kgen(int d, const u16* lar, const u16* aim, const u16* cre, const u16* cim, const u16* ldt,
               u16* ktab, float* ksc, char* smem) {
  const int t = threadIdx.x, lane = t & 63, w = t >> 6;
  f32x4* prm = (f32x4*)smem;
  float* part = (float*)(smem + 2048);
  u16* kd = (u16*)(smem + 2048 + 4 * 644 * 4);
  float* red = (float*)(smem + 2048 + 4 * 644 * 4 + 1296);
  if (t < 128) {
    const float dt = rbf(expf(bf2f(ldt[d])));
    const float ar = -rbf(expf(bf2f(lar[d * NST + t])));
    const float zi = bf2f(aim[d * NST + t]) * dt;
    const float cr = bf2f(cre[d * NST + t]) * dt, ci = bf2f(cim[d * NST + t]) * dt;
    const float zr = ar * dt;
    const float mag = 2.0f * sqrtf(cr * cr + ci * ci);
    f32x4 p = {zr * 1.44269504088896f, zi * 0.159154943091895f, log2f(mag), atan2f(ci, cr) * 0.159154943091895f};
    prm[t] = p;
    const float s = wave_sum(mag);
    if (lane == 0) red[w] = s;
  }
  __syncthreads();
  const int i = lane & 15, kk = lane >> 4;
  const float q = 0.25f * (float)(kk & 1);
  const float pa = (float)(8 * i);
  float pb[3];
#pragma unroll
  for (int tt = 0; tt < 3; ++tt) pb[tt] = (float)hi_pos(16 * tt + i);
  f32x4 acc[3] = {};
#pragma unroll 4
  for (int s = 0; s < 16; ++s) {
    const f32x4 p = prm[32 * w + 2 * s + (kk >> 1)];
    const float ya = __builtin_amdgcn_exp2f(p[0] * pa) * __builtin_amdgcn_cosf(__builtin_amdgcn_fractf(fmaf(p[1], pa, -q)));
    const float ph = p[3] + q;
#pragma unroll
    for (int tt = 0; tt < 3; ++tt) {
      const float xb = __builtin_amdgcn_exp2f(fmaf(p[0], pb[tt], p[2])) *
                       __builtin_amdgcn_cosf(__builtin_amdgcn_fractf(fmaf(p[1], pb[tt], ph)));
      acc[tt] = __builtin_amdgcn_mfma_f32_16x16x4f32(ya, xb, acc[tt], 0, 0, 0);
    }
  }
#pragma unroll
  for (int tt = 0; tt < 3; ++tt) {
    const int jj = 16 * tt + i, hp = hi_pos(jj);
#pragma unroll
    for (int r = 0; r < 4; ++r) {
      const int row = 4 * kk + r;
      if (jj < 40 || (jj == 40 && row == 0)) part[w * 644 + cidx(hp + 8 * row)] = acc[tt][r];
    }
  }
  __syncthreads();
  const float S = red[0] + red[1];
  int e = 0;
  (void)frexpf(S, &e);
  e = (S > 0.0f && S < 3.0e38f) ? e : 14;
  e = max(-100, min(100, e));
  const float scale = ldexpf(1.0f, 14 - e);
  if (t == 0) ksc[d] = ldexpf(1.0f, e - 14);
  for (int c = t; c < 641; c += 256) {
    const float v = (part[c] + part[644 + c]) + (part[2 * 644 + c] + part[3 * 644 + c]);
    kd[c] = __builtin_bit_cast(u16, (_Float16)(v * scale));
  }
  __syncthreads();
  u32 v[9];
#pragma unroll
  for (int e2 = 0; e2 < 9; ++e2) {
    const int l = 2047 - 8 * t - e2;
    const int p = (int)rbf((float)max(l, 0));
    v[e2] = l >= 0 ? (u32)kd[cidx(p)] : 0u;
  }
  u32x4 oe, oo;
#pragma unroll
  for (int j = 0; j < 4; ++j) { oe[j] = v[2 * j] | (v[2 * j + 1] << 16); oo[j] = v[2 * j + 1] | (v[2 * j + 2] << 16); }
  u32x4* dst = (u32x4*)(ktab + (size_t)d * 4096);
  dst[t] = oe;
  dst[256 + t] = oo;
}

DEVI void xpose_stats(int b, const u16* x, u16* xt, i64* st0, i64* st2, char* smem) {
  const int t = threadIdx.x, lane = t & 63, w = t >> 6;
  const int t0 = b * 32;
  constexpr int RS = 1024 + 32;
  u32x4 v[8];
#pragma unroll
  for (int r = 0; r < 8; ++r) v[r] = *(const u32x4*)(x + (size_t)(t0 + w * 8 + r) * DM + lane * 8);
#pragma unroll
  for (int r = 0; r < 8; ++r) {
    *(u32x4*)(smem + (w * 8 + r) * RS + lane * 16) = v[r];
    float s = 0.f, ss = 0.f;
#pragma unroll
    for (int j = 0; j < 4; ++j) { s = dot2bf(v[r][j], 0x3f803f80u, s); ss = dot2bf(v[r][j], v[r][j], ss); }
    s = wave_sum(s); ss = wave_sum(ss);
    if (lane == 0) {
      i64x2 o = {f2fx(s), f2fx(ss)};
      *(i64x2*)(st0 + 2 * (t0 + w * 8 + r)) = o;
      i64x2 z = {0, 0};
      *(i64x2*)(st2 + 2 * (t0 + w * 8 + r)) = z;
    }
  }
  __syncthreads();
  const int g = lane >> 4, q = (lane & 15) >> 2, p = lane & 3;
#pragma unroll
  for (int cf = 0; cf < 8; ++cf) {
    const int c0 = (w * 8 + cf) * 16;
    const char* a = smem + (8 * g + q) * RS + c0 * 2 + 8 * p;
    const u32x4 o = cat2(tr_read(a), tr_read(a + 4 * RS));
    *(u32x4*)(xt + (size_t)(c0 + (lane & 15)) * SEQ + t0 + 8 * g) = o;
  }
}

DEVI void pack_w1(int b, const u16* w1, u16* w1p, float* s1, char* smem) {
  const int t = threadIdx.x, lane = t & 63, w = t >> 6;
  constexpr int RS = 64 + 32;
  u32x4 v[8];
#pragma unroll
  for (int it = 0; it < 8; ++it) v[it] = *(const u32x4*)(w1 + (size_t)(it * 64 + (t >> 2)) * FF + b * 32 + (t & 3) * 8);
#pragma unroll
  for (int it = 0; it < 8; ++it) *(u32x4*)(smem + (it * 64 + (t >> 2)) * RS + (t & 3) * 16) = v[it];
  __syncthreads();
  const int g = lane >> 4, q = (lane & 15) >> 2, p = lane & 3;
  float cs[2] = {0.f, 0.f};
#pragma unroll
  for (int kk = 0; kk < 4; ++kk) {
    const int ks = w * 4 + kk;
#pragma unroll
    for (int h = 0; h < 2; ++h) {
      const char* a = smem + (32 * ks + 8 * g + q) * RS + h * 32 + 8 * p;
      const u32x4 o = cat2(tr_read(a), tr_read(a + 4 * RS));
#pragma unroll
      for (int j = 0; j < 4; ++j) cs[h] = dot2bf(o[j], 0x3f803f80u, cs[h]);
      *(u32x4*)(w1p + ((size_t)((2 * b + h) * 16 + ks) * 64 + lane) * 8) = o;
    }
  }
  float* red = (float*)(smem + 512 * RS);
#pragma unroll
  for (int h = 0; h < 2; ++h) {
    const float s = sum_x32(sum_x16(cs[h]));
    if (lane < 16) red[w * 32 + h * 16 + lane] = s;
  }
  __syncthreads();
  if (t < 32) s1[b * 32 + t] = (red[t] + red[32 + t]) + (red[64 + t] + red[96 + t]);
}

DEVI void pack_w2(int b, const u16* w2, u16* w2p, char* smem) {
  const int t = threadIdx.x, lane = t & 63, w = t >> 6;
  constexpr int RS = 1024 + 32;
  u32x4 v[8];
#pragma unroll
  for (int r = 0; r < 8; ++r) v[r] = *(const u32x4*)(w2 + (size_t)(b * 32 + w * 8 + r) * DM + lane * 8);
#pragma unroll
  for (int r = 0; r < 8; ++r) *(u32x4*)(smem + (w * 8 + r) * RS + lane * 16) = v[r];
  __syncthreads();
  const int g = lane >> 4, q = (lane & 15) >> 2, p = lane & 3;
#pragma unroll
  for (int cf = 0; cf < 8; ++cf) {
    const int nf = w * 8 + cf;
    const char* a = smem + (4 * g + q) * RS + nf * 32 + 8 * p;
    const u32x4 o = cat2(tr_read(a), tr_read(a + 16 * RS));
    *(u32x4*)(w2p + ((size_t)(nf * 56 + b) * 64 + lane) * 8) = o;
  }
}

constexpr int PREP_LDS = 512 * 96 + 512;
__global__ void __launch_bounds__(256) prep_kernel(const u16* x, const u16* lar, const u16* aim, const u16* cre,
                                                   const u16* cim, const u16* ldt, const u16* w1, const u16* w2,
                                                   char* ws) {
  extern __shared__ __attribute__((aligned(16))) char smem[];
  const int b = blockIdx.x;
  if (b < 64) xpose_stats(b, x, (u16*)(ws + WS_XT0), (i64*)(ws + WS_ST0), (i64*)(ws + WS_ST2), smem);
  else if (b < 64 + 512) kgen(b - 64, lar, aim, cre, cim, ldt, (u16*)(ws + WS_KT), (float*)(ws + WS_KSC), smem);
  else if (b < 576 + 56) pack_w1(b - 576, w1, (u16*)(ws + WS_W1P), (float*)(ws + WS_S1), smem);
  else pack_w2(b - 632, w2, (u16*)(ws + WS_W2P), smem);
}

constexpr int CV_KC = 4160;
constexpr int CV_U = 4 * CV_KC;
constexpr int CV_UC = 4608;
constexpr int CV_RED = CV_U + 2 * CV_UC;
constexpr int CONV_LDS = CV_RED + 4 * 8192;

__global__ void __launch_bounds__(256) conv_kernel(const u16* xin, u16* xout, const i64* stats, const u16* ktab,
                                                   const float* ksc, const u16* dskip) {
  extern __shared__ __attribute__((aligned(16))) char smem[];
  const int t = threadIdx.x, lane = t & 63;
  const int w = __builtin_amdgcn_readfirstlane(t >> 6);
  const int c0 = blockIdx.x * 2;
  {
    const u16* src = ktab + (size_t)(c0 + (w >> 1)) * 4096 + (w & 1) * 2048 + lane * 8;
    const u32 dst = lds_addr(smem) + w * CV_KC;
#pragma unroll
    for (int i = 0; i < 4; ++i) glds16(src + i * 512, dst + i * 1024);
  }
  u32x4 xv[2];
  xv[0] = *(const u32x4*)(xin + (size_t)c0 * SEQ + t * 8);
  xv[1] = *(const u32x4*)(xin + (size_t)(c0 + 1) * SEQ + t * 8);
  i64x2 sv[8];
#pragma unroll
  for (int j = 0; j < 8; ++j) sv[j] = *(const i64x2*)(stats + 2 * (t * 8 + j));
  const float dsk0 = bf2f(dskip[c0]), dsk1 = bf2f(dskip[c0 + 1]);
  const float ks0 = ksc[c0], ks1 = ksc[c0 + 1];
  if (t < 16) *(u32x4*)(smem + (t >> 2) * CV_KC + 4096 + (t & 3) * 16) = u32x4{0, 0, 0, 0};
  if (t >= 64 && t < 128) *(u32x4*)(smem + CV_U + ((t - 64) >> 5) * CV_UC + (t & 31) * 16) = u32x4{0, 0, 0, 0};
  u32x4 ud[2], uh[2];
#pragma unroll
  for (int jp = 0; jp < 4; ++jp) {
    float u0[2], u1[2];
#pragma unroll
    for (int h = 0; h < 2; ++h) {
      const int j = 2 * jp + h;
      const float m = fx2f(sv[j][0]) * (1.0f / DM);
      const float var = fmaxf(fx2f(sv[j][1]) * (1.0f / DM) - m * m, 0.0f);
      const float mb = rbf(m);
      const float sd = rbf(__builtin_sqrtf(rbf(rbf(var) + LN_EPS)));
      const float ri = 1.0f / sd;
      const float x0 = h ? hi_bf(xv[0][jp]) : lo_bf(xv[0][jp]);
      const float x1 = h ? hi_bf(xv[1][jp]) : lo_bf(xv[1][jp]);
      u0[h] = rbf(rbf(x0 - mb) * ri);
      u1[h] = rbf(rbf(x1 - mb) * ri);
    }
    uh[0][jp] = pack_h(u0[0], u0[1]); uh[1][jp] = pack_h(u1[0], u1[1]);
    ud[0][jp] = pack_bf(u0[0] * dsk0, u0[1] * dsk0); ud[1][jp] = pack_bf(u1[0] * dsk1, u1[1] * dsk1);
  }
  *(u32x4*)(smem + CV_U + 512 + t * 16) = uh[0];
  *(u32x4*)(smem + CV_U + CV_UC + 512 + t * 16) = uh[1];
  WAIT_VM(0);
  __syncthreads();

  const int r = lane & 15, g = lane >> 4, ch = w >> 1, mu0 = (w & 1) * 4;
  const char* ub = smem + CV_U + ch * CV_UC + 512 + 2 * (16 * r - 16 + 8 * g) - 64 * (mu0 + 3);
  const char* kb = smem + ch * 2 * CV_KC + ((r & 1) ? 0 : CV_KC) + 2 * (2031 - r + 8 * g - ((r & 1) ? 0 : 1)) - 64 * (mu0 + 3 + 56);
  f32x4 acc[8] = {};
#pragma unroll
  for (int mm = 0; mm < 4; ++mm) {
    u32x4 bf[8];
#pragma unroll
    for (int T = 0; T < 8; ++T) bf[T] = *(const u32x4*)(ub + 64 * (3 - mm) + 512 * T);
#pragma unroll
    for (int lam = 0; lam < 8; ++lam) {
      const u32* kp = (const u32*)(kb + 64 * (3 - mm) + 512 * (7 - lam));
      const u32x4 af = {kp[0], kp[1], kp[2], kp[3]};
#pragma unroll
      for (int T = lam; T < 8; ++T) acc[T] = mfma_h(af, bf[T - lam], acc[T]);
    }
  }
#pragma unroll
  for (int T = 0; T < 8; ++T) *(f32x4*)(smem + CV_RED + w * 8192 + T * 1024 + r * 64 + g * 16) = acc[T];
  __syncthreads();
#pragma unroll
  for (int c = 0; c < 2; ++c) {
    const char* p = smem + CV_RED + c * 16384 + t * 32;
    const f32x4 a0 = *(const f32x4*)p, a1 = *(const f32x4*)(p + 16);
    const f32x4 b0 = *(const f32x4*)(p + 8192), b1 = *(const f32x4*)(p + 8192 + 16);
    const float ks = c ? ks1 : ks0;
    float y[8];
#pragma unroll
    for (int j = 0; j < 4; ++j) { y[j] = (a0[j] + b0[j]) * ks; y[4 + j] = (a1[j] + b1[j]) * ks; }
    u32x4 o;
#pragma unroll
    for (int jp = 0; jp < 4; ++jp) {
      const u32 yb = pack_bf(y[2 * jp], y[2 * jp + 1]);
      const u32 s = pack_bf(lo_bf(yb) + lo_bf(ud[c][jp]), hi_bf(yb) + hi_bf(ud[c][jp]));
      o[jp] = pack_bf(lo_bf(xv[c][jp]) + lo_bf(s), hi_bf(xv[c][jp]) + hi_bf(s));
    }
    *(u32x4*)(xout + (size_t)(c0 + c) * SEQ + t * 8) = o;
  }
}

constexpr int M1_ST = 65536;
constexpr int MLP1_LDS = 65536 + 512;

DEVI float gelu_t(float x) {
  const float p = fmaf(x * x, -0.0713548162726f * 1.44269504088896f, -1.59576912160573f * 1.44269504088896f);
  const float e = __builtin_amdgcn_exp2f(x * p);
  return x * __builtin_amdgcn_rcpf(1.0f + e);
}

template <int NF>
DEVI void mlp1_body(const u16* xt, const u16* w1p, const float* s1, const u16* b1, u16* hb, char* smem,
                    int rp, int fs, int w, int lane) {
  const int f0 = (NF == 4) ? w * 4 : 8 + (w - 2) * 3;
  const int ffg = fs * 14 + f0;
  const int g = lane >> 4, c = lane & 15;
  f32x4 s1v[NF]; u32x2 b1v[NF];
#pragma unroll
  for (int j = 0; j < NF; ++j) {
    s1v[j] = *(const f32x4*)(s1 + (ffg + j) * 16 + 4 * g);
    b1v[j] = *(const u32x2*)(b1 + (ffg + j) * 16 + 4 * g);
  }
  u32x4 wf[NF][16];
  {
    const int row = lane >> 3;
    const int swz = (((row >> 1) & 1) << 1);
    const u16* wsrc = w1p + ((size_t)ffg * 16 * 64 + lane) * 8;
#pragma unroll
    for (int ck = 0; ck < 4; ++ck) {
#pragma unroll
      for (int i = 0; i < 4; ++i) {
        const int piece = ck * 16 + i * 4 + w;
        const int sw = swz | ((piece & 1) << 2);
        glds16(xt + (size_t)(piece * 8 + row) * SEQ + rp * 64 + ((lane & 7) ^ sw) * 8, lds_addr(smem) + piece * 1024);
      }
#pragma unroll
      for (int kq = 0; kq < 4; ++kq)
#pragma unroll
        for (int j = 0; j < NF; ++j) wf[j][ck * 4 + kq] = *(const u32x4*)(wsrc + ((size_t)j * 16 + ck * 4 + kq) * 512);
      __builtin_amdgcn_sched_barrier(0);
    }
  }
  f32x4 acc[NF][4] = {};
  f32x4 sa[2] = {}, sq[2] = {};
  const int q = c >> 2, p = lane & 3;
  const int sx = ((q >> 1) & 1) | ((g & 1) << 1);
  const char* xb[4];
#pragma unroll
  for (int i = 0; i < 4; ++i) xb[i] = smem + (8 * g + q) * 128 + ((i ^ sx) * 32) + 8 * p;
  const u32x4 ones = {0x3f803f80u, 0x3f803f80u, 0x3f803f80u, 0x3f803f80u};
#pragma unroll
  for (int ck = 0; ck < 4; ++ck) {
    if (NF == 4) {
      if (ck == 0) WAIT_VM_BAR(63); else if (ck == 1) WAIT_VM_BAR(56); else if (ck == 2) WAIT_VM_BAR(36); else WAIT_VM_BAR(16);
    } else {
      if (ck == 0) WAIT_VM_BAR(60); else if (ck == 1) WAIT_VM_BAR(44); else if (ck == 2) WAIT_VM_BAR(28); else WAIT_VM_BAR(12);
    }
#pragma unroll
    for (int kq = 0; kq < 4; ++kq) {
      const int ks = ck * 4 + kq;
      u32x4 xf[4];
#pragma unroll
      for (int i = 0; i < 4; ++i) xf[i] = cat2(tr_read(xb[i] + ks * 4096), tr_read(xb[i] + ks * 4096 + 512));
#pragma unroll
      for (int j = 0; j < NF; ++j)
#pragma unroll
        for (int i = 0; i < 4; ++i) acc[j][i] = mfma_bf(wf[j][ks], xf[i], acc[j][i]);
      if (NF == 3) {
        const u32x4 xa = (w == 2) ? xf[0] : xf[2], xc = (w == 2) ? xf[1] : xf[3];
        sa[0] = mfma_bf(ones, xa, sa[0]); sq[0] = mfma_bf(xa, xa, sq[0]);
        sa[1] = mfma_bf(ones, xc, sa[1]); sq[1] = mfma_bf(xc, xc, sq[1]);
      }
    }
  }
  f32x2* st = (f32x2*)(smem + M1_ST);
  if (NF == 3) {
#pragma unroll
    for (int h = 0; h < 2; ++h) {
      if (q == g) {
        const float ss = p == 0 ? sq[h][0] : (p == 1 ? sq[h][1] : (p == 2 ? sq[h][2] : sq[h][3]));
        const float m = sa[h][0] * (1.0f / DM);
        const float var = fmaxf(ss * (1.0f / DM) - m * m, 0.0f);
        const float mb = rbf(m);
        const float sd = rbf(__builtin_sqrtf(rbf(rbf(var) + LN_EPS)));
        const float ri = 1.0f / sd;
        const float m2 = rbf((m - mb) * ri);
        const float sd2 = rbf(__builtin_sqrtf(rbf(rbf(var * ri * ri) + LN_EPS)));
        const float ri2 = 1.0f / sd2;
        f32x2 o = {ri * ri2, (mb * ri + m2) * ri2};
        st[((w - 2) * 2 + h) * 16 + c] = o;
      }
    }
  }
  __syncthreads();
#pragma unroll
  for (int i = 0; i < 4; ++i) {
    const f32x2 cs = st[i * 16 + c];
    u32x2 hv[NF];
#pragma unroll
    for (int j = 0; j < NF; ++j) {
      float v[4];
#pragma unroll
      for (int r = 0; r < 4; ++r) {
        const float bb = (r & 1) ? hi_bf(b1v[j][r >> 1]) : lo_bf(b1v[j][r >> 1]);
        v[r] = gelu_t(fmaf(acc[j][i][r], cs[0], fmaf(-cs[1], s1v[j][r], bb)));
      }
      hv[j][0] = pack_bf(v[0], v[1]); hv[j][1] = pack_bf(v[2], v[3]);
    }
    u16* hp = hb + ((size_t)((rp * 4 + i) * 56 + fs * 7) * 64 + lane) * 8;
    if (NF == 4) {
      *(u32x4*)(hp + (size_t)(2 * w) * 512) = cat2(hv[0], hv[1]);
      *(u32x4*)(hp + (size_t)(2 * w + 1) * 512) = cat2(hv[2], hv[3]);
    } else if (w == 2) {
      *(u32x4*)(hp + (size_t)4 * 512) = cat2(hv[0], hv[1]);
      *(u32x2*)(hp + (size_t)5 * 512) = hv[2];
    } else {
      *(u32x2*)(hp + (size_t)5 * 512 + 4) = hv[0];
      *(u32x4*)(hp + (size_t)6 * 512) = cat2(hv[1], hv[2]);
    }
  }
}

__global__ void __launch_bounds__(256) mlp1_kernel(const u16* xt, const u16* w1p, const float* s1, const u16* b1, u16* hb) {
  extern __shared__ __attribute__((aligned(16))) char smem[];
  const int lane = threadIdx.x & 63;
  const int w = __builtin_amdgcn_readfirstlane(threadIdx.x >> 6);
  const int b = blockIdx.x;
  const int rp = (b & 7) * 4 + ((b >> 3) & 3), fs = b >> 5;
  if (w < 2) mlp1_body<4>(xt, w1p, s1, b1, hb, smem, rp, fs, w, lane);
  else mlp1_body<3>(xt, w1p, s1, b1, hb, smem, rp, fs, w, lane);
}

constexpr int M2_X = 49152;
constexpr int MLP2_LDS = 49152 + 8192;

template <bool LAST>
__global__ void __launch_bounds__(256) mlp2_kernel(const u16* hb, const u16* w2p, const u16* b2, const u16* xres,
                                                   u16* xout, i64* stats) {
  extern __shared__ __attribute__((aligned(16))) char smem[];
  const int t = threadIdx.x, lane = t & 63;
  const int w = __builtin_amdgcn_readfirstlane(t >> 6);
  const int b = blockIdx.x;
  const int rp = (b & 7) * 4 + ((b >> 3) & 3), ct = b >> 5;
  const int g = lane >> 4, c = lane & 15;
  const int chn = ct * 64 + w * 16 + c;
  u32x2 xr[4];
#pragma unroll
  for (int i = 0; i < 4; ++i) xr[i] = *(const u32x2*)(xres + (size_t)chn * SEQ + rp * 64 + i * 16 + 4 * g);
  const float bb = bf2f(b2[chn]);
  const u16* hp = hb + ((size_t)(rp * 4 * 56 + w * 14) * 64 + lane) * 8;
  const u16* wp = w2p + ((size_t)(ct * 4 * 56 + w * 14) * 64 + lane) * 8;
  constexpr int PF = 6;
  u32x4 ha[14][4], wb[14][4];
  f32x4 acc[4][4] = {};
#pragma unroll
  for (int s = 0; s < PF; ++s) {
#pragma unroll
    for (int i = 0; i < 4; ++i) {
      wb[s][i] = *(const u32x4*)(wp + ((size_t)i * 56 + s) * 512);
      ha[s][i] = *(const u32x4*)(hp + ((size_t)i * 56 + s) * 512);
    }
  }
  __builtin_amdgcn_sched_barrier(0);
#pragma unroll
  for (int s = 0; s < 14; ++s) {
    if (s + PF < 14) {
#pragma unroll
      for (int i = 0; i < 4; ++i) {
        wb[s + PF][i] = *(const u32x4*)(wp + ((size_t)i * 56 + s + PF) * 512);
        ha[s + PF][i] = *(const u32x4*)(hp + ((size_t)i * 56 + s + PF) * 512);
      }
    }
    __builtin_amdgcn_sched_barrier(0);
#pragma unroll
    for (int i = 0; i < 4; ++i)
#pragma unroll
      for (int j = 0; j < 4; ++j) acc[i][j] = mfma_bf(ha[s][i], wb[s][j], acc[i][j]);
    __builtin_amdgcn_sched_barrier(0);
  }
#pragma unroll
  for (int j = 0; j < 4; ++j) {
    if (j != w) {
      const int rank = (w - j - 1) & 3;
#pragma unroll
      for (int i = 0; i < 4; ++i) *(f32x4*)(smem + ((j * 3 + rank) * 4 + i) * 1024 + lane * 16) = acc[i][j];
    }
  }
  __syncthreads();
  f32x4 o[4];
#pragma unroll
  for (int i = 0; i < 4; ++i) o[i] = w == 0 ? acc[i][0] : (w == 1 ? acc[i][1] : (w == 2 ? acc[i][2] : acc[i][3]));
#pragma unroll
  for (int rk = 0; rk < 3; ++rk)
#pragma unroll
    for (int i = 0; i < 4; ++i) o[i] += *(const f32x4*)(smem + ((w * 3 + rk) * 4 + i) * 1024 + lane * 16);
  {
    const int row = w * 16 + c;
    const int sx = ((row >> 1) & 1) | (((row >> 3) & 1) << 1);
#pragma unroll
    for (int i = 0; i < 4; ++i) {
      u32x2 r;
#pragma unroll
      for (int h = 0; h < 2; ++h) {
        const u32 a = pack_bf(o[i][2 * h], o[i][2 * h + 1]);
        const u32 s = pack_bf(lo_bf(a) + bb, hi_bf(a) + bb);
        r[h] = pack_bf(lo_bf(xr[i][h]) + lo_bf(s), hi_bf(xr[i][h]) + hi_bf(s));
      }
      *(u32x2*)(smem + M2_X + row * 128 + ((i ^ sx) * 32) + g * 8) = r;
    }
  }
  __syncthreads();
  const int q = c >> 2, p = lane & 3;
  const int sx = ((q >> 1) & 1) | ((g & 1) << 1);
  const char* xb = smem + M2_X + (8 * g + q) * 128 + ((w ^ sx) * 32) + 8 * p;
  const u32x4 f0 = cat2(tr_read(xb), tr_read(xb + 512));
  const u32x4 f1 = cat2(tr_read(xb + 4096), tr_read(xb + 4096 + 512));
  if (LAST) {
    u16* op = xout + (size_t)(rp * 64 + w * 16 + c) * DM + ct * 64 + 8 * g;
    *(u32x4*)op = f0;
    *(u32x4*)(op + 32) = f1;
  } else {
#pragma unroll
    for (int h = 0; h < 2; ++h) {
      const int row = h * 32 + (t >> 3);
      const int sr = (((row >> 1) & 1) | (((row >> 3) & 1) << 1)) << 1;
      const u32x4 v = *(const u32x4*)(smem + M2_X + row * 128 + (((t & 7) ^ sr) * 16));
      *(u32x4*)(xout + (size_t)(ct * 64 + row) * SEQ + rp * 64 + (t & 7) * 8) = v;
    }
    float s = 0.f, ss = 0.f;
#pragma unroll
    for (int j = 0; j < 4; ++j) {
      s = dot2bf(f0[j], 0x3f803f80u, s); ss = dot2bf(f0[j], f0[j], ss);
      s = dot2bf(f1[j], 0x3f803f80u, s); ss = dot2bf(f1[j], f1[j], ss);
    }
    s = sum_x32(sum_x16(s)); ss = sum_x32(sum_x16(ss));
    if (lane < 32) {
      const float v = lane < 16 ? s : ss;
      atomicAdd((unsigned long long*)(stats + 2 * (rp * 64 + w * 16 + c) + (lane >> 4)), (unsigned long long)f2fx(v));
    }
  }
}

extern "C" void kernel_launch(void* const* d_in, const int* in_sizes, int n_in, void* d_out, int out_size,
                              void* d_ws, size_t ws_size, hipStream_t stream) {
  (void)in_sizes; (void)n_in; (void)out_size; (void)ws_size;
  const u16* u = (const u16*)d_in[0];
  const u16* lar = (const u16*)d_in[1];
  const u16* aim = (const u16*)d_in[2];
  const u16* cre = (const u16*)d_in[3];
  const u16* cim = (const u16*)d_in[4];
  const u16* ldt = (const u16*)d_in[5];
  const u16* dsk = (const u16*)d_in[6];
  const u16* w1 = (const u16*)d_in[8];
  const u16* b1 = (const u16*)d_in[9];
  const u16* w2 = (const u16*)d_in[10];
  const u16* b2 = (const u16*)d_in[11];
  char* ws = (char*)d_ws;
  (void)hipFuncSetAttribute((const void*)mlp1_kernel, hipFuncAttributeMaxDynamicSharedMemorySize, MLP1_LDS);
  u16* xt0 = (u16*)(ws + WS_XT0); u16* xt1 = (u16*)(ws + WS_XT1);
  u16* xt2 = (u16*)(ws + WS_XT2); u16* xt3 = (u16*)(ws + WS_XT3);
  const u16* kt = (const u16*)(ws + WS_KT); const float* ksc = (const float*)(ws + WS_KSC);
  const u16* w1p = (const u16*)(ws + WS_W1P); const u16* w2p = (const u16*)(ws + WS_W2P);
  const float* s1 = (const float*)(ws + WS_S1);
  u16* hb = (u16*)(ws + WS_H);
  i64* st0 = (i64*)(ws + WS_ST0); i64* st2 = (i64*)(ws + WS_ST2);
  prep_kernel<<<688, 256, PREP_LDS, stream>>>(u, lar, aim, cre, cim, ldt, w1, w2, ws);
  conv_kernel<<<256, 256, CONV_LDS, stream>>>(xt0, xt1, st0, kt, ksc, dsk);
  mlp1_kernel<<<256, 256, MLP1_LDS, stream>>>(xt1, w1p, s1, b1, hb);
  mlp2_kernel<false><<<256, 256, MLP2_LDS, stream>>>(hb, w2p, b2, xt1, xt2, st2);
  conv_kernel<<<256, 256, CONV_LDS, stream>>>(xt2, xt3, st2, kt, ksc, dsk);
  mlp1_kernel<<<256, 256, MLP1_LDS, stream>>>(xt3, w1p, s1, b1, hb);
  mlp2_kernel<true><<<256, 256, MLP2_LDS, stream>>>(hb, w2p, b2, xt3, (u16*)d_out, st2);
}
```

```cpp
#include <hip/hip_runtime.h>
#include <hip/hip_bf16.h>

typedef unsigned short u16;
typedef unsigned int u32;
typedef long long i64;
typedef __attribute__((ext_vector_type(4))) short s16x4;
typedef __attribute__((ext_vector_type(8))) __bf16 bf16x8;
typedef __attribute__((ext_vector_type(2))) __bf16 bf16x2;
typedef __attribute__((ext_vector_type(8))) _Float16 f16x8;
typedef __attribute__((ext_vector_type(2))) _Float16 f16x2;
typedef __attribute__((ext_vector_type(4))) float f32x4;
typedef __attribute__((ext_vector_type(2))) float f32x2;
typedef __attribute__((ext_vector_type(4))) unsigned u32x4;
typedef __attribute__((ext_vector_type(2))) unsigned u32x2;
typedef __attribute__((ext_vector_type(2))) long long i64x2;
typedef __attribute__((address_space(1))) unsigned gu32;
typedef __amdgpu_buffer_rsrc_t rsrc_t;
#define LAS __attribute__((address_space(3)))
#define DEVI __device__ __forceinline__
template <int V> struct IC { static constexpr int v = V; };
template <int I, int N, class F> __device__ __forceinline__ void sfor(F&& f) {
  if constexpr (I < N) { f(IC<I>{}); sfor<I + 1, N>(f); }
}
#define RLX_AGENT __ATOMIC_RELAXED, __HIP_MEMORY_SCOPE_AGENT

constexpr int SEQ = 2048, DM = 512, NST = 128, FF = 1792;
constexpr float LN_EPS = 1e-5f;
constexpr float FX_SCALE = 16777216.0f;
constexpr float FX_INV = 1.0f / 16777216.0f;
constexpr int SC1 = 16;

constexpr int MB = 1024 * 1024;
constexpr int WS_X = 0;
constexpr int WS_KT = 8 * MB;
constexpr int WS_W1P = 12 * MB;
constexpr int WS_W2P = 14 * MB;
constexpr int WS_H = 16 * MB;
constexpr int WS_ST = 32 * MB;
constexpr int WS_S1 = 32 * MB + 131072;
constexpr int WS_KSC = 32 * MB + 196608;
constexpr int WS_CT = 32 * MB + 262144;
constexpr int WS_ONE = 32 * MB + 393216;
constexpr int WS_END = 33 * MB;
constexpr int CT_C = 0, CT_X = 128, CT_H = 256, CT_TMO = 288, CT_LINES = 289;
constexpr int SPIN_CAP = 1 << 16;

#define STAMP(k, i) do {} while (0)

DEVI float bf2f(u16 v) { return __uint_as_float((u32)v << 16); }
DEVI float lo_bf(u32 w) { return __uint_as_float(w << 16); }
DEVI float hi_bf(u32 w) { return __uint_as_float(w & 0xffff0000u); }
DEVI float rbf(float x) { return (float)(__bf16)x; }
DEVI u32 pack_bf(float a, float b) { f32x2 q = {a, b}; return __builtin_bit_cast(u32, __builtin_convertvector(q, bf16x2)); }
DEVI u32 pack_h(float a, float b) { f32x2 q = {a, b}; return __builtin_bit_cast(u32, __builtin_convertvector(q, f16x2)); }
DEVI float dot2bf(u32 a, u32 b, float c) {
  return __builtin_amdgcn_fdot2_f32_bf16(__builtin_bit_cast(bf16x2, a), __builtin_bit_cast(bf16x2, b), c, false);
}
DEVI f32x4 mfma_bf(u32x4 a, u32x4 b, f32x4 c) {
  return __builtin_amdgcn_mfma_f32_16x16x32_bf16(__builtin_bit_cast(bf16x8, a), __builtin_bit_cast(bf16x8, b), c, 0, 0, 0);
}
DEVI f32x4 mfma_h(u32x4 a, u32x4 b, f32x4 c) {
  return __builtin_amdgcn_mfma_f32_16x16x32_f16(__builtin_bit_cast(f16x8, a), __builtin_bit_cast(f16x8, b), c, 0, 0, 0);
}
DEVI u32x2 tr_read(const char* p) {
  return __builtin_bit_cast(u32x2, __builtin_amdgcn_ds_read_tr16_b64_v4i16((LAS s16x4*)p));
}
DEVI u32x4 cat2(u32x2 a, u32x2 b) { return __builtin_shufflevector(a, b, 0, 1, 2, 3); }
DEVI u32 lds_addr(const void* p) { return (u32)(size_t)(const LAS char*)p; }
#define WAIT_VM(n) asm volatile("s_waitcnt vmcnt(" #n ")" ::: "memory")
#define WAIT_VM_BAR(n) asm volatile("s_waitcnt vmcnt(" #n ")\n\ts_barrier" ::: "memory")
#define SCHED() __builtin_amdgcn_sched_barrier(0)
template <int CTRL> DEVI float dpp_add(float v) {
  return v + __uint_as_float(__builtin_amdgcn_update_dpp(0u, __float_as_uint(v), CTRL, 0xf, 0xf, true));
}
DEVI float sum_x16(float v) {
  auto r = __builtin_amdgcn_permlane16_swap(__float_as_uint(v), __float_as_uint(v), false, false);
  return __uint_as_float(r[0]) + __uint_as_float(r[1]);
}
DEVI float sum_x32(float v) {
  auto r = __builtin_amdgcn_permlane32_swap(__float_as_uint(v), __float_as_uint(v), false, false);
  return __uint_as_float(r[0]) + __uint_as_float(r[1]);
}
DEVI float wave_sum(float v) {
  v = dpp_add<0xB1>(v); v = dpp_add<0x4E>(v); v = dpp_add<0x141>(v); v = dpp_add<0x140>(v);
  return sum_x32(sum_x16(v));
}
DEVI float fx2f(u32 lo, u32 hi) { return fmaf((float)(int)hi, 4294967296.0f * FX_INV, (float)lo * FX_INV); }
DEVI i64 f2fx(float v) { return (i64)(v * FX_SCALE); }
DEVI float sel4(int i, float a, float b, float c, float d) {
  asm volatile("" : "+v"(a), "+v"(b), "+v"(c), "+v"(d));
  return (i & 2) ? ((i & 1) ? d : c) : ((i & 1) ? b : a);
}
DEVI float rcp(float x) { return __builtin_amdgcn_rcpf(x); }
DEVI float sqrt_(float x) { return __builtin_amdgcn_sqrtf(x); }

DEVI int cidx(int l) {
  const int hb = (int)(pack_bf((float)l, 0.f) & 0xffffu) - (0x4300 - 128);
  return l < 128 ? l : hb;
}
DEVI f32x2 cmul(f32x2 a, f32x2 b) {
  f32x2 r = {fmaf(a[0], b[0], -a[1] * b[1]), fmaf(a[0], b[1], a[1] * b[0])};
  return r;
}
DEVI f32x2 cexp_(float a2, float br, float p) {
  const float e = __builtin_amdgcn_exp2f(a2 * p), f = __builtin_amdgcn_fractf(br * p);
  f32x2 r = {e * __builtin_amdgcn_cosf(f), e * __builtin_amdgcn_sinf(f)};
  return r;
}

constexpr int KG_RS = 528, KG_X = 16 * KG_RS, KG_KD = 64 * KG_RS, KG_RED = KG_KD + 1296;
DEVI void kgen(int d, const u16* lar, const u16* aim, const u16* cre, const u16* cim, const u16* ldt,
               u16* ktab, float* ksc, char* smem) {
  const int t = threadIdx.x, lane = t & 63, n = t & 127;
  const int w = __builtin_amdgcn_readfirstlane(t >> 6);
  u16* kd = (u16*)(smem + KG_KD);
  float* red = (float*)(smem + KG_RED);
  const u16 r0 = ldt[d], r1 = lar[d * NST + n], r2 = aim[d * NST + n], r3 = cre[d * NST + n], r4 = cim[d * NST + n];
  const float dt = rbf(expf(bf2f(r0)));
  const float cr = bf2f(r3) * dt, ci = bf2f(r4) * dt;
  if (w < 2) {
    const float s = wave_sum(2.0f * sqrt_(cr * cr + ci * ci));
    if (lane == 0) red[w] = s;
  }
  const float a2 = (-rbf(expf(bf2f(r1))) * dt) * 1.44269504088896f;
  const float br = (bf2f(r2) * dt) * 0.159154943091895f;
  const f32x2 e128 = cexp_(a2, br, 128.f);
  char* yp = smem + n * 4;
  char* xp = smem + KG_X + n * 4;
  if (w < 2) {
    const f32x2 e8 = cexp_(a2, br, 8.f);
    f32x2 y = {1.f, 0.f};
#pragma unroll
    for (int b = 0; b < 16; ++b) { *(u32*)(yp + b * KG_RS) = pack_h(y[0], y[1]); y = cmul(y, e8); }
  }
  __syncthreads();
  const float S = red[0] + red[1];
  int e = 0;
  (void)frexpf(S, &e);
  e = (S > 0.0f && S < 3.0e38f) ? e : 14;
  e = max(-100, min(100, e));
  const float scale = ldexpf(2.0f, 14 - e);
  if (t == 0) ksc[d] = ldexpf(1.0f, e - 14);
  f32x2 f = {cr * scale, ci * scale};
  if (w < 2) {
    f = cmul(f, cexp_(a2, br, 512.f));
    const f32x2 e4 = cexp_(a2, br, 4.f);
#pragma unroll
    for (int a = 0; a < 4; ++a) {
      const f32x2 g = cmul(f, e4);
      *(u32*)(xp + (24 + 2 * a) * KG_RS) = pack_h(f[0], -f[1]);
      *(u32*)(xp + (25 + 2 * a) * KG_RS) = pack_h(g[0], -g[1]);
      f = cmul(f, e128);
    }
#pragma unroll
    for (int a = 0; a < 9; ++a) { *(u32*)(xp + (32 + a) * KG_RS) = pack_h(f[0], -f[1]); f = cmul(f, e128); }
  } else {
    const f32x2 e1 = cexp_(a2, br, 1.f), e2 = cexp_(a2, br, 2.f);
#pragma unroll
    for (int a = 0; a < 2; ++a) {
      f32x2 g = f;
#pragma unroll
      for (int r = 0; r < 8; ++r) { *(u32*)(xp + (8 * a + r) * KG_RS) = pack_h(g[0], -g[1]); if (r < 7) g = cmul(g, e1); }
      f = cmul(f, e128);
    }
#pragma unroll
    for (int a = 0; a < 2; ++a) {
      f32x2 g = f;
#pragma unroll
      for (int r = 0; r < 4; ++r) { *(u32*)(xp + (16 + 4 * a + r) * KG_RS) = pack_h(g[0], -g[1]); if (r < 3) g = cmul(g, e2); }
      f = cmul(f, e128);
    }
  }
  __syncthreads();
  if (w < 3) {
    const int i = lane & 15, g = lane >> 4;
    const char* ya = smem + i * KG_RS + g * 16;
    const char* xa = smem + KG_X + (16 * w + i) * KG_RS + g * 16;
    f32x4 acc = {};
#pragma unroll
    for (int ks = 0; ks < 8; ++ks) acc = mfma_h(*(const u32x4*)(ya + ks * 64), *(const u32x4*)(xa + ks * 64), acc);
    const int h1 = i < 8 ? 256 + 128 * (i >> 2) + 2 * (i & 3) : 128 * (i >> 1) + 4 * (i & 1);
    const int hp = w == 0 ? i + (i & 8) * 15 : (w == 1 ? h1 : 1024 + 128 * i);
#pragma unroll
    for (int r = 0; r < 4; ++r) {
      const int pos = hp + 8 * (4 * g + r);
      kd[pos <= 2048 ? cidx(pos) : 644] = __builtin_bit_cast(u16, (_Float16)acc[r]);
    }
  }
  __syncthreads();
  u32 v[9];
#pragma unroll
  for (int e2 = 0; e2 < 9; ++e2) {
    const int l = 2047 - 8 * t - e2;
    v[e2] = kd[cidx(max(l, 0))];
  }
  if (t == 255) v[8] = 0u;
  u32x4 oe, oo;
#pragma unroll
  for (int j = 0; j < 4; ++j) { oe[j] = v[2 * j] | (v[2 * j + 1] << 16); oo[j] = v[2 * j + 1] | (v[2 * j + 2] << 16); }
  u32x4* dst = (u32x4*)(ktab + (size_t)d * 4096);
  dst[t] = oe;
  dst[256 + t] = oo;
}

DEVI void xpose_stats(int b, const u16* x, char* xt, i64* st0, i64* st2, char* smem) {
  const int t = threadIdx.x, lane = t & 63, w = t >> 6;
  const int t0 = b * 32;
  constexpr int RS = 1024 + 32;
  u32x4 v[8];
#pragma unroll
  for (int r = 0; r < 8; ++r) v[r] = *(const u32x4*)(x + (size_t)(t0 + w * 8 + r) * DM + lane * 8);
  float sv = 0.f;
#pragma unroll
  for (int r = 0; r < 8; ++r) {
    *(u32x4*)(smem + (w * 8 + r) * RS + lane * 16) = v[r];
    float s = 0.f, ss = 0.f;
#pragma unroll
    for (int j = 0; j < 4; ++j) { s = dot2bf(v[r][j], 0x3f803f80u, s); ss = dot2bf(v[r][j], v[r][j], ss); }
    s = wave_sum(s); ss = wave_sum(ss);
    sv = lane == 2 * r ? s : (lane == 2 * r + 1 ? ss : sv);
  }
  if (lane < 16) {
    st0[2 * (t0 + w * 8) + lane] = f2fx(sv);
    st2[2 * (t0 + w * 8) + lane] = 0;
  }
  __syncthreads();
  const int g = lane >> 4, q = (lane & 15) >> 2, p = lane & 3;
#pragma unroll
  for (int cf = 0; cf < 8; ++cf) {
    const int c0 = (w * 8 + cf) * 16;
    const char* a = smem + (8 * g + q) * RS + c0 * 2 + 8 * p;
    const u32x4 o = cat2(tr_read(a), tr_read(a + 4 * RS));
    *(u32x4*)(xt + (b >> 1) * 65536 + (c0 + (lane & 15)) * 128 + (b & 1) * 64 + g * 16) = o;
  }
}

DEVI void pack_w1(int b, const u16* w1, u16* w1p, float* s1, char* smem) {
  const int t = threadIdx.x, lane = t & 63, w = t >> 6;
  constexpr int RS = 64 + 32;
  u32x4 v[8];
#pragma unroll
  for (int it = 0; it < 8; ++it) v[it] = *(const u32x4*)(w1 + (size_t)(it * 64 + (t >> 2)) * FF + b * 32 + (t & 3) * 8);
#pragma unroll
  for (int it = 0; it < 8; ++it) *(u32x4*)(smem + (it * 64 + (t >> 2)) * RS + (t & 3) * 16) = v[it];
  __syncthreads();
  const int g = lane >> 4, q = (lane & 15) >> 2, p = lane & 3;
  float cs[2] = {0.f, 0.f};
#pragma unroll
  for (int kk = 0; kk < 4; ++kk) {
    const int ks = w * 4 + kk;
#pragma unroll
    for (int h = 0; h < 2; ++h) {
      const char* a = smem + (32 * ks + 8 * g + q) * RS + h * 32 + 8 * p;
      const u32x4 o = cat2(tr_read(a), tr_read(a + 4 * RS));
#pragma unroll
      for (int j = 0; j < 4; ++j) cs[h] = dot2bf(o[j], 0x3f803f80u, cs[h]);
      *(u32x4*)(w1p + ((size_t)((2 * b + h) * 16 + ks) * 64 + lane) * 8) = o;
    }
  }
  float* red = (float*)(smem + 512 * RS);
#pragma unroll
  for (int h = 0; h < 2; ++h) {
    const float s = sum_x32(sum_x16(cs[h]));
    if (lane < 16) red[w * 32 + h * 16 + lane] = s;
  }
  __syncthreads();
  if (t < 32) s1[b * 32 + t] = (red[t] + red[32 + t]) + (red[64 + t] + red[96 + t]);
}

DEVI void pack_w2(int b, const u16* w2, u16* w2p, char* smem) {
  const int t = threadIdx.x, lane = t & 63, w = t >> 6;
  constexpr int RS = 1024 + 32;
  u32x4 v[8];
#pragma unroll
  for (int r = 0; r < 8; ++r) v[r] = *(const u32x4*)(w2 + (size_t)(b * 32 + w * 8 + r) * DM + lane * 8);
#pragma unroll
  for (int r = 0; r < 8; ++r) *(u32x4*)(smem + (w * 8 + r) * RS + lane * 16) = v[r];
  __syncthreads();
  const int g = lane >> 4, q = (lane & 15) >> 2, p = lane & 3;
#pragma unroll
  for (int cf = 0; cf < 8; ++cf) {
    const int nf = w * 8 + cf;
    const char* a = smem + (4 * g + q) * RS + nf * 32 + 8 * p;
    const u32x4 o = cat2(tr_read(a), tr_read(a + 16 * RS));
    *(u32x4*)(w2p + ((size_t)(nf * 56 + b) * 64 + lane) * 8) = o;
  }
}

constexpr int PREP_LDS = 512 * 96 + 512;
__global__ void __launch_bounds__(256) prep_kernel(const u16* x, const u16* lar, const u16* aim, const u16* cre,
                                                   const u16* cim, const u16* ldt, const u16* w1, const u16* w2,
                                                   char* ws) {
  extern __shared__ __attribute__((aligned(16))) char smem[];
  const int b = blockIdx.x;
  STAMP(0, 0);
  if (b < 64) xpose_stats(b, x, ws + WS_X, (i64*)(ws + WS_ST), (i64*)(ws + WS_ST + 65536), smem);
  else if (b < 120) pack_w1(b - 64, w1, (u16*)(ws + WS_W1P), (float*)(ws + WS_S1), smem);
  else if (b < 176) {
    if (b < 122) { const int i = (b - 120) * 256 + threadIdx.x; if (i < CT_LINES) *(u32*)(ws + WS_CT + i * 256) = 0u; }
    if (b == 122) *(u32x4*)(ws + WS_ONE + threadIdx.x * 16) = u32x4{0x3f803f80u, 0x3f803f80u, 0x3f803f80u, 0x3f803f80u};
    pack_w2(b - 120, w2, (u16*)(ws + WS_W2P), smem);
  } else kgen(b - 176, lar, aim, cre, cim, ldt, (u16*)(ws + WS_KT), (float*)(ws + WS_KSC), smem);
  STAMP(0, (b < 64 ? 4 : (b < 120 ? 6 : (b < 176 ? 7 : 5))));
}

constexpr int L_KC = 4160;
constexpr int L_U = 4 * L_KC, L_UC = 4608;
constexpr int L_XA = 26112, L_XB = L_XA + 65536;
constexpr int L_MISC = L_XB + 65536;
constexpr int L_CST = L_MISC + 512 + 4 * 512;
constexpr int L_SIG = L_CST + 4 * 256;
constexpr int MAIN_LDS = L_SIG + 16;
constexpr int L_RED = L_XB, L_SCR = L_XB + 16384;

DEVI u32x4 ld128(rsrc_t rs, int v, int s) { return __builtin_amdgcn_raw_buffer_load_b128(rs, v, s, SC1); }
DEVI u32x2 ld64(rsrc_t rs, int v, int s) { return __builtin_amdgcn_raw_buffer_load_b64(rs, v, s, SC1); }
DEVI u32x4 ld128p(rsrc_t rs, int v, int s) { return __builtin_amdgcn_raw_buffer_load_b128(rs, v, s, 0); }
DEVI void st128(rsrc_t rs, int v, int s, u32x4 d) { __builtin_amdgcn_raw_buffer_store_b128(d, rs, v, s, SC1); }
DEVI void st64(rsrc_t rs, int v, int s, u32x2 d) { __builtin_amdgcn_raw_buffer_store_b64(d, rs, v, s, SC1); }
template <int OFF> DEVI u32x4 ld128a(rsrc_t rs, int v, int s) {
  u32x4 r;
  asm volatile("buffer_load_dwordx4 %0, %1, %2, %3 offen offset:%4" : "=a"(r) : "v"(v), "s"(rs), "s"(s), "i"(OFF) : "memory");
  return r;
}
template <int OFF> DEVI u32x4 ld128v(rsrc_t rs, int v, int s) {
  u32x4 r;
  asm volatile("buffer_load_dwordx4 %0, %1, %2, %3 offen offset:%4" : "=v"(r) : "v"(v), "s"(rs), "s"(s), "i"(OFF) : "memory");
  return r;
}
DEVI void dma4k(rsrc_t rs, u32 lds_dst, int ve, int vo, int soff) {
  u32 keep;
  asm volatile("s_mov_b32 %0, m0\n\ts_mov_b32 m0, %1\n\ts_nop 3\n\t"
               "buffer_load_dwordx4 %2, %4, %5 offen sc1 lds\n\t"
               "buffer_load_dwordx4 %3, %4, %5 offen offset:1024 sc1 lds\n\t"
               "buffer_load_dwordx4 %2, %4, %5 offen offset:2048 sc1 lds\n\t"
               "buffer_load_dwordx4 %3, %4, %5 offen offset:3072 sc1 lds\n\t"
               "s_mov_b32 m0, %0"
               : "=&s"(keep) : "s"(lds_dst), "v"(ve), "v"(vo), "s"(rs), "s"(soff) : "memory");
}
#define MMA(NAME, OP, CA, CB)                                                                                   \
  template <bool Z> DEVI void NAME(f32x4& d, const u32x4& a, const u32x4& b) {                                  \
    if (Z) asm volatile(OP " %0, %1, %2, 0" : "=&v"(d) : CA(a), CB(b));                                         \
    else asm volatile(OP " %0, %1, %2, %0" : "+v"(d) : CA(a), CB(b));                                           \
  }
MMA(mma_vv, "v_mfma_f32_16x16x32_bf16", "v", "v")
MMA(mma_av, "v_mfma_f32_16x16x32_bf16", "a", "v")
MMA(mma_va, "v_mfma_f32_16x16x32_bf16", "v", "a")
MMA(mma_hh, "v_mfma_f32_16x16x32_f16", "v", "v")
#define MFMA_DONE() do { SCHED(); asm volatile("s_nop 7" ::: "memory"); SCHED(); } while (0)

struct Ctx {
  rsrc_t rs; gu32* ct; char* smem; char* ws; u16* out;
  int lane0, w, b, P, sub, ct_, ff0, hks, eo, f1o, f1s;
  float dsk0, dsk1, ks0, ks1, bb2;
};
DEVI int fresh_lane(const Ctx& c) { int l = c.lane0; asm volatile("" : "+v"(l)); return l; }
DEVI u32 sig_begin(const Ctx& c, int lane) {
  u32 old = 0;
  if (lane == 0) asm volatile("ds_add_rtn_u32 %0, %1, %2" : "=v"(old) : "v"(lds_addr(c.smem) + L_SIG), "v"(1u) : "memory");
  return old;
}
template <bool ALL> DEVI void sig_end(const Ctx& c, int lane, u32 old, int line) {
  asm volatile("s_waitcnt lgkmcnt(0)" : "+v"(old) :: "memory");
  const bool last = (__builtin_amdgcn_readfirstlane(old) & 3u) == 3u;
  const int v = (last && lane < (ALL ? 8 : 1)) ? (ALL ? lane * 2048 : 0) : 0x40000000;
  asm volatile("buffer_atomic_add %0, %1, %2, %3 offen" :: "v"(1u), "v"(v), "s"(c.rs),
               "s"(WS_CT + (line + (ALL ? (c.b >> 3) & 7 : 0)) * 256) : "memory");
}
template <bool ALL> DEVI u32 poll(const Ctx& c, int lane, int line) {
  u32 r;
  asm volatile("buffer_load_dword %0, %1, %2, %3 offen sc1" : "=v"(r) : "v"(ALL ? (lane & 7) * 256 : 0), "s"(c.rs),
               "s"(WS_CT + (line + (ALL ? (c.b & 7) * 8 : 0)) * 256) : "memory");
  return r;
}
template <int YOUNGER> DEVI bool got(u32 v, u32 want) {
  asm volatile("s_waitcnt vmcnt(%1)" : "+v"(v) : "i"(YOUNGER) : "memory");
  return __all(v >= want);
}
template <bool ALL> DEVI void wait_ge(const Ctx& c, int lane, int line, u32 want) {
  int sp = 0;
#pragma nounroll
  while (!got<0>(poll<ALL>(c, lane, line), want)) {
    if (++sp > SPIN_CAP) { __hip_atomic_store(c.ct + CT_TMO * 64, 1u, RLX_AGENT); break; }
  }
  asm volatile("" ::: "memory");
}

struct CIn { u32x2 x0, x1; u32x4 s[4]; };
DEVI void conv_load(const Ctx& c, int lane, CIn& in, int xoff, int stoff, int h) {
  const int sx = xoff + (16 * h + 4 * c.w) * 65536 + c.b * 256;
  const int vx = (lane >> 4) * 65536 + (lane & 15) * 8;
  const int ss = stoff + (1024 * h + 256 * c.w) * 16;
#pragma unroll
  for (int j = 0; j < 4; ++j) in.s[j] = ld128(c.rs, lane * 16 + j * 1024, ss);
  in.x0 = ld64(c.rs, vx, sx);
  in.x1 = ld64(c.rs, vx + 128, sx);
}
DEVI void conv_ln(const Ctx& c, int lane, const CIn& in, int h, f32x4& base0, f32x4& base1) {
  char* scr = c.smem + L_SCR + c.w * 2048;
  constexpr float CH = 4294967296.0f * FX_INV / DM, CL = FX_INV / DM;
  f32x4 m, r;
#pragma unroll
  for (int j = 0; j < 4; ++j) {
    m[j] = fmaf((float)(int)in.s[j][1], CH, (float)in.s[j][0] * CL);
    r[j] = fmaf((float)(int)in.s[j][3], CH, fmaf((float)in.s[j][2], CL, LN_EPS));
  }
#pragma unroll
  for (int j = 0; j < 4; ++j) r[j] = __builtin_amdgcn_rsqf(fmaxf(fmaf(-m[j], m[j], r[j]), LN_EPS));
#pragma unroll
  for (int j = 0; j < 4; ++j) {
    *(float*)(scr + (64 * j + lane) * 4) = r[j];
    *(float*)(scr + 1024 + (64 * j + lane) * 4) = -m[j] * r[j];
  }
  const f32x4 rr = *(const f32x4*)(scr + lane * 16), mr = *(const f32x4*)(scr + 1024 + lane * 16);
  const f32x4 x0 = {lo_bf(in.x0[0]), hi_bf(in.x0[0]), lo_bf(in.x0[1]), hi_bf(in.x0[1])};
  const f32x4 x1 = {lo_bf(in.x1[0]), hi_bf(in.x1[0]), lo_bf(in.x1[1]), hi_bf(in.x1[1])};
  const f32x4 u0 = x0 * rr + mr, u1 = x1 * rr + mr;
  char* up = c.smem + L_U + 512 + (1024 * h + 256 * c.w + 4 * lane) * 2;
  *(u32x2*)up = u32x2{pack_h(u0[0], u0[1]), pack_h(u0[2], u0[3])};
  *(u32x2*)(up + L_UC) = u32x2{pack_h(u1[0], u1[1]), pack_h(u1[2], u1[3])};
  base0 = u0 * c.dsk0 + x0; base1 = u1 * c.dsk1 + x1;
}
struct KB { const char* p[4]; };
template <int MM, int LAM> DEVI u32x4 conv_k1(const KB& kb) {
  const u32* kp = (const u32*)(kb.p[(7 - LAM) >> 1] + 64 * (3 - MM) + 512 * ((7 - LAM) & 1));
  return u32x4{kp[0], kp[1], kp[2], kp[3]};
}
template <int MM, int TB> DEVI u32x4 conv_u1(const char* ub) { return *(const u32x4*)(ub + 64 * (3 - MM) + 512 * TB); }
template <int LO> DEVI void conv_ldk(const KB& kb, u32x4 (&af)[4]) {
  sfor<0, 4>([&](auto L) { af[L.v] = conv_k1<0, LO + L.v>(kb); });
}
constexpr int conv_rank(int DT, int LO, int gr, int lt) {
  int k = 0;
  for (int x = 0; x < lt; ++x) { const int tb = DT + (x & 3) - (LO + 4 * gr + (x >> 2)); k += (tb >= 0 && tb < 4); }
  return k;
}
template <int DT, int TB0, bool ZERO, class F>
DEVI void conv_mma(const char* ub, const KB& kb, f32x4 (&acc)[4], const u32x4 (&af0)[4], F&& hook) {
  constexpr int NG = DT ? 2 : 1, LO = DT ? 1 : 0;
  u32x4 af[2][4], bf[2][4];
  sfor<0, 4>([&](auto L) { af[0][L.v] = af0[L.v]; });
  sfor<0, 4>([&](auto T) { constexpr int tb = DT ? 3 - T.v : T.v; bf[0][tb] = conv_u1<0, TB0 + tb>(ub); });
  SCHED();
  sfor<0, 4 * NG>([&](auto ST) {
    constexpr int st = ST.v, mm = st / NG, gr = st % NG;
    constexpr bool more = st + 1 < 4 * NG;
    constexpr int mn = (st + 1) / NG, gn = (st + 1) % NG;
    constexpr bool nu = more && gr == NG - 1;
    sfor<0, 16>([&](auto LT) {
      constexpr int l = LT.v >> 2, T = LT.v & 3, tb = DT + T - (LO + 4 * gr + l);
      if constexpr (tb >= 0 && tb < 4) {
        constexpr int k = conv_rank(DT, LO, gr, LT.v);
        constexpr bool first = ZERO && st == 0 && (DT ? (l == T) : (l == 0));
        mma_hh<first>(acc[T], af[st & 1][l], bf[mm & 1][tb]);
        sfor<(gr == 1 ? 2 * k : k), (gr == 1 ? 2 * k + 2 : k + 1)>([&](auto J) {
          constexpr int j = J.v;
          if constexpr (nu && j < 8) {
            if constexpr (DT != 0) {
              if constexpr (j & 1) bf[mn & 1][3 - (j >> 1)] = conv_u1<mn, TB0 + 3 - (j >> 1)>(ub);
              else af[(st + 1) & 1][j >> 1] = conv_k1<mn, LO + (j >> 1)>(kb);
            } else {
              if constexpr (j >= 1 && j <= 4) bf[mn & 1][j - 1] = conv_u1<mn, TB0 + j - 1>(ub);
              else af[(st + 1) & 1][j ? j - 4 : 0] = conv_k1<mn, LO + (j ? j - 4 : 0)>(kb);
            }
          } else if constexpr (more && !nu && j < 3) af[(st + 1) & 1][j] = conv_k1<mn, LO + 4 * gn + j>(kb);
        });
        SCHED();
        hook(IC<mm * (DT ? 16 : 10) + gr * 10 + k>{});
      }
    });
  });
}
DEVI void conv_part(const Ctx& c, int lane, const f32x4 (&acc)[4]) {
  char* red = c.smem + L_RED;
#pragma unroll
  for (int T = 0; T < 4; ++T) *(f32x4*)(red + (c.w * 4 + T) * 1024 + (lane & 15) * 64 + (lane >> 4) * 16) = acc[T];
}
DEVI void conv_out(const Ctx& c, int lane, const f32x4& base0, const f32x4& base1, int xoff, int h) {
  const int sx = xoff + (16 * h + 4 * c.w) * 65536 + c.b * 256;
  const int vx = (lane >> 4) * 65536 + (lane & 15) * 8;
  const char* p = c.smem + L_RED + c.w * 1024 + lane * 16;
  const f32x4 a0 = *(const f32x4*)p, b0 = *(const f32x4*)(p + 4096), a1 = *(const f32x4*)(p + 8192), b1 = *(const f32x4*)(p + 12288);
  SCHED();
  const f32x4 y0 = (a0 + b0) * c.ks0 + base0, y1 = (a1 + b1) * c.ks1 + base1;
  st64(c.rs, vx, sx, u32x2{pack_bf(y0[0], y0[1]), pack_bf(y0[2], y0[3])});
  st64(c.rs, vx + 128, sx, u32x2{pack_bf(y1[0], y1[1]), pack_bf(y1[2], y1[3])});
}

DEVI void g1_dma(const Ctx& c, int lane, int ldsx, int soff) {
  const int row = lane >> 3, sw = ((row >> 1) & 1) << 1;
  const int ve = row * 128 + ((lane & 7) ^ sw) * 16, vo = row * 128 + ((lane & 7) ^ (sw | 4)) * 16;
#pragma unroll
  for (int ck = 0; ck < 4; ++ck)
    dma4k(c.rs, lds_addr(c.smem) + ldsx + (4 * ck + c.w) * 4096, ve, vo, soff + (4 * ck + c.w) * 4096);
}
template <class F, class G>
DEVI void g1_mma(const char* const (&xb)[4], const u32x4 (&w1f)[2][16], f32x4 (&acc)[2][4], f32x4& sa, F&& mid, G&& hook) {
  u32x2 xf[2][4][2];
#pragma unroll
  for (int i = 0; i < 4; ++i) { xf[0][i][0] = tr_read(xb[i]); xf[0][i][1] = tr_read(xb[i] + 512); }
  SCHED();
  sfor<0, 16>([&](auto KS) {
    constexpr int ks = KS.v;
    mid(KS);
    sfor<0, 4>([&](auto I) {
      constexpr int i = I.v;
      const u32x4 x = cat2(xf[ks & 1][i][0], xf[ks & 1][i][1]);
      mma_vv<ks == 0>(acc[0][i], w1f[0][ks], x);
      if constexpr (ks < 15) xf[(ks + 1) & 1][i][0] = tr_read(xb[i] + (ks + 1) * 4096);
      SCHED();
      if constexpr (ks < 8) mma_av<ks == 0>(acc[1][i], w1f[1][ks], x); else mma_vv<false>(acc[1][i], w1f[1][ks], x);
      if constexpr (ks < 15) xf[(ks + 1) & 1][i][1] = tr_read(xb[i] + (ks + 1) * 4096 + 512);
      SCHED();
      if constexpr (i == 0) { mma_vv<ks == 0>(sa, x, x); hook(IC<2 * ks>{}); SCHED(); }
      if constexpr (i == 2) { hook(IC<2 * ks + 1>{}); SCHED(); }
    });
  });
  MFMA_DONE();
}
template <class F>
DEVI void g1_epi(const Ctx& c, int lane, f32x4 (&acc)[2][4], const f32x4& sa, int hoff, F&& mid) {
  const int g = lane >> 4, cc = lane & 15, q = cc >> 2, p = lane & 3, w = c.w;
  float* sums = (float*)(c.smem + L_MISC);
  {
    const float d = sel4(p, sa[0], sa[1], sa[2], sa[3]);
    if (q == g) sums[64 + w * 16 + cc] = d;
    const float r = sel4(g, acc[1][1][0], acc[1][2][0], acc[1][3][0], acc[1][0][0]);
    if (w == 3) sums[lane] = r;
  }
  f32x4 s1v[2], b1v[2];
#pragma unroll
  for (int j = 0; j < 2; ++j) {
    s1v[j] = *(const f32x4*)(c.smem + L_CST + w * 256 + j * 64 + g * 16);
    b1v[j] = *(const f32x4*)(c.smem + L_CST + w * 256 + 128 + j * 64 + g * 16);
  }
  __syncthreads();
  f32x2* cs = (f32x2*)(c.smem + L_MISC + 512 + w * 512);
  {
    const float m = sums[lane] * (1.0f / DM);
    const float ve = fmaxf(fmaf(-m, m, fmaf(sums[64 + lane], 1.0f / DM, LN_EPS)), LN_EPS);
    const float r = __builtin_amdgcn_rsqf(fmaf(ve, LN_EPS, ve - LN_EPS));
    f32x2 o = {r, -m * r};
    cs[lane] = o;
  }
  const int v16 = lane * 16 + (w < 3 ? 0 : 0x40000000), v8 = lane * 16 + (w < 3 ? 0x40000000 : c.eo * 8);
  f32x2 k[4];
#pragma unroll
  for (int i = 0; i < 4; ++i) k[i] = cs[((i + w) & 3) * 16 + cc];
  SCHED();
#pragma unroll
  for (int ih = 0; ih < 2; ++ih) {
    f32x4 x[4], e[4];
#pragma unroll
    for (int n = 0; n < 4; ++n) x[n] = k[2 * ih + (n >> 1)][1] * s1v[n & 1] + b1v[n & 1];
    SCHED();
#pragma unroll
    for (int n = 0; n < 4; ++n) x[n] = acc[n & 1][2 * ih + (n >> 1)] * k[2 * ih + (n >> 1)][0] + x[n];
    SCHED();
#pragma unroll
    for (int n = 0; n < 4; ++n) e[n] = x[n] * x[n];
    SCHED();
#pragma unroll
    for (int n = 0; n < 4; ++n) e[n] = e[n] * (-0.0713548162726f * 1.44269504088896f) + (-1.59576912160573f * 1.44269504088896f);
    SCHED();
#pragma unroll
    for (int n = 0; n < 4; ++n) e[n] = e[n] * x[n];
    SCHED();
#pragma unroll
    for (int n = 0; n < 4; ++n)
#pragma unroll
      for (int r = 0; r < 4; ++r) e[n][r] = __builtin_amdgcn_exp2f(e[n][r]);
    SCHED();
#pragma unroll
    for (int n = 0; n < 4; ++n) e[n] = e[n] + 1.0f;
    SCHED();
#pragma unroll
    for (int n = 0; n < 4; ++n)
#pragma unroll
      for (int r = 0; r < 4; ++r) e[n][r] = rcp(e[n][r]);
    SCHED();
#pragma unroll
    for (int n = 0; n < 4; ++n) x[n] = x[n] * e[n];
    SCHED();
    u32x4 hv[2];
#pragma unroll
    for (int n = 0; n < 4; ++n) { hv[n >> 1][2 * (n & 1)] = pack_bf(x[n][0], x[n][1]); hv[n >> 1][2 * (n & 1) + 1] = pack_bf(x[n][2], x[n][3]); }
    const int so0 = hoff + ((2 * ih + w) & 3) * 56 * 1024, so1 = hoff + ((2 * ih + 1 + w) & 3) * 56 * 1024;
    st128(c.rs, v16, so0, hv[0]); st128(c.rs, v16, so1, hv[1]);
    st64(c.rs, v8, so0, u32x2{hv[0][0], hv[0][1]}); st64(c.rs, v8, so1, u32x2{hv[1][0], hv[1][1]});
    SCHED();
    mid(ih);
  }
}

constexpr int PF = 10;
template <int S0, int S1> DEVI void g2_ldh(const Ctx& c, int lane, u32x4 (&hf)[14][2], int ho) {
#pragma unroll
  for (int s = S0; s < S1; ++s)
#pragma unroll
    for (int i = 0; i < 2; ++i) hf[s][i] = ld128(c.rs, lane * 16 + (s & 3) * 1024, ho + i * 56 * 1024 + (s >> 2) * 4096);
}
template <int N> DEVI void ld_w1(const Ctx& c, int v, u32x4 (&w1f)[2][16]) {
  constexpr int ks = N >> 1, j = N & 1;
  const int so = (j ? c.f1o + (ks >> 2) * c.f1s : WS_W1P + c.ff0 * 16384 + (ks >> 2) * 4096);
  if constexpr (j && ks < 8) w1f[j][ks] = ld128a<(ks & 3) * 1024>(c.rs, v, so);
  else w1f[j][ks] = ld128v<(ks & 3) * 1024>(c.rs, v, so);
}
template <int N> DEVI void ld_w2(const Ctx& c, int v, u32x4 (&w2f)[14][4]) {
  constexpr int s = N >> 2, j = N & 3;
  w2f[s][j] = ld128a<(s & 3) * 1024>(c.rs, v, WS_W2P + ((c.ct_ * 4 + ((c.w + j) & 3)) * 56 + 14 * c.w + (s & ~3)) * 1024);
}
template <class F>
DEVI void g2_mma(const Ctx& c, int lane, u32x4 (&hf)[14][2], const u32x4 (&w2f)[14][4], f32x4 (&acc)[2][4], int ho, F&& mid) {
  sfor<0, 14>([&](auto S) {
    constexpr int s = S.v;
    sfor<0, 2>([&](auto I) {
      constexpr int i = I.v;
      sfor<0, 4>([&](auto J) { mma_va<s == 0>(acc[i][J.v], hf[s][i], w2f[s][J.v]); });
      if constexpr (s + PF < 14) hf[s + PF][i] = ld128(c.rs, lane * 16 + ((s + PF) & 3) * 1024, ho + i * 56 * 1024 + ((s + PF) >> 2) * 4096);
      SCHED();
    });
    mid(S);
  });
  MFMA_DONE();
}
template <bool LAST, class F>
DEVI void g2_epi(const Ctx& c, int lane, const f32x4 (&acc)[2][4], const u32x2 (&xr)[2], int h, int xdst, F&& mid) {
  const int g = lane >> 4, cc = lane & 15, q = cc >> 2, p = lane & 3, w = c.w, t = w * 64 + lane;
  char* red = c.smem + L_XA + h * 32768;
#pragma unroll
  for (int jj = 1; jj < 4; ++jj)
#pragma unroll
    for (int i = 0; i < 2; ++i) *(f32x4*)(red + ((((w + jj) & 3) * 3 + jj - 1) * 2 + i) * 1024 + lane * 16) = acc[i][jj];
  __syncthreads();
  char* xt = red + 24576;
  f32x4 pr[2][3];
#pragma unroll
  for (int i = 0; i < 2; ++i)
#pragma unroll
    for (int rk = 0; rk < 3; ++rk) pr[i][rk] = *(const f32x4*)(red + ((w * 3 + rk) * 2 + i) * 1024 + lane * 16);
  mid(0);
#pragma unroll
  for (int i = 0; i < 2; ++i) {
    const f32x4 xv = {lo_bf(xr[i][0]), hi_bf(xr[i][0]), lo_bf(xr[i][1]), hi_bf(xr[i][1])};
    const f32x4 o = ((acc[i][0] + (xv + c.bb2)) + pr[i][0]) + (pr[i][1] + pr[i][2]);
    const u32x2 r = {pack_bf(o[0], o[1]), pack_bf(o[2], o[3])};
    *(u32x2*)(xt + (w * 16 + cc) * 64 + ((i ^ (cc >> 3)) * 32) + g * 8) = r;
  }
  __syncthreads();
  const char* xq = xt + ((w >> 1) * 32 + 8 * g + q) * 64 + (((w & 1) ^ (g & 1)) * 32) + 8 * p;
  const u32x4 f0 = cat2(tr_read(xq), tr_read(xq + 256));
  const int tok = (16 * h + c.P) * 64 + c.sub * 32 + (w & 1) * 16 + cc;
  if (LAST) {
    *(u32x4*)(c.out + (size_t)tok * DM + c.ct_ * 64 + (w >> 1) * 32 + 8 * g) = f0;
  } else {
    const int row = t >> 2;
    const u32x4 v = *(const u32x4*)(xt + row * 64 + (((t & 3) ^ ((row >> 2) & 2)) * 16));
    st128(c.rs, (c.ct_ * 64 + row) * 128 + (t & 3) * 16, xdst, v);
    float s = 0.f, ss = 0.f;
#pragma unroll
    for (int j = 0; j < 4; ++j) { s = dot2bf(f0[j], 0x3f803f80u, s); ss = dot2bf(f0[j], f0[j], ss); }
    s = sum_x16(s); ss = sum_x16(ss);
    (void)__hip_atomic_fetch_add((unsigned long long*)(c.ws + WS_ST + 65536) + 2 * tok + (g & 1),
                                 (unsigned long long)f2fx((g & 1) ? ss : s), RLX_AGENT);
  }
}

template <int LAYER>
DEVI void layer_body(const Ctx& c, u32x4 (&w1f)[2][16], u32x4 (&w2f)[14][4], CIn& inA, CIn& inB) {
  constexpr int xin = WS_X + LAYER * 4 * MB, xmid = xin + 2 * MB, xnext = xin + 4 * MB;
  constexpr int hbuf = WS_H + LAYER * 8 * MB, stin = WS_ST + LAYER * 65536;
  constexpr u32 ep = LAYER + 1;
  constexpr int K1 = 1 + 3 * LAYER, K2 = K1 + 1, K3 = K1 + 2;
  const int w = c.w;
  char* smem = c.smem;
  auto nop = [](auto) {};
  {
    const int lane = fresh_lane(c);
    const int g = lane >> 4, cc = lane & 15, mu0 = (w & 1) * 4, v16 = lane * 16;
    const char* ub = smem + L_U + (w >> 1) * L_UC + 512 + 2 * (16 * cc - 16 + 8 * g) - 64 * (mu0 + 3);
    KB kb;
#pragma unroll
    for (int j = 0; j < 4; ++j) {
      u32 a = lds_addr(smem) + (w >> 1) * 2 * L_KC + ((cc & 1) ? 0 : L_KC) + 2 * (2031 - cc + 8 * g - ((cc & 1) ? 0 : 1)) - 64 * (mu0 + 3 + 56) + 1024 * j;
      asm volatile("" : "+v"(a));
      kb.p[j] = (const char*)(const LAS char*)(size_t)a;
    }
    STAMP(K1, 1);
    f32x4 base0, base1;
    u32x4 a4[4];
    if (LAYER) conv_ldk<0>(kb, a4);
    conv_ln(c, lane, inA, 0, base0, base1);
    __syncthreads();
    if (!LAYER) conv_ldk<0>(kb, a4);
    STAMP(K1, 2);
    f32x4 acc[4];
    u32 pv = 0, o = 0;
    conv_mma<0, 0, true>(ub, kb, acc, a4, [&](auto N) {
      constexpr int n = N.v;
      if constexpr (LAYER && n == 19) { WAIT_VM(0); o = sig_begin(c, lane); SCHED(); }
      if constexpr (LAYER && n == 29) { sig_end<true>(c, lane, o, CT_X + 64); SCHED(); }
    });
    MFMA_DONE();
    conv_part(c, lane, acc);
    conv_ldk<1>(kb, a4);
    __syncthreads();
    conv_out(c, lane, base0, base1, xmid, 0);
    STAMP(K1, 3);
    f32x4 acb[4];
    conv_mma<4, 0, true>(ub, kb, acb, a4, [&](auto N) {
      constexpr int n = N.v;
      if constexpr (!LAYER && (n & 3) == 1) { ld_w1<(n >> 2)>(c, v16, w1f); SCHED(); }
      if constexpr (n == 47) {
        if (LAYER) WAIT_VM(0); else WAIT_VM(12);
        o = sig_begin(c, lane);
        if (LAYER) pv = poll<true>(c, lane, CT_X + 64);
        SCHED();
      }
      if constexpr (n == 57) { sig_end<true>(c, lane, o, CT_C); SCHED(); }
    });
    STAMP(K1, 4);
    if (LAYER) {
      if (!got<1>(pv, 32u)) wait_ge<true>(c, lane, CT_X + 64, 32u);
      conv_load(c, lane, inB, xin, stin, 1);
    }
    conv_ldk<0>(kb, a4);
    STAMP(K1, 5);
    conv_ln(c, lane, inB, 1, base0, base1);
    __syncthreads();
    conv_mma<0, 4, false>(ub, kb, acb, a4, [&](auto N) {
      constexpr int n = N.v;
      if constexpr (!LAYER && (n & 3) == 1) { ld_w1<16 + (n >> 2)>(c, v16, w1f); SCHED(); }
      if constexpr (n == 30) { pv = poll<true>(c, lane, CT_C); SCHED(); }
    });
    MFMA_DONE();
    conv_part(c, lane, acb);
    __syncthreads();
    conv_out(c, lane, base0, base1, xmid, 1);
    STAMP(K1, 6);
    if (!got<(LAYER ? 2 : 4)>(pv, 32u * ep)) wait_ge<true>(c, lane, CT_C, 32u * ep);
    g1_dma(c, lane, L_XA, xmid + c.P * 65536);
    WAIT_VM(16);
    o = sig_begin(c, lane);
    sig_end<true>(c, lane, o, CT_C + 64);
    STAMP(K1, 7);
  }
  u32x4 hf[14][2];
  u32x2 xr[2];
  {
    const int lane = fresh_lane(c);
    const int g = lane >> 4, cc = lane & 15, q = cc >> 2, p = lane & 3, v16 = lane * 16;
    const int sx = ((q >> 1) & 1) | ((g & 1) << 1);
    const char* xb[4];
#pragma unroll
    for (int i = 0; i < 4; ++i) xb[i] = smem + L_XA + (8 * g + q) * 128 + ((((i + w) & 3) ^ sx) * 32) + 8 * p;
    WAIT_VM_BAR(9);
    STAMP(K2, 0);
    f32x4 acc[2][4], sa;
    u32 pv = 0;
    bool gb = false;
    g1_mma(xb, w1f, acc, sa, [&](auto KS) {
      constexpr int ks = KS.v;
      if constexpr (ks == 7) { if (LAYER) WAIT_VM_BAR(1); else WAIT_VM_BAR(15); pv = poll<true>(c, lane, CT_C + 64); SCHED(); }
      if constexpr (ks == 13 && !LAYER) { WAIT_VM(21); SCHED(); }
      if constexpr (ks == 14) {
        gb = got<(LAYER ? 0 : 14)>(pv, 32u * ep);
        if (gb) g1_dma(c, lane, L_XB, xmid + (16 + c.P) * 65536);
        SCHED();
      }
    }, [&](auto N) {
      constexpr int n = N.v;
      if constexpr (!LAYER && n < 6) ld_w1<26 + n>(c, v16, w1f);
      if constexpr (!LAYER && n >= 6 && n < 28) ld_w2<n - 6>(c, v16, w2f);
    });
    STAMP(K2, 1);
    if (!gb) { wait_ge<true>(c, lane, CT_C + 64, 32u * ep); g1_dma(c, lane, L_XB, xmid + (16 + c.P) * 65536); }
    g1_epi(c, lane, acc, sa, hbuf + (c.P * 4 * 56 + c.hks) * 1024, nop);
    STAMP(K2, 2);
  }
  {
    const int lane = fresh_lane(c);
    const int g = lane >> 4, cc = lane & 15, q = cc >> 2, p = lane & 3, v16 = lane * 16;
    const int sx = ((q >> 1) & 1) | ((g & 1) << 1);
    const char* xb[4];
#pragma unroll
    for (int i = 0; i < 4; ++i) xb[i] = smem + L_XB + (8 * g + q) * 128 + ((((i + w) & 3) ^ sx) * 32) + 8 * p;
    WAIT_VM_BAR(8);
    STAMP(K2, 3);
    f32x4 acc[2][4], sa;
    u32 pv = 0, o = 0;
    g1_mma(xb, w1f, acc, sa, [&](auto KS) {
      constexpr int ks = KS.v;
      if constexpr (ks == 6) { if (LAYER) WAIT_VM(0); else WAIT_VM(12); o = sig_begin(c, lane); SCHED(); }
      if constexpr (ks == 7) { sig_end<false>(c, lane, o, CT_H + c.P); SCHED(); }
    }, [&](auto N) {
      constexpr int n = N.v;
      if constexpr (!LAYER) ld_w2<22 + n>(c, v16, w2f);
      if constexpr (!LAYER && n >= 30) ld_w2<24 + n>(c, v16, w2f);
    });
    STAMP(K2, 4);
    pv = poll<false>(c, lane, CT_H + c.P);
    const int ho = hbuf + ((c.P * 4 + c.sub * 2) * 56 + 14 * w) * 1024;
    g1_epi(c, lane, acc, sa, hbuf + ((16 + c.P) * 4 * 56 + c.hks) * 1024, [&](int ih) {
      if (ih == 0) {
        if (!got<4>(pv, 16u * ep)) wait_ge<false>(c, lane, CT_H + c.P, 16u * ep);
        g2_ldh<0, PF>(c, lane, hf, ho);
#pragma unroll
        for (int i = 0; i < 2; ++i) xr[i] = ld64(c.rs, (c.ct_ * 64 + w * 16 + cc) * 128 + i * 32 + g * 8, xmid + c.P * 65536 + c.sub * 64);
        SCHED();
      }
    });
    STAMP(K2, 5);
  }
  {
    const int lane = fresh_lane(c);
    const int g = lane >> 4, cc = lane & 15;
    const int ho = hbuf + ((c.P * 4 + c.sub * 2) * 56 + 14 * w) * 1024, hoB = ho + 64 * 56 * 1024;
    STAMP(K3, 0);
    f32x4 acc[2][4];
    u32 o = 0, pv = 0;
    g2_mma(c, lane, hf, w2f, acc, ho, [&](auto S) {
      constexpr int s = S.v;
      if constexpr (s == 7) { WAIT_VM(8); o = sig_begin(c, lane); SCHED(); }
      if constexpr (s == 8) { sig_end<false>(c, lane, o, CT_H + 16 + c.P); SCHED(); }
    });
    STAMP(K3, 1);
    pv = poll<false>(c, lane, CT_H + 16 + c.P);
    g2_epi<LAYER == 1>(c, lane, acc, xr, 0, xnext + c.P * 65536 + c.sub * 64, nop);
    STAMP(K3, 2);
    if (!got<(LAYER ? 1 : 2)>(pv, 16u * ep)) wait_ge<false>(c, lane, CT_H + 16 + c.P, 16u * ep);
    g2_ldh<0, PF>(c, lane, hf, hoB);
#pragma unroll
    for (int i = 0; i < 2; ++i) xr[i] = ld64(c.rs, (c.ct_ * 64 + w * 16 + cc) * 128 + i * 32 + g * 8, xmid + (16 + c.P) * 65536 + c.sub * 64);
    if (LAYER == 0) {
      WAIT_VM(22);
      o = sig_begin(c, lane);
      sig_end<true>(c, lane, o, CT_X);
    }
    SCHED();
    STAMP(K3, 3);
  }
  {
    const int lane = fresh_lane(c);
    const int hoB = hbuf + (((16 + c.P) * 4 + c.sub * 2) * 56 + 14 * w) * 1024;
    f32x4 acc[2][4];
    u32 pv = 0;
    g2_mma(c, lane, hf, w2f, acc, hoB, [&](auto S) {
      constexpr int s = S.v;
      if constexpr (LAYER == 0 && s == 13) { pv = poll<true>(c, lane, CT_X); SCHED(); }
    });
    STAMP(K3, 4);
    g2_epi<LAYER == 1>(c, lane, acc, xr, 1, xnext + (16 + c.P) * 65536 + c.sub * 64, [&](int) {
      if (LAYER == 0) {
        if (!got<0>(pv, 32u)) wait_ge<true>(c, lane, CT_X, 32u);
        conv_load(c, lane, inA, xnext, WS_ST + 65536, 0);
        SCHED();
      }
    });
    STAMP(K3, 5);
  }
}

__global__ void __launch_bounds__(256, 1) main_kernel(char* ws, const u16* dskip, const u16* b1, const u16* b2, u16* out) {
  extern __shared__ __attribute__((aligned(16))) char smem[];
  STAMP(1, 0);
  Ctx c;
  c.rs = __builtin_amdgcn_make_buffer_rsrc(ws, 0, WS_END, 0x00020000);
  c.ct = (gu32*)(ws + WS_CT);
  c.smem = smem; c.ws = ws; c.out = out;
  const int t = threadIdx.x, lane = t & 63, b = blockIdx.x;
  const int w = __builtin_amdgcn_readfirstlane(t >> 6);
  c.lane0 = lane; c.w = w; c.b = b;
  c.P = (b & 7) * 2 + ((b >> 3) & 1);
  const int idx = b >> 4;
  const int sg = idx >> 1;
  c.eo = idx & 1; c.sub = idx >> 3; c.ct_ = idx & 7;
  c.ff0 = w < 3 ? 14 * sg + 6 * c.eo + 2 * w : 14 * sg + 12 + c.eo;
  c.hks = w < 3 ? 7 * sg + 3 * c.eo + w : 7 * sg + 6;
  dma4k(c.rs, lds_addr(smem) + w * L_KC, lane * 16, lane * 16, WS_KT + b * 16384 + w * 4096);
  CIn inA, inB;
  conv_load(c, lane, inA, WS_X, WS_ST, 0);
  conv_load(c, lane, inB, WS_X, WS_ST, 1);
  const int fi = min(c.ff0 * 16 + (lane & 31), FF - 1);
  const float cs1 = ((const float*)(ws + WS_S1))[fi];
  const u16 cb1 = b1[fi];
  const u16 cb2 = b2[c.ct_ * 64 + w * 16 + (lane & 15)];
  const u32 dsk = *(const u32*)(dskip + 2 * b);
  const f32x2 ksc = *(const f32x2*)(ws + WS_KSC + b * 8);
  SCHED();
  u32x4 w1f[2][16], w2f[14][4];
  c.f1o = w < 3 ? WS_W1P + c.ff0 * 16384 + 16384 : WS_ONE; c.f1s = w < 3 ? 4096 : 0;
  if (t < 16) *(u32x4*)(smem + (t >> 2) * L_KC + 4096 + (t & 3) * 16) = u32x4{0, 0, 0, 0};
  if (t >= 64 && t < 128) *(u32x4*)(smem + L_U + ((t - 64) >> 5) * L_UC + (t & 31) * 16) = u32x4{0, 0, 0, 0};
  if (t == 128) *(u32*)(smem + L_SIG) = 0u;
  ((float*)(smem + L_CST))[t] = lane < 32 ? cs1 : bf2f(cb1);
  const u32 dsks = __builtin_amdgcn_readfirstlane(dsk);
  c.dsk0 = lo_bf(dsks); c.dsk1 = hi_bf(dsks); c.bb2 = bf2f(cb2);
  c.ks0 = __uint_as_float(__builtin_amdgcn_readfirstlane(__float_as_uint(ksc[0])));
  c.ks1 = __uint_as_float(__builtin_amdgcn_readfirstlane(__float_as_uint(ksc[1])));
  layer_body<0>(c, w1f, w2f, inA, inB);
  layer_body<1>(c, w1f, w2f, inA, inB);
}

extern "C" void kernel_launch(void* const* d_in, const int* in_sizes, int n_in, void* d_out, int out_size,
                              void* d_ws, size_t ws_size, hipStream_t stream) {
  (void)in_sizes; (void)n_in; (void)out_size; (void)ws_size;
  char* ws = (char*)d_ws;
  (void)hipFuncSetAttribute((const void*)main_kernel, hipFuncAttributeMaxDynamicSharedMemorySize, MAIN_LDS);
  prep_kernel<<<688, 256, PREP_LDS, stream>>>((const u16*)d_in[0], (const u16*)d_in[1], (const u16*)d_in[2],
                                              (const u16*)d_in[3], (const u16*)d_in[4], (const u16*)d_in[5],
                                              (const u16*)d_in[8], (const u16*)d_in[10], ws);
  main_kernel<<<256, 256, MAIN_LDS, stream>>>(ws, (const u16*)d_in[6], (const u16*)d_in[9], (const u16*)d_in[11], (u16*)d_out);
}
```

```cpp
#include <hip/hip_runtime.h>
#include <hip/hip_bf16.h>

typedef unsigned short u16;
typedef unsigned int u32;
typedef long long i64;
typedef __attribute__((ext_vector_type(4))) short s16x4;
typedef __attribute__((ext_vector_type(8))) __bf16 bf16x8;
typedef __attribute__((ext_vector_type(2))) __bf16 bf16x2;
typedef __attribute__((ext_vector_type(8))) _Float16 f16x8;
typedef __attribute__((ext_vector_type(2))) _Float16 f16x2;
typedef __attribute__((ext_vector_type(4))) float f32x4;
typedef __attribute__((ext_vector_type(2))) float f32x2;
typedef __attribute__((ext_vector_type(4))) unsigned u32x4;
typedef __attribute__((ext_vector_type(2))) unsigned u32x2;
typedef __attribute__((ext_vector_type(2))) long long i64x2;
typedef __attribute__((address_space(1))) unsigned gu32;
typedef __amdgpu_buffer_rsrc_t rsrc_t;
#define LAS __attribute__((address_space(3)))
#define DEVI __device__ __forceinline__
template <int V> struct IC { static constexpr int v = V; };
template <int I, int N, class F> __device__ __forceinline__ void sfor(F&& f) {
  if constexpr (I < N) { f(IC<I>{}); sfor<I + 1, N>(f); }
}
#define RLX_AGENT __ATOMIC_RELAXED, __HIP_MEMORY_SCOPE_AGENT

constexpr int SEQ = 2048, DM = 512, NST = 128, FF = 1792;
constexpr float LN_EPS = 1e-5f;
constexpr float FX_SCALE = 16777216.0f;
constexpr float FX_INV = 1.0f / 16777216.0f;
constexpr int SC1 = 16;

constexpr int MB = 1024 * 1024;
constexpr int WS_X = 0;
constexpr int WS_KT = 8 * MB;
constexpr int WS_W1P = 12 * MB;
constexpr int WS_W2P = 14 * MB;
constexpr int WS_H = 16 * MB;
constexpr int WS_ST = 32 * MB;
constexpr int WS_S1 = 32 * MB + 131072;
constexpr int WS_B2 = 32 * MB + 163840;
constexpr int WS_KSC = 32 * MB + 196608;
constexpr int WS_CT = 32 * MB + 262144;
constexpr int WS_ONE = 32 * MB + 393216;
constexpr int WS_END = 33 * MB;
constexpr int CT_C = 0, CT_X = 128, CT_H = 256, CT_TMO = 288, CT_LINES = 289;
constexpr int SPIN_CAP = 1 << 16;

#define STAMP(k, i) do {} while (0)

DEVI float bf2f(u16 v) { return __uint_as_float((u32)v << 16); }
DEVI float lo_bf(u32 w) { return __uint_as_float(w << 16); }
DEVI float hi_bf(u32 w) { return __uint_as_float(w & 0xffff0000u); }
DEVI float rbf(float x) { return (float)(__bf16)x; }
DEVI u32 pack_bf(float a, float b) { f32x2 q = {a, b}; return __builtin_bit_cast(u32, __builtin_convertvector(q, bf16x2)); }
DEVI u32 pack_h(float a, float b) { f32x2 q = {a, b}; return __builtin_bit_cast(u32, __builtin_convertvector(q, f16x2)); }
DEVI float dot2bf(u32 a, u32 b, float c) {
  return __builtin_amdgcn_fdot2_f32_bf16(__builtin_bit_cast(bf16x2, a), __builtin_bit_cast(bf16x2, b), c, false);
}
DEVI f32x4 mfma_bf(u32x4 a, u32x4 b, f32x4 c) {
  return __builtin_amdgcn_mfma_f32_16x16x32_bf16(__builtin_bit_cast(bf16x8, a), __builtin_bit_cast(bf16x8, b), c, 0, 0, 0);
}
DEVI f32x4 mfma_h(u32x4 a, u32x4 b, f32x4 c) {
  return __builtin_amdgcn_mfma_f32_16x16x32_f16(__builtin_bit_cast(f16x8, a), __builtin_bit_cast(f16x8, b), c, 0, 0, 0);
}
DEVI u32x2 tr_read(const char* p) {
  return __builtin_bit_cast(u32x2, __builtin_amdgcn_ds_read_tr16_b64_v4i16((LAS s16x4*)p));
}
DEVI u32x4 cat2(u32x2 a, u32x2 b) { return __builtin_shufflevector(a, b, 0, 1, 2, 3); }
DEVI u32 lds_addr(const void* p) { return (u32)(size_t)(const LAS char*)p; }
#define WAIT_VM(n) asm volatile("s_waitcnt vmcnt(" #n ")" ::: "memory")
#define WAIT_VM_BAR(n) asm volatile("s_waitcnt vmcnt(" #n ")\n\ts_barrier" ::: "memory")
#define SCHED() __builtin_amdgcn_sched_barrier(0)
template <int CTRL> DEVI float dpp_add(float v) {
  return v + __uint_as_float(__builtin_amdgcn_update_dpp(0u, __float_as_uint(v), CTRL, 0xf, 0xf, true));
}
DEVI float sum_x16(float v) {
  auto r = __builtin_amdgcn_permlane16_swap(__float_as_uint(v), __float_as_uint(v), false, false);
  return __uint_as_float(r[0]) + __uint_as_float(r[1]);
}
DEVI float sum_x32(float v) {
  auto r = __builtin_amdgcn_permlane32_swap(__float_as_uint(v), __float_as_uint(v), false, false);
  return __uint_as_float(r[0]) + __uint_as_float(r[1]);
}
DEVI float wave_sum(float v) {
  v = dpp_add<0xB1>(v); v = dpp_add<0x4E>(v); v = dpp_add<0x141>(v); v = dpp_add<0x140>(v);
  return sum_x32(sum_x16(v));
}
DEVI float fx2f(u32 lo, u32 hi) { return fmaf((float)(int)hi, 4294967296.0f * FX_INV, (float)lo * FX_INV); }
DEVI i64 f2fx(float v) { return (i64)(v * FX_SCALE); }
DEVI float sel4(int i, float a, float b, float c, float d) {
  asm volatile("" : "+v"(a), "+v"(b), "+v"(c), "+v"(d));
  return (i & 2) ? ((i & 1) ? d : c) : ((i & 1) ? b : a);
}
DEVI float rcp(float x) { return __builtin_amdgcn_rcpf(x); }
DEVI float sqrt_(float x) { return __builtin_amdgcn_sqrtf(x); }

DEVI u32 warm_code(int slice, int lane) {
  unsigned long long pc, pe;
  asm volatile("s_getpc_b64 %0\n\ts_getpc_b64 s[90:91]\n\ts_add_u32 s90, s90, code_end_marker@rel32@lo+4\n\t"
               "s_addc_u32 s91, s91, code_end_marker@rel32@hi+12\n\ts_mov_b64 %1, s[90:91]"
               : "=&s"(pc), "=s"(pe) :: "scc", "s90", "s91");
  const unsigned long long a = (pc + (unsigned)(slice * 512 + (lane & 7) * 64)) & ~3ull;
  u32 junk = 0;
  if (pc + (unsigned)(slice * 512 + 512) <= pe) asm volatile("global_load_dword %0, %1, off" : "=v"(junk) : "v"(a) : "memory");
  return junk;
}

DEVI int cidx(int l) {
  const int hb = (int)(pack_bf((float)l, 0.f) & 0xffffu) - (0x4300 - 128);
  return l < 128 ? l : hb;
}
struct CM { f32x2 e, f; };
DEVI f32x2 cmul(f32x2 a, const CM& m) { return a[1] * m.f + a[0] * m.e; }
DEVI CM cexp_(float a2, float br, float p) {
  const float e = __builtin_amdgcn_exp2f(a2 * p), f = __builtin_amdgcn_fractf(br * p);
  const float re = e * __builtin_amdgcn_cosf(f), im = e * __builtin_amdgcn_sinf(f);
  CM r; r.e = f32x2{re, im}; r.f = f32x2{-im, re};
  return r;
}
DEVI void st16(rsrc_t rs, int off, u32x4 v) { __builtin_amdgcn_raw_buffer_store_b128(v, rs, off, 0, SC1); }

constexpr int KG_RS = 528, KG_X = 16 * KG_RS, KG_KD = 64 * KG_RS;
DEVI void kgen(int d, const u16* lar, const u16* aim, const u16* cre, const u16* cim, const u16* ldt,
               const u16* dsk, rsrc_t rs, float* ksc, char* smem) {
  const int t = threadIdx.x, lane = t & 63, n = t & 127;
  const int w = __builtin_amdgcn_readfirstlane(t >> 6);
  const u16 r0 = ldt[d], r1 = lar[d * NST + n], r2 = aim[d * NST + n], r3 = cre[d * NST + n], r4 = cim[d * NST + n];
  const u16 r5 = cre[d * NST + (n ^ 64)], r6 = cim[d * NST + (n ^ 64)], r7 = dsk[d];
  SCHED();
  const int i = lane & 15, g = lane >> 4;
  int wi[4], ix[9];
  {
    const int m8 = -(i >> 3);
    const int h1 = ((256 + 128 * (i >> 2) + 2 * (i & 3)) & ~m8) | ((128 * (i >> 1) + 4 * (i & 1)) & m8);
    const int s0 = -(int)(w == 0), s1 = -(int)(w == 1);
    const int hp = (((i + (i & 8) * 15) & s0) | (h1 & s1) | ((1024 + 128 * i) & ~(s0 | s1))) + 32 * g;
#pragma unroll
    for (int r = 0; r < 4; ++r) { const int pos = hp + 8 * r; wi[r] = 2 * cidx(pos > 2048 ? 2064 : pos); asm volatile("" : "+v"(wi[r])); }
#pragma unroll
    for (int e2 = 0; e2 < 9; ++e2) { ix[e2] = 2 * cidx(max(2047 - 8 * t - e2, 0)); asm volatile("" : "+v"(ix[e2])); }
  }
  SCHED();
  const float dt = rbf(expf(bf2f(r0)));
  const float cr = bf2f(r3), ci = bf2f(r4), cr2 = bf2f(r5), ci2 = bf2f(r6);
  const float S = wave_sum(sqrt_(cr * cr + ci * ci) + sqrt_(cr2 * cr2 + ci2 * ci2)) * (2.002f * dt);
  int e = __builtin_amdgcn_frexp_expf(S);
  e = (S > 0.0f && S < 3.0e38f) ? max(-100, min(100, e)) : 14;
  const float scale = __builtin_amdgcn_ldexpf(2.0f, 14 - e);
  f32x2 ko = {__builtin_amdgcn_ldexpf(1.0f, e - 14), bf2f(r7)};
  asm volatile("" : "+v"(ko));
  STAMP(0, 1);
  const float a2 = (-rbf(expf(bf2f(r1))) * dt) * 1.44269504088896f;
  const float br = (bf2f(r2) * dt) * 0.159154943091895f;
  const CM e128 = cexp_(a2, -br, 128.f);
  char* yp = smem + n * 4;
  char* xp = smem + KG_X + n * 4;
  f32x2 f = {(cr * dt) * scale, -(ci * dt) * scale};
#define PUT(base, row, v) *(u32*)(base + (row) * KG_RS) = pack_h(v[0], v[1])
  if (w < 2) {
    const CM e8 = cexp_(a2, br, 8.f), e4 = cexp_(a2, -br, 4.f);
    f = cmul(f, cexp_(a2, -br, 512.f));
    f32x2 y = {1.f, 0.f};
#pragma unroll
    for (int b = 0; b < 16; ++b) {
      PUT(yp, b, y);
      if (b < 15) y = cmul(y, e8);
      if (b < 4) { const f32x2 g = cmul(f, e4); PUT(xp, 24 + 2 * b, f); PUT(xp, 25 + 2 * b, g); }
      else if (b < 13) PUT(xp, 28 + b, f);
      if (b < 12) f = cmul(f, e128);
    }
  } else {
    const CM e1 = cexp_(a2, -br, 1.f), e2 = cexp_(a2, -br, 2.f);
    f32x2 g1 = cmul(f, e128), g2 = cmul(g1, e128), g3 = cmul(g2, e128);
#pragma unroll
    for (int r = 0; r < 8; ++r) {
      PUT(xp, r, f); PUT(xp, 8 + r, g1);
      if (r < 7) { f = cmul(f, e1); g1 = cmul(g1, e1); }
      if (r < 4) { PUT(xp, 16 + r, g2); PUT(xp, 20 + r, g3); }
      if (r < 3) { g2 = cmul(g2, e2); g3 = cmul(g3, e2); }
    }
  }
#undef PUT
  STAMP(0, 2);
  __syncthreads();
  if (w < 3) {
    const char* ya = smem + i * KG_RS + g * 16;
    const char* xa = smem + KG_X + (16 * w + i) * KG_RS + g * 16;
    u32x4 fa[8], fb[8];
#pragma unroll
    for (int ks = 0; ks < 8; ++ks) { fa[ks] = *(const u32x4*)(ya + ks * 64); fb[ks] = *(const u32x4*)(xa + ks * 64); }
    SCHED();
    f32x4 acc = {};
#pragma unroll
    for (int ks = 0; ks < 8; ++ks) acc = mfma_h(fa[ks], fb[ks], acc);
#pragma unroll
    for (int r = 0; r < 4; ++r) *(u16*)(smem + KG_KD + wi[r]) = __builtin_bit_cast(u16, (_Float16)acc[r]);
  }
  __syncthreads();
  u32 v[9];
#pragma unroll
  for (int e2 = 0; e2 < 9; ++e2) v[e2] = *(const u16*)(smem + KG_KD + ix[e2]);
  if (t == 255) v[8] = 0u;
  u32x4 oe, oo;
#pragma unroll
  for (int j = 0; j < 4; ++j) { oe[j] = v[2 * j] | (v[2 * j + 1] << 16); oo[j] = v[2 * j + 1] | (v[2 * j + 2] << 16); }
  STAMP(0, 3);
  st16(rs, WS_KT + d * 8192 + t * 16, oe);
  st16(rs, WS_KT + d * 8192 + 4096 + t * 16, oo);
  if (t == 0) *(f32x2*)(ksc + 2 * d) = ko;
}

DEVI void xpose_stats(int b, const u16* x, rsrc_t rs, i64* st0, i64* st2, char* smem) {
  const int t = threadIdx.x, lane = t & 63, w = t >> 6;
  const int t0 = b * 32;
  constexpr int RS = 1024 + 32;
  u32x4 v[8];
#pragma unroll
  for (int r = 0; r < 8; ++r) v[r] = *(const u32x4*)(x + (size_t)(t0 + w * 8 + r) * DM + lane * 8);
  float a[16];
#pragma unroll
  for (int r = 0; r < 8; ++r) {
    *(u32x4*)(smem + (w * 8 + r) * RS + lane * 16) = v[r];
    float s = 0.f, ss = 0.f;
#pragma unroll
    for (int j = 0; j < 4; ++j) { s = dot2bf(v[r][j], 0x3f803f80u, s); ss = dot2bf(v[r][j], v[r][j], ss); }
    a[2 * r] = s; a[2 * r + 1] = ss;
  }
  const bool o1 = lane & 1, o2 = lane & 2;
#pragma unroll
  for (int k = 0; k < 8; ++k) {
    const float keep = o1 ? a[2 * k + 1] : a[2 * k], send = o1 ? a[2 * k] : a[2 * k + 1];
    a[k] = keep + __uint_as_float(__builtin_amdgcn_update_dpp(0u, __float_as_uint(send), 0xB1, 0xf, 0xf, true));
  }
#pragma unroll
  for (int k = 0; k < 4; ++k) {
    const float keep = o2 ? a[2 * k + 1] : a[2 * k], send = o2 ? a[2 * k] : a[2 * k + 1];
    a[k] = keep + __uint_as_float(__builtin_amdgcn_update_dpp(0u, __float_as_uint(send), 0x4E, 0xf, 0xf, true));
  }
#pragma unroll
  for (int k = 0; k < 4; ++k) a[k] = sum_x32(sum_x16(dpp_add<0x128>(dpp_add<0x124>(a[k]))));
  if (lane < 16) {
    st0[2 * (t0 + w * 8) + lane] = f2fx(sel4(lane >> 2, a[0], a[1], a[2], a[3]));
    st2[2 * (t0 + w * 8) + lane] = 0;
  }
  __syncthreads();
  const int g = lane >> 4, q = (lane & 15) >> 2, p = lane & 3;
#pragma unroll
  for (int cf = 0; cf < 8; ++cf) {
    const int c0 = (w * 8 + cf) * 16;
    const char* a = smem + (8 * g + q) * RS + c0 * 2 + 8 * p;
    const u32x4 o = cat2(tr_read(a), tr_read(a + 4 * RS));
    st16(rs, WS_X + (b >> 1) * 65536 + (c0 + (lane & 15)) * 128 + (b & 1) * 64 + g * 16, o);
  }
}

DEVI void pack_w1(int b, const u16* w1, const u16* b1, rsrc_t rs, float* s1, char* smem) {
  const int t = threadIdx.x, lane = t & 63, w = t >> 6;
  u32 bv = b1[b * 32 + (t & 31)];
  constexpr int RS = 64 + 32;
  u32x4 v[8];
#pragma unroll
  for (int it = 0; it < 8; ++it) v[it] = *(const u32x4*)(w1 + (size_t)(it * 64 + (t >> 2)) * FF + b * 32 + (t & 3) * 8);
#pragma unroll
  for (int it = 0; it < 8; ++it) *(u32x4*)(smem + (it * 64 + (t >> 2)) * RS + (t & 3) * 16) = v[it];
  asm volatile("" : "+v"(bv));
  __syncthreads();
  const int g = lane >> 4, q = (lane & 15) >> 2, p = lane & 3;
  float cs[2] = {0.f, 0.f};
#pragma unroll
  for (int kk = 0; kk < 4; ++kk) {
    const int ks = w * 4 + kk;
#pragma unroll
    for (int h = 0; h < 2; ++h) {
      const char* a = smem + (32 * ks + 8 * g + q) * RS + h * 32 + 8 * p;
      const u32x4 o = cat2(tr_read(a), tr_read(a + 4 * RS));
#pragma unroll
      for (int j = 0; j < 4; ++j) cs[h] = dot2bf(o[j], 0x3f803f80u, cs[h]);
      st16(rs, WS_W1P + (((2 * b + h) * 16 + ks) * 64 + lane) * 16, o);
    }
  }
  float* red = (float*)(smem + 512 * RS);
#pragma unroll
  for (int h = 0; h < 2; ++h) {
    const float s = sum_x32(sum_x16(cs[h]));
    if (lane < 16) red[w * 32 + h * 16 + lane] = s;
  }
  __syncthreads();
  if (t < 32) {
    s1[b * 64 + (t >> 4) * 32 + (t & 15)] = (red[t] + red[32 + t]) + (red[64 + t] + red[96 + t]);
    s1[b * 64 + (t >> 4) * 32 + 16 + (t & 15)] = lo_bf(bv);
  }
}

DEVI void pack_w2(int b, const u16* w2, rsrc_t rs, char* smem) {
  const int t = threadIdx.x, lane = t & 63, w = t >> 6;
  constexpr int RS = 1024 + 32;
  u32x4 v[8];
#pragma unroll
  for (int r = 0; r < 8; ++r) v[r] = *(const u32x4*)(w2 + (size_t)(b * 32 + w * 8 + r) * DM + lane * 8);
#pragma unroll
  for (int r = 0; r < 8; ++r) *(u32x4*)(smem + (w * 8 + r) * RS + lane * 16) = v[r];
  __syncthreads();
  const int g = lane >> 4, q = (lane & 15) >> 2, p = lane & 3;
#pragma unroll
  for (int cf = 0; cf < 8; ++cf) {
    const int nf = w * 8 + cf;
    const char* a = smem + (4 * g + q) * RS + nf * 32 + 8 * p;
    const u32x4 o = cat2(tr_read(a), tr_read(a + 16 * RS));
    st16(rs, WS_W2P + ((nf * 56 + b) * 64 + lane) * 16, o);
  }
}

constexpr int PREP_LDS = 512 * 96 + 512;
__global__ void __launch_bounds__(256) prep_kernel(const u16* x, const u16* lar, const u16* aim, const u16* cre,
                                                   const u16* cim, const u16* ldt, const u16* dsk, const u16* w1,
                                                   const u16* b1, const u16* w2, const u16* b2, char* ws) {
  extern __shared__ __attribute__((aligned(16))) char smem[];
  const int b = blockIdx.x < 512 ? blockIdx.x + 178 : blockIdx.x - 512;
  const u32 junk = warm_code((blockIdx.x >> 3) * 4 + __builtin_amdgcn_readfirstlane(threadIdx.x >> 6), threadIdx.x);
  STAMP(0, 0);
  const rsrc_t rs = __builtin_amdgcn_make_buffer_rsrc(ws, 0, WS_END, 0x00020000);
  if (b < 178) __builtin_amdgcn_s_setprio(3);
  if (b < 64) xpose_stats(b, x, rs, (i64*)(ws + WS_ST), (i64*)(ws + WS_ST + 65536), smem);
  else if (b < 120) pack_w1(b - 64, w1, b1, rs, (float*)(ws + WS_S1), smem);
  else if (b < 176) {
    pack_w2(b - 120, w2, rs, smem);
    if (b < 122) { const int i = (b - 120) * 256 + threadIdx.x; if (i < CT_LINES) *(u32*)(ws + WS_CT + i * 256) = 0u; }
    if (b == 122) *(u32x4*)(ws + WS_ONE + threadIdx.x * 16) = u32x4{0x3f803f80u, 0x3f803f80u, 0x3f803f80u, 0x3f803f80u};
  } else if (b < 178) {
    ((float*)(ws + WS_B2))[(b - 176) * 256 + threadIdx.x] = bf2f(b2[(b - 176) * 256 + threadIdx.x]);
  } else kgen(b - 178, lar, aim, cre, cim, ldt, dsk, rs, (float*)(ws + WS_KSC), smem);
  asm volatile("s_waitcnt vmcnt(0)" :: "v"(junk) : "memory");
  STAMP(0, (b < 64 ? 4 : (b < 120 ? 6 : (b < 178 ? 7 : 5))));
}

constexpr int L_KC = 4160;
constexpr int L_U = 4 * L_KC, L_UC = 4608;
constexpr int L_XA = 26112, L_XB = L_XA + 65536;
constexpr int L_MISC = L_XB + 65536;
constexpr int L_CST = L_MISC + 512 + 4 * 512;
constexpr int L_SIG = L_CST + 4 * 256;
constexpr int MAIN_LDS = L_SIG + 256;
constexpr int L_RED = L_XB, L_SCR = L_XB + 16384;

DEVI u32x4 ld128(rsrc_t rs, int v, int s) { return __builtin_amdgcn_raw_buffer_load_b128(rs, v, s, SC1); }
DEVI u32x2 ld64(rsrc_t rs, int v, int s) { return __builtin_amdgcn_raw_buffer_load_b64(rs, v, s, SC1); }
DEVI u32x4 ld128p(rsrc_t rs, int v, int s) { return __builtin_amdgcn_raw_buffer_load_b128(rs, v, s, 0); }
DEVI void st128(rsrc_t rs, int v, int s, u32x4 d) { __builtin_amdgcn_raw_buffer_store_b128(d, rs, v, s, SC1); }
DEVI void st64(rsrc_t rs, int v, int s, u32x2 d) { __builtin_amdgcn_raw_buffer_store_b64(d, rs, v, s, SC1); }
template <int OFF> DEVI u32x4 ld128a(rsrc_t rs, int v, int s) {
  u32x4 r;
  asm volatile("buffer_load_dwordx4 %0, %1, %2, %3 offen offset:%4" : "=a"(r) : "v"(v), "s"(rs), "s"(s), "i"(OFF) : "memory");
  return r;
}
template <int OFF> DEVI u32x4 ld128v(rsrc_t rs, int v, int s) {
  u32x4 r;
  asm volatile("buffer_load_dwordx4 %0, %1, %2, %3 offen offset:%4" : "=v"(r) : "v"(v), "s"(rs), "s"(s), "i"(OFF) : "memory");
  return r;
}
template <int OFF> DEVI void dma1k(rsrc_t rs, u32 lds_dst, int v, int soff) {
  asm volatile("s_mov_b32 m0, %0\n\ts_nop 0\n\tbuffer_load_dwordx4 %1, %2, %3 offen offset:%4 sc1 lds"
               :: "s"(lds_dst), "v"(v), "s"(rs), "s"(soff), "i"(OFF) : "memory");
}
#define MMA(NAME, OP, CA, CB)                                                                                   \
  template <bool Z> DEVI void NAME(f32x4& d, const u32x4& a, const u32x4& b) {                                  \
    if (Z) asm volatile(OP " %0, %1, %2, 0" : "=&v"(d) : CA(a), CB(b));                                         \
    else asm volatile(OP " %0, %1, %2, %0" : "+v"(d) : CA(a), CB(b));                                           \
  }
MMA(mma_vv, "v_mfma_f32_16x16x32_bf16", "v", "v")
MMA(mma_av, "v_mfma_f32_16x16x32_bf16", "a", "v")
MMA(mma_va, "v_mfma_f32_16x16x32_bf16", "v", "a")
MMA(mma_hh, "v_mfma_f32_16x16x32_f16", "v", "v")
#define MFMA_DONE() do { SCHED(); asm volatile("s_nop 7" ::: "memory"); SCHED(); } while (0)

struct Ctx {
  rsrc_t rs; gu32* ct; char* smem; char* ws; u16* out;
  int lane0, w, b, P, sub, ct_, ff0, hks, eo, f1o, f1s;
  float dsk0, dsk1, ks0, ks1, bb2, cs1;
};
DEVI int fresh_lane(const Ctx& c) { int l = c.lane0; asm volatile("" : "+v"(l)); return l; }
DEVI u32 sig_begin(const Ctx& c, int lane) {
  u32 old;
  asm volatile("ds_add_rtn_u32 %0, %1, %2" : "=v"(old) : "v"(lds_addr(c.smem) + L_SIG + lane * 4), "v"(1u) : "memory");
  return old;
}
template <bool ALL> DEVI void sig_end(const Ctx& c, int lane, u32 old, int line) {
  asm volatile("s_waitcnt lgkmcnt(0)" : "+v"(old) :: "memory");
  const bool last = (__builtin_amdgcn_readfirstlane(old) & 3u) == 3u;
  const int v = (last && lane < (ALL ? 8 : 1)) ? (ALL ? lane * 2048 : 0) : 0x40000000;
  asm volatile("s_nop 4\n\tbuffer_atomic_add %0, %1, %2, %3 offen" :: "v"(1u), "v"(v), "s"(c.rs),
               "s"(WS_CT + (line + (ALL ? (c.b >> 3) & 7 : 0)) * 256) : "memory");
}
template <bool ALL, int R = 0> DEVI void poll(const Ctx& c, int lane, int line) {
  const int v = ALL ? (lane & 7) * 256 : 0, so = WS_CT + (line + (ALL ? (c.b & 7) * 8 : 0)) * 256;
  if (R == 0) asm volatile("s_nop 4\n\tbuffer_load_dword v254, %0, %1, %2 offen sc1" :: "v"(v), "s"(c.rs), "s"(so) : "memory", "v254");
  else asm volatile("s_nop 4\n\tbuffer_load_dword v255, %0, %1, %2 offen sc1" :: "v"(v), "s"(c.rs), "s"(so) : "memory", "v255");
}
template <int YOUNGER, int R = 0> DEVI bool got(u32 want) {
  unsigned long long m;
  if (R == 0) asm volatile("s_waitcnt vmcnt(%2)\n\tv_cmp_le_u32_e64 %0, %1, v254" : "=s"(m) : "s"(want), "i"(YOUNGER) : "memory");
  else asm volatile("s_waitcnt vmcnt(%2)\n\tv_cmp_le_u32_e64 %0, %1, v255" : "=s"(m) : "s"(want), "i"(YOUNGER) : "memory");
  return m == ~0ull;
}
template <bool ALL> DEVI void wait_ge(const Ctx& c, int lane, int line, u32 want) {
  int sp = 0;
  poll<ALL, 0>(c, lane, line);
  __builtin_amdgcn_s_sleep(6);
  poll<ALL, 1>(c, lane, line);
#pragma nounroll
  for (;;) {
    if (got<1, 0>(want)) break;
    poll<ALL, 0>(c, lane, line);
    if (got<1, 1>(want)) break;
    poll<ALL, 1>(c, lane, line);
    if (++sp > SPIN_CAP) { __hip_atomic_store(c.ct + CT_TMO * 64, 1u, RLX_AGENT); break; }
  }
}

struct CIn { u32x2 x0, x1; u32x4 s[4]; };
DEVI void conv_load(const Ctx& c, int lane, CIn& in, int xoff, int stoff, int h) {
  const int sx = xoff + (16 * h + 4 * c.w) * 65536 + c.b * 256;
  const int vx = (lane >> 4) * 65536 + (lane & 15) * 8;
  const int ss = stoff + (1024 * h + 256 * c.w) * 16;
#pragma unroll
  for (int j = 0; j < 4; ++j) in.s[j] = ld128(c.rs, lane * 16 + j * 1024, ss);
  in.x0 = ld64(c.rs, vx, sx);
  in.x1 = ld64(c.rs, vx + 128, sx);
}
DEVI void conv_ln(const Ctx& c, int lane, const CIn& in, int h, f32x4& base0, f32x4& base1) {
  char* scr = c.smem + L_SCR + c.w * 2048;
  constexpr float CH = 4294967296.0f * FX_INV / DM, CL = FX_INV / DM;
  f32x4 m, r;
#pragma unroll
  for (int j = 0; j < 4; ++j) {
    m[j] = fmaf((float)(int)in.s[j][1], CH, (float)in.s[j][0] * CL);
    r[j] = fmaf((float)(int)in.s[j][3], CH, fmaf((float)in.s[j][2], CL, LN_EPS));
  }
#pragma unroll
  for (int j = 0; j < 4; ++j) r[j] = __builtin_amdgcn_rsqf(fmaxf(fmaf(-m[j], m[j], r[j]), LN_EPS));
#pragma unroll
  for (int j = 0; j < 4; ++j) {
    *(float*)(scr + (64 * j + lane) * 4) = r[j];
    *(float*)(scr + 1024 + (64 * j + lane) * 4) = -m[j] * r[j];
  }
  const f32x4 rr = *(const f32x4*)(scr + lane * 16), mr = *(const f32x4*)(scr + 1024 + lane * 16);
  const f32x4 x0 = {lo_bf(in.x0[0]), hi_bf(in.x0[0]), lo_bf(in.x0[1]), hi_bf(in.x0[1])};
  const f32x4 x1 = {lo_bf(in.x1[0]), hi_bf(in.x1[0]), lo_bf(in.x1[1]), hi_bf(in.x1[1])};
  const f32x4 u0 = x0 * rr + mr, u1 = x1 * rr + mr;
  char* up = c.smem + L_U + 512 + (1024 * h + 256 * c.w + 4 * lane) * 2;
  *(u32x2*)up = u32x2{pack_h(u0[0], u0[1]), pack_h(u0[2], u0[3])};
  *(u32x2*)(up + L_UC) = u32x2{pack_h(u1[0], u1[1]), pack_h(u1[2], u1[3])};
  base0 = u0 * c.dsk0 + x0; base1 = u1 * c.dsk1 + x1;
}
struct KB { const char* p[4]; };
template <int MM, int LAM> DEVI u32x4 conv_k1(const KB& kb) {
  const u32* kp = (const u32*)(kb.p[(7 - LAM) >> 1] + 64 * (3 - MM) + 512 * ((7 - LAM) & 1));
  return u32x4{kp[0], kp[1], kp[2], kp[3]};
}
template <int MM, int TB> DEVI u32x4 conv_u1(const char* ub) { return *(const u32x4*)(ub + 64 * (3 - MM) + 512 * TB); }
template <int LO> DEVI void conv_ldk(const KB& kb, u32x4 (&af)[4]) {
  sfor<0, 4>([&](auto L) { af[L.v] = conv_k1<0, LO + L.v>(kb); });
}
constexpr int conv_rank(int DT, int LO, int gr, int lt) {
  int k = 0;
  for (int x = 0; x < lt; ++x) { const int tb = DT + (x & 3) - (LO + 4 * gr + (x >> 2)); k += (tb >= 0 && tb < 4); }
  return k;
}
template <int DT, int TB0, bool ZERO, class F>
DEVI void conv_mma(const char* ub, const KB& kb, f32x4 (&acc)[4], const u32x4 (&af0)[4], F&& hook) {
  constexpr int NG = DT ? 2 : 1, LO = DT ? 1 : 0;
  u32x4 af[2][4], bf[2][4];
  sfor<0, 4>([&](auto L) { af[0][L.v] = af0[L.v]; });
  sfor<0, 4>([&](auto T) { constexpr int tb = DT ? 3 - T.v : T.v; bf[0][tb] = conv_u1<0, TB0 + tb>(ub); });
  SCHED();
  sfor<0, 4 * NG>([&](auto ST) {
    constexpr int st = ST.v, mm = st / NG, gr = st % NG;
    constexpr bool more = st + 1 < 4 * NG;
    constexpr int mn = (st + 1) / NG, gn = (st + 1) % NG;
    constexpr bool nu = more && gr == NG - 1;
    sfor<0, 16>([&](auto LT) {
      constexpr int l = LT.v >> 2, T = LT.v & 3, tb = DT + T - (LO + 4 * gr + l);
      if constexpr (tb >= 0 && tb < 4) {
        constexpr int k = conv_rank(DT, LO, gr, LT.v);
        constexpr bool first = ZERO && st == 0 && (DT ? (l == T) : (l == 0));
        mma_hh<first>(acc[T], af[st & 1][l], bf[mm & 1][tb]);
        sfor<(gr == 1 ? 2 * k : k), (gr == 1 ? 2 * k + 2 : k + 1)>([&](auto J) {
          constexpr int j = J.v;
          if constexpr (nu && j < 8) {
            if constexpr (DT != 0) {
              if constexpr (j & 1) bf[mn & 1][3 - (j >> 1)] = conv_u1<mn, TB0 + 3 - (j >> 1)>(ub);
              else af[(st + 1) & 1][j >> 1] = conv_k1<mn, LO + (j >> 1)>(kb);
            } else {
              if constexpr (j >= 1 && j <= 4) bf[mn & 1][j - 1] = conv_u1<mn, TB0 + j - 1>(ub);
              else af[(st + 1) & 1][j ? j - 4 : 0] = conv_k1<mn, LO + (j ? j - 4 : 0)>(kb);
            }
          } else if constexpr (more && !nu && j < 3) af[(st + 1) & 1][j] = conv_k1<mn, LO + 4 * gn + j>(kb);
        });
        SCHED();
        hook(IC<mm * (DT ? 16 : 10) + gr * 10 + k>{});
      }
    });
  });
}
DEVI void conv_part(const Ctx& c, int lane, const f32x4 (&acc)[4]) {
  char* red = c.smem + L_RED;
#pragma unroll
  for (int T = 0; T < 4; ++T) *(f32x4*)(red + (c.w * 4 + T) * 1024 + (lane & 15) * 64 + (lane >> 4) * 16) = acc[T];
}
DEVI void conv_out(const Ctx& c, int lane, const f32x4& base0, const f32x4& base1, int xoff, int h) {
  const int sx = xoff + (16 * h + 4 * c.w) * 65536 + c.b * 256;
  const int vx = (lane >> 4) * 65536 + (lane & 15) * 8;
  const char* p = c.smem + L_RED + c.w * 1024 + lane * 16;
  const f32x4 a0 = *(const f32x4*)p, b0 = *(const f32x4*)(p + 4096), a1 = *(const f32x4*)(p + 8192), b1 = *(const f32x4*)(p + 12288);
  SCHED();
  const f32x4 y0 = (a0 + b0) * c.ks0 + base0, y1 = (a1 + b1) * c.ks1 + base1;
  st64(c.rs, vx, sx, u32x2{pack_bf(y0[0], y0[1]), pack_bf(y0[2], y0[3])});
  st64(c.rs, vx + 128, sx, u32x2{pack_bf(y1[0], y1[1]), pack_bf(y1[2], y1[3])});
}

template <int PC> DEVI void g1_dma1(const Ctx& c, int ve, int vo, int ldsx, int soff) {
  dma1k<(PC & 3) * 1024>(c.rs, lds_addr(c.smem) + ldsx + (4 * (PC >> 2) + c.w) * 4096, (PC & 1) ? vo : ve, soff + (4 * (PC >> 2) + c.w) * 4096);
}
template <bool ALL> DEVI void sig_bar(const Ctx& c, int lane, int line) {
  const int v = (c.w == 0 && lane < (ALL ? 8 : 1)) ? (ALL ? lane * 2048 : 0) : 0x40000000;
  asm volatile("s_nop 4\n\tbuffer_atomic_add %0, %1, %2, %3 offen" :: "v"(1u), "v"(v), "s"(c.rs),
               "s"(WS_CT + (line + (ALL ? (c.b >> 3) & 7 : 0)) * 256) : "memory");
}
template <class F, class G>
DEVI void g1_mma(const char* const (&xb)[4], const u32x4 (&w1f)[2][16], f32x4 (&acc)[2][4], f32x4& sa, F&& mid, G&& hook) {
  u32x2 xf[2][4][2];
#pragma unroll
  for (int i = 0; i < 4; ++i) { xf[0][i][0] = tr_read(xb[i]); xf[0][i][1] = tr_read(xb[i] + 512); }
  SCHED();
  sfor<0, 16>([&](auto KS) {
    constexpr int ks = KS.v;
    mid(KS);
    sfor<0, 4>([&](auto I) {
      constexpr int i = I.v;
      const u32x4 x = cat2(xf[ks & 1][i][0], xf[ks & 1][i][1]);
      mma_vv<ks == 0>(acc[0][i], w1f[0][ks], x);
      if constexpr (ks < 15) xf[(ks + 1) & 1][i][0] = tr_read(xb[i] + (ks + 1) * 4096);
      SCHED();
      if constexpr (ks < 8) mma_av<ks == 0>(acc[1][i], w1f[1][ks], x); else mma_vv<false>(acc[1][i], w1f[1][ks], x);
      if constexpr (ks < 15) xf[(ks + 1) & 1][i][1] = tr_read(xb[i] + (ks + 1) * 4096 + 512);
      SCHED();
      if constexpr (i == 0) { mma_vv<ks == 0>(sa, x, x); hook(IC<2 * ks>{}); SCHED(); }
      if constexpr (i == 2) { hook(IC<2 * ks + 1>{}); SCHED(); }
    });
  });
  MFMA_DONE();
}
template <class F>
DEVI void g1_epi(const Ctx& c, int lane, f32x4 (&acc)[2][4], const f32x4& sa, int hoff, F&& hook) {
  const int g = lane >> 4, cc = lane & 15, q = cc >> 2, p = lane & 3, w = c.w;
  float* sums = (float*)(c.smem + L_MISC);
  {
    const float d = sel4(p, sa[0], sa[1], sa[2], sa[3]);
    if (q == g) sums[64 + w * 16 + cc] = d;
    const float r = sel4(g, acc[1][1][0], acc[1][2][0], acc[1][3][0], acc[1][0][0]);
    if (w == 3) sums[lane] = r;
  }
  f32x4 s1v[2], b1v[2];
#pragma unroll
  for (int j = 0; j < 2; ++j) {
    s1v[j] = *(const f32x4*)(c.smem + L_CST + w * 256 + j * 128 + g * 16);
    b1v[j] = *(const f32x4*)(c.smem + L_CST + w * 256 + j * 128 + 64 + g * 16);
  }
  __syncthreads();
  f32x2* cs = (f32x2*)(c.smem + L_MISC + 512 + w * 512);
  {
    const float m = sums[lane] * (1.0f / DM);
    const float ve = fmaxf(fmaf(-m, m, fmaf(sums[64 + lane], 1.0f / DM, LN_EPS)), LN_EPS);
    const float r = __builtin_amdgcn_rsqf(fmaf(ve, LN_EPS, ve - LN_EPS));
    f32x2 o = {r, -m * r};
    cs[lane] = o;
  }
  const int v16 = lane * 16 + (w < 3 ? 0 : 0x40000000), v8 = lane * 16 + (w < 3 ? 0x40000000 : c.eo * 8);
  f32x2 k[4];
#pragma unroll
  for (int i = 0; i < 4; ++i) k[i] = cs[((i + w) & 3) * 16 + cc];
  SCHED();
  hook(IC<-1>{});
  SCHED();
  sfor<0, 2>([&](auto IH) {
    constexpr int ih = IH.v;
    f32x4 x[4], e[4];
#define STAGE(n) do { SCHED(); hook(IC<10 * ih + n>{}); SCHED(); } while (0)
#pragma unroll
    for (int n = 0; n < 4; ++n) x[n] = k[2 * ih + (n >> 1)][1] * s1v[n & 1] + b1v[n & 1];
#pragma unroll
    for (int n = 0; n < 4; ++n) x[n] = acc[n & 1][2 * ih + (n >> 1)] * k[2 * ih + (n >> 1)][0] + x[n];
    STAGE(0);
#pragma unroll
    for (int n = 0; n < 4; ++n) e[n] = x[n] * x[n];
#pragma unroll
    for (int n = 0; n < 4; ++n) e[n] = e[n] * (-0.0713548162726f * 1.44269504088896f) + (-1.59576912160573f * 1.44269504088896f);
    STAGE(1);
#pragma unroll
    for (int n = 0; n < 4; ++n) e[n] = e[n] * x[n];
    STAGE(2);
#pragma unroll
    for (int r = 0; r < 4; ++r) { e[0][r] = __builtin_amdgcn_exp2f(e[0][r]); e[1][r] = __builtin_amdgcn_exp2f(e[1][r]); }
    STAGE(3);
#pragma unroll
    for (int r = 0; r < 4; ++r) { e[2][r] = __builtin_amdgcn_exp2f(e[2][r]); e[3][r] = __builtin_amdgcn_exp2f(e[3][r]); }
    STAGE(4);
#pragma unroll
    for (int n = 0; n < 4; ++n) e[n] = e[n] + 1.0f;
    STAGE(5);
#pragma unroll
    for (int r = 0; r < 4; ++r) { e[0][r] = rcp(e[0][r]); e[1][r] = rcp(e[1][r]); }
    STAGE(6);
#pragma unroll
    for (int r = 0; r < 4; ++r) { e[2][r] = rcp(e[2][r]); e[3][r] = rcp(e[3][r]); }
    STAGE(7);
#pragma unroll
    for (int n = 0; n < 4; ++n) x[n] = x[n] * e[n];
    STAGE(8);
    u32x4 hv[2];
#pragma unroll
    for (int n = 0; n < 4; ++n) { hv[n >> 1][2 * (n & 1)] = pack_bf(x[n][0], x[n][1]); hv[n >> 1][2 * (n & 1) + 1] = pack_bf(x[n][2], x[n][3]); }
    STAGE(9);
    const int so0 = hoff + ((2 * ih + w) & 3) * 56 * 1024, so1 = hoff + ((2 * ih + 1 + w) & 3) * 56 * 1024;
    st128(c.rs, v16, so0, hv[0]); st128(c.rs, v16, so1, hv[1]);
    st64(c.rs, v8, so0, u32x2{hv[0][0], hv[0][1]}); st64(c.rs, v8, so1, u32x2{hv[1][0], hv[1][1]});
    SCHED();
    if constexpr (ih == 0) { hook(IC<-2>{}); SCHED(); }
#undef STAGE
  });
}

constexpr int PF = 10;
template <int S0, int S1> DEVI void g2_ldh(const Ctx& c, int lane, u32x4 (&hf)[14][2], int ho) {
#pragma unroll
  for (int s = S0; s < S1; ++s)
#pragma unroll
    for (int i = 0; i < 2; ++i) hf[s][i] = ld128(c.rs, lane * 16 + (s & 3) * 1024, ho + i * 56 * 1024 + (s >> 2) * 4096);
}
template <int N> DEVI void ld_w1(const Ctx& c, int v, u32x4 (&w1f)[2][16]) {
  constexpr int ks = N >> 1, j = N & 1;
  const int so = (j ? c.f1o + (ks >> 2) * c.f1s : WS_W1P + c.ff0 * 16384 + (ks >> 2) * 4096);
  if constexpr (j && ks < 8) w1f[j][ks] = ld128a<(ks & 3) * 1024>(c.rs, v, so);
  else w1f[j][ks] = ld128v<(ks & 3) * 1024>(c.rs, v, so);
}
template <int N> DEVI void ld_w2(const Ctx& c, int v, u32x4 (&w2f)[14][4]) {
  constexpr int s = N >> 2, j = N & 3;
  w2f[s][j] = ld128a<(s & 3) * 1024>(c.rs, v, WS_W2P + ((c.ct_ * 4 + ((c.w + j) & 3)) * 56 + 14 * c.w + (s & ~3)) * 1024);
}
template <int N> DEVI void g2_ldh1(const Ctx& c, int v16, u32x4 (&hf)[14][2], int ho) {
  constexpr int s = N >> 1, i = N & 1;
  hf[s][i] = ld128(c.rs, v16 + (s & 3) * 1024, ho + i * 56 * 1024 + (s >> 2) * 4096);
}
template <int PRE, int RATE, class F>
DEVI void g2_mma(const Ctx& c, int lane, u32x4 (&hf)[14][2], const u32x4 (&w2f)[14][4], f32x4 (&acc)[2][4], int ho, F&& mid) {
  const int v16 = lane * 16;
  sfor<0, 14>([&](auto S) {
    constexpr int s = S.v;
    sfor<0, 8>([&](auto M) {
      constexpr int i = M.v >> 2, j = M.v & 3;
      mma_va<s == 0>(acc[i][j], hf[s][i], w2f[s][j]);
      constexpr int per = 4 / RATE, n = 2 * (PRE + RATE * s) + M.v / per;
      if constexpr (M.v % per == per - 1 && n < 28) { g2_ldh1<n>(c, v16, hf, ho); SCHED(); }
    });
    SCHED();
    mid(S);
  });
  MFMA_DONE();
}
template <bool LAST, class F>
DEVI void g2_epi(const Ctx& c, int lane, const f32x4 (&acc)[2][4], const u32x2 (&xr)[2], int h, int xdst, F&& mid) {
  const int g = lane >> 4, cc = lane & 15, q = cc >> 2, p = lane & 3, w = c.w, t = w * 64 + lane;
  char* red = c.smem + L_XA + h * 32768;
#pragma unroll
  for (int jj = 1; jj < 4; ++jj)
#pragma unroll
    for (int i = 0; i < 2; ++i) *(f32x4*)(red + ((((w + jj) & 3) * 3 + jj - 1) * 2 + i) * 1024 + lane * 16) = acc[i][jj];
  __syncthreads();
  char* xt = red + 24576;
  f32x4 pr[2][3];
#pragma unroll
  for (int i = 0; i < 2; ++i)
#pragma unroll
    for (int rk = 0; rk < 3; ++rk) pr[i][rk] = *(const f32x4*)(red + ((w * 3 + rk) * 2 + i) * 1024 + lane * 16);
  mid(0);
#pragma unroll
  for (int i = 0; i < 2; ++i) {
    const f32x4 xv = {lo_bf(xr[i][0]), hi_bf(xr[i][0]), lo_bf(xr[i][1]), hi_bf(xr[i][1])};
    const f32x4 o = ((acc[i][0] + (xv + c.bb2)) + pr[i][0]) + (pr[i][1] + pr[i][2]);
    const u32x2 r = {pack_bf(o[0], o[1]), pack_bf(o[2], o[3])};
    *(u32x2*)(xt + (w * 16 + cc) * 64 + ((i ^ (cc >> 3)) * 32) + g * 8) = r;
  }
  __syncthreads();
  const char* xq = xt + ((w >> 1) * 32 + 8 * g + q) * 64 + (((w & 1) ^ (g & 1)) * 32) + 8 * p;
  const u32x4 f0 = cat2(tr_read(xq), tr_read(xq + 256));
  const int tok = (16 * h + c.P) * 64 + c.sub * 32 + (w & 1) * 16 + cc;
  if (LAST) {
    *(u32x4*)(c.out + (size_t)tok * DM + c.ct_ * 64 + (w >> 1) * 32 + 8 * g) = f0;
  } else {
    const int row = t >> 2;
    const u32x4 v = *(const u32x4*)(xt + row * 64 + (((t & 3) ^ ((row >> 2) & 2)) * 16));
    st128(c.rs, (c.ct_ * 64 + row) * 128 + (t & 3) * 16, xdst, v);
    float s = 0.f, ss = 0.f;
#pragma unroll
    for (int j = 0; j < 4; ++j) { s = dot2bf(f0[j], 0x3f803f80u, s); ss = dot2bf(f0[j], f0[j], ss); }
    s = sum_x16(s); ss = sum_x16(ss);
    (void)__hip_atomic_fetch_add((unsigned long long*)(c.ws + WS_ST + 65536) + 2 * tok + (g & 1),
                                 (unsigned long long)f2fx((g & 1) ? ss : s), RLX_AGENT);
  }
}

template <int LAYER>
DEVI void layer_body(const Ctx& c, u32x4 (&w1f)[2][16], u32x4 (&w2f)[14][4], CIn& inA, CIn& inB) {
  constexpr int xin = WS_X + LAYER * 4 * MB, xmid = xin + 2 * MB, xnext = xin + 4 * MB;
  constexpr int hbuf = WS_H + LAYER * 8 * MB, stin = WS_ST + LAYER * 65536;
  constexpr u32 ep = LAYER + 1;
  constexpr int K1 = 1 + 3 * LAYER, K2 = K1 + 1, K3 = K1 + 2;
  const int w = c.w;
  char* smem = c.smem;
  auto nop = [](auto) {};
  {
    const int lane = fresh_lane(c);
    const int g = lane >> 4, cc = lane & 15, mu0 = (w & 1) * 4, v16 = lane * 16;
    const char* ub = smem + L_U + (w >> 1) * L_UC + 512 + 2 * (16 * cc - 16 + 8 * g) - 64 * (mu0 + 3);
    KB kb;
#pragma unroll
    for (int j = 0; j < 4; ++j) {
      u32 a = lds_addr(smem) + (w >> 1) * 2 * L_KC + ((cc & 1) ? 0 : L_KC) + 2 * (2031 - cc + 8 * g - ((cc & 1) ? 0 : 1)) - 64 * (mu0 + 3 + 56) + 1024 * j;
      asm volatile("" : "+v"(a));
      kb.p[j] = (const char*)(const LAS char*)(size_t)a;
    }
    STAMP(K1, 1);
    f32x4 base0, base1;
    u32x4 a4[4];
    if (LAYER) conv_ldk<0>(kb, a4);
    conv_ln(c, lane, inA, 0, base0, base1);
    __syncthreads();
    if (!LAYER) conv_ldk<0>(kb, a4);
    STAMP(K1, 2);
    f32x4 acc[4];
    u32 o = 0;
    conv_mma<0, 0, true>(ub, kb, acc, a4, [&](auto N) {
      constexpr int n = N.v;
      if constexpr (LAYER && n == 19) { WAIT_VM(0); o = sig_begin(c, lane); SCHED(); }
      if constexpr (LAYER && n == 29) { sig_end<true>(c, lane, o, CT_X + 64); SCHED(); }
    });
    MFMA_DONE();
    conv_part(c, lane, acc);
    if (LAYER) conv_ldk<1>(kb, a4);
    else { ((float*)(smem + L_CST))[w * 64 + lane] = c.cs1; WAIT_VM(0); }
    __syncthreads();
    if (!LAYER) conv_ldk<1>(kb, a4);
    conv_out(c, lane, base0, base1, xmid, 0);
    STAMP(K1, 3);
    f32x4 acb[4];
    conv_mma<4, 0, true>(ub, kb, acb, a4, [&](auto N) {
      constexpr int n = N.v;
      if constexpr (!LAYER && (n & 3) == 1) { ld_w1<(n >> 2)>(c, v16, w1f); SCHED(); }
      if constexpr (n == 47) {
        if (LAYER) WAIT_VM(0); else WAIT_VM(12);
        o = sig_begin(c, lane);
        if (LAYER) poll<true>(c, lane, CT_X + 64);
        SCHED();
      }
      if constexpr (n == 57) { sig_end<true>(c, lane, o, CT_C); SCHED(); }
    });
    STAMP(K1, 4);
    if (LAYER) {
      if (!got<1>(32u)) wait_ge<true>(c, lane, CT_X + 64, 32u);
      conv_load(c, lane, inB, xin, stin, 1);
    }
    conv_ldk<0>(kb, a4);
    STAMP(K1, 5);
    conv_ln(c, lane, inB, 1, base0, base1);
    __syncthreads();
    conv_mma<0, 4, false>(ub, kb, acb, a4, [&](auto N) {
      constexpr int n = N.v;
      if constexpr (!LAYER && (n & 3) == 1) { ld_w1<16 + (n >> 2)>(c, v16, w1f); SCHED(); }
      if constexpr (n == 30) { poll<true>(c, lane, CT_C); SCHED(); }
    });
    MFMA_DONE();
    conv_part(c, lane, acb);
    __syncthreads();
    conv_out(c, lane, base0, base1, xmid, 1);
    STAMP(K1, 6);
    if (!got<(LAYER ? 2 : 4)>(32u * ep)) wait_ge<true>(c, lane, CT_C, 32u * ep);
    const int row = lane >> 3, sw = ((row >> 1) & 1) << 1;
    const int ve = row * 128 + ((lane & 7) ^ sw) * 16, vo = row * 128 + ((lane & 7) ^ (sw | 4)) * 16;
    sfor<0, 8>([&](auto N) { g1_dma1<N.v>(c, ve, vo, L_XA, xmid + c.P * 65536); });
    WAIT_VM(8);
    o = sig_begin(c, lane);
    sig_end<true>(c, lane, o, CT_C + 64);
    STAMP(K1, 7);
  }
  u32x4 hf[14][2];
  u32x2 xr[2];
  {
    const int lane = fresh_lane(c);
    const int g = lane >> 4, cc = lane & 15, q = cc >> 2, p = lane & 3, v16 = lane * 16;
    const int sx = ((q >> 1) & 1) | ((g & 1) << 1);
    const char* xb[4];
#pragma unroll
    for (int i = 0; i < 4; ++i) xb[i] = smem + L_XA + (8 * g + q) * 128 + ((((i + w) & 3) ^ sx) * 32) + 8 * p;
    const int row = lane >> 3, sw = ((row >> 1) & 1) << 1;
    const int ve = row * 128 + ((lane & 7) ^ sw) * 16, vo = row * 128 + ((lane & 7) ^ (sw | 4)) * 16;
    sfor<8, 12>([&](auto N) { g1_dma1<N.v>(c, ve, vo, L_XA, xmid + c.P * 65536); });
    WAIT_VM_BAR(9);
    STAMP(K2, 0);
    f32x4 acc[2][4], sa;
    g1_mma(xb, w1f, acc, sa, [&](auto KS) {
      constexpr int ks = KS.v;
      if constexpr (ks == 3) { if (LAYER) WAIT_VM_BAR(8); else WAIT_VM_BAR(10); SCHED(); }
      if constexpr (ks == 7) { if (LAYER) WAIT_VM_BAR(0); else WAIT_VM_BAR(10); SCHED(); }
      if constexpr (ks == 13) { if (!LAYER) WAIT_VM(16); poll<true>(c, lane, CT_C + 64); SCHED(); }
    }, [&](auto N) {
      constexpr int n = N.v;
      if constexpr (n < 4) g1_dma1<12 + n>(c, ve, vo, L_XA, xmid + c.P * 65536);
      if constexpr (!LAYER && n >= 4 && n < 10) ld_w1<22 + n>(c, v16, w1f);
      if constexpr (!LAYER && n >= 10) ld_w2<n - 10>(c, v16, w2f);
    });
    STAMP(K2, 1);
    const int xo = xmid + (16 + c.P) * 65536;
    g1_epi(c, lane, acc, sa, hbuf + (c.P * 4 * 56 + c.hks) * 1024, [&](auto N) {
      constexpr int n = N.v;
      if constexpr (n == -1) { if (!got<(LAYER ? 0 : 6)>(32u * ep)) wait_ge<true>(c, lane, CT_C + 64, 32u * ep); }
      if constexpr (n >= 0 && n < 8) g1_dma1<n>(c, ve, vo, L_XB, xo);
      if constexpr (n >= 10 && n < 18) g1_dma1<n - 2>(c, ve, vo, L_XB, xo);
    });
    STAMP(K2, 2);
  }
  {
    const int lane = fresh_lane(c);
    const int g = lane >> 4, cc = lane & 15, q = cc >> 2, p = lane & 3, v16 = lane * 16;
    const int sx = ((q >> 1) & 1) | ((g & 1) << 1);
    const char* xb[4];
#pragma unroll
    for (int i = 0; i < 4; ++i) xb[i] = smem + L_XB + (8 * g + q) * 128 + ((((i + w) & 3) ^ sx) * 32) + 8 * p;
    WAIT_VM_BAR(16);
    STAMP(K2, 3);
    f32x4 acc[2][4], sa;
    g1_mma(xb, w1f, acc, sa, [&](auto KS) {
      constexpr int ks = KS.v;
      if constexpr (ks == 6) {
        if (LAYER) WAIT_VM_BAR(0); else WAIT_VM_BAR(12);
        sig_bar<false>(c, lane, CT_H + c.P);
        SCHED();
      }
      if constexpr (ks == 15) {
#pragma unroll
        for (int i = 0; i < 2; ++i) xr[i] = ld64(c.rs, (c.ct_ * 64 + w * 16 + cc) * 128 + i * 32 + g * 8, xmid + c.P * 65536 + c.sub * 64);
        SCHED();
      }
    }, [&](auto N) {
      constexpr int n = N.v;
      if constexpr (!LAYER) ld_w2<22 + n>(c, v16, w2f);
      if constexpr (!LAYER && n >= 30) ld_w2<24 + n>(c, v16, w2f);
    });
    STAMP(K2, 4);
    poll<false>(c, lane, CT_H + c.P);
    const int ho = hbuf + ((c.P * 4 + c.sub * 2) * 56 + 14 * w) * 1024;
    g1_epi(c, lane, acc, sa, hbuf + ((16 + c.P) * 4 * 56 + c.hks) * 1024, [&](auto N) {
      constexpr int n = N.v;
      if constexpr (n == -1) { if (!got<0>(16u * ep)) wait_ge<false>(c, lane, CT_H + c.P, 16u * ep); }
      if constexpr (n >= 0) g2_ldh1<n>(c, v16, hf, ho);
    });
    STAMP(K2, 5);
  }
  {
    const int lane = fresh_lane(c);
    const int g = lane >> 4, cc = lane & 15;
    const int ho = hbuf + ((c.P * 4 + c.sub * 2) * 56 + 14 * w) * 1024, hoB = ho + 64 * 56 * 1024;
    STAMP(K3, 0);
    f32x4 acc[2][4];
    u32 o = 0;
    g2_mma<10, 1>(c, lane, hf, w2f, acc, ho, [&](auto S) {
      constexpr int s = S.v;
      if constexpr (s == 6) { WAIT_VM(8); o = sig_begin(c, lane); SCHED(); }
      if constexpr (s == 7) { sig_end<false>(c, lane, o, CT_H + 16 + c.P); SCHED(); }
    });
    STAMP(K3, 1);
    poll<false>(c, lane, CT_H + 16 + c.P);
    g2_epi<LAYER == 1>(c, lane, acc, xr, 0, xnext + c.P * 65536 + c.sub * 64, nop);
    STAMP(K3, 2);
    if (!got<(LAYER ? 1 : 2)>(16u * ep)) wait_ge<false>(c, lane, CT_H + 16 + c.P, 16u * ep);
    g2_ldh<0, 4>(c, lane, hf, hoB);
#pragma unroll
    for (int i = 0; i < 2; ++i) xr[i] = ld64(c.rs, (c.ct_ * 64 + w * 16 + cc) * 128 + i * 32 + g * 8, xmid + (16 + c.P) * 65536 + c.sub * 64);
    if (LAYER == 0) {
      WAIT_VM(10);
      o = sig_begin(c, lane);
      sig_end<true>(c, lane, o, CT_X);
    }
    SCHED();
    STAMP(K3, 3);
  }
  {
    const int lane = fresh_lane(c);
    const int hoB = hbuf + (((16 + c.P) * 4 + c.sub * 2) * 56 + 14 * w) * 1024;
    f32x4 acc[2][4];
    g2_mma<4, 2>(c, lane, hf, w2f, acc, hoB, [&](auto S) {
      constexpr int s = S.v;
      if constexpr (LAYER == 0 && s == 13) { poll<true>(c, lane, CT_X); SCHED(); }
    });
    STAMP(K3, 4);
    g2_epi<LAYER == 1>(c, lane, acc, xr, 1, xnext + (16 + c.P) * 65536 + c.sub * 64, [&](int) {
      if (LAYER == 0) {
        if (!got<0>(32u)) wait_ge<true>(c, lane, CT_X, 32u);
        conv_load(c, lane, inA, xnext, WS_ST + 65536, 0);
        SCHED();
      }
    });
    STAMP(K3, 5);
  }
}

__global__ void __launch_bounds__(256, 1) main_kernel(char* ws, u16* out) {
  extern __shared__ __attribute__((aligned(16))) char smem[];
  STAMP(1, 0);
  Ctx c;
  c.rs = __builtin_amdgcn_make_buffer_rsrc(ws, 0, WS_END, 0x00020000);
  c.ct = (gu32*)(ws + WS_CT);
  c.smem = smem; c.ws = ws; c.out = out;
  const int t = threadIdx.x, lane = t & 63, b = blockIdx.x;
  const int w = __builtin_amdgcn_readfirstlane(t >> 6);
  c.lane0 = lane; c.w = w; c.b = b;
  c.P = (b & 7) * 2 + ((b >> 3) & 1);
  const int idx = b >> 4;
  const int sg = idx >> 1;
  c.eo = idx & 1; c.sub = idx >> 3; c.ct_ = idx & 7;
  c.ff0 = w < 3 ? 14 * sg + 6 * c.eo + 2 * w : 14 * sg + 12 + c.eo;
  c.hks = w < 3 ? 7 * sg + 3 * c.eo + w : 7 * sg + 6;
  CIn inA, inB;
  const f32x4 ksc = *(const f32x4*)(ws + WS_KSC + b * 16);
  dma1k<2048>(c.rs, lds_addr(smem) + w * L_KC, lane * 16, WS_KT + b * 16384 + w * 4096);
  dma1k<3072>(c.rs, lds_addr(smem) + w * L_KC, lane * 16, WS_KT + b * 16384 + w * 4096);
  conv_load(c, lane, inA, WS_X, WS_ST, 0);
  conv_load(c, lane, inB, WS_X, WS_ST, 1);
  const float cs1 = ((const float*)(ws + WS_S1))[min(c.ff0 * 32 + lane, FF * 2 - 1)];
  c.bb2 = ((const float*)(ws + WS_B2))[c.ct_ * 64 + w * 16 + (lane & 15)];
  SCHED();
  dma1k<0>(c.rs, lds_addr(smem) + w * L_KC, lane * 16, WS_KT + b * 16384 + w * 4096);
  dma1k<1024>(c.rs, lds_addr(smem) + w * L_KC, lane * 16, WS_KT + b * 16384 + w * 4096);
  u32x4 w1f[2][16], w2f[14][4];
  c.f1o = w < 3 ? WS_W1P + c.ff0 * 16384 + 16384 : WS_ONE; c.f1s = w < 3 ? 4096 : 0;
  if (t < 16) *(u32x4*)(smem + (t >> 2) * L_KC + 4096 + (t & 3) * 16) = u32x4{0, 0, 0, 0};
  if (t >= 64 && t < 128) *(u32x4*)(smem + L_U + ((t - 64) >> 5) * L_UC + (t & 31) * 16) = u32x4{0, 0, 0, 0};
  if (t == 128) *(u32*)(smem + L_SIG) = 0u;
  c.ks0 = __uint_as_float(__builtin_amdgcn_readfirstlane(__float_as_uint(ksc[0])));
  c.dsk0 = __uint_as_float(__builtin_amdgcn_readfirstlane(__float_as_uint(ksc[1])));
  c.ks1 = __uint_as_float(__builtin_amdgcn_readfirstlane(__float_as_uint(ksc[2])));
  c.dsk1 = __uint_as_float(__builtin_amdgcn_readfirstlane(__float_as_uint(ksc[3])));
  c.cs1 = cs1;
  layer_body<0>(c, w1f, w2f, inA, inB);
  layer_body<1>(c, w1f, w2f, inA, inB);
}

extern "C" __global__ void code_end_marker() {}

extern "C" void kernel_launch(void* const* d_in, const int* in_sizes, int n_in, void* d_out, int out_size,
                              void* d_ws, size_t ws_size, hipStream_t stream) {
  (void)in_sizes; (void)n_in; (void)out_size; (void)ws_size;
  char* ws = (char*)d_ws;
  (void)hipFuncSetAttribute((const void*)main_kernel, hipFuncAttributeMaxDynamicSharedMemorySize, MAIN_LDS);
  prep_kernel<<<690, 256, PREP_LDS, stream>>>((const u16*)d_in[0], (const u16*)d_in[1], (const u16*)d_in[2],
                                              (const u16*)d_in[3], (const u16*)d_in[4], (const u16*)d_in[5],
                                              (const u16*)d_in[6], (const u16*)d_in[8], (const u16*)d_in[9],
                                              (const u16*)d_in[10], (const u16*)d_in[11], ws);
  main_kernel<<<256, 256, MAIN_LDS, stream>>>(ws, (u16*)d_out);
}
```

```cpp
#include <hip/hip_runtime.h>
#include <hip/hip_bf16.h>

typedef unsigned short u16;
typedef unsigned int u32;
typedef long long i64;
typedef __attribute__((ext_vector_type(4))) short s16x4;
typedef __attribute__((ext_vector_type(8))) __bf16 bf16x8;
typedef __attribute__((ext_vector_type(2))) __bf16 bf16x2;
typedef __attribute__((ext_vector_type(8))) _Float16 f16x8;
typedef __attribute__((ext_vector_type(2))) _Float16 f16x2;
typedef __attribute__((ext_vector_type(4))) float f32x4;
typedef __attribute__((ext_vector_type(2))) float f32x2;
typedef __attribute__((ext_vector_type(4))) unsigned u32x4;
typedef __attribute__((ext_vector_type(2))) unsigned u32x2;
typedef __attribute__((ext_vector_type(2))) long long i64x2;
typedef __attribute__((address_space(1))) unsigned gu32;
typedef __amdgpu_buffer_rsrc_t rsrc_t;
#define LAS __attribute__((address_space(3)))
#define DEVI __device__ __forceinline__
template <int V> struct IC { static constexpr int v = V; };
template <int I, int N, class F> __device__ __forceinline__ void sfor(F&& f) {
  if constexpr (I < N) { f(IC<I>{}); sfor<I + 1, N>(f); }
}
#define RLX_AGENT __ATOMIC_RELAXED, __HIP_MEMORY_SCOPE_AGENT

constexpr int SEQ = 2048, DM = 512, NST = 128, FF = 1792;
constexpr float LN_EPS = 1e-5f;
constexpr float FX_SCALE = 16777216.0f;
constexpr float FX_INV = 1.0f / 16777216.0f;
constexpr int SC1 = 16;

constexpr int MB = 1024 * 1024;
constexpr int WS_X = 0;
constexpr int WS_KT = 8 * MB;
constexpr int WS_W1P = 12 * MB;
constexpr int WS_W2P = 14 * MB;
constexpr int WS_H = 16 * MB;
constexpr int WS_ST = 32 * MB;
constexpr int WS_S1 = 32 * MB + 131072;
constexpr int WS_B2 = 32 * MB + 163840;
constexpr int WS_KSC = 32 * MB + 196608;
constexpr int WS_CT = 32 * MB + 262144;
constexpr int WS_ONE = 32 * MB + 393216;
constexpr int WS_END = 33 * MB;
constexpr int CT_C = 0, CT_X = 128, CT_H = 256, CT_TMO = 288, CT_LINES = 289;
constexpr int SPIN_CAP = 1 << 16;

#define STAMP(k, i) do {} while (0)

DEVI float bf2f(u16 v) { return __uint_as_float((u32)v << 16); }
DEVI float lo_bf(u32 w) { return __uint_as_float(w << 16); }
DEVI float hi_bf(u32 w) { return __uint_as_float(w & 0xffff0000u); }
DEVI float rbf(float x) { return (float)(__bf16)x; }
DEVI u32 pack_bf(float a, float b) { f32x2 q = {a, b}; return __builtin_bit_cast(u32, __builtin_convertvector(q, bf16x2)); }
DEVI u32 pack_h(float a, float b) { f32x2 q = {a, b}; return __builtin_bit_cast(u32, __builtin_convertvector(q, f16x2)); }
DEVI float dot2bf(u32 a, u32 b, float c) {
  return __builtin_amdgcn_fdot2_f32_bf16(__builtin_bit_cast(bf16x2, a), __builtin_bit_cast(bf16x2, b), c, false);
}
DEVI f32x4 mfma_bf(u32x4 a, u32x4 b, f32x4 c) {
  return __builtin_amdgcn_mfma_f32_16x16x32_bf16(__builtin_bit_cast(bf16x8, a), __builtin_bit_cast(bf16x8, b), c, 0, 0, 0);
}
DEVI f32x4 mfma_h(u32x4 a, u32x4 b, f32x4 c) {
  return __builtin_amdgcn_mfma_f32_16x16x32_f16(__builtin_bit_cast(f16x8, a), __builtin_bit_cast(f16x8, b), c, 0, 0, 0);
}
DEVI u32x2 tr_read(const char* p) {
  return __builtin_bit_cast(u32x2, __builtin_amdgcn_ds_read_tr16_b64_v4i16((LAS s16x4*)p));
}
DEVI u32x4 cat2(u32x2 a, u32x2 b) { return __builtin_shufflevector(a, b, 0, 1, 2, 3); }
DEVI u32 lds_addr(const void* p) { return (u32)(size_t)(const LAS char*)p; }
#define WAIT_VM(n) asm volatile("s_waitcnt vmcnt(" #n ")" ::: "memory")
#define WAIT_VM_BAR(n) asm volatile("s_waitcnt vmcnt(" #n ")\n\ts_barrier" ::: "memory")
#define SCHED() __builtin_amdgcn_sched_barrier(0)
template <int CTRL> DEVI float dpp_add(float v) {
  return v + __uint_as_float(__builtin_amdgcn_update_dpp(0u, __float_as_uint(v), CTRL, 0xf, 0xf, true));
}
DEVI float sum_x16(float v) {
  auto r = __builtin_amdgcn_permlane16_swap(__float_as_uint(v), __float_as_uint(v), false, false);
  return __uint_as_float(r[0]) + __uint_as_float(r[1]);
}
DEVI float sum_x32(float v) {
  auto r = __builtin_amdgcn_permlane32_swap(__float_as_uint(v), __float_as_uint(v), false, false);
  return __uint_as_float(r[0]) + __uint_as_float(r[1]);
}
DEVI float wave_sum(float v) {
  v = dpp_add<0xB1>(v); v = dpp_add<0x4E>(v); v = dpp_add<0x141>(v); v = dpp_add<0x140>(v);
  return sum_x32(sum_x16(v));
}
DEVI float fx2f(u32 lo, u32 hi) { return fmaf((float)(int)hi, 4294967296.0f * FX_INV, (float)lo * FX_INV); }
DEVI i64 f2fx(float v) { return (i64)(v * FX_SCALE); }
DEVI float sel4(int i, float a, float b, float c, float d) {
  asm volatile("" : "+v"(a), "+v"(b), "+v"(c), "+v"(d));
  return (i & 2) ? ((i & 1) ? d : c) : ((i & 1) ? b : a);
}
DEVI float rcp(float x) { return __builtin_amdgcn_rcpf(x); }
DEVI float sqrt_(float x) { return __builtin_amdgcn_sqrtf(x); }

DEVI u32 warm_code(int slice, int lane) {
  unsigned long long pc, pe;
  asm volatile("s_getpc_b64 %0\n\ts_getpc_b64 s[90:91]\n\ts_add_u32 s90, s90, code_end_marker@rel32@lo+4\n\t"
               "s_addc_u32 s91, s91, code_end_marker@rel32@hi+12\n\ts_mov_b64 %1, s[90:91]"
               : "=&s"(pc), "=s"(pe) :: "scc", "s90", "s91");
  const unsigned long long a = (pc + (unsigned)(slice * 512 + (lane & 7) * 64)) & ~3ull;
  u32 junk = 0;
  if (pc + (unsigned)(slice * 512 + 512) <= pe) asm volatile("global_load_dword %0, %1, off" : "=v"(junk) : "v"(a) : "memory");
  return junk;
}

DEVI int cidx(int l) {
  const int hb = (int)(pack_bf((float)l, 0.f) & 0xffffu) - (0x4300 - 128);
  return l < 128 ? l : hb;
}
struct CM { f32x2 e, f; };
DEVI f32x2 cmul(f32x2 a, const CM& m) { return a[1] * m.f + a[0] * m.e; }
DEVI CM cexp_(float a2, float br, float p) {
  const float e = __builtin_amdgcn_exp2f(a2 * p), f = __builtin_amdgcn_fractf(br * p);
  const float re = e * __builtin_amdgcn_cosf(f), im = e * __builtin_amdgcn_sinf(f);
  CM r; r.e = f32x2{re, im}; r.f = f32x2{-im, re};
  return r;
}
DEVI void st16(rsrc_t rs, int off, u32x4 v) { __builtin_amdgcn_raw_buffer_store_b128(v, rs, off, 0, SC1); }

constexpr int KG_RS = 528, KG_X = 16 * KG_RS, KG_KD = 64 * KG_RS;
DEVI void kgen(int d, const u16* lar, const u16* aim, const u16* cre, const u16* cim, const u16* ldt,
               const u16* dsk, rsrc_t rs, float* ksc, char* smem) {
  const int t = threadIdx.x, lane = t & 63, n = t & 127;
  const int w = __builtin_amdgcn_readfirstlane(t >> 6);
  const u16 r0 = ldt[d], r1 = lar[d * NST + n], r2 = aim[d * NST + n], r3 = cre[d * NST + n], r4 = cim[d * NST + n];
  const u16 r5 = cre[d * NST + (n ^ 64)], r6 = cim[d * NST + (n ^ 64)], r7 = dsk[d];
  SCHED();
  const int i = lane & 15, g = lane >> 4;
  int wi[4], ix[9];
  {
    const int m8 = -(i >> 3);
    const int h1 = ((256 + 128 * (i >> 2) + 2 * (i & 3)) & ~m8) | ((128 * (i >> 1) + 4 * (i & 1)) & m8);
    const int s0 = -(int)(w == 0), s1 = -(int)(w == 1);
    const int hp = (((i + (i & 8) * 15) & s0) | (h1 & s1) | ((1024 + 128 * i) & ~(s0 | s1))) + 32 * g;
#pragma unroll
    for (int r = 0; r < 4; ++r) { const int pos = hp + 8 * r; wi[r] = 2 * cidx(pos > 2048 ? 2064 : pos); asm volatile("" : "+v"(wi[r])); }
#pragma unroll
    for (int e2 = 0; e2 < 9; ++e2) { ix[e2] = 2 * cidx(max(2047 - 8 * t - e2, 0)); asm volatile("" : "+v"(ix[e2])); }
  }
  SCHED();
  const float dt = rbf(expf(bf2f(r0)));
  const float cr = bf2f(r3), ci = bf2f(r4), cr2 = bf2f(r5), ci2 = bf2f(r6);
  const float S = wave_sum(sqrt_(cr * cr + ci * ci) + sqrt_(cr2 * cr2 + ci2 * ci2)) * (2.002f * dt);
  int e = __builtin_amdgcn_frexp_expf(S);
  e = (S > 0.0f && S < 3.0e38f) ? max(-100, min(100, e)) : 14;
  const float scale = __builtin_amdgcn_ldexpf(2.0f, 14 - e);
  f32x2 ko = {__builtin_amdgcn_ldexpf(1.0f, e - 14), bf2f(r7)};
  asm volatile("" : "+v"(ko));
  STAMP(0, 1);
  const float a2 = (-rbf(expf(bf2f(r1))) * dt) * 1.44269504088896f;
  const float br = (bf2f(r2) * dt) * 0.159154943091895f;
  const CM e128 = cexp_(a2, -br, 128.f);
  char* yp = smem + n * 4;
  char* xp = smem + KG_X + n * 4;
  f32x2 f = {(cr * dt) * scale, -(ci * dt) * scale};
#define PUT(base, row, v) *(u32*)(base + (row) * KG_RS) = pack_h(v[0], v[1])
  if (w < 2) {
    const CM e8 = cexp_(a2, br, 8.f), e4 = cexp_(a2, -br, 4.f);
    f = cmul(f, cexp_(a2, -br, 512.f));
    f32x2 y = {1.f, 0.f};
#pragma unroll
    for (int b = 0; b < 16; ++b) {
      PUT(yp, b, y);
      if (b < 15) y = cmul(y, e8);
      if (b < 4) { const f32x2 g = cmul(f, e4); PUT(xp, 24 + 2 * b, f); PUT(xp, 25 + 2 * b, g); }
      else if (b < 13) PUT(xp, 28 + b, f);
      if (b < 12) f = cmul(f, e128);
    }
  } else {
    const CM e1 = cexp_(a2, -br, 1.f), e2 = cexp_(a2, -br, 2.f);
    f32x2 g1 = cmul(f, e128), g2 = cmul(g1, e128), g3 = cmul(g2, e128);
#pragma unroll
    for (int r = 0; r < 8; ++r) {
      PUT(xp, r, f); PUT(xp, 8 + r, g1);
      if (r < 7) { f = cmul(f, e1); g1 = cmul(g1, e1); }
      if (r < 4) { PUT(xp, 16 + r, g2); PUT(xp, 20 + r, g3); }
      if (r < 3) { g2 = cmul(g2, e2); g3 = cmul(g3, e2); }
    }
  }
#undef PUT
  STAMP(0, 2);
  __syncthreads();
  if (w < 3) {
    const char* ya = smem + i * KG_RS + g * 16;
    const char* xa = smem + KG_X + (16 * w + i) * KG_RS + g * 16;
    u32x4 fa[8], fb[8];
#pragma unroll
    for (int ks = 0; ks < 8; ++ks) { fa[ks] = *(const u32x4*)(ya + ks * 64); fb[ks] = *(const u32x4*)(xa + ks * 64); }
    SCHED();
    f32x4 acc = {};
#pragma unroll
    for (int ks = 0; ks < 8; ++ks) acc = mfma_h(fa[ks], fb[ks], acc);
#pragma unroll
    for (int r = 0; r < 4; ++r) *(u16*)(smem + KG_KD + wi[r]) = __builtin_bit_cast(u16, (_Float16)acc[r]);
  }
  __syncthreads();
  u32 v[9];
#pragma unroll
  for (int e2 = 0; e2 < 9; ++e2) v[e2] = *(const u16*)(smem + KG_KD + ix[e2]);
  if (t == 255) v[8] = 0u;
  u32x4 oe, oo;
#pragma unroll
  for (int j = 0; j < 4; ++j) { oe[j] = v[2 * j] | (v[2 * j + 1] << 16); oo[j] = v[2 * j + 1] | (v[2 * j + 2] << 16); }
  STAMP(0, 3);
  st16(rs, WS_KT + d * 8192 + t * 16, oe);
  st16(rs, WS_KT + d * 8192 + 4096 + t * 16, oo);
  if (t == 0) *(f32x2*)(ksc + 2 * d) = ko;
}

DEVI void xpose_stats(int b, const u16* x, rsrc_t rs, i64* st0, i64* st2, char* smem) {
  const int t = threadIdx.x, lane = t & 63, w = t >> 6;
  const int t0 = b * 32;
  constexpr int RS = 1024 + 32;
  u32x4 v[8];
#pragma unroll
  for (int r = 0; r < 8; ++r) v[r] = *(const u32x4*)(x + (size_t)(t0 + w * 8 + r) * DM + lane * 8);
  float a[16];
#pragma unroll
  for (int r = 0; r < 8; ++r) {
    *(u32x4*)(smem + (w * 8 + r) * RS + lane * 16) = v[r];
    float s = 0.f, ss = 0.f;
#pragma unroll
    for (int j = 0; j < 4; ++j) { s = dot2bf(v[r][j], 0x3f803f80u, s); ss = dot2bf(v[r][j], v[r][j], ss); }
    a[2 * r] = s; a[2 * r + 1] = ss;
  }
  const bool o1 = lane & 1, o2 = lane & 2;
#pragma unroll
  for (int k = 0; k < 8; ++k) {
    const float keep = o1 ? a[2 * k + 1] : a[2 * k], send = o1 ? a[2 * k] : a[2 * k + 1];
    a[k] = keep + __uint_as_float(__builtin_amdgcn_update_dpp(0u, __float_as_uint(send), 0xB1, 0xf, 0xf, true));
  }
#pragma unroll
  for (int k = 0; k < 4; ++k) {
    const float keep = o2 ? a[2 * k + 1] : a[2 * k], send = o2 ? a[2 * k] : a[2 * k + 1];
    a[k] = keep + __uint_as_float(__builtin_amdgcn_update_dpp(0u, __float_as_uint(send), 0x4E, 0xf, 0xf, true));
  }
#pragma unroll
  for (int k = 0; k < 4; ++k) a[k] = sum_x32(sum_x16(dpp_add<0x128>(dpp_add<0x124>(a[k]))));
  if (lane < 16) {
    st0[2 * (t0 + w * 8) + lane] = f2fx(sel4(lane >> 2, a[0], a[1], a[2], a[3]));
    st2[2 * (t0 + w * 8) + lane] = 0;
  }
  __syncthreads();
  const int g = lane >> 4, q = (lane & 15) >> 2, p = lane & 3;
#pragma unroll
  for (int cf = 0; cf < 8; ++cf) {
    const int c0 = (w * 8 + cf) * 16;
    const char* a = smem + (8 * g + q) * RS + c0 * 2 + 8 * p;
    const u32x4 o = cat2(tr_read(a), tr_read(a + 4 * RS));
    st16(rs, WS_X + (b >> 1) * 65536 + (c0 + (lane & 15)) * 128 + (b & 1) * 64 + g * 16, o);
  }
}

DEVI void pack_w1(int b, const u16* w1, const u16* b1, rsrc_t rs, float* s1, char* smem) {
  const int t = threadIdx.x, lane = t & 63, w = t >> 6;
  u32 bv = b1[b * 32 + (t & 31)];
  constexpr int RS = 64 + 32;
  u32x4 v[8];
#pragma unroll
  for (int it = 0; it < 8; ++it) v[it] = *(const u32x4*)(w1 + (size_t)(it * 64 + (t >> 2)) * FF + b * 32 + (t & 3) * 8);
#pragma unroll
  for (int it = 0; it < 8; ++it) *(u32x4*)(smem + (it * 64 + (t >> 2)) * RS + (t & 3) * 16) = v[it];
  asm volatile("" : "+v"(bv));
  __syncthreads();
  const int g = lane >> 4, q = (lane & 15) >> 2, p = lane & 3;
  float cs[2] = {0.f, 0.f};
#pragma unroll
  for (int kk = 0; kk < 4; ++kk) {
    const int ks = w * 4 + kk;
#pragma unroll
    for (int h = 0; h < 2; ++h) {
      const char* a = smem + (32 * ks + 8 * g + q) * RS + h * 32 + 8 * p;
      const u32x4 o = cat2(tr_read(a), tr_read(a + 4 * RS));
#pragma unroll
      for (int j = 0; j < 4; ++j) cs[h] = dot2bf(o[j], 0x3f803f80u, cs[h]);
      st16(rs, WS_W1P + (((2 * b + h) * 16 + ks) * 64 + lane) * 16, o);
    }
  }
  float* red = (float*)(smem + 512 * RS);
#pragma unroll
  for (int h = 0; h < 2; ++h) {
    const float s = sum_x32(sum_x16(cs[h]));
    if (lane < 16) red[w * 32 + h * 16 + lane] = s;
  }
  __syncthreads();
  if (t < 32) {
    s1[b * 64 + (t >> 4) * 32 + (t & 15)] = (red[t] + red[32 + t]) + (red[64 + t] + red[96 + t]);
    s1[b * 64 + (t >> 4) * 32 + 16 + (t & 15)] = lo_bf(bv);
  }
}

DEVI void pack_w2(int b, const u16* w2, rsrc_t rs, char* smem) {
  const int t = threadIdx.x, lane = t & 63, w = t >> 6;
  constexpr int RS = 1024 + 32;
  u32x4 v[8];
#pragma unroll
  for (int r = 0; r < 8; ++r) v[r] = *(const u32x4*)(w2 + (size_t)(b * 32 + w * 8 + r) * DM + lane * 8);
#pragma unroll
  for (int r = 0; r < 8; ++r) *(u32x4*)(smem + (w * 8 + r) * RS + lane * 16) = v[r];
  __syncthreads();
  const int g = lane >> 4, q = (lane & 15) >> 2, p = lane & 3;
#pragma unroll
  for (int cf = 0; cf < 8; ++cf) {
    const int nf = w * 8 + cf;
    const char* a = smem + (4 * g + q) * RS + nf * 32 + 8 * p;
    const u32x4 o = cat2(tr_read(a), tr_read(a + 16 * RS));
    st16(rs, WS_W2P + ((nf * 56 + b) * 64 + lane) * 16, o);
  }
}

constexpr int PREP_LDS = 512 * 96 + 512;
__global__ void __launch_bounds__(256) prep_kernel(const u16* x, const u16* lar, const u16* aim, const u16* cre,
                                                   const u16* cim, const u16* ldt, const u16* dsk, const u16* w1,
                                                   const u16* b1, const u16* w2, const u16* b2, char* ws, const char* out) {
  extern __shared__ __attribute__((aligned(16))) char smem[];
  const int b = blockIdx.x < 512 ? blockIdx.x + 178 : blockIdx.x - 512;
  const u32 junk = warm_code((blockIdx.x >> 3) * 4 + __builtin_amdgcn_readfirstlane(threadIdx.x >> 6), threadIdx.x);
  u32 junk2 = 0;
  if (blockIdx.x >= 512 && blockIdx.x < 528 && threadIdx.x < 20) {
    const char* a = threadIdx.x < 17 ? ws + threadIdx.x * (2 * MB) : out + (threadIdx.x - 17) * (MB - 64);
    asm volatile("global_load_dword %0, %1, off" : "=v"(junk2) : "v"(a) : "memory");
  }
  STAMP(0, 0);
  const rsrc_t rs = __builtin_amdgcn_make_buffer_rsrc(ws, 0, WS_END, 0x00020000);
  if (b < 178) __builtin_amdgcn_s_setprio(3);
  if (b < 64) xpose_stats(b, x, rs, (i64*)(ws + WS_ST), (i64*)(ws + WS_ST + 65536), smem);
  else if (b < 120) pack_w1(b - 64, w1, b1, rs, (float*)(ws + WS_S1), smem);
  else if (b < 176) {
    pack_w2(b - 120, w2, rs, smem);
    if (b < 122) { const int i = (b - 120) * 256 + threadIdx.x; if (i < CT_LINES) *(u32*)(ws + WS_CT + i * 256) = 0u; }
    if (b == 122) *(u32x4*)(ws + WS_ONE + threadIdx.x * 16) = u32x4{0x3f803f80u, 0x3f803f80u, 0x3f803f80u, 0x3f803f80u};
  } else if (b < 178) {
    ((float*)(ws + WS_B2))[(b - 176) * 256 + threadIdx.x] = bf2f(b2[(b - 176) * 256 + threadIdx.x]);
  } else kgen(b - 178, lar, aim, cre, cim, ldt, dsk, rs, (float*)(ws + WS_KSC), smem);
  asm volatile("s_waitcnt vmcnt(0)" :: "v"(junk), "v"(junk2) : "memory");
  STAMP(0, (b < 64 ? 4 : (b < 120 ? 6 : (b < 178 ? 7 : 5))));
}

constexpr int L_KC = 4160;
constexpr int L_U = 4 * L_KC, L_UC = 4608;
constexpr int L_XA = 26112, L_XB = L_XA + 65536;
constexpr int L_MISC = L_XB + 65536;
constexpr int L_CST = L_MISC + 512 + 4 * 512;
constexpr int L_SIG = L_CST + 4 * 256;
constexpr int MAIN_LDS = L_SIG + 256;
constexpr int L_RED = L_XB, L_SCR = L_XB + 16384;

DEVI u32x4 ld128(rsrc_t rs, int v, int s) { return __builtin_amdgcn_raw_buffer_load_b128(rs, v, s, SC1); }
DEVI u32x2 ld64(rsrc_t rs, int v, int s) { return __builtin_amdgcn_raw_buffer_load_b64(rs, v, s, SC1); }
DEVI u32x4 ld128p(rsrc_t rs, int v, int s) { return __builtin_amdgcn_raw_buffer_load_b128(rs, v, s, 0); }
DEVI void st128(rsrc_t rs, int v, int s, u32x4 d) { __builtin_amdgcn_raw_buffer_store_b128(d, rs, v, s, SC1); }
DEVI void st64(rsrc_t rs, int v, int s, u32x2 d) { __builtin_amdgcn_raw_buffer_store_b64(d, rs, v, s, SC1); }
template <int OFF> DEVI u32x4 ld128a(rsrc_t rs, int v, int s) {
  u32x4 r;
  asm volatile("buffer_load_dwordx4 %0, %1, %2, %3 offen offset:%4" : "=a"(r) : "v"(v), "s"(rs), "s"(s), "i"(OFF) : "memory");
  return r;
}
template <int OFF> DEVI u32x4 ld128v(rsrc_t rs, int v, int s) {
  u32x4 r;
  asm volatile("buffer_load_dwordx4 %0, %1, %2, %3 offen offset:%4" : "=v"(r) : "v"(v), "s"(rs), "s"(s), "i"(OFF) : "memory");
  return r;
}
template <int OFF> DEVI void dma1k(rsrc_t rs, u32 lds_dst, int v, int soff) {
  asm volatile("s_mov_b32 m0, %0\n\ts_nop 0\n\tbuffer_load_dwordx4 %1, %2, %3 offen offset:%4 sc1 lds"
               :: "s"(lds_dst), "v"(v), "s"(rs), "s"(soff), "i"(OFF) : "memory");
}
#define MMA(NAME, OP, CA, CB)                                                                                   \
  template <bool Z> DEVI void NAME(f32x4& d, const u32x4& a, const u32x4& b) {                                  \
    if (Z) asm volatile(OP " %0, %1, %2, 0" : "=&v"(d) : CA(a), CB(b));                                         \
    else asm volatile(OP " %0, %1, %2, %0" : "+v"(d) : CA(a), CB(b));                                           \
  }
MMA(mma_vv, "v_mfma_f32_16x16x32_bf16", "v", "v")
MMA(mma_av, "v_mfma_f32_16x16x32_bf16", "a", "v")
MMA(mma_va, "v_mfma_f32_16x16x32_bf16", "v", "a")
MMA(mma_hh, "v_mfma_f32_16x16x32_f16", "v", "v")
#define MFMA_DONE() do { SCHED(); asm volatile("s_nop 7" ::: "memory"); SCHED(); } while (0)

struct Ctx {
  rsrc_t rs; gu32* ct; char* smem; char* ws; u16* out;
  int lane0, w, b, P, sub, ct_, ff0, hks, eo, f1o, f1s;
  float dsk0, dsk1, ks0, ks1, bb2, cs1;
};
DEVI int fresh_lane(const Ctx& c) { int l = c.lane0; asm volatile("" : "+v"(l)); return l; }
DEVI u32 sig_begin(const Ctx& c, int lane) {
  u32 old;
  asm volatile("ds_add_rtn_u32 %0, %1, %2" : "=v"(old) : "v"(lds_addr(c.smem) + L_SIG + lane * 4), "v"(1u) : "memory");
  return old;
}
template <bool ALL> DEVI void sig_end(const Ctx& c, int lane, u32 old, int line) {
  asm volatile("s_waitcnt lgkmcnt(0)" : "+v"(old) :: "memory");
  const bool last = (__builtin_amdgcn_readfirstlane(old) & 3u) == 3u;
  const int v = (last && lane < (ALL ? 8 : 1)) ? (ALL ? lane * 2048 : 0) : 0x40000000;
  asm volatile("s_nop 4\n\tbuffer_atomic_add %0, %1, %2, %3 offen" :: "v"(1u), "v"(v), "s"(c.rs),
               "s"(WS_CT + (line + (ALL ? (c.b >> 3) & 7 : 0)) * 256) : "memory");
}
template <bool ALL, int R = 0> DEVI void poll(const Ctx& c, int lane, int line) {
  const int v = ALL ? (lane & 7) * 256 : 0, so = WS_CT + (line + (ALL ? (c.b & 7) * 8 : 0)) * 256;
  if (R == 0) asm volatile("s_nop 4\n\tbuffer_load_dword v254, %0, %1, %2 offen sc1" :: "v"(v), "s"(c.rs), "s"(so) : "memory", "v254");
  else asm volatile("s_nop 4\n\tbuffer_load_dword v255, %0, %1, %2 offen sc1" :: "v"(v), "s"(c.rs), "s"(so) : "memory", "v255");
}
template <int YOUNGER, int R = 0> DEVI bool got(u32 want) {
  unsigned long long m;
  if (R == 0) asm volatile("s_waitcnt vmcnt(%2)\n\tv_cmp_le_u32_e64 %0, %1, v254" : "=s"(m) : "s"(want), "i"(YOUNGER) : "memory");
  else asm volatile("s_waitcnt vmcnt(%2)\n\tv_cmp_le_u32_e64 %0, %1, v255" : "=s"(m) : "s"(want), "i"(YOUNGER) : "memory");
  return m == ~0ull;
}
template <bool ALL> DEVI void wait_ge(const Ctx& c, int lane, int line, u32 want) {
  int sp = 0;
  poll<ALL, 0>(c, lane, line);
  __builtin_amdgcn_s_sleep(6);
  poll<ALL, 1>(c, lane, line);
#pragma nounroll
  for (;;) {
    if (got<1, 0>(want)) break;
    poll<ALL, 0>(c, lane, line);
    if (got<1, 1>(want)) break;
    poll<ALL, 1>(c, lane, line);
    if (++sp > SPIN_CAP) { __hip_atomic_store(c.ct + CT_TMO * 64, 1u, RLX_AGENT); break; }
  }
}

struct CIn { u32x2 x0, x1; u32x4 s[4]; };
DEVI void conv_load(const Ctx& c, int lane, CIn& in, int xoff, int stoff, int h) {
  const int sx = xoff + (16 * h + 4 * c.w) * 65536 + c.b * 256;
  const int vx = (lane >> 4) * 65536 + (lane & 15) * 8;
  const int ss = stoff + (1024 * h + 256 * c.w) * 16;
#pragma unroll
  for (int j = 0; j < 4; ++j) in.s[j] = ld128(c.rs, lane * 16 + j * 1024, ss);
  in.x0 = ld64(c.rs, vx, sx);
  in.x1 = ld64(c.rs, vx + 128, sx);
}
DEVI void conv_ln(const Ctx& c, int lane, const CIn& in, int h, f32x4& base0, f32x4& base1) {
  char* scr = c.smem + L_SCR + c.w * 2048;
  constexpr float CH = 4294967296.0f * FX_INV / DM, CL = FX_INV / DM;
  f32x4 m, r;
#pragma unroll
  for (int j = 0; j < 4; ++j) {
    m[j] = fmaf((float)(int)in.s[j][1], CH, (float)in.s[j][0] * CL);
    r[j] = fmaf((float)(int)in.s[j][3], CH, fmaf((float)in.s[j][2], CL, LN_EPS));
  }
#pragma unroll
  for (int j = 0; j < 4; ++j) r[j] = __builtin_amdgcn_rsqf(fmaxf(fmaf(-m[j], m[j], r[j]), LN_EPS));
#pragma unroll
  for (int j = 0; j < 4; ++j) {
    *(float*)(scr + (64 * j + lane) * 4) = r[j];
    *(float*)(scr + 1024 + (64 * j + lane) * 4) = -m[j] * r[j];
  }
  const f32x4 rr = *(const f32x4*)(scr + lane * 16), mr = *(const f32x4*)(scr + 1024 + lane * 16);
  const f32x4 x0 = {lo_bf(in.x0[0]), hi_bf(in.x0[0]), lo_bf(in.x0[1]), hi_bf(in.x0[1])};
  const f32x4 x1 = {lo_bf(in.x1[0]), hi_bf(in.x1[0]), lo_bf(in.x1[1]), hi_bf(in.x1[1])};
  const f32x4 u0 = x0 * rr + mr, u1 = x1 * rr + mr;
  char* up = c.smem + L_U + 512 + (1024 * h + 256 * c.w + 4 * lane) * 2;
  *(u32x2*)up = u32x2{pack_h(u0[0], u0[1]), pack_h(u0[2], u0[3])};
  *(u32x2*)(up + L_UC) = u32x2{pack_h(u1[0], u1[1]), pack_h(u1[2], u1[3])};
  base0 = u0 * c.dsk0 + x0; base1 = u1 * c.dsk1 + x1;
}
struct KB { const char* p[4]; };
template <int MM, int LAM> DEVI u32x4 conv_k1(const KB& kb) {
  const u32* kp = (const u32*)(kb.p[(7 - LAM) >> 1] + 64 * (3 - MM) + 512 * ((7 - LAM) & 1));
  return u32x4{kp[0], kp[1], kp[2], kp[3]};
}
template <int MM, int TB> DEVI u32x4 conv_u1(const char* ub) { return *(const u32x4*)(ub + 64 * (3 - MM) + 512 * TB); }
template <int LO> DEVI void conv_ldk(const KB& kb, u32x4 (&af)[4]) {
  sfor<0, 4>([&](auto L) { af[L.v] = conv_k1<0, LO + L.v>(kb); });
}
constexpr int conv_rank(int DT, int LO, int gr, int lt) {
  int k = 0;
  for (int x = 0; x < lt; ++x) { const int tb = DT + (x & 3) - (LO + 4 * gr + (x >> 2)); k += (tb >= 0 && tb < 4); }
  return k;
}
template <int DT, int TB0, bool ZERO, class F>
DEVI void conv_mma(const char* ub, const KB& kb, f32x4 (&acc)[4], const u32x4 (&af0)[4], F&& hook) {
  constexpr int NG = DT ? 2 : 1, LO = DT ? 1 : 0;
  u32x4 af[2][4], bf[2][4];
  sfor<0, 4>([&](auto L) { af[0][L.v] = af0[L.v]; });
  sfor<0, 4>([&](auto T) { constexpr int tb = DT ? 3 - T.v : T.v; bf[0][tb] = conv_u1<0, TB0 + tb>(ub); });
  SCHED();
  sfor<0, 4 * NG>([&](auto ST) {
    constexpr int st = ST.v, mm = st / NG, gr = st % NG;
    constexpr bool more = st + 1 < 4 * NG;
    constexpr int mn = (st + 1) / NG, gn = (st + 1) % NG;
    constexpr bool nu = more && gr == NG - 1;
    sfor<0, 16>([&](auto LT) {
      constexpr int l = LT.v >> 2, T = LT.v & 3, tb = DT + T - (LO + 4 * gr + l);
      if constexpr (tb >= 0 && tb < 4) {
        constexpr int k = conv_rank(DT, LO, gr, LT.v);
        constexpr bool first = ZERO && st == 0 && (DT ? (l == T) : (l == 0));
        mma_hh<first>(acc[T], af[st & 1][l], bf[mm & 1][tb]);
        sfor<(gr == 1 ? 2 * k : k), (gr == 1 ? 2 * k + 2 : k + 1)>([&](auto J) {
          constexpr int j = J.v;
          if constexpr (nu && j < 8) {
            if constexpr (DT != 0) {
              if constexpr (j & 1) bf[mn & 1][3 - (j >> 1)] = conv_u1<mn, TB0 + 3 - (j >> 1)>(ub);
              else af[(st + 1) & 1][j >> 1] = conv_k1<mn, LO + (j >> 1)>(kb);
            } else {
              if constexpr (j >= 1 && j <= 4) bf[mn & 1][j - 1] = conv_u1<mn, TB0 + j - 1>(ub);
              else af[(st + 1) & 1][j ? j - 4 : 0] = conv_k1<mn, LO + (j ? j - 4 : 0)>(kb);
            }
          } else if constexpr (more && !nu && j < 3) af[(st + 1) & 1][j] = conv_k1<mn, LO + 4 * gn + j>(kb);
        });
        SCHED();
        hook(IC<mm * (DT ? 16 : 10) + gr * 10 + k>{});
      }
    });
  });
}
DEVI void conv_part(const Ctx& c, int lane, const f32x4 (&acc)[4]) {
  char* red = c.smem + L_RED;
#pragma unroll
  for (int T = 0; T < 4; ++T) *(f32x4*)(red + (c.w * 4 + T) * 1024 + (lane & 15) * 64 + (lane >> 4) * 16) = acc[T];
}
DEVI void conv_out(const Ctx& c, int lane, const f32x4& base0, const f32x4& base1, int xoff, int h) {
  const int sx = xoff + (16 * h + 4 * c.w) * 65536 + c.b * 256;
  const int vx = (lane >> 4) * 65536 + (lane & 15) * 8;
  const char* p = c.smem + L_RED + c.w * 1024 + lane * 16;
  const f32x4 a0 = *(const f32x4*)p, b0 = *(const f32x4*)(p + 4096), a1 = *(const f32x4*)(p + 8192), b1 = *(const f32x4*)(p + 12288);
  SCHED();
  const f32x4 y0 = (a0 + b0) * c.ks0 + base0, y1 = (a1 + b1) * c.ks1 + base1;
  st64(c.rs, vx, sx, u32x2{pack_bf(y0[0], y0[1]), pack_bf(y0[2], y0[3])});
  st64(c.rs, vx + 128, sx, u32x2{pack_bf(y1[0], y1[1]), pack_bf(y1[2], y1[3])});
}

template <int PC> DEVI void g1_dma1(const Ctx& c, int ve, int vo, int ldsx, int soff) {
  dma1k<(PC & 3) * 1024>(c.rs, lds_addr(c.smem) + ldsx + (4 * (PC >> 2) + c.w) * 4096, (PC & 1) ? vo : ve, soff + (4 * (PC >> 2) + c.w) * 4096);
}
template <bool ALL> DEVI void sig_bar(const Ctx& c, int lane, int line) {
  const int v = (c.w == 0 && lane < (ALL ? 8 : 1)) ? (ALL ? lane * 2048 : 0) : 0x40000000;
  asm volatile("s_nop 4\n\tbuffer_atomic_add %0, %1, %2, %3 offen" :: "v"(1u), "v"(v), "s"(c.rs),
               "s"(WS_CT + (line + (ALL ? (c.b >> 3) & 7 : 0)) * 256) : "memory");
}
template <class F, class G>
DEVI void g1_mma(const char* const (&xb)[4], const u32x4 (&w1f)[2][16], f32x4 (&acc)[2][4], f32x4& sa, F&& mid, G&& hook) {
  u32x2 xf[2][4][2];
#pragma unroll
  for (int i = 0; i < 4; ++i) { xf[0][i][0] = tr_read(xb[i]); xf[0][i][1] = tr_read(xb[i] + 512); }
  SCHED();
  sfor<0, 16>([&](auto KS) {
    constexpr int ks = KS.v;
    mid(KS);
    sfor<0, 4>([&](auto I) {
      constexpr int i = I.v;
      const u32x4 x = cat2(xf[ks & 1][i][0], xf[ks & 1][i][1]);
      mma_vv<ks == 0>(acc[0][i], w1f[0][ks], x);
      if constexpr (ks < 15) xf[(ks + 1) & 1][i][0] = tr_read(xb[i] + (ks + 1) * 4096);
      SCHED();
      if constexpr (ks < 8) mma_av<ks == 0>(acc[1][i], w1f[1][ks], x); else mma_vv<false>(acc[1][i], w1f[1][ks], x);
      if constexpr (ks < 15) xf[(ks + 1) & 1][i][1] = tr_read(xb[i] + (ks + 1) * 4096 + 512);
      SCHED();
      if constexpr (i == 0) { mma_vv<ks == 0>(sa, x, x); hook(IC<2 * ks>{}); SCHED(); }
      if constexpr (i == 2) { hook(IC<2 * ks + 1>{}); SCHED(); }
    });
  });
  MFMA_DONE();
}
template <class F>
DEVI void g1_epi(const Ctx& c, int lane, f32x4 (&acc)[2][4], const f32x4& sa, int hoff, F&& hook) {
  const int g = lane >> 4, cc = lane & 15, q = cc >> 2, p = lane & 3, w = c.w;
  float* sums = (float*)(c.smem + L_MISC);
  {
    const float d = sel4(p, sa[0], sa[1], sa[2], sa[3]);
    if (q == g) sums[64 + w * 16 + cc] = d;
    const float r = sel4(g, acc[1][1][0], acc[1][2][0], acc[1][3][0], acc[1][0][0]);
    if (w == 3) sums[lane] = r;
  }
  f32x4 s1v[2], b1v[2];
#pragma unroll
  for (int j = 0; j < 2; ++j) {
    s1v[j] = *(const f32x4*)(c.smem + L_CST + w * 256 + j * 128 + g * 16);
    b1v[j] = *(const f32x4*)(c.smem + L_CST + w * 256 + j * 128 + 64 + g * 16);
  }
  __syncthreads();
  f32x2* cs = (f32x2*)(c.smem + L_MISC + 512 + w * 512);
  {
    const float m = sums[lane] * (1.0f / DM);
    const float ve = fmaxf(fmaf(-m, m, fmaf(sums[64 + lane], 1.0f / DM, LN_EPS)), LN_EPS);
    const float r = __builtin_amdgcn_rsqf(fmaf(ve, LN_EPS, ve - LN_EPS));
    f32x2 o = {r, -m * r};
    cs[lane] = o;
  }
  const int v16 = lane * 16 + (w < 3 ? 0 : 0x40000000), v8 = lane * 16 + (w < 3 ? 0x40000000 : c.eo * 8);
  f32x2 k[4];
#pragma unroll
  for (int i = 0; i < 4; ++i) k[i] = cs[((i + w) & 3) * 16 + cc];
  SCHED();
  hook(IC<-1>{});
  SCHED();
  sfor<0, 2>([&](auto IH) {
    constexpr int ih = IH.v;
    f32x4 x[4], e[4];
#define STAGE(n) do { SCHED(); hook(IC<10 * ih + n>{}); SCHED(); } while (0)
#pragma unroll
    for (int n = 0; n < 4; ++n) x[n] = k[2 * ih + (n >> 1)][1] * s1v[n & 1] + b1v[n & 1];
#pragma unroll
    for (int n = 0; n < 4; ++n) x[n] = acc[n & 1][2 * ih + (n >> 1)] * k[2 * ih + (n >> 1)][0] + x[n];
    STAGE(0);
#pragma unroll
    for (int n = 0; n < 4; ++n) e[n] = x[n] * x[n];
#pragma unroll
    for (int n = 0; n < 4; ++n) e[n] = e[n] * (-0.0713548162726f * 1.44269504088896f) + (-1.59576912160573f * 1.44269504088896f);
    STAGE(1);
#pragma unroll
    for (int n = 0; n < 4; ++n) e[n] = e[n] * x[n];
    STAGE(2);
#pragma unroll
    for (int r = 0; r < 4; ++r) { e[0][r] = __builtin_amdgcn_exp2f(e[0][r]); e[1][r] = __builtin_amdgcn_exp2f(e[1][r]); }
    STAGE(3);
#pragma unroll
    for (int r = 0; r < 4; ++r) { e[2][r] = __builtin_amdgcn_exp2f(e[2][r]); e[3][r] = __builtin_amdgcn_exp2f(e[3][r]); }
    STAGE(4);
#pragma unroll
    for (int n = 0; n < 4; ++n) e[n] = e[n] + 1.0f;
    STAGE(5);
#pragma unroll
    for (int r = 0; r < 4; ++r) { e[0][r] = rcp(e[0][r]); e[1][r] = rcp(e[1][r]); }
    STAGE(6);
#pragma unroll
    for (int r = 0; r < 4; ++r) { e[2][r] = rcp(e[2][r]); e[3][r] = rcp(e[3][r]); }
    STAGE(7);
#pragma unroll
    for (int n = 0; n < 4; ++n) x[n] = x[n] * e[n];
    STAGE(8);
    u32x4 hv[2];
#pragma unroll
    for (int n = 0; n < 4; ++n) { hv[n >> 1][2 * (n & 1)] = pack_bf(x[n][0], x[n][1]); hv[n >> 1][2 * (n & 1) + 1] = pack_bf(x[n][2], x[n][3]); }
    STAGE(9);
    const int so0 = hoff + ((2 * ih + w) & 3) * 56 * 1024, so1 = hoff + ((2 * ih + 1 + w) & 3) * 56 * 1024;
    st128(c.rs, v16, so0, hv[0]); st128(c.rs, v16, so1, hv[1]);
    st64(c.rs, v8, so0, u32x2{hv[0][0], hv[0][1]}); st64(c.rs, v8, so1, u32x2{hv[1][0], hv[1][1]});
    SCHED();
    if constexpr (ih == 0) { hook(IC<-2>{}); SCHED(); }
#undef STAGE
  });
}

constexpr int PF = 10;
template <int S0, int S1> DEVI void g2_ldh(const Ctx& c, int lane, u32x4 (&hf)[14][2], int ho) {
#pragma unroll
  for (int s = S0; s < S1; ++s)
#pragma unroll
    for (int i = 0; i < 2; ++i) hf[s][i] = ld128(c.rs, lane * 16 + (s & 3) * 1024, ho + i * 56 * 1024 + (s >> 2) * 4096);
}
template <int N> DEVI void ld_w1(const Ctx& c, int v, u32x4 (&w1f)[2][16]) {
  constexpr int ks = N >> 1, j = N & 1;
  const int so = (j ? c.f1o + (ks >> 2) * c.f1s : WS_W1P + c.ff0 * 16384 + (ks >> 2) * 4096);
  if constexpr (j && ks < 8) w1f[j][ks] = ld128a<(ks & 3) * 1024>(c.rs, v, so);
  else w1f[j][ks] = ld128v<(ks & 3) * 1024>(c.rs, v, so);
}
template <int N> DEVI void ld_w2(const Ctx& c, int v, u32x4 (&w2f)[14][4]) {
  constexpr int s = N >> 2, j = N & 3;
  w2f[s][j] = ld128a<(s & 3) * 1024>(c.rs, v, WS_W2P + ((c.ct_ * 4 + ((c.w + j) & 3)) * 56 + 14 * c.w + (s & ~3)) * 1024);
}
template <int N> DEVI void g2_ldh1(const Ctx& c, int v16, u32x4 (&hf)[14][2], int ho) {
  constexpr int s = N >> 1, i = N & 1;
  hf[s][i] = ld128(c.rs, v16 + (s & 3) * 1024, ho + i * 56 * 1024 + (s >> 2) * 4096);
}
template <int PRE, int RATE, class F>
DEVI void g2_mma(const Ctx& c, int lane, u32x4 (&hf)[14][2], const u32x4 (&w2f)[14][4], f32x4 (&acc)[2][4], int ho, F&& mid) {
  const int v16 = lane * 16;
  sfor<0, 14>([&](auto S) {
    constexpr int s = S.v;
    sfor<0, 8>([&](auto M) {
      constexpr int i = M.v >> 2, j = M.v & 3;
      mma_va<s == 0>(acc[i][j], hf[s][i], w2f[s][j]);
      constexpr int per = 4 / RATE, n = 2 * (PRE + RATE * s) + M.v / per;
      if constexpr (M.v % per == per - 1 && n < 28) { g2_ldh1<n>(c, v16, hf, ho); SCHED(); }
    });
    SCHED();
    mid(S);
  });
  MFMA_DONE();
}
template <bool LAST, class F>
DEVI void g2_epi(const Ctx& c, int lane, const f32x4 (&acc)[2][4], const u32x2 (&xr)[2], int h, int xdst, F&& mid) {
  const int g = lane >> 4, cc = lane & 15, q = cc >> 2, p = lane & 3, w = c.w, t = w * 64 + lane;
  char* red = c.smem + L_XA + h * 32768;
#pragma unroll
  for (int jj = 1; jj < 4; ++jj)
#pragma unroll
    for (int i = 0; i < 2; ++i) *(f32x4*)(red + ((((w + jj) & 3) * 3 + jj - 1) * 2 + i) * 1024 + lane * 16) = acc[i][jj];
  __syncthreads();
  char* xt = red + 24576;
  f32x4 pr[2][3];
#pragma unroll
  for (int i = 0; i < 2; ++i)
#pragma unroll
    for (int rk = 0; rk < 3; ++rk) pr[i][rk] = *(const f32x4*)(red + ((w * 3 + rk) * 2 + i) * 1024 + lane * 16);
  mid(0);
#pragma unroll
  for (int i = 0; i < 2; ++i) {
    const f32x4 xv = {lo_bf(xr[i][0]), hi_bf(xr[i][0]), lo_bf(xr[i][1]), hi_bf(xr[i][1])};
    const f32x4 o = ((acc[i][0] + (xv + c.bb2)) + pr[i][0]) + (pr[i][1] + pr[i][2]);
    const u32x2 r = {pack_bf(o[0], o[1]), pack_bf(o[2], o[3])};
    *(u32x2*)(xt + (w * 16 + cc) * 64 + ((i ^ (cc >> 3)) * 32) + g * 8) = r;
  }
  __syncthreads();
  const char* xq = xt + ((w >> 1) * 32 + 8 * g + q) * 64 + (((w & 1) ^ (g & 1)) * 32) + 8 * p;
  const u32x4 f0 = cat2(tr_read(xq), tr_read(xq + 256));
  const int tok = (16 * h + c.P) * 64 + c.sub * 32 + (w & 1) * 16 + cc;
  if (LAST) {
    *(u32x4*)(c.out + (size_t)tok * DM + c.ct_ * 64 + (w >> 1) * 32 + 8 * g) = f0;
  } else {
    const int row = t >> 2;
    const u32x4 v = *(const u32x4*)(xt + row * 64 + (((t & 3) ^ ((row >> 2) & 2)) * 16));
    st128(c.rs, (c.ct_ * 64 + row) * 128 + (t & 3) * 16, xdst, v);
    float s = 0.f, ss = 0.f;
#pragma unroll
    for (int j = 0; j < 4; ++j) { s = dot2bf(f0[j], 0x3f803f80u, s); ss = dot2bf(f0[j], f0[j], ss); }
    s = sum_x16(s); ss = sum_x16(ss);
    (void)__hip_atomic_fetch_add((unsigned long long*)(c.ws + WS_ST + 65536) + 2 * tok + (g & 1),
                                 (unsigned long long)f2fx((g & 1) ? ss : s), RLX_AGENT);
  }
}

template <int LAYER>
DEVI void layer_body(const Ctx& c, u32x4 (&w1f)[2][16], u32x4 (&w2f)[14][4], CIn& inA, CIn& inB) {
  constexpr int xin = WS_X + LAYER * 4 * MB, xmid = xin + 2 * MB, xnext = xin + 4 * MB;
  constexpr int hbuf = WS_H + LAYER * 8 * MB, stin = WS_ST + LAYER * 65536;
  constexpr u32 ep = LAYER + 1;
  constexpr int K1 = 1 + 3 * LAYER, K2 = K1 + 1, K3 = K1 + 2;
  const int w = c.w;
  char* smem = c.smem;
  auto nop = [](auto) {};
  {
    const int lane = fresh_lane(c);
    const int g = lane >> 4, cc = lane & 15, mu0 = (w & 1) * 4, v16 = lane * 16;
    const char* ub = smem + L_U + (w >> 1) * L_UC + 512 + 2 * (16 * cc - 16 + 8 * g) - 64 * (mu0 + 3);
    KB kb;
#pragma unroll
    for (int j = 0; j < 4; ++j) {
      u32 a = lds_addr(smem) + (w >> 1) * 2 * L_KC + ((cc & 1) ? 0 : L_KC) + 2 * (2031 - cc + 8 * g - ((cc & 1) ? 0 : 1)) - 64 * (mu0 + 3 + 56) + 1024 * j;
      asm volatile("" : "+v"(a));
      kb.p[j] = (const char*)(const LAS char*)(size_t)a;
    }
    STAMP(K1, 1);
    f32x4 base0, base1;
    u32x4 a4[4];
    if (LAYER) conv_ldk<0>(kb, a4);
    conv_ln(c, lane, inA, 0, base0, base1);
    __syncthreads();
    if (!LAYER) conv_ldk<0>(kb, a4);
    STAMP(K1, 2);
    f32x4 acc[4];
    u32 o = 0;
    conv_mma<0, 0, true>(ub, kb, acc, a4, [&](auto N) {
      constexpr int n = N.v;
      if constexpr (LAYER && n == 19) { WAIT_VM(0); o = sig_begin(c, lane); SCHED(); }
      if constexpr (LAYER && n == 29) { sig_end<true>(c, lane, o, CT_X + 64); SCHED(); }
    });
    MFMA_DONE();
    conv_part(c, lane, acc);
    if (LAYER) conv_ldk<1>(kb, a4);
    else { ((float*)(smem + L_CST))[w * 64 + lane] = c.cs1; WAIT_VM(0); }
    __syncthreads();
    if (!LAYER) conv_ldk<1>(kb, a4);
    conv_out(c, lane, base0, base1, xmid, 0);
    STAMP(K1, 3);
    f32x4 acb[4];
    conv_mma<4, 0, true>(ub, kb, acb, a4, [&](auto N) {
      constexpr int n = N.v;
      if constexpr (!LAYER && (n & 3) == 1) { ld_w1<(n >> 2)>(c, v16, w1f); SCHED(); }
      if constexpr (n == 47) {
        if (LAYER) WAIT_VM(0); else WAIT_VM(12);
        o = sig_begin(c, lane);
        if (LAYER) poll<true>(c, lane, CT_X + 64);
        SCHED();
      }
      if constexpr (n == 57) { sig_end<true>(c, lane, o, CT_C); SCHED(); }
    });
    STAMP(K1, 4);
    if (LAYER) {
      if (!got<1>(32u)) wait_ge<true>(c, lane, CT_X + 64, 32u);
      conv_load(c, lane, inB, xin, stin, 1);
    }
    conv_ldk<0>(kb, a4);
    STAMP(K1, 5);
    conv_ln(c, lane, inB, 1, base0, base1);
    __syncthreads();
    conv_mma<0, 4, false>(ub, kb, acb, a4, [&](auto N) {
      constexpr int n = N.v;
      if constexpr (!LAYER && (n & 3) == 1) { ld_w1<16 + (n >> 2)>(c, v16, w1f); SCHED(); }
      if constexpr (n == 30) { poll<true>(c, lane, CT_C); SCHED(); }
    });
    MFMA_DONE();
    conv_part(c, lane, acb);
    __syncthreads();
    conv_out(c, lane, base0, base1, xmid, 1);
    STAMP(K1, 6);
    if (!got<(LAYER ? 2 : 4)>(32u * ep)) wait_ge<true>(c, lane, CT_C, 32u * ep);
    const int row = lane >> 3, sw = ((row >> 1) & 1) << 1;
    const int ve = row * 128 + ((lane & 7) ^ sw) * 16, vo = row * 128 + ((lane & 7) ^ (sw | 4)) * 16;
    sfor<0, 8>([&](auto N) { g1_dma1<N.v>(c, ve, vo, L_XA, xmid + c.P * 65536); });
    WAIT_VM(8);
    o = sig_begin(c, lane);
    sig_end<true>(c, lane, o, CT_C + 64);
    STAMP(K1, 7);
  }
  u32x4 hf[14][2];
  u32x2 xr[2];
  {
    const int lane = fresh_lane(c);
    const int g = lane >> 4, cc = lane & 15, q = cc >> 2, p = lane & 3, v16 = lane * 16;
    const int sx = ((q >> 1) & 1) | ((g & 1) << 1);
    const char* xb[4];
#pragma unroll
    for (int i = 0; i < 4; ++i) xb[i] = smem + L_XA + (8 * g + q) * 128 + ((((i + w) & 3) ^ sx) * 32) + 8 * p;
    const int row = lane >> 3, sw = ((row >> 1) & 1) << 1;
    const int ve = row * 128 + ((lane & 7) ^ sw) * 16, vo = row * 128 + ((lane & 7) ^ (sw | 4)) * 16;
    sfor<8, 12>([&](auto N) { g1_dma1<N.v>(c, ve, vo, L_XA, xmid + c.P * 65536); });
    WAIT_VM_BAR(9);
    STAMP(K2, 0);
    f32x4 acc[2][4], sa;
    g1_mma(xb, w1f, acc, sa, [&](auto KS) {
      constexpr int ks = KS.v;
      if constexpr (ks == 3) { if (LAYER) WAIT_VM_BAR(8); else WAIT_VM_BAR(10); SCHED(); }
      if constexpr (ks == 7) { if (LAYER) WAIT_VM_BAR(0); else WAIT_VM_BAR(10); SCHED(); }
      if constexpr (ks == 13) { if (!LAYER) WAIT_VM(16); poll<true>(c, lane, CT_C + 64); SCHED(); }
    }, [&](auto N) {
      constexpr int n = N.v;
      if constexpr (n < 4) g1_dma1<12 + n>(c, ve, vo, L_XA, xmid + c.P * 65536);
      if constexpr (!LAYER && n >= 4 && n < 10) ld_w1<22 + n>(c, v16, w1f);
      if constexpr (!LAYER && n >= 10) ld_w2<n - 10>(c, v16, w2f);
    });
    STAMP(K2, 1);
    const int xo = xmid + (16 + c.P) * 65536;
    g1_epi(c, lane, acc, sa, hbuf + (c.P * 4 * 56 + c.hks) * 1024, [&](auto N) {
      constexpr int n = N.v;
      if constexpr (n == -1) { if (!got<(LAYER ? 0 : 6)>(32u * ep)) wait_ge<true>(c, lane, CT_C + 64, 32u * ep); }
      if constexpr (n >= 0 && n < 8) g1_dma1<n>(c, ve, vo, L_XB, xo);
      if constexpr (n >= 10 && n < 18) g1_dma1<n - 2>(c, ve, vo, L_XB, xo);
    });
    STAMP(K2, 2);
  }
  {
    const int lane = fresh_lane(c);
    const int g = lane >> 4, cc = lane & 15, q = cc >> 2, p = lane & 3, v16 = lane * 16;
    const int sx = ((q >> 1) & 1) | ((g & 1) << 1);
    const char* xb[4];
#pragma unroll
    for (int i = 0; i < 4; ++i) xb[i] = smem + L_XB + (8 * g + q) * 128 + ((((i + w) & 3) ^ sx) * 32) + 8 * p;
    WAIT_VM_BAR(16);
    STAMP(K2, 3);
    f32x4 acc[2][4], sa;
    g1_mma(xb, w1f, acc, sa, [&](auto KS) {
      constexpr int ks = KS.v;
      if constexpr (ks == 6) {
        if (LAYER) WAIT_VM_BAR(0); else WAIT_VM_BAR(12);
        sig_bar<false>(c, lane, CT_H + c.P);
        SCHED();
      }
      if constexpr (ks == 15) {
#pragma unroll
        for (int i = 0; i < 2; ++i) xr[i] = ld64(c.rs, (c.ct_ * 64 + w * 16 + cc) * 128 + i * 32 + g * 8, xmid + c.P * 65536 + c.sub * 64);
        SCHED();
      }
    }, [&](auto N) {
      constexpr int n = N.v;
      if constexpr (!LAYER) ld_w2<22 + n>(c, v16, w2f);
      if constexpr (!LAYER && n >= 30) ld_w2<24 + n>(c, v16, w2f);
    });
    STAMP(K2, 4);
    poll<false>(c, lane, CT_H + c.P);
    const int ho = hbuf + ((c.P * 4 + c.sub * 2) * 56 + 14 * w) * 1024;
    g1_epi(c, lane, acc, sa, hbuf + ((16 + c.P) * 4 * 56 + c.hks) * 1024, [&](auto N) {
      constexpr int n = N.v;
      if constexpr (n == -1) { if (!got<0>(16u * ep)) wait_ge<false>(c, lane, CT_H + c.P, 16u * ep); }
      if constexpr (n >= 0) g2_ldh1<n>(c, v16, hf, ho);
    });
    STAMP(K2, 5);
  }
  {
    const int lane = fresh_lane(c);
    const int g = lane >> 4, cc = lane & 15;
    const int ho = hbuf + ((c.P * 4 + c.sub * 2) * 56 + 14 * w) * 1024, hoB = ho + 64 * 56 * 1024;
    STAMP(K3, 0);
    f32x4 acc[2][4];
    u32 o = 0;
    g2_mma<10, 1>(c, lane, hf, w2f, acc, ho, [&](auto S) {
      constexpr int s = S.v;
      if constexpr (s == 6) { WAIT_VM(8); o = sig_begin(c, lane); SCHED(); }
      if constexpr (s == 7) { sig_end<false>(c, lane, o, CT_H + 16 + c.P); SCHED(); }
    });
    STAMP(K3, 1);
    poll<false>(c, lane, CT_H + 16 + c.P);
    g2_epi<LAYER == 1>(c, lane, acc, xr, 0, xnext + c.P * 65536 + c.sub * 64, nop);
    STAMP(K3, 2);
    if (!got<(LAYER ? 1 : 2)>(16u * ep)) wait_ge<false>(c, lane, CT_H + 16 + c.P, 16u * ep);
    g2_ldh<0, 4>(c, lane, hf, hoB);
#pragma unroll
    for (int i = 0; i < 2; ++i) xr[i] = ld64(c.rs, (c.ct_ * 64 + w * 16 + cc) * 128 + i * 32 + g * 8, xmid + (16 + c.P) * 65536 + c.sub * 64);
    if (LAYER == 0) {
      WAIT_VM(10);
      o = sig_begin(c, lane);
      sig_end<true>(c, lane, o, CT_X);
    }
    SCHED();
    STAMP(K3, 3);
  }
  {
    const int lane = fresh_lane(c);
    const int hoB = hbuf + (((16 + c.P) * 4 + c.sub * 2) * 56 + 14 * w) * 1024;
    f32x4 acc[2][4];
    g2_mma<4, 2>(c, lane, hf, w2f, acc, hoB, [&](auto S) {
      constexpr int s = S.v;
      if constexpr (LAYER == 0 && s == 13) { poll<true>(c, lane, CT_X); SCHED(); }
    });
    STAMP(K3, 4);
    g2_epi<LAYER == 1>(c, lane, acc, xr, 1, xnext + (16 + c.P) * 65536 + c.sub * 64, [&](int) {
      if (LAYER == 0) {
        if (!got<0>(32u)) wait_ge<true>(c, lane, CT_X, 32u);
        conv_load(c, lane, inA, xnext, WS_ST + 65536, 0);
        SCHED();
      }
    });
    STAMP(K3, 5);
  }
}

__global__ void __launch_bounds__(256, 1) main_kernel(char* ws, u16* out) {
  extern __shared__ __attribute__((aligned(16))) char smem[];
  STAMP(1, 0);
  Ctx c;
  c.rs = __builtin_amdgcn_make_buffer_rsrc(ws, 0, WS_END, 0x00020000);
  c.ct = (gu32*)(ws + WS_CT);
  c.smem = smem; c.ws = ws; c.out = out;
  const int t = threadIdx.x, lane = t & 63, b = blockIdx.x;
  const int w = __builtin_amdgcn_readfirstlane(t >> 6);
  c.lane0 = lane; c.w = w; c.b = b;
  c.P = (b & 7) * 2 + ((b >> 3) & 1);
  const int idx = b >> 4;
  const int sg = idx >> 1;
  c.eo = idx & 1; c.sub = idx >> 3; c.ct_ = idx & 7;
  c.ff0 = w < 3 ? 14 * sg + 6 * c.eo + 2 * w : 14 * sg + 12 + c.eo;
  c.hks = w < 3 ? 7 * sg + 3 * c.eo + w : 7 * sg + 6;
  CIn inA, inB;
  const f32x4 ksc = *(const f32x4*)(ws + WS_KSC + b * 16);
  dma1k<2048>(c.rs, lds_addr(smem) + w * L_KC, lane * 16, WS_KT + b * 16384 + w * 4096);
  dma1k<3072>(c.rs, lds_addr(smem) + w * L_KC, lane * 16, WS_KT + b * 16384 + w * 4096);
  conv_load(c, lane, inA, WS_X, WS_ST, 0);
  conv_load(c, lane, inB, WS_X, WS_ST, 1);
  const float cs1 = ((const float*)(ws + WS_S1))[min(c.ff0 * 32 + lane, FF * 2 - 1)];
  c.bb2 = ((const float*)(ws + WS_B2))[c.ct_ * 64 + w * 16 + (lane & 15)];
  SCHED();
  dma1k<0>(c.rs, lds_addr(smem) + w * L_KC, lane * 16, WS_KT + b * 16384 + w * 4096);
  dma1k<1024>(c.rs, lds_addr(smem) + w * L_KC, lane * 16, WS_KT + b * 16384 + w * 4096);
  u32x4 w1f[2][16], w2f[14][4];
  c.f1o = w < 3 ? WS_W1P + c.ff0 * 16384 + 16384 : WS_ONE; c.f1s = w < 3 ? 4096 : 0;
  if (t < 16) *(u32x4*)(smem + (t >> 2) * L_KC + 4096 + (t & 3) * 16) = u32x4{0, 0, 0, 0};
  if (t >= 64 && t < 128) *(u32x4*)(smem + L_U + ((t - 64) >> 5) * L_UC + (t & 31) * 16) = u32x4{0, 0, 0, 0};
  if (t == 128) *(u32*)(smem + L_SIG) = 0u;
  c.ks0 = __uint_as_float(__builtin_amdgcn_readfirstlane(__float_as_uint(ksc[0])));
  c.dsk0 = __uint_as_float(__builtin_amdgcn_readfirstlane(__float_as_uint(ksc[1])));
  c.ks1 = __uint_as_float(__builtin_amdgcn_readfirstlane(__float_as_uint(ksc[2])));
  c.dsk1 = __uint_as_float(__builtin_amdgcn_readfirstlane(__float_as_uint(ksc[3])));
  c.cs1 = cs1;
  layer_body<0>(c, w1f, w2f, inA, inB);
  layer_body<1>(c, w1f, w2f, inA, inB);
}

extern "C" __global__ void code_end_marker() {}

extern "C" void kernel_launch(void* const* d_in, const int* in_sizes, int n_in, void* d_out, int out_size,
                              void* d_ws, size_t ws_size, hipStream_t stream) {
  (void)in_sizes; (void)n_in; (void)out_size; (void)ws_size;
  char* ws = (char*)d_ws;
  (void)hipFuncSetAttribute((const void*)main_kernel, hipFuncAttributeMaxDynamicSharedMemorySize, MAIN_LDS);
  prep_kernel<<<690, 256, PREP_LDS, stream>>>((const u16*)d_in[0], (const u16*)d_in[1], (const u16*)d_in[2],
                                              (const u16*)d_in[3], (const u16*)d_in[4], (const u16*)d_in[5],
                                              (const u16*)d_in[6], (const u16*)d_in[8], (const u16*)d_in[9],
                                              (const u16*)d_in[10], (const u16*)d_in[11], ws, (const char*)d_out);
  main_kernel<<<256, 256, MAIN_LDS, stream>>>(ws, (u16*)d_out);
}
```

```cpp
#include <hip/hip_runtime.h>
#include <hip/hip_bf16.h>

typedef unsigned short u16;
typedef unsigned int u32;
typedef long long i64;
typedef __attribute__((ext_vector_type(4))) short s16x4;
typedef __attribute__((ext_vector_type(8))) __bf16 bf16x8;
typedef __attribute__((ext_vector_type(2))) __bf16 bf16x2;
typedef __attribute__((ext_vector_type(8))) _Float16 f16x8;
typedef __attribute__((ext_vector_type(2))) _Float16 f16x2;
typedef __attribute__((ext_vector_type(4))) float f32x4;
typedef __attribute__((ext_vector_type(2))) float f32x2;
typedef __attribute__((ext_vector_type(4))) unsigned u32x4;
typedef __attribute__((ext_vector_type(2))) unsigned u32x2;
typedef __attribute__((ext_vector_type(2))) long long i64x2;
typedef __attribute__((address_space(1))) unsigned gu32;
typedef __amdgpu_buffer_rsrc_t rsrc_t;
#define LAS __attribute__((address_space(3)))
#define DEVI __device__ __forceinline__
template <int V> struct IC { static constexpr int v = V; };
template <int I, int N, class F> __device__ __forceinline__ void sfor(F&& f) {
  if constexpr (I < N) { f(IC<I>{}); sfor<I + 1, N>(f); }
}
#define RLX_AGENT __ATOMIC_RELAXED, __HIP_MEMORY_SCOPE_AGENT

constexpr int SEQ = 2048, DM = 512, NST = 128, FF = 1792;
constexpr float LN_EPS = 1e-5f;
constexpr float FX_SCALE = 16777216.0f;
constexpr float FX_INV = 1.0f / 16777216.0f;
constexpr int SC1 = 16;

constexpr int MB = 1024 * 1024;
constexpr int WS_X = 0;
constexpr int WS_W1P = 12 * MB;
constexpr int WS_W2P = 14 * MB;
constexpr int WS_H = 16 * MB;
constexpr int WS_ST = 32 * MB;
constexpr int WS_S1 = 32 * MB + 131072;
constexpr int WS_GEN = 32 * MB + 196608;
constexpr int WS_F0 = WS_GEN + 256, WS_F1 = WS_F0 + 16384;
constexpr int WS_CT = 32 * MB + 262144;
constexpr int WS_ONE = 32 * MB + 393216;
constexpr int WS_END = 33 * MB;
constexpr int CT_C = 0, CT_X = 128, CT_H = 256, CT_TMO = 288;
constexpr int SPIN_CAP = 1 << 16;

#define STAMP(k, i) do {} while (0)

DEVI float bf2f(u16 v) { return __uint_as_float((u32)v << 16); }
DEVI float lo_bf(u32 w) { return __uint_as_float(w << 16); }
DEVI float hi_bf(u32 w) { return __uint_as_float(w & 0xffff0000u); }
DEVI float rbf(float x) { return (float)(__bf16)x; }
DEVI u32 pack_bf(float a, float b) { f32x2 q = {a, b}; return __builtin_bit_cast(u32, __builtin_convertvector(q, bf16x2)); }
DEVI u32 pack_h(float a, float b) { f32x2 q = {a, b}; return __builtin_bit_cast(u32, __builtin_convertvector(q, f16x2)); }
DEVI float dot2bf(u32 a, u32 b, float c) {
  return __builtin_amdgcn_fdot2_f32_bf16(__builtin_bit_cast(bf16x2, a), __builtin_bit_cast(bf16x2, b), c, false);
}
DEVI f32x4 mfma_bf(u32x4 a, u32x4 b, f32x4 c) {
  return __builtin_amdgcn_mfma_f32_16x16x32_bf16(__builtin_bit_cast(bf16x8, a), __builtin_bit_cast(bf16x8, b), c, 0, 0, 0);
}
DEVI f32x4 mfma_h(u32x4 a, u32x4 b, f32x4 c) {
  return __builtin_amdgcn_mfma_f32_16x16x32_f16(__builtin_bit_cast(f16x8, a), __builtin_bit_cast(f16x8, b), c, 0, 0, 0);
}
DEVI u32x2 tr_read(const char* p) {
  return __builtin_bit_cast(u32x2, __builtin_amdgcn_ds_read_tr16_b64_v4i16((LAS s16x4*)p));
}
DEVI u32x4 cat2(u32x2 a, u32x2 b) { return __builtin_shufflevector(a, b, 0, 1, 2, 3); }
DEVI u32 lds_addr(const void* p) { return (u32)(size_t)(const LAS char*)p; }
#define WAIT_VM(n) asm volatile("s_waitcnt vmcnt(" #n ")" ::: "memory")
#define WAIT_VM_BAR(n) asm volatile("s_waitcnt vmcnt(" #n ")\n\ts_barrier" ::: "memory")
#define SCHED() __builtin_amdgcn_sched_barrier(0)
template <int CTRL> DEVI float dpp_add(float v) {
  return v + __uint_as_float(__builtin_amdgcn_update_dpp(0u, __float_as_uint(v), CTRL, 0xf, 0xf, true));
}
DEVI float sum_x16(float v) {
  auto r = __builtin_amdgcn_permlane16_swap(__float_as_uint(v), __float_as_uint(v), false, false);
  return __uint_as_float(r[0]) + __uint_as_float(r[1]);
}
DEVI float sum_x32(float v) {
  auto r = __builtin_amdgcn_permlane32_swap(__float_as_uint(v), __float_as_uint(v), false, false);
  return __uint_as_float(r[0]) + __uint_as_float(r[1]);
}
DEVI float wave_sum(float v) {
  v = dpp_add<0xB1>(v); v = dpp_add<0x4E>(v); v = dpp_add<0x141>(v); v = dpp_add<0x140>(v);
  return sum_x32(sum_x16(v));
}
DEVI float fx2f(u32 lo, u32 hi) { return fmaf((float)(int)hi, 4294967296.0f * FX_INV, (float)lo * FX_INV); }
DEVI i64 f2fx(float v) { return (i64)(v * FX_SCALE); }
DEVI float sel4(int i, float a, float b, float c, float d) {
  asm volatile("" : "+v"(a), "+v"(b), "+v"(c), "+v"(d));
  return (i & 2) ? ((i & 1) ? d : c) : ((i & 1) ? b : a);
}
DEVI float rcp(float x) { return __builtin_amdgcn_rcpf(x); }
DEVI float sqrt_(float x) { return __builtin_amdgcn_sqrtf(x); }

DEVI u32 warm_code(int slice, int lane) {
  unsigned long long pc, pe;
  asm volatile("s_getpc_b64 %0\n\ts_getpc_b64 s[90:91]\n\ts_add_u32 s90, s90, code_end_marker@rel32@lo+4\n\t"
               "s_addc_u32 s91, s91, code_end_marker@rel32@hi+12\n\ts_mov_b64 %1, s[90:91]"
               : "=&s"(pc), "=s"(pe) :: "scc", "s90", "s91");
  const unsigned long long a = (pc + (unsigned)(slice * 512 + (lane & 7) * 64)) & ~3ull;
  u32 junk = 0;
  if (pc + (unsigned)(slice * 512 + 512) <= pe) asm volatile("global_load_dword %0, %1, off" : "=v"(junk) : "v"(a) : "memory");
  return junk;
}

DEVI int cidx(int l) {
  const int hb = (int)(pack_bf((float)l, 0.f) & 0xffffu) - (0x4300 - 128);
  return l < 128 ? l : hb;
}
struct CM { f32x2 e, f; };
DEVI f32x2 cmul(f32x2 a, const CM& m) { return a[1] * m.f + a[0] * m.e; }
DEVI CM cexp_(float a2, float br, float p) {
  const float e = __builtin_amdgcn_exp2f(a2 * p), f = __builtin_amdgcn_fractf(br * p);
  const float re = e * __builtin_amdgcn_cosf(f), im = e * __builtin_amdgcn_sinf(f);
  CM r; r.e = f32x2{re, im}; r.f = f32x2{-im, re};
  return r;
}
DEVI void st16(rsrc_t rs, int off, u32x4 v) { __builtin_amdgcn_raw_buffer_store_b128(v, rs, off, 0, SC1); }

DEVI void xpose_load(int b, const u16* x, u32x4 (&v)[8]) {
  const int lane = threadIdx.x & 63, w = threadIdx.x >> 6;
#pragma unroll
  for (int r = 0; r < 8; ++r) v[r] = *(const u32x4*)(x + (size_t)(b * 32 + w * 8 + r) * DM + lane * 8);
}
DEVI void xpose(int b, const u32x4 (&v)[8], rsrc_t rs, char* smem) {
  const int t = threadIdx.x, lane = t & 63, w = t >> 6;
  constexpr int RS = 1024 + 32;
#pragma unroll
  for (int r = 0; r < 8; ++r) *(u32x4*)(smem + (w * 8 + r) * RS + lane * 16) = v[r];
  __syncthreads();
  const int g = lane >> 4, q = (lane & 15) >> 2, p = lane & 3;
#pragma unroll
  for (int cf = 0; cf < 8; ++cf) {
    const int c0 = (w * 8 + cf) * 16;
    const char* pa = smem + (8 * g + q) * RS + c0 * 2 + 8 * p;
    const u32x4 o = cat2(tr_read(pa), tr_read(pa + 4 * RS));
    st16(rs, WS_X + (b >> 1) * 65536 + (c0 + (lane & 15)) * 128 + (b & 1) * 64 + g * 16, o);
  }
}
DEVI void row_stats(int b, const u32x4 (&v)[8], rsrc_t rs) {
  const int t = threadIdx.x, lane = t & 63, w = t >> 6;
  const int t0 = b * 32;
  float a[16];
#pragma unroll
  for (int r = 0; r < 8; ++r) {
    float s = 0.f, ss = 0.f;
#pragma unroll
    for (int j = 0; j < 4; ++j) { s = dot2bf(v[r][j], 0x3f803f80u, s); ss = dot2bf(v[r][j], v[r][j], ss); }
    a[2 * r] = s; a[2 * r + 1] = ss;
  }
  const bool o1 = lane & 1, o2 = lane & 2;
#pragma unroll
  for (int k = 0; k < 8; ++k) {
    const float keep = o1 ? a[2 * k + 1] : a[2 * k], send = o1 ? a[2 * k] : a[2 * k + 1];
    a[k] = keep + __uint_as_float(__builtin_amdgcn_update_dpp(0u, __float_as_uint(send), 0xB1, 0xf, 0xf, true));
  }
#pragma unroll
  for (int k = 0; k < 4; ++k) {
    const float keep = o2 ? a[2 * k + 1] : a[2 * k], send = o2 ? a[2 * k] : a[2 * k + 1];
    a[k] = keep + __uint_as_float(__builtin_amdgcn_update_dpp(0u, __float_as_uint(send), 0x4E, 0xf, 0xf, true));
  }
#pragma unroll
  for (int k = 0; k < 4; ++k) a[k] = sum_x32(sum_x16(dpp_add<0x128>(dpp_add<0x124>(a[k]))));
  const i64 sv = lane < 16 ? f2fx(sel4(lane >> 2, a[0], a[1], a[2], a[3])) : 0;
  if (lane < 32)
    __builtin_amdgcn_raw_buffer_store_b64(__builtin_bit_cast(u32x2, sv), rs, (lane >> 4) * 65536 + (t0 + w * 8) * 16 + (lane & 15) * 8, WS_ST, SC1);
  if (t < 5) __builtin_amdgcn_raw_buffer_store_b32(0u, rs, (b * 5 + t) * 256, WS_CT, SC1);
}

DEVI void pack_w1_load(int b, int kh, const u16* w1, u32x4 (&v)[8]) {
  const int t = threadIdx.x;
#pragma unroll
  for (int it = 0; it < 4; ++it) v[it] = *(const u32x4*)(w1 + (size_t)(kh * 256 + it * 64 + (t >> 2)) * FF + b * 32 + (t & 3) * 8);
}
DEVI void pack_w1(int b, int kh, const u32x4 (&v)[8], rsrc_t rs, char* smem) {
  const int t = threadIdx.x, lane = t & 63, w = t >> 6;
  constexpr int RS = 64 + 32;
#pragma unroll
  for (int it = 0; it < 4; ++it) *(u32x4*)(smem + (it * 64 + (t >> 2)) * RS + (t & 3) * 16) = v[it];
  __syncthreads();
  const int g = lane >> 4, q = (lane & 15) >> 2, p = lane & 3;
  float cs[2] = {0.f, 0.f};
#pragma unroll
  for (int kk = 0; kk < 2; ++kk) {
    const int ks = w * 2 + kk;
#pragma unroll
    for (int h = 0; h < 2; ++h) {
      const char* a = smem + (32 * ks + 8 * g + q) * RS + h * 32 + 8 * p;
      const u32x4 o = cat2(tr_read(a), tr_read(a + 4 * RS));
#pragma unroll
      for (int j = 0; j < 4; ++j) cs[h] = dot2bf(o[j], 0x3f803f80u, cs[h]);
      st16(rs, WS_W1P + (((2 * b + h) * 16 + kh * 8 + ks) * 64 + lane) * 16, o);
    }
  }
  const float s0 = sum_x32(sum_x16(cs[0])), s1 = sum_x32(sum_x16(cs[1]));
  if (lane < 32) __builtin_amdgcn_raw_buffer_store_b32(__float_as_uint(lane < 16 ? s0 : s1), rs, ((b * 32 + lane) * 8 + kh * 4 + w) * 4, WS_S1, SC1);
  if (b == 0 && kh == 0) st16(rs, WS_ONE + t * 16, u32x4{0x3f803f80u, 0x3f803f80u, 0x3f803f80u, 0x3f803f80u});
}

DEVI void pack_w2_load(int b, const u16* w2, u32x4 (&v)[2]) {
  const int t = threadIdx.x;
#pragma unroll
  for (int r = 0; r < 2; ++r) v[r] = *(const u32x4*)(w2 + (size_t)((b >> 2) * 32 + r * 16 + (t >> 4)) * DM + (b & 3) * 128 + (t & 15) * 8);
}
constexpr int W2_RS = 256 + 32;
DEVI void pack_w2_put(const u32x4 (&v)[2], char* smem) {
  const int t = threadIdx.x;
#pragma unroll
  for (int r = 0; r < 2; ++r) *(u32x4*)(smem + (r * 16 + (t >> 4)) * W2_RS + (t & 15) * 16) = v[r];
}
DEVI void pack_w2_out(int b, rsrc_t rs, const char* smem) {
  const int t = threadIdx.x, lane = t & 63, w = t >> 6;
  const int g = lane >> 4, q = (lane & 15) >> 2, p = lane & 3;
#pragma unroll
  for (int cf = 0; cf < 2; ++cf) {
    const char* a = smem + (4 * g + q) * W2_RS + (w * 2 + cf) * 32 + 8 * p;
    const u32x4 o = cat2(tr_read(a), tr_read(a + 16 * W2_RS));
    st16(rs, b < 224 ? WS_W2P + ((((b & 3) * 8 + w * 2 + cf) * 56 + (b >> 2)) * 64 + lane) * 16 : 0x40000000, o);
  }
}

constexpr int L_KC = 4160;
constexpr int L_U = 4 * L_KC, L_UC = 4608;
constexpr int L_XA = 26112, L_XB = L_XA + 65536;
constexpr int L_MISC = L_XB + 65536;
constexpr int L_CST = L_MISC + 512 + 4 * 512;
constexpr int L_SIG = L_CST + 4 * 256;
constexpr int MAIN_LDS = L_SIG + 256;
constexpr int L_RED = L_XB, L_SCR = L_XB + 16384;

DEVI u32x4 ld128(rsrc_t rs, int v, int s) { return __builtin_amdgcn_raw_buffer_load_b128(rs, v, s, SC1); }
DEVI u32x2 ld64(rsrc_t rs, int v, int s) { return __builtin_amdgcn_raw_buffer_load_b64(rs, v, s, SC1); }
DEVI u32x4 ld128p(rsrc_t rs, int v, int s) { return __builtin_amdgcn_raw_buffer_load_b128(rs, v, s, 0); }
DEVI void st128(rsrc_t rs, int v, int s, u32x4 d) { __builtin_amdgcn_raw_buffer_store_b128(d, rs, v, s, SC1); }
DEVI void st64(rsrc_t rs, int v, int s, u32x2 d) { __builtin_amdgcn_raw_buffer_store_b64(d, rs, v, s, SC1); }
template <int OFF> DEVI u32x4 ld128a(rsrc_t rs, int v, int s) {
  u32x4 r;
  asm volatile("buffer_load_dwordx4 %0, %1, %2, %3 offen offset:%4" : "=a"(r) : "v"(v), "s"(rs), "s"(s), "i"(OFF) : "memory");
  return r;
}
template <int OFF> DEVI u32x4 ld128v(rsrc_t rs, int v, int s) {
  u32x4 r;
  asm volatile("buffer_load_dwordx4 %0, %1, %2, %3 offen offset:%4" : "=v"(r) : "v"(v), "s"(rs), "s"(s), "i"(OFF) : "memory");
  return r;
}
template <int OFF> DEVI void dma1k(rsrc_t rs, u32 lds_dst, int v, int soff) {
  asm volatile("s_mov_b32 m0, %0\n\ts_nop 0\n\tbuffer_load_dwordx4 %1, %2, %3 offen offset:%4 sc1 lds"
               :: "s"(lds_dst), "v"(v), "s"(rs), "s"(soff), "i"(OFF) : "memory");
}
#define MMA(NAME, OP, CA, CB)                                                                                   \
  template <bool Z> DEVI void NAME(f32x4& d, const u32x4& a, const u32x4& b) {                                  \
    if (Z) asm volatile(OP " %0, %1, %2, 0" : "=&v"(d) : CA(a), CB(b));                                         \
    else asm volatile(OP " %0, %1, %2, %0" : "+v"(d) : CA(a), CB(b));                                           \
  }
MMA(mma_vv, "v_mfma_f32_16x16x32_bf16", "v", "v")
MMA(mma_av, "v_mfma_f32_16x16x32_bf16", "a", "v")
MMA(mma_va, "v_mfma_f32_16x16x32_bf16", "v", "a")
MMA(mma_hh, "v_mfma_f32_16x16x32_f16", "v", "v")
#define MFMA_DONE() do { SCHED(); asm volatile("s_nop 7" ::: "memory"); SCHED(); } while (0)

struct Ctx {
  rsrc_t rs; gu32* ct; char* smem; char* ws; u16* out;
  int lane0, w, b, P, sub, ct_, ff0, hks, eo, f1o, f1s;
  float dsk0, dsk1, ks0, ks1, bb2, cs1;
  u32 tag;
};
DEVI int fresh_lane(const Ctx& c) { int l = c.lane0; asm volatile("" : "+v"(l)); return l; }
DEVI u32 sig_begin(const Ctx& c, int lane) {
  u32 old;
  asm volatile("ds_add_rtn_u32 %0, %1, %2" : "=v"(old) : "v"(lds_addr(c.smem) + L_SIG + lane * 4), "v"(1u) : "memory");
  return old;
}
template <bool ALL> DEVI void sig_end(const Ctx& c, int lane, u32 old, int line) {
  asm volatile("s_waitcnt lgkmcnt(0)" : "+v"(old) :: "memory");
  const bool last = (__builtin_amdgcn_readfirstlane(old) & 3u) == 3u;
  const int v = (last && lane < (ALL ? 8 : 1)) ? (ALL ? lane * 2048 : 0) : 0x40000000;
  asm volatile("s_nop 4\n\tbuffer_atomic_add %0, %1, %2, %3 offen" :: "v"(1u), "v"(v), "s"(c.rs),
               "s"(WS_CT + (line + (ALL ? (c.b >> 3) & 7 : 0)) * 256) : "memory");
}
template <bool ALL, int R = 0> DEVI void poll(const Ctx& c, int lane, int line) {
  const int v = ALL ? (lane & 7) * 256 : 0, so = WS_CT + (line + (ALL ? (c.b & 7) * 8 : 0)) * 256;
  if (R == 0) asm volatile("s_nop 4\n\tbuffer_load_dword v254, %0, %1, %2 offen sc1" :: "v"(v), "s"(c.rs), "s"(so) : "memory", "v254");
  else asm volatile("s_nop 4\n\tbuffer_load_dword v255, %0, %1, %2 offen sc1" :: "v"(v), "s"(c.rs), "s"(so) : "memory", "v255");
}
template <int YOUNGER, int R = 0> DEVI bool got(u32 want) {
  unsigned long long m;
  if (R == 0) asm volatile("s_waitcnt vmcnt(%2)\n\tv_cmp_le_u32_e64 %0, %1, v254" : "=s"(m) : "s"(want), "i"(YOUNGER) : "memory");
  else asm volatile("s_waitcnt vmcnt(%2)\n\tv_cmp_le_u32_e64 %0, %1, v255" : "=s"(m) : "s"(want), "i"(YOUNGER) : "memory");
  return m == ~0ull;
}
template <bool ALL> DEVI void wait_ge(const Ctx& c, int lane, int line, u32 want) {
  int sp = 0;
  poll<ALL, 0>(c, lane, line);
  __builtin_amdgcn_s_sleep(6);
  poll<ALL, 1>(c, lane, line);
#pragma nounroll
  for (;;) {
    if (got<1, 0>(want)) break;
    poll<ALL, 0>(c, lane, line);
    if (got<1, 1>(want)) break;
    poll<ALL, 1>(c, lane, line);
    if (++sp > SPIN_CAP) { __hip_atomic_store(c.ct + CT_TMO * 64, 1u, RLX_AGENT); break; }
  }
}

DEVI void poll0(const Ctx& c, int lane, int set) {
  asm volatile("s_nop 4\n\tbuffer_load_dwordx2 v[254:255], %0, %1, %2 offen sc1" :: "v"((lane & 7) * 256 + (lane >> 3) * 8), "s"(c.rs),
               "s"(set + (c.b & 7) * 2048) : "memory", "v254", "v255");
}
template <int YOUNGER> DEVI bool got0(u32 tag) {
  unsigned long long m, m2;
  asm volatile("s_waitcnt vmcnt(%3)\n\tv_cmp_eq_u32_e64 %0, %2, v254\n\tv_cmp_eq_u32_e64 %1, %2, v255"
               : "=&s"(m), "=s"(m2) : "s"(tag), "i"(YOUNGER) : "memory");
  return (m & m2) == ~0ull;
}
DEVI void wait0(const Ctx& c, int lane, int set) {
  int sp = 0;
#pragma nounroll
  do {
    poll0(c, lane, set);
    if (++sp > SPIN_CAP) { __hip_atomic_store(c.ct + CT_TMO * 64, 1u, RLX_AGENT); break; }
  } while (!got0<0>(c.tag));
}

struct CIn { u32x2 x0, x1; u32x4 s[4]; };
DEVI void conv_load(const Ctx& c, int lane, CIn& in, int xoff, int stoff, int h) {
  const int sx = xoff + (16 * h + 4 * c.w) * 65536 + c.b * 256;
  const int vx = (lane >> 4) * 65536 + (lane & 15) * 8;
  const int ss = stoff + (1024 * h + 256 * c.w) * 16;
#pragma unroll
  for (int j = 0; j < 4; ++j) in.s[j] = ld128(c.rs, lane * 16 + j * 1024, ss);
  in.x0 = ld64(c.rs, vx, sx);
  in.x1 = ld64(c.rs, vx + 128, sx);
}
DEVI void conv_ln(const Ctx& c, int lane, const CIn& in, int h, f32x4& base0, f32x4& base1) {
  char* scr = c.smem + L_SCR + c.w * 2048;
  constexpr float CH = 4294967296.0f * FX_INV / DM, CL = FX_INV / DM;
  f32x4 m, r;
#pragma unroll
  for (int j = 0; j < 4; ++j) {
    m[j] = fmaf((float)(int)in.s[j][1], CH, (float)in.s[j][0] * CL);
    r[j] = fmaf((float)(int)in.s[j][3], CH, fmaf((float)in.s[j][2], CL, LN_EPS));
  }
#pragma unroll
  for (int j = 0; j < 4; ++j) r[j] = __builtin_amdgcn_rsqf(fmaxf(fmaf(-m[j], m[j], r[j]), LN_EPS));
#pragma unroll
  for (int j = 0; j < 4; ++j) {
    *(float*)(scr + (64 * j + lane) * 4) = r[j];
    *(float*)(scr + 1024 + (64 * j + lane) * 4) = -m[j] * r[j];
  }
  const f32x4 rr = *(const f32x4*)(scr + lane * 16), mr = *(const f32x4*)(scr + 1024 + lane * 16);
  const f32x4 x0 = {lo_bf(in.x0[0]), hi_bf(in.x0[0]), lo_bf(in.x0[1]), hi_bf(in.x0[1])};
  const f32x4 x1 = {lo_bf(in.x1[0]), hi_bf(in.x1[0]), lo_bf(in.x1[1]), hi_bf(in.x1[1])};
  const f32x4 u0 = x0 * rr + mr, u1 = x1 * rr + mr;
  char* up = c.smem + L_U + 512 + (1024 * h + 256 * c.w + 4 * lane) * 2;
  *(u32x2*)up = u32x2{pack_h(u0[0], u0[1]), pack_h(u0[2], u0[3])};
  *(u32x2*)(up + L_UC) = u32x2{pack_h(u1[0], u1[1]), pack_h(u1[2], u1[3])};
  base0 = u0 * c.dsk0 + x0; base1 = u1 * c.dsk1 + x1;
}
struct KB { const char* p[4]; };
template <int MM, int LAM> DEVI u32x4 conv_k1(const KB& kb) {
  const u32* kp = (const u32*)(kb.p[(7 - LAM) >> 1] + 64 * (3 - MM) + 512 * ((7 - LAM) & 1));
  return u32x4{kp[0], kp[1], kp[2], kp[3]};
}
template <int MM, int TB> DEVI u32x4 conv_u1(const char* ub) { return *(const u32x4*)(ub + 64 * (3 - MM) + 512 * TB); }
template <int LO> DEVI void conv_ldk(const KB& kb, u32x4 (&af)[4]) {
  sfor<0, 4>([&](auto L) { af[L.v] = conv_k1<0, LO + L.v>(kb); });
}
constexpr int conv_rank(int DT, int LO, int gr, int lt) {
  int k = 0;
  for (int x = 0; x < lt; ++x) { const int tb = DT + (x & 3) - (LO + 4 * gr + (x >> 2)); k += (tb >= 0 && tb < 4); }
  return k;
}
template <int DT, int TB0, bool ZERO, class F>
DEVI void conv_mma(const char* ub, const KB& kb, f32x4 (&acc)[4], const u32x4 (&af0)[4], F&& hook) {
  constexpr int NG = DT ? 2 : 1, LO = DT ? 1 : 0;
  u32x4 af[2][4], bf[2][4];
  sfor<0, 4>([&](auto L) { af[0][L.v] = af0[L.v]; });
  sfor<0, 4>([&](auto T) { constexpr int tb = DT ? 3 - T.v : T.v; bf[0][tb] = conv_u1<0, TB0 + tb>(ub); });
  SCHED();
  sfor<0, 4 * NG>([&](auto ST) {
    constexpr int st = ST.v, mm = st / NG, gr = st % NG;
    constexpr bool more = st + 1 < 4 * NG;
    constexpr int mn = (st + 1) / NG, gn = (st + 1) % NG;
    constexpr bool nu = more && gr == NG - 1;
    sfor<0, 16>([&](auto LT) {
      constexpr int l = LT.v >> 2, T = LT.v & 3, tb = DT + T - (LO + 4 * gr + l);
      if constexpr (tb >= 0 && tb < 4) {
        constexpr int k = conv_rank(DT, LO, gr, LT.v);
        constexpr bool first = ZERO && st == 0 && (DT ? (l == T) : (l == 0));
        mma_hh<first>(acc[T], af[st & 1][l], bf[mm & 1][tb]);
        sfor<(gr == 1 ? 2 * k : k), (gr == 1 ? 2 * k + 2 : k + 1)>([&](auto J) {
          constexpr int j = J.v;
          if constexpr (nu && j < 8) {
            if constexpr (DT != 0) {
              if constexpr (j & 1) bf[mn & 1][3 - (j >> 1)] = conv_u1<mn, TB0 + 3 - (j >> 1)>(ub);
              else af[(st + 1) & 1][j >> 1] = conv_k1<mn, LO + (j >> 1)>(kb);
            } else {
              if constexpr (j >= 1 && j <= 4) bf[mn & 1][j - 1] = conv_u1<mn, TB0 + j - 1>(ub);
              else af[(st + 1) & 1][j ? j - 4 : 0] = conv_k1<mn, LO + (j ? j - 4 : 0)>(kb);
            }
          } else if constexpr (more && !nu && j < 3) af[(st + 1) & 1][j] = conv_k1<mn, LO + 4 * gn + j>(kb);
        });
        SCHED();
        hook(IC<mm * (DT ? 16 : 10) + gr * 10 + k>{});
      }
    });
  });
}
DEVI void conv_part(const Ctx& c, int lane, const f32x4 (&acc)[4]) {
  char* red = c.smem + L_RED;
#pragma unroll
  for (int T = 0; T < 4; ++T) *(f32x4*)(red + (c.w * 4 + T) * 1024 + (lane & 15) * 64 + (lane >> 4) * 16) = acc[T];
}
DEVI void conv_out(const Ctx& c, int lane, const f32x4& base0, const f32x4& base1, int xoff, int h) {
  const int sx = xoff + (16 * h + 4 * c.w) * 65536 + c.b * 256;
  const int vx = (lane >> 4) * 65536 + (lane & 15) * 8;
  const char* p = c.smem + L_RED + c.w * 1024 + lane * 16;
  const f32x4 a0 = *(const f32x4*)p, b0 = *(const f32x4*)(p + 4096), a1 = *(const f32x4*)(p + 8192), b1 = *(const f32x4*)(p + 12288);
  SCHED();
  const f32x4 y0 = (a0 + b0) * c.ks0 + base0, y1 = (a1 + b1) * c.ks1 + base1;
  st64(c.rs, vx, sx, u32x2{pack_bf(y0[0], y0[1]), pack_bf(y0[2], y0[3])});
  st64(c.rs, vx + 128, sx, u32x2{pack_bf(y1[0], y1[1]), pack_bf(y1[2], y1[3])});
}

template <int PC> DEVI void g1_dma1(const Ctx& c, int ve, int vo, int ldsx, int soff) {
  dma1k<(PC & 3) * 1024>(c.rs, lds_addr(c.smem) + ldsx + (4 * (PC >> 2) + c.w) * 4096, (PC & 1) ? vo : ve, soff + (4 * (PC >> 2) + c.w) * 4096);
}
template <bool ALL> DEVI void sig_bar(const Ctx& c, int lane, int line) {
  const int v = (c.w == 0 && lane < (ALL ? 8 : 1)) ? (ALL ? lane * 2048 : 0) : 0x40000000;
  asm volatile("s_nop 4\n\tbuffer_atomic_add %0, %1, %2, %3 offen" :: "v"(1u), "v"(v), "s"(c.rs),
               "s"(WS_CT + (line + (ALL ? (c.b >> 3) & 7 : 0)) * 256) : "memory");
}
template <class F, class G>
DEVI void g1_mma(const char* const (&xb)[4], const u32x4 (&w1f)[2][16], f32x4 (&acc)[2][4], f32x4& sa, F&& mid, G&& hook) {
  u32x2 xf[2][4][2];
#pragma unroll
  for (int i = 0; i < 4; ++i) { xf[0][i][0] = tr_read(xb[i]); xf[0][i][1] = tr_read(xb[i] + 512); }
  SCHED();
  sfor<0, 16>([&](auto KS) {
    constexpr int ks = KS.v;
    mid(KS);
    sfor<0, 4>([&](auto I) {
      constexpr int i = I.v;
      const u32x4 x = cat2(xf[ks & 1][i][0], xf[ks & 1][i][1]);
      mma_vv<ks == 0>(acc[0][i], w1f[0][ks], x);
      if constexpr (ks < 15) xf[(ks + 1) & 1][i][0] = tr_read(xb[i] + (ks + 1) * 4096);
      SCHED();
      if constexpr (ks < 8) mma_av<ks == 0>(acc[1][i], w1f[1][ks], x); else mma_vv<false>(acc[1][i], w1f[1][ks], x);
      if constexpr (ks < 15) xf[(ks + 1) & 1][i][1] = tr_read(xb[i] + (ks + 1) * 4096 + 512);
      SCHED();
      if constexpr (i == 0) { mma_vv<ks == 0>(sa, x, x); hook(IC<2 * ks>{}); SCHED(); }
      if constexpr (i == 2) { hook(IC<2 * ks + 1>{}); SCHED(); }
    });
  });
  MFMA_DONE();
}
template <class F>
DEVI void g1_epi(const Ctx& c, int lane, f32x4 (&acc)[2][4], const f32x4& sa, int hoff, F&& hook) {
  const int g = lane >> 4, cc = lane & 15, q = cc >> 2, p = lane & 3, w = c.w;
  float* sums = (float*)(c.smem + L_MISC);
  {
    const float d = sel4(p, sa[0], sa[1], sa[2], sa[3]);
    if (q == g) sums[64 + w * 16 + cc] = d;
    const float r = sel4(g, acc[1][1][0], acc[1][2][0], acc[1][3][0], acc[1][0][0]);
    if (w == 3) sums[lane] = r;
  }
  f32x4 s1v[2], b1v[2];
#pragma unroll
  for (int j = 0; j < 2; ++j) {
    s1v[j] = *(const f32x4*)(c.smem + L_CST + w * 256 + j * 128 + g * 16);
    b1v[j] = *(const f32x4*)(c.smem + L_CST + w * 256 + j * 128 + 64 + g * 16);
  }
  __syncthreads();
  f32x2* cs = (f32x2*)(c.smem + L_MISC + 512 + w * 512);
  {
    const float m = sums[lane] * (1.0f / DM);
    const float ve = fmaxf(fmaf(-m, m, fmaf(sums[64 + lane], 1.0f / DM, LN_EPS)), LN_EPS);
    const float r = __builtin_amdgcn_rsqf(fmaf(ve, LN_EPS, ve - LN_EPS));
    f32x2 o = {r, -m * r};
    cs[lane] = o;
  }
  const int v16 = lane * 16 + (w < 3 ? 0 : 0x40000000), v8 = lane * 16 + (w < 3 ? 0x40000000 : c.eo * 8);
  f32x2 k[4];
#pragma unroll
  for (int i = 0; i < 4; ++i) k[i] = cs[((i + w) & 3) * 16 + cc];
  SCHED();
  hook(IC<-1>{});
  SCHED();
  sfor<0, 2>([&](auto IH) {
    constexpr int ih = IH.v;
    f32x4 x[4], e[4];
#define STAGE(n) do { SCHED(); hook(IC<10 * ih + n>{}); SCHED(); } while (0)
#pragma unroll
    for (int n = 0; n < 4; ++n) x[n] = k[2 * ih + (n >> 1)][1] * s1v[n & 1] + b1v[n & 1];
#pragma unroll
    for (int n = 0; n < 4; ++n) x[n] = acc[n & 1][2 * ih + (n >> 1)] * k[2 * ih + (n >> 1)][0] + x[n];
    STAGE(0);
#pragma unroll
    for (int n = 0; n < 4; ++n) e[n] = x[n] * x[n];
#pragma unroll
    for (int n = 0; n < 4; ++n) e[n] = e[n] * (-0.0713548162726f * 1.44269504088896f) + (-1.59576912160573f * 1.44269504088896f);
    STAGE(1);
#pragma unroll
    for (int n = 0; n < 4; ++n) e[n] = e[n] * x[n];
    STAGE(2);
#pragma unroll
    for (int r = 0; r < 4; ++r) { e[0][r] = __builtin_amdgcn_exp2f(e[0][r]); e[1][r] = __builtin_amdgcn_exp2f(e[1][r]); }
    STAGE(3);
#pragma unroll
    for (int r = 0; r < 4; ++r) { e[2][r] = __builtin_amdgcn_exp2f(e[2][r]); e[3][r] = __builtin_amdgcn_exp2f(e[3][r]); }
    STAGE(4);
#pragma unroll
    for (int n = 0; n < 4; ++n) e[n] = e[n] + 1.0f;
    STAGE(5);
#pragma unroll
    for (int r = 0; r < 4; ++r) { e[0][r] = rcp(e[0][r]); e[1][r] = rcp(e[1][r]); }
    STAGE(6);
#pragma unroll
    for (int r = 0; r < 4; ++r) { e[2][r] = rcp(e[2][r]); e[3][r] = rcp(e[3][r]); }
    STAGE(7);
#pragma unroll
    for (int n = 0; n < 4; ++n) x[n] = x[n] * e[n];
    STAGE(8);
    u32x4 hv[2];
#pragma unroll
    for (int n = 0; n < 4; ++n) { hv[n >> 1][2 * (n & 1)] = pack_bf(x[n][0], x[n][1]); hv[n >> 1][2 * (n & 1) + 1] = pack_bf(x[n][2], x[n][3]); }
    STAGE(9);
    const int so0 = hoff + ((2 * ih + w) & 3) * 56 * 1024, so1 = hoff + ((2 * ih + 1 + w) & 3) * 56 * 1024;
    st128(c.rs, v16, so0, hv[0]); st128(c.rs, v16, so1, hv[1]);
    st64(c.rs, v8, so0, u32x2{hv[0][0], hv[0][1]}); st64(c.rs, v8, so1, u32x2{hv[1][0], hv[1][1]});
    SCHED();
    if constexpr (ih == 0) { hook(IC<-2>{}); SCHED(); }
#undef STAGE
  });
}

constexpr int PF = 10;
template <int S0, int S1> DEVI void g2_ldh(const Ctx& c, int lane, u32x4 (&hf)[14][2], int ho) {
#pragma unroll
  for (int s = S0; s < S1; ++s)
#pragma unroll
    for (int i = 0; i < 2; ++i) hf[s][i] = ld128(c.rs, lane * 16 + (s & 3) * 1024, ho + i * 56 * 1024 + (s >> 2) * 4096);
}
template <int N> DEVI void ld_w1(const Ctx& c, int v, u32x4 (&w1f)[2][16]) {
  constexpr int ks = N >> 1, j = N & 1;
  const int so = (j ? c.f1o + (ks >> 2) * c.f1s : WS_W1P + c.ff0 * 16384 + (ks >> 2) * 4096);
  if constexpr (j && ks < 8) w1f[j][ks] = ld128a<(ks & 3) * 1024>(c.rs, v, so);
  else w1f[j][ks] = ld128v<(ks & 3) * 1024>(c.rs, v, so);
}
template <int N> DEVI void ld_w2(const Ctx& c, int v, u32x4 (&w2f)[14][4]) {
  constexpr int s = N >> 2, j = N & 3;
  w2f[s][j] = ld128a<(s & 3) * 1024>(c.rs, v, WS_W2P + ((c.ct_ * 4 + ((c.w + j) & 3)) * 56 + 14 * c.w + (s & ~3)) * 1024);
}
template <int N> DEVI void g2_ldh1(const Ctx& c, int v16, u32x4 (&hf)[14][2], int ho) {
  constexpr int s = N >> 1, i = N & 1;
  hf[s][i] = ld128(c.rs, v16 + (s & 3) * 1024, ho + i * 56 * 1024 + (s >> 2) * 4096);
}
template <int PRE, int RATE, class F>
DEVI void g2_mma(const Ctx& c, int lane, u32x4 (&hf)[14][2], const u32x4 (&w2f)[14][4], f32x4 (&acc)[2][4], int ho, F&& mid) {
  const int v16 = lane * 16;
  sfor<0, 14>([&](auto S) {
    constexpr int s = S.v;
    sfor<0, 8>([&](auto M) {
      constexpr int i = M.v >> 2, j = M.v & 3;
      mma_va<s == 0>(acc[i][j], hf[s][i], w2f[s][j]);
      constexpr int per = 4 / RATE, n = 2 * (PRE + RATE * s) + M.v / per;
      if constexpr (M.v % per == per - 1 && n < 28) { g2_ldh1<n>(c, v16, hf, ho); SCHED(); }
    });
    SCHED();
    mid(S);
  });
  MFMA_DONE();
}
template <bool LAST, class F>
DEVI void g2_epi(const Ctx& c, int lane, const f32x4 (&acc)[2][4], const u32x2 (&xr)[2], int h, int xdst, F&& mid) {
  const int g = lane >> 4, cc = lane & 15, q = cc >> 2, p = lane & 3, w = c.w, t = w * 64 + lane;
  char* red = c.smem + L_XA + h * 32768;
#pragma unroll
  for (int jj = 1; jj < 4; ++jj)
#pragma unroll
    for (int i = 0; i < 2; ++i) *(f32x4*)(red + ((((w + jj) & 3) * 3 + jj - 1) * 2 + i) * 1024 + lane * 16) = acc[i][jj];
  __syncthreads();
  char* xt = red + 24576;
  f32x4 pr[2][3];
#pragma unroll
  for (int i = 0; i < 2; ++i)
#pragma unroll
    for (int rk = 0; rk < 3; ++rk) pr[i][rk] = *(const f32x4*)(red + ((w * 3 + rk) * 2 + i) * 1024 + lane * 16);
  mid(0);
#pragma unroll
  for (int i = 0; i < 2; ++i) {
    const f32x4 xv = {lo_bf(xr[i][0]), hi_bf(xr[i][0]), lo_bf(xr[i][1]), hi_bf(xr[i][1])};
    const f32x4 o = ((acc[i][0] + (xv + c.bb2)) + pr[i][0]) + (pr[i][1] + pr[i][2]);
    const u32x2 r = {pack_bf(o[0], o[1]), pack_bf(o[2], o[3])};
    *(u32x2*)(xt + (w * 16 + cc) * 64 + ((i ^ (cc >> 3)) * 32) + g * 8) = r;
  }
  __syncthreads();
  const char* xq = xt + ((w >> 1) * 32 + 8 * g + q) * 64 + (((w & 1) ^ (g & 1)) * 32) + 8 * p;
  const u32x4 f0 = cat2(tr_read(xq), tr_read(xq + 256));
  const int tok = (16 * h + c.P) * 64 + c.sub * 32 + (w & 1) * 16 + cc;
  if (LAST) {
    *(u32x4*)(c.out + (size_t)tok * DM + c.ct_ * 64 + (w >> 1) * 32 + 8 * g) = f0;
  } else {
    const int row = t >> 2;
    const u32x4 v = *(const u32x4*)(xt + row * 64 + (((t & 3) ^ ((row >> 2) & 2)) * 16));
    st128(c.rs, (c.ct_ * 64 + row) * 128 + (t & 3) * 16, xdst, v);
    float s = 0.f, ss = 0.f;
#pragma unroll
    for (int j = 0; j < 4; ++j) { s = dot2bf(f0[j], 0x3f803f80u, s); ss = dot2bf(f0[j], f0[j], ss); }
    s = sum_x16(s); ss = sum_x16(ss);
    (void)__hip_atomic_fetch_add((unsigned long long*)(c.ws + WS_ST + 65536) + 2 * tok + (g & 1),
                                 (unsigned long long)f2fx((g & 1) ? ss : s), RLX_AGENT);
  }
}

template <int LAYER>
DEVI void layer_body(const Ctx& c, u32x4 (&w1f)[2][16], u32x4 (&w2f)[14][4], CIn& inA, CIn& inB) {
  constexpr int xin = WS_X + LAYER * 4 * MB, xmid = xin + 2 * MB, xnext = xin + 4 * MB;
  constexpr int hbuf = WS_H + LAYER * 8 * MB, stin = WS_ST + LAYER * 65536;
  constexpr u32 ep = LAYER + 1;
  constexpr int K1 = 1 + 3 * LAYER, K2 = K1 + 1, K3 = K1 + 2;
  const int w = c.w;
  char* smem = c.smem;
  auto nop = [](auto) {};
  {
    const int lane = fresh_lane(c);
    const int g = lane >> 4, cc = lane & 15, mu0 = (w & 1) * 4, v16 = lane * 16;
    const char* ub = smem + L_U + (w >> 1) * L_UC + 512 + 2 * (16 * cc - 16 + 8 * g) - 64 * (mu0 + 3);
    KB kb;
#pragma unroll
    for (int j = 0; j < 4; ++j) {
      u32 a = lds_addr(smem) + (w >> 1) * 2 * L_KC + ((cc & 1) ? 0 : L_KC) + 2 * (2031 - cc + 8 * g - ((cc & 1) ? 0 : 1)) - 64 * (mu0 + 3 + 56) + 1024 * j;
      asm volatile("" : "+v"(a));
      kb.p[j] = (const char*)(const LAS char*)(size_t)a;
    }
    STAMP(K1, 1);
    f32x4 base0, base1;
    u32x4 a4[4];
    conv_ldk<0>(kb, a4);
    conv_ln(c, lane, inA, 0, base0, base1);
    if (!LAYER) poll0(c, lane, WS_F1);
    __syncthreads();
    STAMP(K1, 2);
    f32x4 acc[4];
    u32 o = 0;
    conv_mma<0, 0, true>(ub, kb, acc, a4, [&](auto N) {
      constexpr int n = N.v;
      if constexpr (LAYER && n == 19) { WAIT_VM(0); o = sig_begin(c, lane); SCHED(); }
      if constexpr (LAYER && n == 29) { sig_end<true>(c, lane, o, CT_X + 64); SCHED(); }
    });
    MFMA_DONE();
    conv_part(c, lane, acc);
    conv_ldk<1>(kb, a4);
    __syncthreads();
    f32x4 s1p, s1q;
    if (!LAYER) {
      if (!got0<0>(c.tag)) wait0(c, lane, WS_F1);
      asm volatile("" :: "v"(inB.x1));
      asm volatile("s_nop 4\n\tbuffer_load_dwordx4 %0, %2, %3, %4 offen sc1\n\tbuffer_load_dwordx4 %1, %2, %3, %4 offen offset:16 sc1"
                   : "=&v"(s1p), "=&v"(s1q)
                   : "v"(min(c.ff0 * 16 + (lane & 15) + (lane >> 5) * 16, FF - 1) * 32), "s"(c.rs), "s"(WS_S1) : "memory");
    }
    conv_out(c, lane, base0, base1, xmid, 0);
    STAMP(K1, 3);
    f32x4 acb[4];
    conv_mma<4, 0, true>(ub, kb, acb, a4, [&](auto N) {
      constexpr int n = N.v;
      if constexpr (!LAYER && (n & 3) == 1) { ld_w1<(n >> 2)>(c, v16, w1f); SCHED(); }
      if constexpr (n == 47) {
        if (LAYER) WAIT_VM(0); else WAIT_VM(12);
        o = sig_begin(c, lane);
        if (LAYER) poll<true>(c, lane, CT_X + 64);
        SCHED();
      }
      if constexpr (n == 57) { sig_end<true>(c, lane, o, CT_C); SCHED(); }
    });
    STAMP(K1, 4);
    if (!LAYER) {
      asm volatile("" : "+v"(s1p), "+v"(s1q));
      s1p += s1q;
      ((float*)(smem + L_CST))[w * 64 + lane] = (lane & 16) ? c.cs1 : (s1p[0] + s1p[1]) + (s1p[2] + s1p[3]);
    }
    if (LAYER) {
      if (!got<1>(32u)) wait_ge<true>(c, lane, CT_X + 64, 32u);
      conv_load(c, lane, inB, xin, stin, 1);
    }
    conv_ldk<0>(kb, a4);
    STAMP(K1, 5);
    conv_ln(c, lane, inB, 1, base0, base1);
    __syncthreads();
    conv_mma<0, 4, false>(ub, kb, acb, a4, [&](auto N) {
      constexpr int n = N.v;
      if constexpr (!LAYER && (n & 3) == 1) { ld_w1<16 + (n >> 2)>(c, v16, w1f); SCHED(); }
      if constexpr (n == 30) { poll<true>(c, lane, CT_C); SCHED(); }
    });
    MFMA_DONE();
    conv_part(c, lane, acb);
    __syncthreads();
    conv_out(c, lane, base0, base1, xmid, 1);
    STAMP(K1, 6);
    if (!got<(LAYER ? 2 : 4)>(32u * ep)) wait_ge<true>(c, lane, CT_C, 32u * ep);
    const int row = lane >> 3, sw = ((row >> 1) & 1) << 1;
    const int ve = row * 128 + ((lane & 7) ^ sw) * 16, vo = row * 128 + ((lane & 7) ^ (sw | 4)) * 16;
    if (!LAYER && c.b == 0 && w == 0) __hip_atomic_store((gu32*)(c.ws + WS_GEN), c.tag, RLX_AGENT);
    sfor<0, 8>([&](auto N) { g1_dma1<N.v>(c, ve, vo, L_XA, xmid + c.P * 65536); });
    WAIT_VM(8);
    o = sig_begin(c, lane);
    sig_end<true>(c, lane, o, CT_C + 64);
    STAMP(K1, 7);
  }
  u32x4 hf[14][2];
  u32x2 xr[2];
  {
    const int lane = fresh_lane(c);
    const int g = lane >> 4, cc = lane & 15, q = cc >> 2, p = lane & 3, v16 = lane * 16;
    const int sx = ((q >> 1) & 1) | ((g & 1) << 1);
    const char* xb[4];
#pragma unroll
    for (int i = 0; i < 4; ++i) xb[i] = smem + L_XA + (8 * g + q) * 128 + ((((i + w) & 3) ^ sx) * 32) + 8 * p;
    const int row = lane >> 3, sw = ((row >> 1) & 1) << 1;
    const int ve = row * 128 + ((lane & 7) ^ sw) * 16, vo = row * 128 + ((lane & 7) ^ (sw | 4)) * 16;
    sfor<8, 12>([&](auto N) { g1_dma1<N.v>(c, ve, vo, L_XA, xmid + c.P * 65536); });
    WAIT_VM_BAR(9);
    STAMP(K2, 0);
    f32x4 acc[2][4], sa;
    g1_mma(xb, w1f, acc, sa, [&](auto KS) {
      constexpr int ks = KS.v;
      if constexpr (ks == 3) { if (LAYER) WAIT_VM_BAR(8); else WAIT_VM_BAR(10); SCHED(); }
      if constexpr (ks == 7) { if (LAYER) WAIT_VM_BAR(0); else WAIT_VM_BAR(10); SCHED(); }
      if constexpr (ks == 13) { if (!LAYER) WAIT_VM(16); poll<true>(c, lane, CT_C + 64); SCHED(); }
    }, [&](auto N) {
      constexpr int n = N.v;
      if constexpr (n < 4) g1_dma1<12 + n>(c, ve, vo, L_XA, xmid + c.P * 65536);
      if constexpr (!LAYER && n >= 4 && n < 10) ld_w1<22 + n>(c, v16, w1f);
      if constexpr (!LAYER && n >= 10) ld_w2<n - 10>(c, v16, w2f);
    });
    STAMP(K2, 1);
    const int xo = xmid + (16 + c.P) * 65536;
    g1_epi(c, lane, acc, sa, hbuf + (c.P * 4 * 56 + c.hks) * 1024, [&](auto N) {
      constexpr int n = N.v;
      if constexpr (n == -1) { if (!got<(LAYER ? 0 : 6)>(32u * ep)) wait_ge<true>(c, lane, CT_C + 64, 32u * ep); }
      if constexpr (n >= 0 && n < 8) g1_dma1<n>(c, ve, vo, L_XB, xo);
      if constexpr (n >= 10 && n < 18) g1_dma1<n - 2>(c, ve, vo, L_XB, xo);
    });
    STAMP(K2, 2);
  }
  {
    const int lane = fresh_lane(c);
    const int g = lane >> 4, cc = lane & 15, q = cc >> 2, p = lane & 3, v16 = lane * 16;
    const int sx = ((q >> 1) & 1) | ((g & 1) << 1);
    const char* xb[4];
#pragma unroll
    for (int i = 0; i < 4; ++i) xb[i] = smem + L_XB + (8 * g + q) * 128 + ((((i + w) & 3) ^ sx) * 32) + 8 * p;
    WAIT_VM_BAR(16);
    STAMP(K2, 3);
    f32x4 acc[2][4], sa;
    g1_mma(xb, w1f, acc, sa, [&](auto KS) {
      constexpr int ks = KS.v;
      if constexpr (ks == 6) {
        if (LAYER) WAIT_VM_BAR(0); else WAIT_VM_BAR(12);
        sig_bar<false>(c, lane, CT_H + c.P);
        SCHED();
      }
      if constexpr (ks == 15) {
#pragma unroll
        for (int i = 0; i < 2; ++i) xr[i] = ld64(c.rs, (c.ct_ * 64 + w * 16 + cc) * 128 + i * 32 + g * 8, xmid + c.P * 65536 + c.sub * 64);
        SCHED();
      }
    }, [&](auto N) {
      constexpr int n = N.v;
      if constexpr (!LAYER) ld_w2<22 + n>(c, v16, w2f);
      if constexpr (!LAYER && n >= 30) ld_w2<24 + n>(c, v16, w2f);
    });
    STAMP(K2, 4);
    poll<false>(c, lane, CT_H + c.P);
    const int ho = hbuf + ((c.P * 4 + c.sub * 2) * 56 + 14 * w) * 1024;
    g1_epi(c, lane, acc, sa, hbuf + ((16 + c.P) * 4 * 56 + c.hks) * 1024, [&](auto N) {
      constexpr int n = N.v;
      if constexpr (n == -1) { if (!got<0>(16u * ep)) wait_ge<false>(c, lane, CT_H + c.P, 16u * ep); }
      if constexpr (n >= 0) g2_ldh1<n>(c, v16, hf, ho);
    });
    STAMP(K2, 5);
  }
  {
    const int lane = fresh_lane(c);
    const int g = lane >> 4, cc = lane & 15;
    const int ho = hbuf + ((c.P * 4 + c.sub * 2) * 56 + 14 * w) * 1024, hoB = ho + 64 * 56 * 1024;
    STAMP(K3, 0);
    f32x4 acc[2][4];
    u32 o = 0;
    g2_mma<10, 1>(c, lane, hf, w2f, acc, ho, [&](auto S) {
      constexpr int s = S.v;
      if constexpr (s == 6) { WAIT_VM(8); o = sig_begin(c, lane); SCHED(); }
      if constexpr (s == 7) { sig_end<false>(c, lane, o, CT_H + 16 + c.P); SCHED(); }
    });
    STAMP(K3, 1);
    poll<false>(c, lane, CT_H + 16 + c.P);
    g2_epi<LAYER == 1>(c, lane, acc, xr, 0, xnext + c.P * 65536 + c.sub * 64, nop);
    STAMP(K3, 2);
    if (!got<(LAYER ? 1 : 2)>(16u * ep)) wait_ge<false>(c, lane, CT_H + 16 + c.P, 16u * ep);
    g2_ldh<0, 4>(c, lane, hf, hoB);
#pragma unroll
    for (int i = 0; i < 2; ++i) xr[i] = ld64(c.rs, (c.ct_ * 64 + w * 16 + cc) * 128 + i * 32 + g * 8, xmid + (16 + c.P) * 65536 + c.sub * 64);
    if (LAYER == 0) {
      WAIT_VM(10);
      o = sig_begin(c, lane);
      sig_end<true>(c, lane, o, CT_X);
    }
    SCHED();
    STAMP(K3, 3);
  }
  {
    const int lane = fresh_lane(c);
    const int hoB = hbuf + (((16 + c.P) * 4 + c.sub * 2) * 56 + 14 * w) * 1024;
    f32x4 acc[2][4];
    g2_mma<4, 2>(c, lane, hf, w2f, acc, hoB, [&](auto S) {
      constexpr int s = S.v;
      if constexpr (LAYER == 0 && s == 13) { poll<true>(c, lane, CT_X); SCHED(); }
    });
    STAMP(K3, 4);
    g2_epi<LAYER == 1>(c, lane, acc, xr, 1, xnext + (16 + c.P) * 65536 + c.sub * 64, [&](int) {
      if (LAYER == 0) {
        if (!got<0>(32u)) wait_ge<true>(c, lane, CT_X, 32u);
        conv_load(c, lane, inA, xnext, WS_ST + 65536, 0);
        SCHED();
      }
    });
    STAMP(K3, 5);
  }
}

constexpr int KG_RS = 528, KG_X = 16 * KG_RS, KG_CH = 64 * KG_RS + 1296;
__global__ void __launch_bounds__(256, 1) main_kernel(const u16* x, const u16* lar, const u16* aim, const u16* cre,
                                                      const u16* cim, const u16* ldt, const u16* dsk, const u16* w1,
                                                      const u16* b1, const u16* w2, const u16* b2, char* ws, u16* out) {
  extern __shared__ __attribute__((aligned(16))) char smem[];
  const int t = threadIdx.x, lane = t & 63, b = blockIdx.x;
  const int w = __builtin_amdgcn_readfirstlane(t >> 6);
  const u32 junk = warm_code((b >> 3) * 4 + w, lane);
  STAMP(0, 0);
  Ctx c;
  c.rs = __builtin_amdgcn_make_buffer_rsrc(ws, 0, WS_END, 0x00020000);
  c.ct = (gu32*)(ws + WS_CT);
  c.smem = smem; c.ws = ws; c.out = out;
  c.lane0 = lane; c.w = w; c.b = b;
  c.P = (b & 7) * 2 + ((b >> 3) & 1);
  const int idx = b >> 4;
  const int sg = idx >> 1;
  c.eo = idx & 1; c.sub = idx >> 3; c.ct_ = idx & 7;
  c.ff0 = w < 3 ? 14 * sg + 6 * c.eo + 2 * w : 14 * sg + 12 + c.eo;
  c.hks = w < 3 ? 7 * sg + 3 * c.eo + w : 7 * sg + 6;
  c.f1o = w < 3 ? WS_W1P + c.ff0 * 16384 + 16384 : WS_ONE; c.f1s = w < 3 ? 4096 : 0;
  CIn inA, inB;
  {
    const int ch = w >> 1, d = 2 * b + ch, n = t & 127;
    const u16 r0 = ldt[d], r1 = lar[d * NST + n], r2 = aim[d * NST + n], r3 = cre[d * NST + n], r4 = cim[d * NST + n];
    const u16 r5 = cre[d * NST + (n ^ 64)], r6 = cim[d * NST + (n ^ 64)];
    const u32 gen = __hip_atomic_load((gu32*)(ws + WS_GEN), RLX_AGENT);
    u32x4 rv[8], rw[2];
    const bool early = !(b & 64);
    if (early) xpose_load(b & 63, x, rv);
    else if ((b & 63) < 56) pack_w1_load(b & 63, b >> 7, w1, rv);
    pack_w2_load(min(b, 223), w2, rw);
    const u32 r7 = *(const u32*)(dsk + 2 * b);
    u32 r8 = b1[min(c.ff0 * 16 + (lane & 15) + (lane >> 5) * 16, FF - 1)], r9 = b2[c.ct_ * 64 + w * 16 + (lane & 15)];
    SCHED();
    char* kg = smem + L_XA + ch * KG_CH;
    char* stg = smem + L_XA + 2 * KG_CH;
    const int i = lane & 15, g = lane >> 4;
    int ix[9];
#pragma unroll
    for (int e2 = 0; e2 < 9; ++e2) { ix[e2] = 2 * cidx(max(2047 - 8 * t - e2, 0)); asm volatile("" : "+v"(ix[e2])); }
    int wi[2][4];
    {
      const int m8 = -(i >> 3), s1 = -(w & 1);
      const int h1 = ((256 + 128 * (i >> 2) + 2 * (i & 3)) & ~m8) | ((128 * (i >> 1) + 4 * (i & 1)) & m8);
      const int hp = (((i + (i & 8) * 15) & ~s1) | (h1 & s1)) + 32 * g, hq = 1024 + 128 * i + 32 * g;
#pragma unroll
      for (int r = 0; r < 4; ++r) {
        wi[0][r] = 2 * cidx(hp + 8 * r); wi[1][r] = 2 * cidx(hq + 8 * r > 2048 ? 2064 : hq + 8 * r);
        asm volatile("" : "+v"(wi[0][r]), "+v"(wi[1][r]));
      }
    }
    if (t < 16) *(u32x4*)(smem + (t >> 2) * L_KC + 4096 + (t & 3) * 16) = u32x4{0, 0, 0, 0};
    if (t >= 64 && t < 128) *(u32x4*)(smem + L_U + ((t - 64) >> 5) * L_UC + (t & 31) * 16) = u32x4{0, 0, 0, 0};
    if (t == 128) *(u32*)(smem + L_SIG) = 0u;
    SCHED();
    STAMP(0, 1);
    const float dt = rbf(expf(bf2f(r0)));
    const float cr = bf2f(r3), ci = bf2f(r4), cr2 = bf2f(r5), ci2 = bf2f(r6);
    const float S = wave_sum(sqrt_(cr * cr + ci * ci) + sqrt_(cr2 * cr2 + ci2 * ci2)) * (2.002f * dt);
    int e = __builtin_amdgcn_frexp_expf(S);
    e = (S > 0.0f && S < 3.0e38f) ? max(-100, min(100, e)) : 14;
    const float scale = __builtin_amdgcn_ldexpf(2.0f, 14 - e);
    if (n == 0) *(float*)(smem + L_MISC + ch * 4) = __builtin_amdgcn_ldexpf(1.0f, e - 14);
    const float a2 = (-rbf(expf(bf2f(r1))) * dt) * 1.44269504088896f;
    const float br = (bf2f(r2) * dt) * 0.159154943091895f;
    {
      char* yp = kg + n * 4;
      char* xp = kg + KG_X + n * 4;
      const CM e128 = cexp_(a2, -br, 128.f), e8 = cexp_(a2, br, 8.f), e1 = cexp_(a2, -br, 1.f), e2 = cexp_(a2, -br, 2.f);
      const CM e4 = cexp_(a2, -br, 4.f);
      f32x2 f0 = {(cr * dt) * scale, -(ci * dt) * scale};
      f32x2 f = cmul(f0, cexp_(a2, -br, 512.f)), y = {1.f, 0.f};
      f32x2 g1 = cmul(f0, e128), g2 = cmul(g1, e128), g3 = cmul(g2, e128);
#define PUT(base, row, v) *(u32*)(base + (row) * KG_RS) = pack_h(v[0], v[1])
      auto chain = [&](auto K) {
        constexpr int k = K.v;
        PUT(yp, k, y);
        if (k < 15) y = cmul(y, e8);
        if (k < 4) { const f32x2 q4 = cmul(f, e4); PUT(xp, 24 + 2 * k, f); PUT(xp, 25 + 2 * k, q4); }
        else if (k < 13) PUT(xp, 28 + k, f);
        if (k < 12) f = cmul(f, e128);
        if (k < 8) { PUT(xp, k, f0); PUT(xp, 8 + k, g1); }
        if (k < 7) { f0 = cmul(f0, e1); g1 = cmul(g1, e1); }
        if (k >= 8 && k < 12) { PUT(xp, 8 + k, g2); PUT(xp, 12 + k, g3); }
        if (k >= 8 && k < 11) { g2 = cmul(g2, e2); g3 = cmul(g3, e2); }
      };
      c.tag = __builtin_amdgcn_readfirstlane(gen) + 1u;
      sfor<0, 2>(chain);
      asm volatile("" : "+v"(y), "+v"(f), "+v"(f0), "+v"(g1));
      if (b < 64) xpose(b, rv, c.rs, stg);
      else if (early) row_stats(b & 63, rv, c.rs);
      asm volatile("" : "+v"(y), "+v"(f), "+v"(f0), "+v"(g1));
      sfor<2, 12>(chain);
      asm volatile("" : "+v"(y), "+v"(f));
      if (early) {
        WAIT_VM(0);
        __syncthreads();
        if (w == 0 && lane < 8)
          __hip_atomic_store((gu32*)(ws + WS_F0 + lane * 2048 + (b & 7) * 256 + ((b >> 3) & 7) * 8 + (b >> 7) * 4), c.tag, RLX_AGENT);
      }
      asm volatile("" : "+v"(y), "+v"(f));
      STAMP(0, 2);
      sfor<12, 16>(chain);
      STAMP(0, 3);
      if (!early && (b & 63) < 56) pack_w1(b & 63, b >> 7, rv, c.rs, stg);
#undef PUT
    }
    pack_w2_put(rw, smem + L_XB + 55552);
    __syncthreads();
    STAMP(0, 4);
    {
      const char* ya = kg + i * KG_RS + g * 16;
      const char* xa = kg + KG_X + (16 * (w & 1) + i) * KG_RS + g * 16;
      const char* xc = kg + KG_X + (32 + i) * KG_RS + g * 16;
      u32x4 fa[8], fb[8], fc[8];
#pragma unroll
      for (int ks = 0; ks < 8; ++ks) { fa[ks] = *(const u32x4*)(ya + ks * 64); fb[ks] = *(const u32x4*)(xa + ks * 64); }
#pragma unroll
      for (int ks = 0; ks < 8; ++ks) fc[ks] = *(const u32x4*)(xc + ks * 64);
      f32x4 ac0, ac1;
      SCHED();
      sfor<0, 8>([&](auto KS) { mma_hh<KS.v == 0>(ac0, fa[KS.v], fb[KS.v]); });
      sfor<0, 8>([&](auto KS) { mma_hh<KS.v == 0>(ac1, fa[KS.v], fc[KS.v]); });
      MFMA_DONE();
#pragma unroll
      for (int r = 0; r < 4; ++r) {
        *(u16*)(kg + 64 * KG_RS + wi[0][r]) = __builtin_bit_cast(u16, (_Float16)ac0[r]);
        *(u16*)(kg + 64 * KG_RS + wi[1][r]) = __builtin_bit_cast(u16, (_Float16)ac1[r]);
      }
    }
    if (!early) WAIT_VM(0);
    __syncthreads();
    if (w == 1 && !early && lane < 8)
      __hip_atomic_store((gu32*)(ws + WS_F1 + lane * 2048 + (b & 7) * 256 + ((b >> 3) & 7) * 8 + (b >> 7) * 4), c.tag, RLX_AGENT);
    if (w == 0) poll0(c, lane, WS_F0);
#pragma unroll
    for (int c2 = 0; c2 < 2; ++c2) {
      u32 v[9];
#pragma unroll
      for (int e2 = 0; e2 < 9; ++e2) v[e2] = *(const u16*)(smem + L_XA + c2 * KG_CH + 64 * KG_RS + ix[e2]);
      if (t == 255) v[8] = 0u;
      u32x4 oe, oo;
#pragma unroll
      for (int j = 0; j < 4; ++j) { oe[j] = v[2 * j] | (v[2 * j + 1] << 16); oo[j] = v[2 * j + 1] | (v[2 * j + 2] << 16); }
      *(u32x4*)(smem + c2 * 2 * L_KC + t * 16) = oe;
      *(u32x4*)(smem + c2 * 2 * L_KC + L_KC + t * 16) = oo;
    }
    const f32x2 ksc = *(const f32x2*)(smem + L_MISC);
    c.ks0 = __uint_as_float(__builtin_amdgcn_readfirstlane(__float_as_uint(ksc[0])));
    c.ks1 = __uint_as_float(__builtin_amdgcn_readfirstlane(__float_as_uint(ksc[1])));
    const u32 ds = __builtin_amdgcn_readfirstlane(r7);
    c.dsk0 = lo_bf(ds); c.dsk1 = hi_bf(ds);
    c.cs1 = lo_bf(r8); c.bb2 = lo_bf(r9);
    asm volatile("" :: "v"(junk));
    STAMP(0, 5);
    if (w == 0 && !got0<0>(c.tag)) wait0(c, lane, WS_F0);
    __syncthreads();
    conv_load(c, lane, inA, WS_X, WS_ST, 0);
    conv_load(c, lane, inB, WS_X, WS_ST, 1);
    pack_w2_out(b, c.rs, smem + L_XB + 55552);
    STAMP(0, 6);
  }
  u32x4 w1f[2][16], w2f[14][4];
  layer_body<0>(c, w1f, w2f, inA, inB);
  layer_body<1>(c, w1f, w2f, inA, inB);
}

extern "C" __global__ void code_end_marker() {}

extern "C" void kernel_launch(void* const* d_in, const int* in_sizes, int n_in, void* d_out, int out_size,
                              void* d_ws, size_t ws_size, hipStream_t stream) {
  (void)in_sizes; (void)n_in; (void)out_size; (void)ws_size;
  char* ws = (char*)d_ws;
  (void)hipFuncSetAttribute((const void*)main_kernel, hipFuncAttributeMaxDynamicSharedMemorySize, MAIN_LDS);
  main_kernel<<<256, 256, MAIN_LDS, stream>>>((const u16*)d_in[0], (const u16*)d_in[1], (const u16*)d_in[2],
                                              (const u16*)d_in[3], (const u16*)d_in[4], (const u16*)d_in[5],
                                              (const u16*)d_in[6], (const u16*)d_in[8], (const u16*)d_in[9],
                                              (const u16*)d_in[10], (const u16*)d_in[11], ws, (u16*)d_out);
}
```

```cpp
#include <hip/hip_runtime.h>
#include <hip/hip_bf16.h>

typedef unsigned short u16;
typedef unsigned int u32;
typedef long long i64;
typedef __attribute__((ext_vector_type(4))) short s16x4;
typedef __attribute__((ext_vector_type(8))) __bf16 bf16x8;
typedef __attribute__((ext_vector_type(2))) __bf16 bf16x2;
typedef __attribute__((ext_vector_type(8))) _Float16 f16x8;
typedef __attribute__((ext_vector_type(2))) _Float16 f16x2;
typedef __attribute__((ext_vector_type(4))) float f32x4;
typedef __attribute__((ext_vector_type(2))) float f32x2;
typedef __attribute__((ext_vector_type(4))) unsigned u32x4;
typedef __attribute__((ext_vector_type(2))) unsigned u32x2;
typedef __attribute__((ext_vector_type(2))) long long i64x2;
typedef __attribute__((address_space(1))) unsigned gu32;
typedef __amdgpu_buffer_rsrc_t rsrc_t;
#define LAS __attribute__((address_space(3)))
#define DEVI __device__ __forceinline__
template <int V> struct IC { static constexpr int v = V; };
template <int I, int N, class F> __device__ __forceinline__ void sfor(F&& f) {
  if constexpr (I < N) { f(IC<I>{}); sfor<I + 1, N>(f); }
}
#define RLX_AGENT __ATOMIC_RELAXED, __HIP_MEMORY_SCOPE_AGENT

constexpr int SEQ = 2048, DM = 512, NST = 128, FF = 1792;
constexpr float LN_EPS = 1e-5f;
constexpr float FX_SCALE = 16777216.0f;
constexpr float FX_INV = 1.0f / 16777216.0f;
constexpr int SC1 = 16;

constexpr int MB = 1024 * 1024;
constexpr int WS_X = 0;
constexpr int WS_W1P = 12 * MB;
constexpr int WS_W2P = 14 * MB;
constexpr int WS_H = 16 * MB;
constexpr int WS_ST = 32 * MB;
constexpr int WS_S1 = 32 * MB + 131072;
constexpr int WS_GEN = 32 * MB + 196608;
constexpr int WS_F0 = WS_GEN + 256, WS_F1 = WS_F0 + 16384;
constexpr int WS_CT = 32 * MB + 262144;
constexpr int WS_ONE = 32 * MB + 393216;
constexpr int WS_END = 33 * MB;
constexpr int CT_C = 0, CT_X = 128, CT_H = 256, CT_TMO = 288;
constexpr int SPIN_CAP = 1 << 16;

#define STAMP(k, i) do {} while (0)

DEVI float bf2f(u16 v) { return __uint_as_float((u32)v << 16); }
DEVI float lo_bf(u32 w) { return __uint_as_float(w << 16); }
DEVI float hi_bf(u32 w) { return __uint_as_float(w & 0xffff0000u); }
DEVI float rbf(float x) { return (float)(__bf16)x; }
DEVI u32 pack_bf(float a, float b) { f32x2 q = {a, b}; return __builtin_bit_cast(u32, __builtin_convertvector(q, bf16x2)); }
DEVI u32 pack_h(float a, float b) { f32x2 q = {a, b}; return __builtin_bit_cast(u32, __builtin_convertvector(q, f16x2)); }
DEVI float dot2bf(u32 a, u32 b, float c) {
  return __builtin_amdgcn_fdot2_f32_bf16(__builtin_bit_cast(bf16x2, a), __builtin_bit_cast(bf16x2, b), c, false);
}
DEVI f32x4 mfma_bf(u32x4 a, u32x4 b, f32x4 c) {
  return __builtin_amdgcn_mfma_f32_16x16x32_bf16(__builtin_bit_cast(bf16x8, a), __builtin_bit_cast(bf16x8, b), c, 0, 0, 0);
}
DEVI f32x4 mfma_h(u32x4 a, u32x4 b, f32x4 c) {
  return __builtin_amdgcn_mfma_f32_16x16x32_f16(__builtin_bit_cast(f16x8, a), __builtin_bit_cast(f16x8, b), c, 0, 0, 0);
}
DEVI u32x2 tr_read(const char* p) {
  return __builtin_bit_cast(u32x2, __builtin_amdgcn_ds_read_tr16_b64_v4i16((LAS s16x4*)p));
}
DEVI u32x4 cat2(u32x2 a, u32x2 b) { return __builtin_shufflevector(a, b, 0, 1, 2, 3); }
DEVI u32 lds_addr(const void* p) { return (u32)(size_t)(const LAS char*)p; }
#define WAIT_VM(n) asm volatile("s_waitcnt vmcnt(" #n ")" ::: "memory")
#define WAIT_VM_BAR(n) asm volatile("s_waitcnt vmcnt(" #n ")\n\ts_barrier" ::: "memory")
#define SCHED() __builtin_amdgcn_sched_barrier(0)
template <int CTRL> DEVI float dpp_add(float v) {
  return v + __uint_as_float(__builtin_amdgcn_update_dpp(0u, __float_as_uint(v), CTRL, 0xf, 0xf, true));
}
DEVI float sum_x16(float v) {
  auto r = __builtin_amdgcn_permlane16_swap(__float_as_uint(v), __float_as_uint(v), false, false);
  return __uint_as_float(r[0]) + __uint_as_float(r[1]);
}
DEVI float sum_x32(float v) {
  auto r = __builtin_amdgcn_permlane32_swap(__float_as_uint(v), __float_as_uint(v), false, false);
  return __uint_as_float(r[0]) + __uint_as_float(r[1]);
}
DEVI float wave_sum(float v) {
  v = dpp_add<0xB1>(v); v = dpp_add<0x4E>(v); v = dpp_add<0x141>(v); v = dpp_add<0x140>(v);
  return sum_x32(sum_x16(v));
}
DEVI float fx2f(u32 lo, u32 hi) { return fmaf((float)(int)hi, 4294967296.0f * FX_INV, (float)lo * FX_INV); }
DEVI i64 f2fx(float v) { return (i64)(v * FX_SCALE); }
DEVI float sel4(int i, float a, float b, float c, float d) {
  asm volatile("" : "+v"(a), "+v"(b), "+v"(c), "+v"(d));
  return (i & 2) ? ((i & 1) ? d : c) : ((i & 1) ? b : a);
}
DEVI float rcp(float x) { return __builtin_amdgcn_rcpf(x); }
DEVI float sqrt_(float x) { return __builtin_amdgcn_sqrtf(x); }

DEVI u32 warm_code(int slice, int lane) {
  unsigned long long pc, pe;
  asm volatile("s_getpc_b64 %0\n\ts_getpc_b64 s[90:91]\n\ts_add_u32 s90, s90, code_end_marker@rel32@lo+4\n\t"
               "s_addc_u32 s91, s91, code_end_marker@rel32@hi+12\n\ts_mov_b64 %1, s[90:91]"
               : "=&s"(pc), "=s"(pe) :: "scc", "s90", "s91");
  const unsigned long long a = (pc + (unsigned)(slice * 512 + (lane & 7) * 64)) & ~3ull;
  u32 junk = 0;
  if (pc + (unsigned)(slice * 512 + 512) <= pe) asm volatile("global_load_dword %0, %1, off" : "=v"(junk) : "v"(a) : "memory");
  return junk;
}

DEVI int cidx(int l) {
  const int hb = (int)(pack_bf((float)l, 0.f) & 0xffffu) - (0x4300 - 128);
  return l < 128 ? l : hb;
}
struct CM { f32x2 e, f; };
DEVI f32x2 cmul(f32x2 a, const CM& m) { return a[1] * m.f + a[0] * m.e; }
DEVI CM cexp_(float a2, float br, float p) {
  const float e = __builtin_amdgcn_exp2f(a2 * p), f = __builtin_amdgcn_fractf(br * p);
  const float re = e * __builtin_amdgcn_cosf(f), im = e * __builtin_amdgcn_sinf(f);
  CM r; r.e = f32x2{re, im}; r.f = f32x2{-im, re};
  return r;
}
DEVI void st16(rsrc_t rs, int off, u32x4 v) { __builtin_amdgcn_raw_buffer_store_b128(v, rs, off, 0, SC1); }

DEVI void xpose_load(int b, const u16* x, u32x4 (&v)[8]) {
  const int lane = threadIdx.x & 63, w = threadIdx.x >> 6;
#pragma unroll
  for (int r = 0; r < 8; ++r) v[r] = *(const u32x4*)(x + (size_t)(b * 32 + w * 8 + r) * DM + lane * 8);
}
DEVI void xpose(int b, const u32x4 (&v)[8], rsrc_t rs, char* smem) {
  const int t = threadIdx.x, lane = t & 63, w = t >> 6;
  constexpr int RS = 1024 + 32;
#pragma unroll
  for (int r = 0; r < 8; ++r) *(u32x4*)(smem + (w * 8 + r) * RS + lane * 16) = v[r];
  __syncthreads();
  const int g = lane >> 4, q = (lane & 15) >> 2, p = lane & 3;
#pragma unroll
  for (int cf = 0; cf < 8; ++cf) {
    const int c0 = (w * 8 + cf) * 16;
    const char* pa = smem + (8 * g + q) * RS + c0 * 2 + 8 * p;
    const u32x4 o = cat2(tr_read(pa), tr_read(pa + 4 * RS));
    st16(rs, WS_X + (b >> 1) * 65536 + (c0 + (lane & 15)) * 128 + (b & 1) * 64 + g * 16, o);
  }
}
DEVI void row_stats(int b, const u32x4 (&v)[8], rsrc_t rs) {
  const int t = threadIdx.x, lane = t & 63, w = t >> 6;
  const int t0 = b * 32;
  float a[16];
#pragma unroll
  for (int r = 0; r < 8; ++r) {
    float s = 0.f, ss = 0.f;
#pragma unroll
    for (int j = 0; j < 4; ++j) { s = dot2bf(v[r][j], 0x3f803f80u, s); ss = dot2bf(v[r][j], v[r][j], ss); }
    a[2 * r] = s; a[2 * r + 1] = ss;
  }
  const bool o1 = lane & 1, o2 = lane & 2;
#pragma unroll
  for (int k = 0; k < 8; ++k) {
    const float keep = o1 ? a[2 * k + 1] : a[2 * k], send = o1 ? a[2 * k] : a[2 * k + 1];
    a[k] = keep + __uint_as_float(__builtin_amdgcn_update_dpp(0u, __float_as_uint(send), 0xB1, 0xf, 0xf, true));
  }
#pragma unroll
  for (int k = 0; k < 4; ++k) {
    const float keep = o2 ? a[2 * k + 1] : a[2 * k], send = o2 ? a[2 * k] : a[2 * k + 1];
    a[k] = keep + __uint_as_float(__builtin_amdgcn_update_dpp(0u, __float_as_uint(send), 0x4E, 0xf, 0xf, true));
  }
#pragma unroll
  for (int k = 0; k < 4; ++k) a[k] = sum_x32(sum_x16(dpp_add<0x128>(dpp_add<0x124>(a[k]))));
  const i64 sv = lane < 16 ? f2fx(sel4(lane >> 2, a[0], a[1], a[2], a[3])) : 0;
  if (lane < 32)
    __builtin_amdgcn_raw_buffer_store_b64(__builtin_bit_cast(u32x2, sv), rs, (lane >> 4) * 65536 + (t0 + w * 8) * 16 + (lane & 15) * 8, WS_ST, SC1);
  if (t < 5) __builtin_amdgcn_raw_buffer_store_b32(0u, rs, (b * 5 + t) * 256, WS_CT, SC1);
}

DEVI void pack_w1_load(int b, int kh, const u16* w1, u32x4 (&v)[8]) {
  const int t = threadIdx.x;
#pragma unroll
  for (int it = 0; it < 4; ++it) v[it] = *(const u32x4*)(w1 + (size_t)(kh * 256 + it * 64 + (t >> 2)) * FF + b * 32 + (t & 3) * 8);
}
DEVI void pack_w1(int b, int kh, const u32x4 (&v)[8], rsrc_t rs, char* smem) {
  const int t = threadIdx.x, lane = t & 63, w = t >> 6;
  constexpr int RS = 64 + 32;
#pragma unroll
  for (int it = 0; it < 4; ++it) *(u32x4*)(smem + (it * 64 + (t >> 2)) * RS + (t & 3) * 16) = v[it];
  __syncthreads();
  const int g = lane >> 4, q = (lane & 15) >> 2, p = lane & 3;
  float cs[2] = {0.f, 0.f};
#pragma unroll
  for (int kk = 0; kk < 2; ++kk) {
    const int ks = w * 2 + kk;
#pragma unroll
    for (int h = 0; h < 2; ++h) {
      const char* a = smem + (32 * ks + 8 * g + q) * RS + h * 32 + 8 * p;
      const u32x4 o = cat2(tr_read(a), tr_read(a + 4 * RS));
#pragma unroll
      for (int j = 0; j < 4; ++j) cs[h] = dot2bf(o[j], 0x3f803f80u, cs[h]);
      st16(rs, WS_W1P + (((2 * b + h) * 16 + kh * 8 + ks) * 64 + lane) * 16, o);
    }
  }
  const float s0 = sum_x32(sum_x16(cs[0])), s1 = sum_x32(sum_x16(cs[1]));
  if (lane < 32) __builtin_amdgcn_raw_buffer_store_b32(__float_as_uint(lane < 16 ? s0 : s1), rs, ((b * 32 + lane) * 8 + kh * 4 + w) * 4, WS_S1, SC1);
  if (b == 0 && kh == 0) st16(rs, WS_ONE + t * 16, u32x4{0x3f803f80u, 0x3f803f80u, 0x3f803f80u, 0x3f803f80u});
}

DEVI void pack_w2_load(int b, const u16* w2, u32x4 (&v)[2]) {
  const int t = threadIdx.x;
#pragma unroll
  for (int r = 0; r < 2; ++r) v[r] = *(const u32x4*)(w2 + (size_t)((b >> 2) * 32 + r * 16 + (t >> 4)) * DM + (b & 3) * 128 + (t & 15) * 8);
}
constexpr int W2_RS = 256 + 32;
DEVI void pack_w2_put(const u32x4 (&v)[2], char* smem) {
  const int t = threadIdx.x;
#pragma unroll
  for (int r = 0; r < 2; ++r) *(u32x4*)(smem + (r * 16 + (t >> 4)) * W2_RS + (t & 15) * 16) = v[r];
}
DEVI void pack_w2_out(int b, rsrc_t rs, const char* smem) {
  const int t = threadIdx.x, lane = t & 63, w = t >> 6;
  const int g = lane >> 4, q = (lane & 15) >> 2, p = lane & 3;
#pragma unroll
  for (int cf = 0; cf < 2; ++cf) {
    const char* a = smem + (4 * g + q) * W2_RS + (w * 2 + cf) * 32 + 8 * p;
    const u32x4 o = cat2(tr_read(a), tr_read(a + 16 * W2_RS));
    st16(rs, b < 224 ? WS_W2P + ((((b & 3) * 8 + w * 2 + cf) * 56 + (b >> 2)) * 64 + lane) * 16 : 0x40000000, o);
  }
}

constexpr int L_KC = 4160;
constexpr int L_U = 4 * L_KC, L_UC = 4608;
constexpr int L_XA = 26112, L_XB = L_XA + 65536;
constexpr int L_MISC = L_XB + 65536;
constexpr int L_CST = L_MISC + 512 + 4 * 512;
constexpr int L_SIG = L_CST + 4 * 256;
constexpr int MAIN_LDS = L_SIG + 256;
constexpr int L_RED = L_XB, L_SCR = L_XB + 16384;

DEVI u32x4 ld128(rsrc_t rs, int v, int s) { return __builtin_amdgcn_raw_buffer_load_b128(rs, v, s, SC1); }
DEVI u32x2 ld64(rsrc_t rs, int v, int s) { return __builtin_amdgcn_raw_buffer_load_b64(rs, v, s, SC1); }
DEVI void st128(rsrc_t rs, int v, int s, u32x4 d) { __builtin_amdgcn_raw_buffer_store_b128(d, rs, v, s, SC1); }
DEVI void st64(rsrc_t rs, int v, int s, u32x2 d) { __builtin_amdgcn_raw_buffer_store_b64(d, rs, v, s, SC1); }
template <int OFF> DEVI u32x4 ld128a(rsrc_t rs, int v, int s) {
  u32x4 r;
  asm volatile("buffer_load_dwordx4 %0, %1, %2, %3 offen offset:%4 sc1" : "=a"(r) : "v"(v), "s"(rs), "s"(s), "i"(OFF) : "memory");
  return r;
}
template <int OFF> DEVI u32x4 ld128v(rsrc_t rs, int v, int s) {
  u32x4 r;
  asm volatile("buffer_load_dwordx4 %0, %1, %2, %3 offen offset:%4 sc1" : "=v"(r) : "v"(v), "s"(rs), "s"(s), "i"(OFF) : "memory");
  return r;
}
template <int OFF> DEVI void dma1k(rsrc_t rs, u32 lds_dst, int v, int soff) {
  asm volatile("s_mov_b32 m0, %0\n\ts_nop 0\n\tbuffer_load_dwordx4 %1, %2, %3 offen offset:%4 sc1 lds"
               :: "s"(lds_dst), "v"(v), "s"(rs), "s"(soff), "i"(OFF) : "memory");
}
#define MMA(NAME, OP, CA, CB)                                                                                   \
  template <bool Z> DEVI void NAME(f32x4& d, const u32x4& a, const u32x4& b) {                                  \
    if (Z) asm volatile(OP " %0, %1, %2, 0" : "=&v"(d) : CA(a), CB(b));                                         \
    else asm volatile(OP " %0, %1, %2, %0" : "+v"(d) : CA(a), CB(b));                                           \
  }
MMA(mma_vv, "v_mfma_f32_16x16x32_bf16", "v", "v")
MMA(mma_av, "v_mfma_f32_16x16x32_bf16", "a", "v")
MMA(mma_va, "v_mfma_f32_16x16x32_bf16", "v", "a")
MMA(mma_hh, "v_mfma_f32_16x16x32_f16", "v", "v")
#define MFMA_DONE() do { SCHED(); asm volatile("s_nop 7" ::: "memory"); SCHED(); } while (0)

struct Ctx {
  rsrc_t rs; gu32* ct; char* smem; char* ws; u16* out;
  int lane0, w, b, P, sub, ct_, ff0, hks, eo, f1o, f1s;
  float dsk0, dsk1, ks0, ks1, bb2, cs1;
  u32 tag;
};
DEVI int fresh_lane(const Ctx& c) { int l = c.lane0; asm volatile("" : "+v"(l)); return l; }
DEVI u32 sig_begin(const Ctx& c, int lane) {
  u32 old;
  asm volatile("ds_add_rtn_u32 %0, %1, %2" : "=v"(old) : "v"(lds_addr(c.smem) + L_SIG + lane * 4), "v"(1u) : "memory");
  return old;
}
template <bool ALL> DEVI void sig_end(const Ctx& c, int lane, u32 old, int line) {
  asm volatile("s_waitcnt lgkmcnt(0)" : "+v"(old) :: "memory");
  const bool last = (__builtin_amdgcn_readfirstlane(old) & 3u) == 3u;
  const int v = (last && lane < (ALL ? 8 : 1)) ? (ALL ? lane * 2048 : 0) : 0x40000000;
  asm volatile("s_nop 4\n\tbuffer_atomic_add %0, %1, %2, %3 offen" :: "v"(1u), "v"(v), "s"(c.rs),
               "s"(WS_CT + (line + (ALL ? (c.b >> 3) & 7 : 0)) * 256) : "memory");
}
template <bool ALL, int R = 0> DEVI void poll(const Ctx& c, int lane, int line) {
  const int v = ALL ? (lane & 7) * 256 : 0, so = WS_CT + (line + (ALL ? (c.b & 7) * 8 : 0)) * 256;
  if (R == 0) asm volatile("s_nop 4\n\tbuffer_load_dword v254, %0, %1, %2 offen sc1" :: "v"(v), "s"(c.rs), "s"(so) : "memory", "v254");
  else asm volatile("s_nop 4\n\tbuffer_load_dword v255, %0, %1, %2 offen sc1" :: "v"(v), "s"(c.rs), "s"(so) : "memory", "v255");
}
template <int YOUNGER, int R = 0> DEVI bool got(u32 want) {
  unsigned long long m;
  if (R == 0) asm volatile("s_waitcnt vmcnt(%2)\n\tv_cmp_le_u32_e64 %0, %1, v254" : "=s"(m) : "s"(want), "i"(YOUNGER) : "memory");
  else asm volatile("s_waitcnt vmcnt(%2)\n\tv_cmp_le_u32_e64 %0, %1, v255" : "=s"(m) : "s"(want), "i"(YOUNGER) : "memory");
  return m == ~0ull;
}
template <bool ALL> DEVI void wait_ge(const Ctx& c, int lane, int line, u32 want) {
  int sp = 0;
  poll<ALL, 0>(c, lane, line);
  __builtin_amdgcn_s_sleep(6);
  poll<ALL, 1>(c, lane, line);
#pragma nounroll
  for (;;) {
    if (got<1, 0>(want)) break;
    poll<ALL, 0>(c, lane, line);
    if (got<1, 1>(want)) break;
    poll<ALL, 1>(c, lane, line);
    if (++sp > SPIN_CAP) { __hip_atomic_store(c.ct + CT_TMO * 64, 1u, RLX_AGENT); break; }
  }
}

DEVI void poll0(const Ctx& c, int lane, int set) {
  asm volatile("s_nop 4\n\tbuffer_load_dwordx2 v[254:255], %0, %1, %2 offen sc1" :: "v"((lane & 7) * 256 + (lane >> 3) * 8), "s"(c.rs),
               "s"(set + (c.b & 7) * 2048) : "memory", "v254", "v255");
}
template <int YOUNGER> DEVI bool got0(u32 tag) {
  unsigned long long m, m2;
  asm volatile("s_waitcnt vmcnt(%3)\n\tv_cmp_eq_u32_e64 %0, %2, v254\n\tv_cmp_eq_u32_e64 %1, %2, v255"
               : "=&s"(m), "=s"(m2) : "s"(tag), "i"(YOUNGER) : "memory");
  return (m & m2) == ~0ull;
}
DEVI void wait0(const Ctx& c, int lane, int set) {
  int sp = 0;
#pragma nounroll
  do {
    poll0(c, lane, set);
    if (++sp > SPIN_CAP) { __hip_atomic_store(c.ct + CT_TMO * 64, 1u, RLX_AGENT); break; }
  } while (!got0<0>(c.tag));
}

struct CIn { u32x2 x0, x1; u32x4 s[4]; };
DEVI void conv_load(const Ctx& c, int lane, CIn& in, int xoff, int stoff, int h) {
  const int sx = xoff + (16 * h + 4 * c.w) * 65536 + c.b * 256;
  const int vx = (lane >> 4) * 65536 + (lane & 15) * 8;
  const int ss = stoff + (1024 * h + 256 * c.w) * 16;
#pragma unroll
  for (int j = 0; j < 4; ++j) in.s[j] = ld128(c.rs, lane * 16 + j * 1024, ss);
  in.x0 = ld64(c.rs, vx, sx);
  in.x1 = ld64(c.rs, vx + 128, sx);
}
DEVI void conv_ln(const Ctx& c, int lane, const CIn& in, int h, f32x4& base0, f32x4& base1) {
  char* scr = c.smem + L_SCR + c.w * 2048;
  constexpr float CH = 4294967296.0f * FX_INV / DM, CL = FX_INV / DM;
  f32x4 m, r;
#pragma unroll
  for (int j = 0; j < 4; ++j) {
    m[j] = fmaf((float)(int)in.s[j][1], CH, (float)in.s[j][0] * CL);
    r[j] = fmaf((float)(int)in.s[j][3], CH, fmaf((float)in.s[j][2], CL, LN_EPS));
  }
#pragma unroll
  for (int j = 0; j < 4; ++j) r[j] = __builtin_amdgcn_rsqf(fmaxf(fmaf(-m[j], m[j], r[j]), LN_EPS));
#pragma unroll
  for (int j = 0; j < 4; ++j) {
    *(float*)(scr + (64 * j + lane) * 4) = r[j];
    *(float*)(scr + 1024 + (64 * j + lane) * 4) = -m[j] * r[j];
  }
  const f32x4 rr = *(const f32x4*)(scr + lane * 16), mr = *(const f32x4*)(scr + 1024 + lane * 16);
  const f32x4 x0 = {lo_bf(in.x0[0]), hi_bf(in.x0[0]), lo_bf(in.x0[1]), hi_bf(in.x0[1])};
  const f32x4 x1 = {lo_bf(in.x1[0]), hi_bf(in.x1[0]), lo_bf(in.x1[1]), hi_bf(in.x1[1])};
  const f32x4 u0 = x0 * rr + mr, u1 = x1 * rr + mr;
  char* up = c.smem + L_U + 512 + (1024 * h + 256 * c.w + 4 * lane) * 2;
  *(u32x2*)up = u32x2{pack_h(u0[0], u0[1]), pack_h(u0[2], u0[3])};
  *(u32x2*)(up + L_UC) = u32x2{pack_h(u1[0], u1[1]), pack_h(u1[2], u1[3])};
  base0 = u0 * c.dsk0 + x0; base1 = u1 * c.dsk1 + x1;
}
struct KB { const char* p[4]; };
template <int MM, int LAM> DEVI u32x4 conv_k1(const KB& kb) {
  const u32* kp = (const u32*)(kb.p[(7 - LAM) >> 1] + 64 * (3 - MM) + 512 * ((7 - LAM) & 1));
  return u32x4{kp[0], kp[1], kp[2], kp[3]};
}
template <int MM, int TB> DEVI u32x4 conv_u1(const char* ub) { return *(const u32x4*)(ub + 64 * (3 - MM) + 512 * TB); }
template <int LO> DEVI void conv_ldk(const KB& kb, u32x4 (&af)[4]) {
  sfor<0, 4>([&](auto L) { af[L.v] = conv_k1<0, LO + L.v>(kb); });
}
constexpr int conv_rank(int DT, int LO, int gr, int lt) {
  int k = 0;
  for (int x = 0; x < lt; ++x) { const int tb = DT + (x & 3) - (LO + 4 * gr + (x >> 2)); k += (tb >= 0 && tb < 4); }
  return k;
}
template <int DT, int TB0, bool ZERO, class F>
DEVI void conv_mma(const char* ub, const KB& kb, f32x4 (&acc)[4], const u32x4 (&af0)[4], F&& hook) {
  constexpr int NG = DT ? 2 : 1, LO = DT ? 1 : 0;
  u32x4 af[2][4], bf[2][4];
  sfor<0, 4>([&](auto L) { af[0][L.v] = af0[L.v]; });
  sfor<0, 4>([&](auto T) { constexpr int tb = DT ? 3 - T.v : T.v; bf[0][tb] = conv_u1<0, TB0 + tb>(ub); });
  SCHED();
  sfor<0, 4 * NG>([&](auto ST) {
    constexpr int st = ST.v, mm = st / NG, gr = st % NG;
    constexpr bool more = st + 1 < 4 * NG;
    constexpr int mn = (st + 1) / NG, gn = (st + 1) % NG;
    constexpr bool nu = more && gr == NG - 1;
    sfor<0, 16>([&](auto LT) {
      constexpr int l = LT.v >> 2, T = LT.v & 3, tb = DT + T - (LO + 4 * gr + l);
      if constexpr (tb >= 0 && tb < 4) {
        constexpr int k = conv_rank(DT, LO, gr, LT.v);
        constexpr bool first = ZERO && st == 0 && (DT ? (l == T) : (l == 0));
        mma_hh<first>(acc[T], af[st & 1][l], bf[mm & 1][tb]);
        sfor<(gr == 1 ? 2 * k : k), (gr == 1 ? 2 * k + 2 : k + 1)>([&](auto J) {
          constexpr int j = J.v;
          if constexpr (nu && j < 8) {
            if constexpr (DT != 0) {
              if constexpr (j & 1) bf[mn & 1][3 - (j >> 1)] = conv_u1<mn, TB0 + 3 - (j >> 1)>(ub);
              else af[(st + 1) & 1][j >> 1] = conv_k1<mn, LO + (j >> 1)>(kb);
            } else {
              if constexpr (j >= 1 && j <= 4) bf[mn & 1][j - 1] = conv_u1<mn, TB0 + j - 1>(ub);
              else af[(st + 1) & 1][j ? j - 4 : 0] = conv_k1<mn, LO + (j ? j - 4 : 0)>(kb);
            }
          } else if constexpr (more && !nu && j < 3) af[(st + 1) & 1][j] = conv_k1<mn, LO + 4 * gn + j>(kb);
        });
        SCHED();
        hook(IC<mm * (DT ? 16 : 10) + gr * 10 + k>{});
      }
    });
  });
}
DEVI void conv_part(const Ctx& c, int lane, const f32x4 (&acc)[4]) {
  char* red = c.smem + L_RED;
#pragma unroll
  for (int T = 0; T < 4; ++T) *(f32x4*)(red + (c.w * 4 + T) * 1024 + (lane & 15) * 64 + (lane >> 4) * 16) = acc[T];
}
DEVI void conv_out(const Ctx& c, int lane, const f32x4& base0, const f32x4& base1, int xoff, int h) {
  const int sx = xoff + (16 * h + 4 * c.w) * 65536 + c.b * 256;
  const int vx = (lane >> 4) * 65536 + (lane & 15) * 8;
  const char* p = c.smem + L_RED + c.w * 1024 + lane * 16;
  const f32x4 a0 = *(const f32x4*)p, b0 = *(const f32x4*)(p + 4096), a1 = *(const f32x4*)(p + 8192), b1 = *(const f32x4*)(p + 12288);
  SCHED();
  const f32x4 y0 = (a0 + b0) * c.ks0 + base0, y1 = (a1 + b1) * c.ks1 + base1;
  st64(c.rs, vx, sx, u32x2{pack_bf(y0[0], y0[1]), pack_bf(y0[2], y0[3])});
  st64(c.rs, vx + 128, sx, u32x2{pack_bf(y1[0], y1[1]), pack_bf(y1[2], y1[3])});
}

template <int PC> DEVI void g1_dma1(const Ctx& c, int ve, int vo, int ldsx, int soff) {
  dma1k<(PC & 3) * 1024>(c.rs, lds_addr(c.smem) + ldsx + (4 * (PC >> 2) + c.w) * 4096, (PC & 1) ? vo : ve, soff + (4 * (PC >> 2) + c.w) * 4096);
}
template <bool ALL> DEVI void sig_bar(const Ctx& c, int lane, int line) {
  const int v = (c.w == 0 && lane < (ALL ? 8 : 1)) ? (ALL ? lane * 2048 : 0) : 0x40000000;
  asm volatile("s_nop 4\n\tbuffer_atomic_add %0, %1, %2, %3 offen" :: "v"(1u), "v"(v), "s"(c.rs),
               "s"(WS_CT + (line + (ALL ? (c.b >> 3) & 7 : 0)) * 256) : "memory");
}
template <class F, class G>
DEVI void g1_mma(const char* const (&xb)[4], const u32x4 (&w1f)[2][16], f32x4 (&acc)[2][4], f32x4& sa, F&& mid, G&& hook) {
  u32x2 xf[2][4][2];
#pragma unroll
  for (int i = 0; i < 4; ++i) { xf[0][i][0] = tr_read(xb[i]); xf[0][i][1] = tr_read(xb[i] + 512); }
  SCHED();
  sfor<0, 16>([&](auto KS) {
    constexpr int ks = KS.v;
    mid(KS);
    sfor<0, 4>([&](auto I) {
      constexpr int i = I.v;
      const u32x4 x = cat2(xf[ks & 1][i][0], xf[ks & 1][i][1]);
      mma_vv<ks == 0>(acc[0][i], w1f[0][ks], x);
      if constexpr (ks < 15) xf[(ks + 1) & 1][i][0] = tr_read(xb[i] + (ks + 1) * 4096);
      SCHED();
      if constexpr (ks < 8) mma_av<ks == 0>(acc[1][i], w1f[1][ks], x); else mma_vv<false>(acc[1][i], w1f[1][ks], x);
      if constexpr (ks < 15) xf[(ks + 1) & 1][i][1] = tr_read(xb[i] + (ks + 1) * 4096 + 512);
      SCHED();
      if constexpr (i == 0) { mma_vv<ks == 0>(sa, x, x); hook(IC<2 * ks>{}); SCHED(); }
      if constexpr (i == 2) { hook(IC<2 * ks + 1>{}); SCHED(); }
    });
  });
  MFMA_DONE();
}
template <class F>
DEVI void g1_epi(const Ctx& c, int lane, f32x4 (&acc)[2][4], const f32x4& sa, int hoff, F&& hook) {
  const int g = lane >> 4, cc = lane & 15, q = cc >> 2, p = lane & 3, w = c.w;
  float* sums = (float*)(c.smem + L_MISC);
  {
    const float d = sel4(p, sa[0], sa[1], sa[2], sa[3]);
    if (q == g) sums[64 + w * 16 + cc] = d;
    const float r = sel4(g, acc[1][1][0], acc[1][2][0], acc[1][3][0], acc[1][0][0]);
    if (w == 3) sums[lane] = r;
  }
  f32x4 s1v[2], b1v[2];
#pragma unroll
  for (int j = 0; j < 2; ++j) {
    s1v[j] = *(const f32x4*)(c.smem + L_CST + w * 256 + j * 128 + g * 16);
    b1v[j] = *(const f32x4*)(c.smem + L_CST + w * 256 + j * 128 + 64 + g * 16);
  }
  __syncthreads();
  f32x2* cs = (f32x2*)(c.smem + L_MISC + 512 + w * 512);
  {
    const float m = sums[lane] * (1.0f / DM);
    const float ve = fmaxf(fmaf(-m, m, fmaf(sums[64 + lane], 1.0f / DM, LN_EPS)), LN_EPS);
    const float r = __builtin_amdgcn_rsqf(fmaf(ve, LN_EPS, ve - LN_EPS));
    f32x2 o = {r, -m * r};
    cs[lane] = o;
  }
  const int v16 = lane * 16 + (w < 3 ? 0 : 0x40000000), v8 = lane * 16 + (w < 3 ? 0x40000000 : c.eo * 8);
  f32x2 k[4];
#pragma unroll
  for (int i = 0; i < 4; ++i) k[i] = cs[((i + w) & 3) * 16 + cc];
  SCHED();
  hook(IC<-1>{});
  SCHED();
  sfor<0, 2>([&](auto IH) {
    constexpr int ih = IH.v;
    f32x4 x[4], e[4];
#define STAGE(n) do { SCHED(); hook(IC<10 * ih + n>{}); SCHED(); } while (0)
#pragma unroll
    for (int n = 0; n < 4; ++n) x[n] = k[2 * ih + (n >> 1)][1] * s1v[n & 1] + b1v[n & 1];
#pragma unroll
    for (int n = 0; n < 4; ++n) x[n] = acc[n & 1][2 * ih + (n >> 1)] * k[2 * ih + (n >> 1)][0] + x[n];
    STAGE(0);
#pragma unroll
    for (int n = 0; n < 4; ++n) e[n] = x[n] * x[n];
#pragma unroll
    for (int n = 0; n < 4; ++n) e[n] = e[n] * (-0.0713548162726f * 1.44269504088896f) + (-1.59576912160573f * 1.44269504088896f);
    STAGE(1);
#pragma unroll
    for (int n = 0; n < 4; ++n) e[n] = e[n] * x[n];
    STAGE(2);
#pragma unroll
    for (int r = 0; r < 4; ++r) { e[0][r] = __builtin_amdgcn_exp2f(e[0][r]); e[1][r] = __builtin_amdgcn_exp2f(e[1][r]); }
    STAGE(3);
#pragma unroll
    for (int r = 0; r < 4; ++r) { e[2][r] = __builtin_amdgcn_exp2f(e[2][r]); e[3][r] = __builtin_amdgcn_exp2f(e[3][r]); }
    STAGE(4);
#pragma unroll
    for (int n = 0; n < 4; ++n) e[n] = e[n] + 1.0f;
    STAGE(5);
#pragma unroll
    for (int r = 0; r < 4; ++r) { e[0][r] = rcp(e[0][r]); e[1][r] = rcp(e[1][r]); }
    STAGE(6);
#pragma unroll
    for (int r = 0; r < 4; ++r) { e[2][r] = rcp(e[2][r]); e[3][r] = rcp(e[3][r]); }
    STAGE(7);
#pragma unroll
    for (int n = 0; n < 4; ++n) x[n] = x[n] * e[n];
    STAGE(8);
    u32x4 hv[2];
#pragma unroll
    for (int n = 0; n < 4; ++n) { hv[n >> 1][2 * (n & 1)] = pack_bf(x[n][0], x[n][1]); hv[n >> 1][2 * (n & 1) + 1] = pack_bf(x[n][2], x[n][3]); }
    STAGE(9);
    const int so0 = hoff + ((2 * ih + w) & 3) * 56 * 1024, so1 = hoff + ((2 * ih + 1 + w) & 3) * 56 * 1024;
    st128(c.rs, v16, so0, hv[0]); st128(c.rs, v16, so1, hv[1]);
    st64(c.rs, v8, so0, u32x2{hv[0][0], hv[0][1]}); st64(c.rs, v8, so1, u32x2{hv[1][0], hv[1][1]});
    SCHED();
    if constexpr (ih == 0) { hook(IC<-2>{}); SCHED(); }
#undef STAGE
  });
}

constexpr int PF = 10;
template <int S0, int S1> DEVI void g2_ldh(const Ctx& c, int lane, u32x4 (&hf)[14][2], int ho) {
#pragma unroll
  for (int s = S0; s < S1; ++s)
#pragma unroll
    for (int i = 0; i < 2; ++i) hf[s][i] = ld128(c.rs, lane * 16 + (s & 3) * 1024, ho + i * 56 * 1024 + (s >> 2) * 4096);
}
template <int N> DEVI void ld_w1(const Ctx& c, int v, u32x4 (&w1f)[2][16]) {
  constexpr int ks = N >> 1, j = N & 1;
  const int so = (j ? c.f1o + (ks >> 2) * c.f1s : WS_W1P + c.ff0 * 16384 + (ks >> 2) * 4096);
  if constexpr (j && ks < 8) w1f[j][ks] = ld128a<(ks & 3) * 1024>(c.rs, v, so);
  else w1f[j][ks] = ld128v<(ks & 3) * 1024>(c.rs, v, so);
}
template <int N> DEVI void ld_w2(const Ctx& c, int v, u32x4 (&w2f)[14][4]) {
  constexpr int s = N >> 2, j = N & 3;
  w2f[s][j] = ld128a<(s & 3) * 1024>(c.rs, v, WS_W2P + ((c.ct_ * 4 + ((c.w + j) & 3)) * 56 + 14 * c.w + (s & ~3)) * 1024);
}
template <int N> DEVI void g2_ldh1(const Ctx& c, int v16, u32x4 (&hf)[14][2], int ho) {
  constexpr int s = N >> 1, i = N & 1;
  hf[s][i] = ld128(c.rs, v16 + (s & 3) * 1024, ho + i * 56 * 1024 + (s >> 2) * 4096);
}
template <int PRE, int RATE, class F>
DEVI void g2_mma(const Ctx& c, int lane, u32x4 (&hf)[14][2], const u32x4 (&w2f)[14][4], f32x4 (&acc)[2][4], int ho, F&& mid) {
  const int v16 = lane * 16;
  sfor<0, 14>([&](auto S) {
    constexpr int s = S.v;
    sfor<0, 8>([&](auto M) {
      constexpr int i = M.v >> 2, j = M.v & 3;
      mma_va<s == 0>(acc[i][j], hf[s][i], w2f[s][j]);
      constexpr int per = 4 / RATE, n = 2 * (PRE + RATE * s) + M.v / per;
      if constexpr (M.v % per == per - 1 && n < 28) { g2_ldh1<n>(c, v16, hf, ho); SCHED(); }
    });
    SCHED();
    mid(S);
  });
  MFMA_DONE();
}
template <bool LAST, class F>
DEVI void g2_epi(const Ctx& c, int lane, const f32x4 (&acc)[2][4], const u32x2 (&xr)[2], int h, int xdst, F&& mid) {
  const int g = lane >> 4, cc = lane & 15, q = cc >> 2, p = lane & 3, w = c.w, t = w * 64 + lane;
  char* red = c.smem + L_XA + h * 32768;
#pragma unroll
  for (int jj = 1; jj < 4; ++jj)
#pragma unroll
    for (int i = 0; i < 2; ++i) *(f32x4*)(red + ((((w + jj) & 3) * 3 + jj - 1) * 2 + i) * 1024 + lane * 16) = acc[i][jj];
  __syncthreads();
  char* xt = red + 24576;
  f32x4 pr[2][3];
#pragma unroll
  for (int i = 0; i < 2; ++i)
#pragma unroll
    for (int rk = 0; rk < 3; ++rk) pr[i][rk] = *(const f32x4*)(red + ((w * 3 + rk) * 2 + i) * 1024 + lane * 16);
  mid(0);
#pragma unroll
  for (int i = 0; i < 2; ++i) {
    const f32x4 xv = {lo_bf(xr[i][0]), hi_bf(xr[i][0]), lo_bf(xr[i][1]), hi_bf(xr[i][1])};
    const f32x4 o = ((acc[i][0] + (xv + c.bb2)) + pr[i][0]) + (pr[i][1] + pr[i][2]);
    const u32x2 r = {pack_bf(o[0], o[1]), pack_bf(o[2], o[3])};
    *(u32x2*)(xt + (w * 16 + cc) * 64 + ((i ^ (cc >> 3)) * 32) + g * 8) = r;
  }
  __syncthreads();
  const char* xq = xt + ((w >> 1) * 32 + 8 * g + q) * 64 + (((w & 1) ^ (g & 1)) * 32) + 8 * p;
  const u32x4 f0 = cat2(tr_read(xq), tr_read(xq + 256));
  const int tok = (16 * h + c.P) * 64 + c.sub * 32 + (w & 1) * 16 + cc;
  if (LAST) {
    *(u32x4*)(c.out + (size_t)tok * DM + c.ct_ * 64 + (w >> 1) * 32 + 8 * g) = f0;
  } else {
    const int row = t >> 2;
    const u32x4 v = *(const u32x4*)(xt + row * 64 + (((t & 3) ^ ((row >> 2) & 2)) * 16));
    st128(c.rs, (c.ct_ * 64 + row) * 128 + (t & 3) * 16, xdst, v);
    float s = 0.f, ss = 0.f;
#pragma unroll
    for (int j = 0; j < 4; ++j) { s = dot2bf(f0[j], 0x3f803f80u, s); ss = dot2bf(f0[j], f0[j], ss); }
    s = sum_x16(s); ss = sum_x16(ss);
    (void)__hip_atomic_fetch_add((unsigned long long*)(c.ws + WS_ST + 65536) + 2 * tok + (g & 1),
                                 (unsigned long long)f2fx((g & 1) ? ss : s), RLX_AGENT);
  }
}

template <int LAYER>
DEVI void layer_body(const Ctx& c, u32x4 (&w1f)[2][16], u32x4 (&w2f)[14][4], CIn& inA, CIn& inB) {
  constexpr int xin = WS_X + LAYER * 4 * MB, xmid = xin + 2 * MB, xnext = xin + 4 * MB;
  constexpr int hbuf = WS_H + LAYER * 8 * MB, stin = WS_ST + LAYER * 65536;
  constexpr u32 ep = LAYER + 1;
  constexpr int K1 = 1 + 3 * LAYER, K2 = K1 + 1, K3 = K1 + 2;
  const int w = c.w;
  char* smem = c.smem;
  auto nop = [](auto) {};
  {
    const int lane = fresh_lane(c);
    const int g = lane >> 4, cc = lane & 15, mu0 = (w & 1) * 4, v16 = lane * 16;
    const char* ub = smem + L_U + (w >> 1) * L_UC + 512 + 2 * (16 * cc - 16 + 8 * g) - 64 * (mu0 + 3);
    KB kb;
#pragma unroll
    for (int j = 0; j < 4; ++j) {
      u32 a = lds_addr(smem) + (w >> 1) * 2 * L_KC + ((cc & 1) ? 0 : L_KC) + 2 * (2031 - cc + 8 * g - ((cc & 1) ? 0 : 1)) - 64 * (mu0 + 3 + 56) + 1024 * j;
      asm volatile("" : "+v"(a));
      kb.p[j] = (const char*)(const LAS char*)(size_t)a;
    }
    STAMP(K1, 1);
    f32x4 base0, base1;
    u32x4 a4[4];
    conv_ldk<0>(kb, a4);
    conv_ln(c, lane, inA, 0, base0, base1);
    if (!LAYER) poll0(c, lane, WS_F1);
    __syncthreads();
    STAMP(K1, 2);
    f32x4 acc[4];
    u32 o = 0;
    conv_mma<0, 0, true>(ub, kb, acc, a4, [&](auto N) {
      constexpr int n = N.v;
      if constexpr (LAYER && n == 19) { WAIT_VM(0); o = sig_begin(c, lane); SCHED(); }
      if constexpr (LAYER && n == 29) { sig_end<true>(c, lane, o, CT_X + 64); SCHED(); }
    });
    MFMA_DONE();
    conv_part(c, lane, acc);
    conv_ldk<1>(kb, a4);
    __syncthreads();
    f32x4 s1p, s1q;
    if (!LAYER) {
      if (!got0<0>(c.tag)) wait0(c, lane, WS_F1);
      asm volatile("" :: "v"(inB.x1));
      asm volatile("s_nop 4\n\tbuffer_load_dwordx4 %0, %2, %3, %4 offen sc1\n\tbuffer_load_dwordx4 %1, %2, %3, %4 offen offset:16 sc1"
                   : "=&v"(s1p), "=&v"(s1q)
                   : "v"(min(c.ff0 * 16 + (lane & 15) + (lane >> 5) * 16, FF - 1) * 32), "s"(c.rs), "s"(WS_S1) : "memory");
    }
    conv_out(c, lane, base0, base1, xmid, 0);
    STAMP(K1, 3);
    f32x4 acb[4];
    conv_mma<4, 0, true>(ub, kb, acb, a4, [&](auto N) {
      constexpr int n = N.v;
      if constexpr (!LAYER && (n & 3) == 1) { ld_w1<(n >> 2)>(c, v16, w1f); SCHED(); }
      if constexpr (n == 47) {
        if (LAYER) WAIT_VM(0); else WAIT_VM(12);
        o = sig_begin(c, lane);
        if (LAYER) poll<true>(c, lane, CT_X + 64);
        SCHED();
      }
      if constexpr (n == 57) { sig_end<true>(c, lane, o, CT_C); SCHED(); }
    });
    STAMP(K1, 4);
    if (!LAYER) {
      asm volatile("" : "+v"(s1p), "+v"(s1q));
      s1p += s1q;
      ((float*)(smem + L_CST))[w * 64 + lane] = (lane & 16) ? c.cs1 : (s1p[0] + s1p[1]) + (s1p[2] + s1p[3]);
    }
    if (LAYER) {
      if (!got<1>(32u)) wait_ge<true>(c, lane, CT_X + 64, 32u);
      conv_load(c, lane, inB, xin, stin, 1);
    }
    conv_ldk<0>(kb, a4);
    STAMP(K1, 5);
    conv_ln(c, lane, inB, 1, base0, base1);
    __syncthreads();
    conv_mma<0, 4, false>(ub, kb, acb, a4, [&](auto N) {
      constexpr int n = N.v;
      if constexpr (!LAYER && (n & 3) == 1) { ld_w1<16 + (n >> 2)>(c, v16, w1f); SCHED(); }
      if constexpr (n == 30) { poll<true>(c, lane, CT_C); SCHED(); }
    });
    MFMA_DONE();
    conv_part(c, lane, acb);
    __syncthreads();
    conv_out(c, lane, base0, base1, xmid, 1);
    STAMP(K1, 6);
    if (!got<(LAYER ? 2 : 4)>(32u * ep)) wait_ge<true>(c, lane, CT_C, 32u * ep);
    const int row = lane >> 3, sw = ((row >> 1) & 1) << 1;
    const int ve = row * 128 + ((lane & 7) ^ sw) * 16, vo = row * 128 + ((lane & 7) ^ (sw | 4)) * 16;
    if (!LAYER && c.b == 0 && w == 0) __hip_atomic_store((gu32*)(c.ws + WS_GEN), c.tag, RLX_AGENT);
    sfor<0, 8>([&](auto N) { g1_dma1<N.v>(c, ve, vo, L_XA, xmid + c.P * 65536); });
    WAIT_VM(8);
    o = sig_begin(c, lane);
    sig_end<true>(c, lane, o, CT_C + 64);
    STAMP(K1, 7);
  }
  u32x4 hf[14][2];
  u32x2 xr[2];
  {
    const int lane = fresh_lane(c);
    const int g = lane >> 4, cc = lane & 15, q = cc >> 2, p = lane & 3, v16 = lane * 16;
    const int sx = ((q >> 1) & 1) | ((g & 1) << 1);
    const char* xb[4];
#pragma unroll
    for (int i = 0; i < 4; ++i) xb[i] = smem + L_XA + (8 * g + q) * 128 + ((((i + w) & 3) ^ sx) * 32) + 8 * p;
    const int row = lane >> 3, sw = ((row >> 1) & 1) << 1;
    const int ve = row * 128 + ((lane & 7) ^ sw) * 16, vo = row * 128 + ((lane & 7) ^ (sw | 4)) * 16;
    sfor<8, 12>([&](auto N) { g1_dma1<N.v>(c, ve, vo, L_XA, xmid + c.P * 65536); });
    WAIT_VM_BAR(9);
    STAMP(K2, 0);
    f32x4 acc[2][4], sa;
    g1_mma(xb, w1f, acc, sa, [&](auto KS) {
      constexpr int ks = KS.v;
      if constexpr (ks == 3) { if (LAYER) WAIT_VM_BAR(8); else WAIT_VM_BAR(10); SCHED(); }
      if constexpr (ks == 7) { if (LAYER) WAIT_VM_BAR(0); else WAIT_VM_BAR(10); SCHED(); }
      if constexpr (ks == 13) { if (!LAYER) WAIT_VM(16); poll<true>(c, lane, CT_C + 64); SCHED(); }
    }, [&](auto N) {
      constexpr int n = N.v;
      if constexpr (n < 4) g1_dma1<12 + n>(c, ve, vo, L_XA, xmid + c.P * 65536);
      if constexpr (!LAYER && n >= 4 && n < 10) ld_w1<22 + n>(c, v16, w1f);
      if constexpr (!LAYER && n >= 10) ld_w2<n - 10>(c, v16, w2f);
    });
    STAMP(K2, 1);
    const int xo = xmid + (16 + c.P) * 65536;
    g1_epi(c, lane, acc, sa, hbuf + (c.P * 4 * 56 + c.hks) * 1024, [&](auto N) {
      constexpr int n = N.v;
      if constexpr (n == -1) { if (!got<(LAYER ? 0 : 6)>(32u * ep)) wait_ge<true>(c, lane, CT_C + 64, 32u * ep); }
      if constexpr (n >= 0 && n < 8) g1_dma1<n>(c, ve, vo, L_XB, xo);
      if constexpr (n >= 10 && n < 18) g1_dma1<n - 2>(c, ve, vo, L_XB, xo);
    });
    STAMP(K2, 2);
  }
  {
    const int lane = fresh_lane(c);
    const int g = lane >> 4, cc = lane & 15, q = cc >> 2, p = lane & 3, v16 = lane * 16;
    const int sx = ((q >> 1) & 1) | ((g & 1) << 1);
    const char* xb[4];
#pragma unroll
    for (int i = 0; i < 4; ++i) xb[i] = smem + L_XB + (8 * g + q) * 128 + ((((i + w) & 3) ^ sx) * 32) + 8 * p;
    WAIT_VM_BAR(16);
    STAMP(K2, 3);
    f32x4 acc[2][4], sa;
    g1_mma(xb, w1f, acc, sa, [&](auto KS) {
      constexpr int ks = KS.v;
      if constexpr (ks == 6) {
        if (LAYER) WAIT_VM_BAR(0); else WAIT_VM_BAR(12);
        sig_bar<false>(c, lane, CT_H + c.P);
        SCHED();
      }
      if constexpr (ks == 15) {
#pragma unroll
        for (int i = 0; i < 2; ++i) xr[i] = ld64(c.rs, (c.ct_ * 64 + w * 16 + cc) * 128 + i * 32 + g * 8, xmid + c.P * 65536 + c.sub * 64);
        SCHED();
      }
    }, [&](auto N) {
      constexpr int n = N.v;
      if constexpr (!LAYER) ld_w2<22 + n>(c, v16, w2f);
      if constexpr (!LAYER && n >= 30) ld_w2<24 + n>(c, v16, w2f);
    });
    STAMP(K2, 4);
    poll<false>(c, lane, CT_H + c.P);
    const int ho = hbuf + ((c.P * 4 + c.sub * 2) * 56 + 14 * w) * 1024;
    g1_epi(c, lane, acc, sa, hbuf + ((16 + c.P) * 4 * 56 + c.hks) * 1024, [&](auto N) {
      constexpr int n = N.v;
      if constexpr (n == -1) { if (!got<0>(16u * ep)) wait_ge<false>(c, lane, CT_H + c.P, 16u * ep); }
      if constexpr (n >= 0) g2_ldh1<n>(c, v16, hf, ho);
    });
    STAMP(K2, 5);
  }
  {
    const int lane = fresh_lane(c);
    const int g = lane >> 4, cc = lane & 15;
    const int ho = hbuf + ((c.P * 4 + c.sub * 2) * 56 + 14 * w) * 1024, hoB = ho + 64 * 56 * 1024;
    STAMP(K3, 0);
    f32x4 acc[2][4];
    u32 o = 0;
    g2_mma<10, 1>(c, lane, hf, w2f, acc, ho, [&](auto S) {
      constexpr int s = S.v;
      if constexpr (s == 6) { WAIT_VM(8); o = sig_begin(c, lane); SCHED(); }
      if constexpr (s == 7) { sig_end<false>(c, lane, o, CT_H + 16 + c.P); SCHED(); }
    });
    STAMP(K3, 1);
    poll<false>(c, lane, CT_H + 16 + c.P);
    g2_epi<LAYER == 1>(c, lane, acc, xr, 0, xnext + c.P * 65536 + c.sub * 64, nop);
    STAMP(K3, 2);
    if (!got<(LAYER ? 1 : 2)>(16u * ep)) wait_ge<false>(c, lane, CT_H + 16 + c.P, 16u * ep);
    g2_ldh<0, 4>(c, lane, hf, hoB);
#pragma unroll
    for (int i = 0; i < 2; ++i) xr[i] = ld64(c.rs, (c.ct_ * 64 + w * 16 + cc) * 128 + i * 32 + g * 8, xmid + (16 + c.P) * 65536 + c.sub * 64);
    if (LAYER == 0) {
      WAIT_VM(10);
      o = sig_begin(c, lane);
      sig_end<true>(c, lane, o, CT_X);
    }
    SCHED();
    STAMP(K3, 3);
  }
  {
    const int lane = fresh_lane(c);
    const int hoB = hbuf + (((16 + c.P) * 4 + c.sub * 2) * 56 + 14 * w) * 1024;
    f32x4 acc[2][4];
    g2_mma<4, 2>(c, lane, hf, w2f, acc, hoB, [&](auto S) {
      constexpr int s = S.v;
      if constexpr (LAYER == 0 && s == 13) { poll<true>(c, lane, CT_X); SCHED(); }
    });
    STAMP(K3, 4);
    g2_epi<LAYER == 1>(c, lane, acc, xr, 1, xnext + (16 + c.P) * 65536 + c.sub * 64, [&](int) {
      if (LAYER == 0) {
        if (!got<0>(32u)) wait_ge<true>(c, lane, CT_X, 32u);
        conv_load(c, lane, inA, xnext, WS_ST + 65536, 0);
        SCHED();
      }
    });
    STAMP(K3, 5);
  }
}

constexpr int KG_RS = 528, KG_X = 16 * KG_RS, KG_CH = 64 * KG_RS + 1296;
__global__ void __launch_bounds__(256, 1) main_kernel(const u16* x, const u16* lar, const u16* aim, const u16* cre,
                                                      const u16* cim, const u16* ldt, const u16* dsk, const u16* w1,
                                                      const u16* b1, const u16* w2, const u16* b2, char* ws, u16* out) {
  extern __shared__ __attribute__((aligned(16))) char smem[];
  const int t = threadIdx.x, lane = t & 63, b = blockIdx.x;
  const int w = __builtin_amdgcn_readfirstlane(t >> 6);
  const u32 junk = warm_code((b >> 3) * 4 + w, lane);
  STAMP(0, 0);
  Ctx c;
  c.rs = __builtin_amdgcn_make_buffer_rsrc(ws, 0, WS_END, 0x00020000);
  c.ct = (gu32*)(ws + WS_CT);
  c.smem = smem; c.ws = ws; c.out = out;
  c.lane0 = lane; c.w = w; c.b = b;
  c.P = (b & 7) * 2 + ((b >> 3) & 1);
  const int idx = b >> 4;
  const int sg = idx >> 1;
  c.eo = idx & 1; c.sub = idx >> 3; c.ct_ = idx & 7;
  c.ff0 = w < 3 ? 14 * sg + 6 * c.eo + 2 * w : 14 * sg + 12 + c.eo;
  c.hks = w < 3 ? 7 * sg + 3 * c.eo + w : 7 * sg + 6;
  c.f1o = w < 3 ? WS_W1P + c.ff0 * 16384 + 16384 : WS_ONE; c.f1s = w < 3 ? 4096 : 0;
  CIn inA, inB;
  {
    const int ch = w >> 1, d = 2 * b + ch, n = t & 127;
    const u16 r0 = ldt[d], r1 = lar[d * NST + n], r2 = aim[d * NST + n], r3 = cre[d * NST + n], r4 = cim[d * NST + n];
    const u16 r5 = cre[d * NST + (n ^ 64)], r6 = cim[d * NST + (n ^ 64)];
    const u32 gen = __hip_atomic_load((gu32*)(ws + WS_GEN), RLX_AGENT);
    u32x4 rv[8], rw[2];
    const bool early = !(b & 64);
    if (early) xpose_load(b & 63, x, rv);
    else if ((b & 63) < 56) pack_w1_load(b & 63, b >> 7, w1, rv);
    pack_w2_load(min(b, 223), w2, rw);
    const u32 r7 = *(const u32*)(dsk + 2 * b);
    u32 r8 = b1[min(c.ff0 * 16 + (lane & 15) + (lane >> 5) * 16, FF - 1)], r9 = b2[c.ct_ * 64 + w * 16 + (lane & 15)];
    SCHED();
    char* kg = smem + L_XA + ch * KG_CH;
    char* stg = smem + L_XA + 2 * KG_CH;
    const int i = lane & 15, g = lane >> 4;
    int ix[9];
#pragma unroll
    for (int e2 = 0; e2 < 9; ++e2) { ix[e2] = 2 * cidx(max(2047 - 8 * t - e2, 0)); asm volatile("" : "+v"(ix[e2])); }
    int wi[2][4];
    {
      const int m8 = -(i >> 3), s1 = -(w & 1);
      const int h1 = ((256 + 128 * (i >> 2) + 2 * (i & 3)) & ~m8) | ((128 * (i >> 1) + 4 * (i & 1)) & m8);
      const int hp = (((i + (i & 8) * 15) & ~s1) | (h1 & s1)) + 32 * g, hq = 1024 + 128 * i + 32 * g;
#pragma unroll
      for (int r = 0; r < 4; ++r) {
        wi[0][r] = 2 * cidx(hp + 8 * r); wi[1][r] = 2 * cidx(hq + 8 * r > 2048 ? 2064 : hq + 8 * r);
        asm volatile("" : "+v"(wi[0][r]), "+v"(wi[1][r]));
      }
    }
    if (t < 16) *(u32x4*)(smem + (t >> 2) * L_KC + 4096 + (t & 3) * 16) = u32x4{0, 0, 0, 0};
    if (t >= 64 && t < 128) *(u32x4*)(smem + L_U + ((t - 64) >> 5) * L_UC + (t & 31) * 16) = u32x4{0, 0, 0, 0};
    if (t == 128) *(u32*)(smem + L_SIG) = 0u;
    SCHED();
    STAMP(0, 1);
    const float dt = rbf(expf(bf2f(r0)));
    const float cr = bf2f(r3), ci = bf2f(r4), cr2 = bf2f(r5), ci2 = bf2f(r6);
    const float S = wave_sum(sqrt_(cr * cr + ci * ci) + sqrt_(cr2 * cr2 + ci2 * ci2)) * (2.002f * dt);
    int e = __builtin_amdgcn_frexp_expf(S);
    e = (S > 0.0f && S < 3.0e38f) ? max(-100, min(100, e)) : 14;
    const float scale = __builtin_amdgcn_ldexpf(2.0f, 14 - e);
    if (n == 0) *(float*)(smem + L_MISC + ch * 4) = __builtin_amdgcn_ldexpf(1.0f, e - 14);
    const float a2 = (-rbf(expf(bf2f(r1))) * dt) * 1.44269504088896f;
    const float br = (bf2f(r2) * dt) * 0.159154943091895f;
    {
      char* yp = kg + n * 4;
      char* xp = kg + KG_X + n * 4;
      const CM e128 = cexp_(a2, -br, 128.f), e8 = cexp_(a2, br, 8.f), e1 = cexp_(a2, -br, 1.f), e2 = cexp_(a2, -br, 2.f);
      const CM e4 = cexp_(a2, -br, 4.f);
      f32x2 f0 = {(cr * dt) * scale, -(ci * dt) * scale};
      f32x2 f = cmul(f0, cexp_(a2, -br, 512.f)), y = {1.f, 0.f};
      f32x2 g1 = cmul(f0, e128), g2 = cmul(g1, e128), g3 = cmul(g2, e128);
#define PUT(base, row, v) *(u32*)(base + (row) * KG_RS) = pack_h(v[0], v[1])
      auto chain = [&](auto K) {
        constexpr int k = K.v;
        PUT(yp, k, y);
        if (k < 15) y = cmul(y, e8);
        if (k < 4) { const f32x2 q4 = cmul(f, e4); PUT(xp, 24 + 2 * k, f); PUT(xp, 25 + 2 * k, q4); }
        else if (k < 13) PUT(xp, 28 + k, f);
        if (k < 12) f = cmul(f, e128);
        if (k < 8) { PUT(xp, k, f0); PUT(xp, 8 + k, g1); }
        if (k < 7) { f0 = cmul(f0, e1); g1 = cmul(g1, e1); }
        if (k >= 8 && k < 12) { PUT(xp, 8 + k, g2); PUT(xp, 12 + k, g3); }
        if (k >= 8 && k < 11) { g2 = cmul(g2, e2); g3 = cmul(g3, e2); }
      };
      c.tag = __builtin_amdgcn_readfirstlane(gen) * 0x9E3779B1u + 0x7F4A7C15u;
      sfor<0, 2>(chain);
      asm volatile("" : "+v"(y), "+v"(f), "+v"(f0), "+v"(g1));
      if (b < 64) xpose(b, rv, c.rs, stg);
      else if (early) row_stats(b & 63, rv, c.rs);
      asm volatile("" : "+v"(y), "+v"(f), "+v"(f0), "+v"(g1));
      sfor<2, 12>(chain);
      asm volatile("" : "+v"(y), "+v"(f));
      if (early) {
        WAIT_VM(0);
        __syncthreads();
        if (w == 0 && lane < 8)
          __hip_atomic_store((gu32*)(ws + WS_F0 + lane * 2048 + (b & 7) * 256 + ((b >> 3) & 7) * 8 + (b >> 7) * 4), c.tag, RLX_AGENT);
      }
      asm volatile("" : "+v"(y), "+v"(f));
      STAMP(0, 2);
      sfor<12, 16>(chain);
      STAMP(0, 3);
      if (!early && (b & 63) < 56) pack_w1(b & 63, b >> 7, rv, c.rs, stg);
#undef PUT
    }
    pack_w2_put(rw, smem + L_XB + 55552);
    __syncthreads();
    STAMP(0, 4);
    {
      const char* ya = kg + i * KG_RS + g * 16;
      const char* xa = kg + KG_X + (16 * (w & 1) + i) * KG_RS + g * 16;
      const char* xc = kg + KG_X + (32 + i) * KG_RS + g * 16;
      u32x4 fa[8], fb[8], fc[8];
#pragma unroll
      for (int ks = 0; ks < 8; ++ks) { fa[ks] = *(const u32x4*)(ya + ks * 64); fb[ks] = *(const u32x4*)(xa + ks * 64); }
#pragma unroll
      for (int ks = 0; ks < 8; ++ks) fc[ks] = *(const u32x4*)(xc + ks * 64);
      f32x4 ac0, ac1;
      SCHED();
      sfor<0, 8>([&](auto KS) { mma_hh<KS.v == 0>(ac0, fa[KS.v], fb[KS.v]); });
      sfor<0, 8>([&](auto KS) { mma_hh<KS.v == 0>(ac1, fa[KS.v], fc[KS.v]); });
      MFMA_DONE();
#pragma unroll
      for (int r = 0; r < 4; ++r) {
        *(u16*)(kg + 64 * KG_RS + wi[0][r]) = __builtin_bit_cast(u16, (_Float16)ac0[r]);
        *(u16*)(kg + 64 * KG_RS + wi[1][r]) = __builtin_bit_cast(u16, (_Float16)ac1[r]);
      }
    }
    if (!early) WAIT_VM(0);
    __syncthreads();
    if (w == 1 && !early && lane < 8)
      __hip_atomic_store((gu32*)(ws + WS_F1 + lane * 2048 + (b & 7) * 256 + ((b >> 3) & 7) * 8 + (b >> 7) * 4), c.tag, RLX_AGENT);
    if (w == 0) poll0(c, lane, WS_F0);
#pragma unroll
    for (int c2 = 0; c2 < 2; ++c2) {
      u32 v[9];
#pragma unroll
      for (int e2 = 0; e2 < 9; ++e2) v[e2] = *(const u16*)(smem + L_XA + c2 * KG_CH + 64 * KG_RS + ix[e2]);
      if (t == 255) v[8] = 0u;
      u32x4 oe, oo;
#pragma unroll
      for (int j = 0; j < 4; ++j) { oe[j] = v[2 * j] | (v[2 * j + 1] << 16); oo[j] = v[2 * j + 1] | (v[2 * j + 2] << 16); }
      *(u32x4*)(smem + c2 * 2 * L_KC + t * 16) = oe;
      *(u32x4*)(smem + c2 * 2 * L_KC + L_KC + t * 16) = oo;
    }
    const f32x2 ksc = *(const f32x2*)(smem + L_MISC);
    c.ks0 = __uint_as_float(__builtin_amdgcn_readfirstlane(__float_as_uint(ksc[0])));
    c.ks1 = __uint_as_float(__builtin_amdgcn_readfirstlane(__float_as_uint(ksc[1])));
    const u32 ds = __builtin_amdgcn_readfirstlane(r7);
    c.dsk0 = lo_bf(ds); c.dsk1 = hi_bf(ds);
    c.cs1 = lo_bf(r8); c.bb2 = lo_bf(r9);
    asm volatile("" :: "v"(junk));
    STAMP(0, 5);
    if (w == 0 && !got0<0>(c.tag)) wait0(c, lane, WS_F0);
    __syncthreads();
    conv_load(c, lane, inA, WS_X, WS_ST, 0);
    conv_load(c, lane, inB, WS_X, WS_ST, 1);
    pack_w2_out(b, c.rs, smem + L_XB + 55552);
    STAMP(0, 6);
  }
  u32x4 w1f[2][16], w2f[14][4];
  layer_body<0>(c, w1f, w2f, inA, inB);
  layer_body<1>(c, w1f, w2f, inA, inB);
}

extern "C" __global__ void code_end_marker() {}

extern "C" void kernel_launch(void* const* d_in, const int* in_sizes, int n_in, void* d_out, int out_size,
                              void* d_ws, size_t ws_size, hipStream_t stream) {
  (void)in_sizes; (void)n_in; (void)out_size; (void)ws_size;
  char* ws = (char*)d_ws + ((ws_size - (size_t)WS_END) & ~(size_t)(2 * MB - 1));
  (void)hipFuncSetAttribute((const void*)main_kernel, hipFuncAttributeMaxDynamicSharedMemorySize, MAIN_LDS);
  main_kernel<<<256, 256, MAIN_LDS, stream>>>((const u16*)d_in[0], (const u16*)d_in[1], (const u16*)d_in[2],
                                              (const u16*)d_in[3], (const u16*)d_in[4], (const u16*)d_in[5],
                                              (const u16*)d_in[6], (const u16*)d_in[8], (const u16*)d_in[9],
                                              (const u16*)d_in[10], (const u16*)d_in[11], ws, (u16*)d_out);
}
```

```cpp
#include <hip/hip_runtime.h>
#include <hip/hip_bf16.h>

typedef unsigned short u16;
typedef unsigned int u32;
typedef long long i64;
typedef __attribute__((ext_vector_type(4))) short s16x4;
typedef __attribute__((ext_vector_type(8))) __bf16 bf16x8;
typedef __attribute__((ext_vector_type(2))) __bf16 bf16x2;
typedef __attribute__((ext_vector_type(8))) _Float16 f16x8;
typedef __attribute__((ext_vector_type(2))) _Float16 f16x2;
typedef __attribute__((ext_vector_type(4))) float f32x4;
typedef __attribute__((ext_vector_type(2))) float f32x2;
typedef __attribute__((ext_vector_type(4))) unsigned u32x4;
typedef __attribute__((ext_vector_type(2))) unsigned u32x2;
typedef __attribute__((ext_vector_type(2))) long long i64x2;
typedef __attribute__((address_space(1))) unsigned gu32;
typedef __amdgpu_buffer_rsrc_t rsrc_t;
#define LAS __attribute__((address_space(3)))
#define DEVI __device__ __forceinline__
template <int V> struct IC { static constexpr int v = V; };
template <int I, int N, class F> __device__ __forceinline__ void sfor(F&& f) {
  if constexpr (I < N) { f(IC<I>{}); sfor<I + 1, N>(f); }
}
#define RLX_AGENT __ATOMIC_RELAXED, __HIP_MEMORY_SCOPE_AGENT

constexpr int SEQ = 2048, DM = 512, NST = 128, FF = 1792;
constexpr float LN_EPS = 1e-5f;
constexpr float FX_SCALE = 16777216.0f;
constexpr float FX_INV = 1.0f / 16777216.0f;
constexpr int SC1 = 16;

constexpr int MB = 1024 * 1024;
constexpr int WS_X = 0;
constexpr int WS_W1P = 12 * MB;
constexpr int WS_W2P = 14 * MB;
constexpr int WS_H = 16 * MB;
constexpr int WS_ST = 32 * MB;
constexpr int WS_S1 = 32 * MB + 131072;
constexpr int WS_GEN = 32 * MB + 196608;
constexpr int WS_F0 = WS_GEN + 256, WS_F1 = WS_F0 + 16384;
constexpr int WS_CT = 32 * MB + 262144;
constexpr int WS_ONE = 32 * MB + 393216;
constexpr int WS_END = 33 * MB;
constexpr int CT_C = 0, CT_X = 128, CT_H = 256, CT_TMO = 288;
constexpr int SPIN_CAP = 1 << 16;

#define STAMP(k, i) do {} while (0)

DEVI float bf2f(u16 v) { return __uint_as_float((u32)v << 16); }
DEVI float lo_bf(u32 w) { return __uint_as_float(w << 16); }
DEVI float hi_bf(u32 w) { return __uint_as_float(w & 0xffff0000u); }
DEVI float rbf(float x) { return (float)(__bf16)x; }
DEVI u32 pack_bf(float a, float b) { f32x2 q = {a, b}; return __builtin_bit_cast(u32, __builtin_convertvector(q, bf16x2)); }
DEVI u32 pack_h(float a, float b) { f32x2 q = {a, b}; return __builtin_bit_cast(u32, __builtin_convertvector(q, f16x2)); }
DEVI float dot2bf(u32 a, u32 b, float c) {
  return __builtin_amdgcn_fdot2_f32_bf16(__builtin_bit_cast(bf16x2, a), __builtin_bit_cast(bf16x2, b), c, false);
}
DEVI f32x4 mfma_bf(u32x4 a, u32x4 b, f32x4 c) {
  return __builtin_amdgcn_mfma_f32_16x16x32_bf16(__builtin_bit_cast(bf16x8, a), __builtin_bit_cast(bf16x8, b), c, 0, 0, 0);
}
DEVI f32x4 mfma_h(u32x4 a, u32x4 b, f32x4 c) {
  return __builtin_amdgcn_mfma_f32_16x16x32_f16(__builtin_bit_cast(f16x8, a), __builtin_bit_cast(f16x8, b), c, 0, 0, 0);
}
DEVI u32x2 tr_read(const char* p) {
  return __builtin_bit_cast(u32x2, __builtin_amdgcn_ds_read_tr16_b64_v4i16((LAS s16x4*)p));
}
DEVI u32x4 cat2(u32x2 a, u32x2 b) { return __builtin_shufflevector(a, b, 0, 1, 2, 3); }
DEVI u32 lds_addr(const void* p) { return (u32)(size_t)(const LAS char*)p; }
#define WAIT_VM(n) asm volatile("s_waitcnt vmcnt(" #n ")" ::: "memory")
#define WAIT_VM_BAR(n) asm volatile("s_waitcnt vmcnt(" #n ")\n\ts_barrier" ::: "memory")
#define SCHED() __builtin_amdgcn_sched_barrier(0)
template <int CTRL> DEVI float dpp_add(float v) {
  return v + __uint_as_float(__builtin_amdgcn_update_dpp(0u, __float_as_uint(v), CTRL, 0xf, 0xf, true));
}
DEVI float sum_x16(float v) {
  auto r = __builtin_amdgcn_permlane16_swap(__float_as_uint(v), __float_as_uint(v), false, false);
  return __uint_as_float(r[0]) + __uint_as_float(r[1]);
}
DEVI float sum_x32(float v) {
  auto r = __builtin_amdgcn_permlane32_swap(__float_as_uint(v), __float_as_uint(v), false, false);
  return __uint_as_float(r[0]) + __uint_as_float(r[1]);
}
DEVI float wave_sum(float v) {
  v = dpp_add<0xB1>(v); v = dpp_add<0x4E>(v); v = dpp_add<0x141>(v); v = dpp_add<0x140>(v);
  return sum_x32(sum_x16(v));
}
DEVI float fx2f(u32 lo, u32 hi) { return fmaf((float)(int)hi, 4294967296.0f * FX_INV, (float)lo * FX_INV); }
DEVI i64 f2fx(float v) { return (i64)(v * FX_SCALE); }
DEVI float sel4(int i, float a, float b, float c, float d) {
  asm volatile("" : "+v"(a), "+v"(b), "+v"(c), "+v"(d));
  return (i & 2) ? ((i & 1) ? d : c) : ((i & 1) ? b : a);
}
DEVI float rcp(float x) { return __builtin_amdgcn_rcpf(x); }
DEVI float sqrt_(float x) { return __builtin_amdgcn_sqrtf(x); }

DEVI u32 warm_code(int slice, int lane) {
  unsigned long long pc, pe;
  asm volatile("s_getpc_b64 %0\n\ts_getpc_b64 s[90:91]\n\ts_add_u32 s90, s90, code_end_marker@rel32@lo+4\n\t"
               "s_addc_u32 s91, s91, code_end_marker@rel32@hi+12\n\ts_mov_b64 %1, s[90:91]"
               : "=&s"(pc), "=s"(pe) :: "scc", "s90", "s91");
  const unsigned long long a = (pc + (unsigned)(slice * 512 + (lane & 7) * 64)) & ~3ull;
  u32 junk = 0;
  if (pc + (unsigned)(slice * 512 + 512) <= pe) asm volatile("global_load_dword %0, %1, off" : "=v"(junk) : "v"(a) : "memory");
  return junk;
}

DEVI int cidx(int l) {
  const int hb = (int)(pack_bf((float)l, 0.f) & 0xffffu) - (0x4300 - 128);
  return l < 128 ? l : hb;
}
struct CM { f32x2 e, f; };
DEVI f32x2 cmul(f32x2 a, const CM& m) { return a[1] * m.f + a[0] * m.e; }
DEVI CM cexp_(float a2, float br, float p) {
  const float e = __builtin_amdgcn_exp2f(a2 * p), f = __builtin_amdgcn_fractf(br * p);
  const float re = e * __builtin_amdgcn_cosf(f), im = e * __builtin_amdgcn_sinf(f);
  CM r; r.e = f32x2{re, im}; r.f = f32x2{-im, re};
  return r;
}
DEVI void st16(rsrc_t rs, int off, u32x4 v) { __builtin_amdgcn_raw_buffer_store_b128(v, rs, off, 0, SC1); }

DEVI void xpose_load(int b, const u16* x, u32x4 (&v)[8]) {
  const int lane = threadIdx.x & 63, w = threadIdx.x >> 6;
#pragma unroll
  for (int r = 0; r < 8; ++r) v[r] = *(const u32x4*)(x + (size_t)(b * 32 + w * 8 + r) * DM + lane * 8);
}
DEVI void xpose(int b, const u32x4 (&v)[8], rsrc_t rs, char* smem) {
  const int t = threadIdx.x, lane = t & 63, w = t >> 6;
  constexpr int RS = 1024 + 32;
#pragma unroll
  for (int r = 0; r < 8; ++r) *(u32x4*)(smem + (w * 8 + r) * RS + lane * 16) = v[r];
  __syncthreads();
  const int g = lane >> 4, q = (lane & 15) >> 2, p = lane & 3;
#pragma unroll
  for (int cf = 0; cf < 8; ++cf) {
    const int c0 = (w * 8 + cf) * 16;
    const char* pa = smem + (8 * g + q) * RS + c0 * 2 + 8 * p;
    const u32x4 o = cat2(tr_read(pa), tr_read(pa + 4 * RS));
    st16(rs, WS_X + (b >> 1) * 65536 + (c0 + (lane & 15)) * 128 + (b & 1) * 64 + g * 16, o);
  }
}
DEVI void row_stats(int b, const u32x4 (&v)[8], rsrc_t rs) {
  const int t = threadIdx.x, lane = t & 63, w = t >> 6;
  const int t0 = b * 32;
  float a[16];
#pragma unroll
  for (int r = 0; r < 8; ++r) {
    float s = 0.f, ss = 0.f;
#pragma unroll
    for (int j = 0; j < 4; ++j) { s = dot2bf(v[r][j], 0x3f803f80u, s); ss = dot2bf(v[r][j], v[r][j], ss); }
    a[2 * r] = s; a[2 * r + 1] = ss;
  }
  const bool o1 = lane & 1, o2 = lane & 2;
#pragma unroll
  for (int k = 0; k < 8; ++k) {
    const float keep = o1 ? a[2 * k + 1] : a[2 * k], send = o1 ? a[2 * k] : a[2 * k + 1];
    a[k] = keep + __uint_as_float(__builtin_amdgcn_update_dpp(0u, __float_as_uint(send), 0xB1, 0xf, 0xf, true));
  }
#pragma unroll
  for (int k = 0; k < 4; ++k) {
    const float keep = o2 ? a[2 * k + 1] : a[2 * k], send = o2 ? a[2 * k] : a[2 * k + 1];
    a[k] = keep + __uint_as_float(__builtin_amdgcn_update_dpp(0u, __float_as_uint(send), 0x4E, 0xf, 0xf, true));
  }
#pragma unroll
  for (int k = 0; k < 4; ++k) a[k] = sum_x32(sum_x16(dpp_add<0x128>(dpp_add<0x124>(a[k]))));
  const i64 sv = lane < 16 ? f2fx(sel4(lane >> 2, a[0], a[1], a[2], a[3])) : 0;
  if (lane < 32)
    __builtin_amdgcn_raw_buffer_store_b64(__builtin_bit_cast(u32x2, sv), rs, (lane >> 4) * 65536 + (t0 + w * 8) * 16 + (lane & 15) * 8, WS_ST, SC1);
  if (t < 5) __builtin_amdgcn_raw_buffer_store_b32(0u, rs, (b * 5 + t) * 256, WS_CT, SC1);
}

DEVI void pack_w1_load(int b, int kh, const u16* w1, u32x4 (&v)[8]) {
  const int t = threadIdx.x;
#pragma unroll
  for (int it = 0; it < 4; ++it) v[it] = *(const u32x4*)(w1 + (size_t)(kh * 256 + it * 64 + (t >> 2)) * FF + b * 32 + (t & 3) * 8);
}
DEVI void pack_w1(int b, int kh, const u32x4 (&v)[8], rsrc_t rs, char* smem) {
  const int t = threadIdx.x, lane = t & 63, w = t >> 6;
  constexpr int RS = 64 + 32;
#pragma unroll
  for (int it = 0; it < 4; ++it) *(u32x4*)(smem + (it * 64 + (t >> 2)) * RS + (t & 3) * 16) = v[it];
  __syncthreads();
  const int g = lane >> 4, q = (lane & 15) >> 2, p = lane & 3;
  float cs[2] = {0.f, 0.f};
#pragma unroll
  for (int kk = 0; kk < 2; ++kk) {
    const int ks = w * 2 + kk;
#pragma unroll
    for (int h = 0; h < 2; ++h) {
      const char* a = smem + (32 * ks + 8 * g + q) * RS + h * 32 + 8 * p;
      const u32x4 o = cat2(tr_read(a), tr_read(a + 4 * RS));
#pragma unroll
      for (int j = 0; j < 4; ++j) cs[h] = dot2bf(o[j], 0x3f803f80u, cs[h]);
      st16(rs, WS_W1P + (((2 * b + h) * 16 + kh * 8 + ks) * 64 + lane) * 16, o);
    }
  }
  const float s0 = sum_x32(sum_x16(cs[0])), s1 = sum_x32(sum_x16(cs[1]));
  if (lane < 32) __builtin_amdgcn_raw_buffer_store_b32(__float_as_uint(lane < 16 ? s0 : s1), rs, ((b * 32 + lane) * 8 + kh * 4 + w) * 4, WS_S1, SC1);
  if (b == 0 && kh == 0) st16(rs, WS_ONE + t * 16, u32x4{0x3f803f80u, 0x3f803f80u, 0x3f803f80u, 0x3f803f80u});
}

DEVI void pack_w2_load(int b, const u16* w2, u32x4 (&v)[2]) {
  const int t = threadIdx.x;
#pragma unroll
  for (int r = 0; r < 2; ++r) v[r] = *(const u32x4*)(w2 + (size_t)((b >> 2) * 32 + r * 16 + (t >> 4)) * DM + (b & 3) * 128 + (t & 15) * 8);
}
constexpr int W2_RS = 256 + 32;
DEVI void pack_w2_put(const u32x4 (&v)[2], char* smem) {
  const int t = threadIdx.x;
#pragma unroll
  for (int r = 0; r < 2; ++r) *(u32x4*)(smem + (r * 16 + (t >> 4)) * W2_RS + (t & 15) * 16) = v[r];
}
DEVI void pack_w2_out(int b, rsrc_t rs, const char* smem) {
  const int t = threadIdx.x, lane = t & 63, w = t >> 6;
  const int g = lane >> 4, q = (lane & 15) >> 2, p = lane & 3;
#pragma unroll
  for (int cf = 0; cf < 2; ++cf) {
    const char* a = smem + (4 * g + q) * W2_RS + (w * 2 + cf) * 32 + 8 * p;
    const u32x4 o = cat2(tr_read(a), tr_read(a + 16 * W2_RS));
    st16(rs, b < 224 ? WS_W2P + ((((b & 3) * 8 + w * 2 + cf) * 56 + (b >> 2)) * 64 + lane) * 16 : 0x40000000, o);
  }
}

constexpr int L_KC = 4160;
constexpr int L_U = 4 * L_KC, L_UC = 4608;
constexpr int L_XA = 26112, L_XB = L_XA + 65536;
constexpr int L_MISC = L_XB + 65536;
constexpr int L_CST = L_MISC + 512 + 4 * 512;
constexpr int L_SIG = L_CST + 4 * 256;
constexpr int MAIN_LDS = L_SIG + 256;
constexpr int L_RED = L_XB, L_SCR = L_XB + 16384;

DEVI u32x4 ld128(rsrc_t rs, int v, int s) { return __builtin_amdgcn_raw_buffer_load_b128(rs, v, s, SC1); }
DEVI u32x2 ld64(rsrc_t rs, int v, int s) { return __builtin_amdgcn_raw_buffer_load_b64(rs, v, s, SC1); }
DEVI void st128(rsrc_t rs, int v, int s, u32x4 d) { __builtin_amdgcn_raw_buffer_store_b128(d, rs, v, s, SC1); }
DEVI void st64(rsrc_t rs, int v, int s, u32x2 d) { __builtin_amdgcn_raw_buffer_store_b64(d, rs, v, s, SC1); }
template <int OFF> DEVI u32x4 ld128a(rsrc_t rs, int v, int s) {
  u32x4 r;
  asm volatile("buffer_load_dwordx4 %0, %1, %2, %3 offen offset:%4 sc1" : "=a"(r) : "v"(v), "s"(rs), "s"(s), "i"(OFF) : "memory");
  return r;
}
template <int OFF> DEVI u32x4 ld128v(rsrc_t rs, int v, int s) {
  u32x4 r;
  asm volatile("buffer_load_dwordx4 %0, %1, %2, %3 offen offset:%4 sc1" : "=v"(r) : "v"(v), "s"(rs), "s"(s), "i"(OFF) : "memory");
  return r;
}
template <int OFF> DEVI void dma1k(rsrc_t rs, u32 lds_dst, int v, int soff) {
  asm volatile("s_mov_b32 m0, %0\n\ts_nop 0\n\tbuffer_load_dwordx4 %1, %2, %3 offen offset:%4 sc1 lds"
               :: "s"(lds_dst), "v"(v), "s"(rs), "s"(soff), "i"(OFF) : "memory");
}
#define MMA(NAME, OP, CA, CB)                                                                                   \
  template <bool Z> DEVI void NAME(f32x4& d, const u32x4& a, const u32x4& b) {                                  \
    if (Z) asm volatile(OP " %0, %1, %2, 0" : "=&v"(d) : CA(a), CB(b));                                         \
    else asm volatile(OP " %0, %1, %2, %0" : "+v"(d) : CA(a), CB(b));                                           \
  }
MMA(mma_vv, "v_mfma_f32_16x16x32_bf16", "v", "v")
MMA(mma_av, "v_mfma_f32_16x16x32_bf16", "a", "v")
MMA(mma_va, "v_mfma_f32_16x16x32_bf16", "v", "a")
MMA(mma_hh, "v_mfma_f32_16x16x32_f16", "v", "v")
#define MFMA_DONE() do { SCHED(); asm volatile("s_nop 7" ::: "memory"); SCHED(); } while (0)

struct Ctx {
  rsrc_t rs; gu32* ct; char* smem; char* ws; u16* out;
  int lane0, w, b, P, sub, ct_, ff0, hks, eo, f1o, f1s;
  float dsk0, dsk1, ks0, ks1, bb2, cs1;
  u32 tag;
};
DEVI int fresh_lane(const Ctx& c) { int l = c.lane0; asm volatile("" : "+v"(l)); return l; }
DEVI u32 sig_begin(const Ctx& c, int lane) {
  u32 old;
  asm volatile("ds_add_rtn_u32 %0, %1, %2" : "=v"(old) : "v"(lds_addr(c.smem) + L_SIG + lane * 4), "v"(1u) : "memory");
  return old;
}
template <bool ALL> DEVI void sig_end(const Ctx& c, int lane, u32 old, int line) {
  asm volatile("s_waitcnt lgkmcnt(0)" : "+v"(old) :: "memory");
  const bool last = (__builtin_amdgcn_readfirstlane(old) & 3u) == 3u;
  const int v = (last && lane < (ALL ? 8 : 1)) ? (ALL ? lane * 2048 : 0) : 0x40000000;
  asm volatile("s_nop 4\n\tbuffer_atomic_add %0, %1, %2, %3 offen" :: "v"(1u), "v"(v), "s"(c.rs),
               "s"(WS_CT + (line + (ALL ? (c.b >> 3) & 7 : 0)) * 256) : "memory");
}
template <bool ALL, int R = 0> DEVI void poll(const Ctx& c, int lane, int line) {
  const int v = ALL ? (lane & 7) * 256 : 0, so = WS_CT + (line + (ALL ? (c.b & 7) * 8 : 0)) * 256;
  if (R == 0) asm volatile("s_nop 4\n\tbuffer_load_dword v254, %0, %1, %2 offen sc1" :: "v"(v), "s"(c.rs), "s"(so) : "memory", "v254");
  else asm volatile("s_nop 4\n\tbuffer_load_dword v255, %0, %1, %2 offen sc1" :: "v"(v), "s"(c.rs), "s"(so) : "memory", "v255");
}
template <int YOUNGER, int R = 0> DEVI bool got(u32 want) {
  unsigned long long m;
  if (R == 0) asm volatile("s_waitcnt vmcnt(%2)\n\tv_cmp_le_u32_e64 %0, %1, v254" : "=s"(m) : "s"(want), "i"(YOUNGER) : "memory");
  else asm volatile("s_waitcnt vmcnt(%2)\n\tv_cmp_le_u32_e64 %0, %1, v255" : "=s"(m) : "s"(want), "i"(YOUNGER) : "memory");
  return m == ~0ull;
}
template <bool ALL> DEVI void wait_ge(const Ctx& c, int lane, int line, u32 want) {
  int sp = 0;
#pragma nounroll
  do {
    if (c.w) __builtin_amdgcn_s_sleep(8);
    poll<ALL, 0>(c, lane, line);
    if (++sp > SPIN_CAP) { __hip_atomic_store(c.ct + CT_TMO * 64, 1u, RLX_AGENT); break; }
  } while (!got<0, 0>(want));
}

DEVI void poll0(const Ctx& c, int lane, int set) {
  asm volatile("s_nop 4\n\tbuffer_load_dwordx2 v[254:255], %0, %1, %2 offen sc1" :: "v"((lane & 7) * 256 + (lane >> 3) * 8), "s"(c.rs),
               "s"(set + (c.b & 7) * 2048) : "memory", "v254", "v255");
}
template <int YOUNGER> DEVI bool got0(u32 tag) {
  unsigned long long m, m2;
  asm volatile("s_waitcnt vmcnt(%3)\n\tv_cmp_eq_u32_e64 %0, %2, v254\n\tv_cmp_eq_u32_e64 %1, %2, v255"
               : "=&s"(m), "=s"(m2) : "s"(tag), "i"(YOUNGER) : "memory");
  return (m & m2) == ~0ull;
}
DEVI void wait0(const Ctx& c, int lane, int set) {
  int sp = 0;
#pragma nounroll
  do {
    poll0(c, lane, set);
    if (++sp > SPIN_CAP) { __hip_atomic_store(c.ct + CT_TMO * 64, 1u, RLX_AGENT); break; }
  } while (!got0<0>(c.tag));
}

struct CIn { u32x2 x0, x1; u32x4 s[4]; };
DEVI void conv_load(const Ctx& c, int lane, CIn& in, int xoff, int stoff, int h) {
  const int sx = xoff + (16 * h + 4 * c.w) * 65536 + c.b * 256;
  const int vx = (lane >> 4) * 65536 + (lane & 15) * 8;
  const int ss = stoff + (1024 * h + 256 * c.w) * 16;
#pragma unroll
  for (int j = 0; j < 4; ++j) in.s[j] = ld128(c.rs, lane * 16 + j * 1024, ss);
  in.x0 = ld64(c.rs, vx, sx);
  in.x1 = ld64(c.rs, vx + 128, sx);
}
DEVI void conv_ln(const Ctx& c, int lane, const CIn& in, int h, f32x4& base0, f32x4& base1) {
  char* scr = c.smem + L_SCR + c.w * 2048;
  constexpr float CH = 4294967296.0f * FX_INV / DM, CL = FX_INV / DM;
  f32x4 m, r;
#pragma unroll
  for (int j = 0; j < 4; ++j) {
    m[j] = fmaf((float)(int)in.s[j][1], CH, (float)in.s[j][0] * CL);
    r[j] = fmaf((float)(int)in.s[j][3], CH, fmaf((float)in.s[j][2], CL, LN_EPS));
  }
#pragma unroll
  for (int j = 0; j < 4; ++j) r[j] = __builtin_amdgcn_rsqf(fmaxf(fmaf(-m[j], m[j], r[j]), LN_EPS));
#pragma unroll
  for (int j = 0; j < 4; ++j) {
    *(float*)(scr + (64 * j + lane) * 4) = r[j];
    *(float*)(scr + 1024 + (64 * j + lane) * 4) = -m[j] * r[j];
  }
  const f32x4 rr = *(const f32x4*)(scr + lane * 16), mr = *(const f32x4*)(scr + 1024 + lane * 16);
  const f32x4 x0 = {lo_bf(in.x0[0]), hi_bf(in.x0[0]), lo_bf(in.x0[1]), hi_bf(in.x0[1])};
  const f32x4 x1 = {lo_bf(in.x1[0]), hi_bf(in.x1[0]), lo_bf(in.x1[1]), hi_bf(in.x1[1])};
  const f32x4 u0 = x0 * rr + mr, u1 = x1 * rr + mr;
  char* up = c.smem + L_U + 512 + (1024 * h + 256 * c.w + 4 * lane) * 2;
  *(u32x2*)up = u32x2{pack_h(u0[0], u0[1]), pack_h(u0[2], u0[3])};
  *(u32x2*)(up + L_UC) = u32x2{pack_h(u1[0], u1[1]), pack_h(u1[2], u1[3])};
  base0 = u0 * c.dsk0 + x0; base1 = u1 * c.dsk1 + x1;
}
struct KB { const char* p[4]; };
template <int MM, int LAM> DEVI u32x4 conv_k1(const KB& kb) {
  const u32* kp = (const u32*)(kb.p[(7 - LAM) >> 1] + 64 * (3 - MM) + 512 * ((7 - LAM) & 1));
  return u32x4{kp[0], kp[1], kp[2], kp[3]};
}
template <int MM, int TB> DEVI u32x4 conv_u1(const char* ub) { return *(const u32x4*)(ub + 64 * (3 - MM) + 512 * TB); }
template <int LO> DEVI void conv_ldk(const KB& kb, u32x4 (&af)[4]) {
  sfor<0, 4>([&](auto L) { af[L.v] = conv_k1<0, LO + L.v>(kb); });
}
constexpr int conv_rank(int DT, int LO, int gr, int lt) {
  int k = 0;
  for (int x = 0; x < lt; ++x) { const int tb = DT + (x & 3) - (LO + 4 * gr + (x >> 2)); k += (tb >= 0 && tb < 4); }
  return k;
}
template <int DT, int TB0, bool ZERO, class F>
DEVI void conv_mma(const char* ub, const KB& kb, f32x4 (&acc)[4], const u32x4 (&af0)[4], F&& hook) {
  constexpr int NG = DT ? 2 : 1, LO = DT ? 1 : 0;
  u32x4 af[2][4], bf[2][4];
  sfor<0, 4>([&](auto L) { af[0][L.v] = af0[L.v]; });
  sfor<0, 4>([&](auto T) { constexpr int tb = DT ? 3 - T.v : T.v; bf[0][tb] = conv_u1<0, TB0 + tb>(ub); });
  SCHED();
  sfor<0, 4 * NG>([&](auto ST) {
    constexpr int st = ST.v, mm = st / NG, gr = st % NG;
    constexpr bool more = st + 1 < 4 * NG;
    constexpr int mn = (st + 1) / NG, gn = (st + 1) % NG;
    constexpr bool nu = more && gr == NG - 1;
    sfor<0, 16>([&](auto LT) {
      constexpr int l = LT.v >> 2, T = LT.v & 3, tb = DT + T - (LO + 4 * gr + l);
      if constexpr (tb >= 0 && tb < 4) {
        constexpr int k = conv_rank(DT, LO, gr, LT.v);
        constexpr bool first = ZERO && st == 0 && (DT ? (l == T) : (l == 0));
        mma_hh<first>(acc[T], af[st & 1][l], bf[mm & 1][tb]);
        sfor<(gr == 1 ? 2 * k : k), (gr == 1 ? 2 * k + 2 : k + 1)>([&](auto J) {
          constexpr int j = J.v;
          if constexpr (nu && j < 8) {
            if constexpr (DT != 0) {
              if constexpr (j & 1) bf[mn & 1][3 - (j >> 1)] = conv_u1<mn, TB0 + 3 - (j >> 1)>(ub);
              else af[(st + 1) & 1][j >> 1] = conv_k1<mn, LO + (j >> 1)>(kb);
            } else {
              if constexpr (j >= 1 && j <= 4) bf[mn & 1][j - 1] = conv_u1<mn, TB0 + j - 1>(ub);
              else af[(st + 1) & 1][j ? j - 4 : 0] = conv_k1<mn, LO + (j ? j - 4 : 0)>(kb);
            }
          } else if constexpr (more && !nu && j < 3) af[(st + 1) & 1][j] = conv_k1<mn, LO + 4 * gn + j>(kb);
        });
        SCHED();
        hook(IC<mm * (DT ? 16 : 10) + gr * 10 + k>{});
      }
    });
  });
}
DEVI void conv_part(const Ctx& c, int lane, const f32x4 (&acc)[4]) {
  char* red = c.smem + L_RED;
#pragma unroll
  for (int T = 0; T < 4; ++T) *(f32x4*)(red + (c.w * 4 + T) * 1024 + (lane & 15) * 64 + (lane >> 4) * 16) = acc[T];
}
DEVI void conv_out(const Ctx& c, int lane, const f32x4& base0, const f32x4& base1, int xoff, int h) {
  const int sx = xoff + (16 * h + 4 * c.w) * 65536 + c.b * 256;
  const int vx = (lane >> 4) * 65536 + (lane & 15) * 8;
  const char* p = c.smem + L_RED + c.w * 1024 + lane * 16;
  const f32x4 a0 = *(const f32x4*)p, b0 = *(const f32x4*)(p + 4096), a1 = *(const f32x4*)(p + 8192), b1 = *(const f32x4*)(p + 12288);
  SCHED();
  const f32x4 y0 = (a0 + b0) * c.ks0 + base0, y1 = (a1 + b1) * c.ks1 + base1;
  st64(c.rs, vx, sx, u32x2{pack_bf(y0[0], y0[1]), pack_bf(y0[2], y0[3])});
  st64(c.rs, vx + 128, sx, u32x2{pack_bf(y1[0], y1[1]), pack_bf(y1[2], y1[3])});
}

template <int PC> DEVI void g1_dma1(const Ctx& c, int ve, int vo, int ldsx, int soff) {
  dma1k<(PC & 3) * 1024>(c.rs, lds_addr(c.smem) + ldsx + (4 * (PC >> 2) + c.w) * 4096, (PC & 1) ? vo : ve, soff + (4 * (PC >> 2) + c.w) * 4096);
}
template <bool ALL> DEVI void sig_bar(const Ctx& c, int lane, int line) {
  const int v = (c.w == 0 && lane < (ALL ? 8 : 1)) ? (ALL ? lane * 2048 : 0) : 0x40000000;
  asm volatile("s_nop 4\n\tbuffer_atomic_add %0, %1, %2, %3 offen" :: "v"(1u), "v"(v), "s"(c.rs),
               "s"(WS_CT + (line + (ALL ? (c.b >> 3) & 7 : 0)) * 256) : "memory");
}
template <class F, class G>
DEVI void g1_mma(const char* const (&xb)[4], const u32x4 (&w1f)[2][16], f32x4 (&acc)[2][4], f32x4& sa, F&& mid, G&& hook) {
  u32x2 xf[2][4][2];
#pragma unroll
  for (int i = 0; i < 4; ++i) { xf[0][i][0] = tr_read(xb[i]); xf[0][i][1] = tr_read(xb[i] + 512); }
  SCHED();
  sfor<0, 16>([&](auto KS) {
    constexpr int ks = KS.v;
    mid(KS);
    sfor<0, 4>([&](auto I) {
      constexpr int i = I.v;
      const u32x4 x = cat2(xf[ks & 1][i][0], xf[ks & 1][i][1]);
      mma_vv<ks == 0>(acc[0][i], w1f[0][ks], x);
      if constexpr (ks < 15) xf[(ks + 1) & 1][i][0] = tr_read(xb[i] + (ks + 1) * 4096);
      SCHED();
      if constexpr (ks < 8) mma_av<ks == 0>(acc[1][i], w1f[1][ks], x); else mma_vv<false>(acc[1][i], w1f[1][ks], x);
      if constexpr (ks < 15) xf[(ks + 1) & 1][i][1] = tr_read(xb[i] + (ks + 1) * 4096 + 512);
      SCHED();
      if constexpr (i == 0) { mma_vv<ks == 0>(sa, x, x); hook(IC<2 * ks>{}); SCHED(); }
      if constexpr (i == 2) { hook(IC<2 * ks + 1>{}); SCHED(); }
    });
  });
  MFMA_DONE();
}
template <class F>
DEVI void g1_epi(const Ctx& c, int lane, f32x4 (&acc)[2][4], const f32x4& sa, int hoff, F&& hook) {
  const int g = lane >> 4, cc = lane & 15, q = cc >> 2, p = lane & 3, w = c.w;
  float* sums = (float*)(c.smem + L_MISC);
  {
    const float d = sel4(p, sa[0], sa[1], sa[2], sa[3]);
    if (q == g) sums[64 + w * 16 + cc] = d;
    const float r = sel4(g, acc[1][1][0], acc[1][2][0], acc[1][3][0], acc[1][0][0]);
    if (w == 3) sums[lane] = r;
  }
  f32x4 s1v[2], b1v[2];
#pragma unroll
  for (int j = 0; j < 2; ++j) {
    s1v[j] = *(const f32x4*)(c.smem + L_CST + w * 256 + j * 128 + g * 16);
    b1v[j] = *(const f32x4*)(c.smem + L_CST + w * 256 + j * 128 + 64 + g * 16);
  }
  __syncthreads();
  f32x2* cs = (f32x2*)(c.smem + L_MISC + 512 + w * 512);
  {
    const float m = sums[lane] * (1.0f / DM);
    const float ve = fmaxf(fmaf(-m, m, fmaf(sums[64 + lane], 1.0f / DM, LN_EPS)), LN_EPS);
    const float r = __builtin_amdgcn_rsqf(fmaf(ve, LN_EPS, ve - LN_EPS));
    f32x2 o = {r, -m * r};
    cs[lane] = o;
  }
  const int v16 = lane * 16 + (w < 3 ? 0 : 0x40000000), v8 = lane * 16 + (w < 3 ? 0x40000000 : c.eo * 8);
  f32x2 k[4];
#pragma unroll
  for (int i = 0; i < 4; ++i) k[i] = cs[((i + w) & 3) * 16 + cc];
  SCHED();
  hook(IC<-1>{});
  SCHED();
  sfor<0, 2>([&](auto IH) {
    constexpr int ih = IH.v;
    f32x4 x[4], e[4];
#define STAGE(n) do { SCHED(); hook(IC<10 * ih + n>{}); SCHED(); } while (0)
#pragma unroll
    for (int n = 0; n < 4; ++n) x[n] = k[2 * ih + (n >> 1)][1] * s1v[n & 1] + b1v[n & 1];
#pragma unroll
    for (int n = 0; n < 4; ++n) x[n] = acc[n & 1][2 * ih + (n >> 1)] * k[2 * ih + (n >> 1)][0] + x[n];
    STAGE(0);
#pragma unroll
    for (int n = 0; n < 4; ++n) e[n] = x[n] * x[n];
#pragma unroll
    for (int n = 0; n < 4; ++n) e[n] = e[n] * (-0.0713548162726f * 1.44269504088896f) + (-1.59576912160573f * 1.44269504088896f);
    STAGE(1);
#pragma unroll
    for (int n = 0; n < 4; ++n) e[n] = e[n] * x[n];
    STAGE(2);
#pragma unroll
    for (int r = 0; r < 4; ++r) { e[0][r] = __builtin_amdgcn_exp2f(e[0][r]); e[1][r] = __builtin_amdgcn_exp2f(e[1][r]); }
    STAGE(3);
#pragma unroll
    for (int r = 0; r < 4; ++r) { e[2][r] = __builtin_amdgcn_exp2f(e[2][r]); e[3][r] = __builtin_amdgcn_exp2f(e[3][r]); }
    STAGE(4);
#pragma unroll
    for (int n = 0; n < 4; ++n) e[n] = e[n] + 1.0f;
    STAGE(5);
#pragma unroll
    for (int r = 0; r < 4; ++r) { e[0][r] = rcp(e[0][r]); e[1][r] = rcp(e[1][r]); }
    STAGE(6);
#pragma unroll
    for (int r = 0; r < 4; ++r) { e[2][r] = rcp(e[2][r]); e[3][r] = rcp(e[3][r]); }
    STAGE(7);
#pragma unroll
    for (int n = 0; n < 4; ++n) x[n] = x[n] * e[n];
    STAGE(8);
    u32x4 hv[2];
#pragma unroll
    for (int n = 0; n < 4; ++n) { hv[n >> 1][2 * (n & 1)] = pack_bf(x[n][0], x[n][1]); hv[n >> 1][2 * (n & 1) + 1] = pack_bf(x[n][2], x[n][3]); }
    STAGE(9);
    const int so0 = hoff + ((2 * ih + w) & 3) * 56 * 1024, so1 = hoff + ((2 * ih + 1 + w) & 3) * 56 * 1024;
    st128(c.rs, v16, so0, hv[0]); st128(c.rs, v16, so1, hv[1]);
    st64(c.rs, v8, so0, u32x2{hv[0][0], hv[0][1]}); st64(c.rs, v8, so1, u32x2{hv[1][0], hv[1][1]});
    SCHED();
    if constexpr (ih == 0) { hook(IC<-2>{}); SCHED(); }
#undef STAGE
  });
}

constexpr int PF = 10;
template <int S0, int S1> DEVI void g2_ldh(const Ctx& c, int lane, u32x4 (&hf)[14][2], int ho) {
#pragma unroll
  for (int s = S0; s < S1; ++s)
#pragma unroll
    for (int i = 0; i < 2; ++i) hf[s][i] = ld128(c.rs, lane * 16 + (s & 3) * 1024, ho + i * 56 * 1024 + (s >> 2) * 4096);
}
template <int N> DEVI void ld_w1(const Ctx& c, int v, u32x4 (&w1f)[2][16]) {
  constexpr int ks = N >> 1, j = N & 1;
  const int so = (j ? c.f1o + (ks >> 2) * c.f1s : WS_W1P + c.ff0 * 16384 + (ks >> 2) * 4096);
  if constexpr (j && ks < 8) w1f[j][ks] = ld128a<(ks & 3) * 1024>(c.rs, v, so);
  else w1f[j][ks] = ld128v<(ks & 3) * 1024>(c.rs, v, so);
}
template <int N> DEVI void ld_w2(const Ctx& c, int v, u32x4 (&w2f)[14][4]) {
  constexpr int s = N >> 2, j = N & 3;
  w2f[s][j] = ld128a<(s & 3) * 1024>(c.rs, v, WS_W2P + ((c.ct_ * 4 + ((c.w + j) & 3)) * 56 + 14 * c.w + (s & ~3)) * 1024);
}
template <int N> DEVI void g2_ldh1(const Ctx& c, int v16, u32x4 (&hf)[14][2], int ho) {
  constexpr int s = N >> 1, i = N & 1;
  hf[s][i] = ld128(c.rs, v16 + (s & 3) * 1024, ho + i * 56 * 1024 + (s >> 2) * 4096);
}
template <int PRE, int RATE, class F>
DEVI void g2_mma(const Ctx& c, int lane, u32x4 (&hf)[14][2], const u32x4 (&w2f)[14][4], f32x4 (&acc)[2][4], int ho, F&& mid) {
  const int v16 = lane * 16;
  sfor<0, 14>([&](auto S) {
    constexpr int s = S.v;
    sfor<0, 8>([&](auto M) {
      constexpr int i = M.v >> 2, j = M.v & 3;
      mma_va<s == 0>(acc[i][j], hf[s][i], w2f[s][j]);
      constexpr int per = 4 / RATE, n = 2 * (PRE + RATE * s) + M.v / per;
      if constexpr (M.v % per == per - 1 && n < 28) { g2_ldh1<n>(c, v16, hf, ho); SCHED(); }
    });
    SCHED();
    mid(S);
  });
  MFMA_DONE();
}
template <bool LAST, class F>
DEVI void g2_epi(const Ctx& c, int lane, const f32x4 (&acc)[2][4], const u32x2 (&xr)[2], int h, int xdst, F&& mid) {
  const int g = lane >> 4, cc = lane & 15, q = cc >> 2, p = lane & 3, w = c.w, t = w * 64 + lane;
  char* red = c.smem + L_XA + h * 32768;
#pragma unroll
  for (int jj = 1; jj < 4; ++jj)
#pragma unroll
    for (int i = 0; i < 2; ++i) *(f32x4*)(red + ((((w + jj) & 3) * 3 + jj - 1) * 2 + i) * 1024 + lane * 16) = acc[i][jj];
  __syncthreads();
  char* xt = red + 24576;
  f32x4 pr[2][3];
#pragma unroll
  for (int i = 0; i < 2; ++i)
#pragma unroll
    for (int rk = 0; rk < 3; ++rk) pr[i][rk] = *(const f32x4*)(red + ((w * 3 + rk) * 2 + i) * 1024 + lane * 16);
  mid(0);
#pragma unroll
  for (int i = 0; i < 2; ++i) {
    const f32x4 xv = {lo_bf(xr[i][0]), hi_bf(xr[i][0]), lo_bf(xr[i][1]), hi_bf(xr[i][1])};
    const f32x4 o = ((acc[i][0] + (xv + c.bb2)) + pr[i][0]) + (pr[i][1] + pr[i][2]);
    const u32x2 r = {pack_bf(o[0], o[1]), pack_bf(o[2], o[3])};
    *(u32x2*)(xt + (w * 16 + cc) * 64 + ((i ^ (cc >> 3)) * 32) + g * 8) = r;
  }
  __syncthreads();
  const char* xq = xt + ((w >> 1) * 32 + 8 * g + q) * 64 + (((w & 1) ^ (g & 1)) * 32) + 8 * p;
  const u32x4 f0 = cat2(tr_read(xq), tr_read(xq + 256));
  const int tok = (16 * h + c.P) * 64 + c.sub * 32 + (w & 1) * 16 + cc;
  if (LAST) {
    *(u32x4*)(c.out + (size_t)tok * DM + c.ct_ * 64 + (w >> 1) * 32 + 8 * g) = f0;
  } else {
    const int row = t >> 2;
    const u32x4 v = *(const u32x4*)(xt + row * 64 + (((t & 3) ^ ((row >> 2) & 2)) * 16));
    st128(c.rs, (c.ct_ * 64 + row) * 128 + (t & 3) * 16, xdst, v);
    float s = 0.f, ss = 0.f;
#pragma unroll
    for (int j = 0; j < 4; ++j) { s = dot2bf(f0[j], 0x3f803f80u, s); ss = dot2bf(f0[j], f0[j], ss); }
    s = sum_x16(s); ss = sum_x16(ss);
    (void)__hip_atomic_fetch_add((unsigned long long*)(c.ws + WS_ST + 65536) + 2 * tok + (g & 1),
                                 (unsigned long long)f2fx((g & 1) ? ss : s), RLX_AGENT);
  }
}

template <int LAYER>
DEVI void layer_body(const Ctx& c, u32x4 (&w1f)[2][16], u32x4 (&w2f)[14][4], CIn& inA, CIn& inB) {
  constexpr int xin = WS_X + LAYER * 4 * MB, xmid = xin + 2 * MB, xnext = xin + 4 * MB;
  constexpr int hbuf = WS_H + LAYER * 8 * MB, stin = WS_ST + LAYER * 65536;
  constexpr u32 ep = LAYER + 1;
  constexpr int K1 = 1 + 3 * LAYER, K2 = K1 + 1, K3 = K1 + 2;
  const int w = c.w;
  char* smem = c.smem;
  auto nop = [](auto) {};
  {
    const int lane = fresh_lane(c);
    const int g = lane >> 4, cc = lane & 15, mu0 = (w & 1) * 4, v16 = lane * 16;
    const char* ub = smem + L_U + (w >> 1) * L_UC + 512 + 2 * (16 * cc - 16 + 8 * g) - 64 * (mu0 + 3);
    KB kb;
#pragma unroll
    for (int j = 0; j < 4; ++j) {
      u32 a = lds_addr(smem) + (w >> 1) * 2 * L_KC + ((cc & 1) ? 0 : L_KC) + 2 * (2031 - cc + 8 * g - ((cc & 1) ? 0 : 1)) - 64 * (mu0 + 3 + 56) + 1024 * j;
      asm volatile("" : "+v"(a));
      kb.p[j] = (const char*)(const LAS char*)(size_t)a;
    }
    STAMP(K1, 1);
    f32x4 base0, base1;
    u32x4 a4[4];
    conv_ldk<0>(kb, a4);
    conv_ln(c, lane, inA, 0, base0, base1);
    if (!LAYER) poll0(c, lane, WS_F1);
    __syncthreads();
    STAMP(K1, 2);
    f32x4 acc[4];
    u32 o = 0;
    conv_mma<0, 0, true>(ub, kb, acc, a4, [&](auto N) {
      constexpr int n = N.v;
      if constexpr (LAYER && n == 19) { WAIT_VM(0); o = sig_begin(c, lane); SCHED(); }
      if constexpr (LAYER && n == 29) { sig_end<true>(c, lane, o, CT_X + 64); SCHED(); }
    });
    MFMA_DONE();
    conv_part(c, lane, acc);
    conv_ldk<1>(kb, a4);
    __syncthreads();
    f32x4 s1p, s1q;
    if (!LAYER) {
      if (!got0<0>(c.tag)) wait0(c, lane, WS_F1);
      asm volatile("" :: "v"(inB.x1));
      asm volatile("s_nop 4\n\tbuffer_load_dwordx4 %0, %2, %3, %4 offen sc1\n\tbuffer_load_dwordx4 %1, %2, %3, %4 offen offset:16 sc1"
                   : "=&v"(s1p), "=&v"(s1q)
                   : "v"(min(c.ff0 * 16 + (lane & 15) + (lane >> 5) * 16, FF - 1) * 32), "s"(c.rs), "s"(WS_S1) : "memory");
    }
    conv_out(c, lane, base0, base1, xmid, 0);
    STAMP(K1, 3);
    f32x4 acb[4];
    conv_mma<4, 0, true>(ub, kb, acb, a4, [&](auto N) {
      constexpr int n = N.v;
      if constexpr (!LAYER && (n & 3) == 1) { ld_w1<(n >> 2)>(c, v16, w1f); SCHED(); }
      if constexpr (n == 47) {
        if (LAYER) WAIT_VM(0); else WAIT_VM(12);
        o = sig_begin(c, lane);
        if (LAYER) poll<true>(c, lane, CT_X + 64);
        SCHED();
      }
      if constexpr (n == 57) { sig_end<true>(c, lane, o, CT_C); SCHED(); }
    });
    STAMP(K1, 4);
    if (!LAYER) {
      asm volatile("" : "+v"(s1p), "+v"(s1q));
      s1p += s1q;
      ((float*)(smem + L_CST))[w * 64 + lane] = (lane & 16) ? c.cs1 : (s1p[0] + s1p[1]) + (s1p[2] + s1p[3]);
    }
    if (LAYER) {
      if (!got<1>(32u)) wait_ge<true>(c, lane, CT_X + 64, 32u);
      conv_load(c, lane, inB, xin, stin, 1);
    }
    conv_ldk<0>(kb, a4);
    STAMP(K1, 5);
    conv_ln(c, lane, inB, 1, base0, base1);
    __syncthreads();
    conv_mma<0, 4, false>(ub, kb, acb, a4, [&](auto N) {
      constexpr int n = N.v;
      if constexpr (!LAYER && (n & 3) == 1) { ld_w1<16 + (n >> 2)>(c, v16, w1f); SCHED(); }
      if constexpr (n == 30) { poll<true>(c, lane, CT_C); SCHED(); }
    });
    MFMA_DONE();
    conv_part(c, lane, acb);
    __syncthreads();
    conv_out(c, lane, base0, base1, xmid, 1);
    STAMP(K1, 6);
    if (!got<(LAYER ? 2 : 4)>(32u * ep)) wait_ge<true>(c, lane, CT_C, 32u * ep);
    const int row = lane >> 3, sw = ((row >> 1) & 1) << 1;
    const int ve = row * 128 + ((lane & 7) ^ sw) * 16, vo = row * 128 + ((lane & 7) ^ (sw | 4)) * 16;
    if (!LAYER && c.b == 0 && w == 0) __hip_atomic_store((gu32*)(c.ws + WS_GEN), c.tag, RLX_AGENT);
    sfor<0, 8>([&](auto N) { g1_dma1<N.v>(c, ve, vo, L_XA, xmid + c.P * 65536); });
    WAIT_VM(8);
    o = sig_begin(c, lane);
    sig_end<true>(c, lane, o, CT_C + 64);
    STAMP(K1, 7);
  }
  u32x4 hf[14][2];
  u32x2 xr[2];
  {
    const int lane = fresh_lane(c);
    const int g = lane >> 4, cc = lane & 15, q = cc >> 2, p = lane & 3, v16 = lane * 16;
    const int sx = ((q >> 1) & 1) | ((g & 1) << 1);
    const char* xb[4];
#pragma unroll
    for (int i = 0; i < 4; ++i) xb[i] = smem + L_XA + (8 * g + q) * 128 + ((((i + w) & 3) ^ sx) * 32) + 8 * p;
    const int row = lane >> 3, sw = ((row >> 1) & 1) << 1;
    const int ve = row * 128 + ((lane & 7) ^ sw) * 16, vo = row * 128 + ((lane & 7) ^ (sw | 4)) * 16;
    sfor<8, 12>([&](auto N) { g1_dma1<N.v>(c, ve, vo, L_XA, xmid + c.P * 65536); });
    WAIT_VM_BAR(9);
    STAMP(K2, 0);
    f32x4 acc[2][4], sa;
    g1_mma(xb, w1f, acc, sa, [&](auto KS) {
      constexpr int ks = KS.v;
      if constexpr (ks == 3) { if (LAYER) WAIT_VM_BAR(8); else WAIT_VM_BAR(10); SCHED(); }
      if constexpr (ks == 7) { if (LAYER) WAIT_VM_BAR(0); else WAIT_VM_BAR(10); SCHED(); }
      if constexpr (ks == 13) { if (!LAYER) WAIT_VM(16); poll<true>(c, lane, CT_C + 64); SCHED(); }
    }, [&](auto N) {
      constexpr int n = N.v;
      if constexpr (n < 4) g1_dma1<12 + n>(c, ve, vo, L_XA, xmid + c.P * 65536);
      if constexpr (!LAYER && n >= 4 && n < 10) ld_w1<22 + n>(c, v16, w1f);
      if constexpr (!LAYER && n >= 10) ld_w2<n - 10>(c, v16, w2f);
    });
    STAMP(K2, 1);
    const int xo = xmid + (16 + c.P) * 65536;
    g1_epi(c, lane, acc, sa, hbuf + (c.P * 4 * 56 + c.hks) * 1024, [&](auto N) {
      constexpr int n = N.v;
      if constexpr (n == -1) { if (!got<(LAYER ? 0 : 6)>(32u * ep)) wait_ge<true>(c, lane, CT_C + 64, 32u * ep); }
      if constexpr (n >= 0 && n < 8) g1_dma1<n>(c, ve, vo, L_XB, xo);
      if constexpr (n >= 10 && n < 18) g1_dma1<n - 2>(c, ve, vo, L_XB, xo);
    });
    STAMP(K2, 2);
  }
  {
    const int lane = fresh_lane(c);
    const int g = lane >> 4, cc = lane & 15, q = cc >> 2, p = lane & 3, v16 = lane * 16;
    const int sx = ((q >> 1) & 1) | ((g & 1) << 1);
    const char* xb[4];
#pragma unroll
    for (int i = 0; i < 4; ++i) xb[i] = smem + L_XB + (8 * g + q) * 128 + ((((i + w) & 3) ^ sx) * 32) + 8 * p;
    WAIT_VM_BAR(16);
    STAMP(K2, 3);
    f32x4 acc[2][4], sa;
    g1_mma(xb, w1f, acc, sa, [&](auto KS) {
      constexpr int ks = KS.v;
      if constexpr (ks == 6) {
        if (LAYER) WAIT_VM_BAR(0); else WAIT_VM_BAR(12);
        sig_bar<false>(c, lane, CT_H + c.P);
        SCHED();
      }
      if constexpr (ks == 15) {
#pragma unroll
        for (int i = 0; i < 2; ++i) xr[i] = ld64(c.rs, (c.ct_ * 64 + w * 16 + cc) * 128 + i * 32 + g * 8, xmid + c.P * 65536 + c.sub * 64);
        SCHED();
      }
    }, [&](auto N) {
      constexpr int n = N.v;
      if constexpr (!LAYER) ld_w2<22 + n>(c, v16, w2f);
      if constexpr (!LAYER && n >= 30) ld_w2<24 + n>(c, v16, w2f);
    });
    STAMP(K2, 4);
    poll<false>(c, lane, CT_H + c.P);
    const int ho = hbuf + ((c.P * 4 + c.sub * 2) * 56 + 14 * w) * 1024;
    g1_epi(c, lane, acc, sa, hbuf + ((16 + c.P) * 4 * 56 + c.hks) * 1024, [&](auto N) {
      constexpr int n = N.v;
      if constexpr (n == -1) { if (!got<0>(16u * ep)) wait_ge<false>(c, lane, CT_H + c.P, 16u * ep); }
      if constexpr (n >= 0) g2_ldh1<n>(c, v16, hf, ho);
    });
    STAMP(K2, 5);
  }
  {
    const int lane = fresh_lane(c);
    const int g = lane >> 4, cc = lane & 15;
    const int ho = hbuf + ((c.P * 4 + c.sub * 2) * 56 + 14 * w) * 1024, hoB = ho + 64 * 56 * 1024;
    STAMP(K3, 0);
    f32x4 acc[2][4];
    u32 o = 0;
    g2_mma<10, 1>(c, lane, hf, w2f, acc, ho, [&](auto S) {
      constexpr int s = S.v;
      if constexpr (s == 6) { WAIT_VM(8); o = sig_begin(c, lane); SCHED(); }
      if constexpr (s == 7) { sig_end<false>(c, lane, o, CT_H + 16 + c.P); SCHED(); }
    });
    STAMP(K3, 1);
    poll<false>(c, lane, CT_H + 16 + c.P);
    g2_epi<LAYER == 1>(c, lane, acc, xr, 0, xnext + c.P * 65536 + c.sub * 64, nop);
    STAMP(K3, 2);
    if (!got<(LAYER ? 1 : 2)>(16u * ep)) wait_ge<false>(c, lane, CT_H + 16 + c.P, 16u * ep);
    g2_ldh<0, 4>(c, lane, hf, hoB);
#pragma unroll
    for (int i = 0; i < 2; ++i) xr[i] = ld64(c.rs, (c.ct_ * 64 + w * 16 + cc) * 128 + i * 32 + g * 8, xmid + (16 + c.P) * 65536 + c.sub * 64);
    if (LAYER == 0) {
      WAIT_VM(10);
      o = sig_begin(c, lane);
      sig_end<true>(c, lane, o, CT_X);
    }
    SCHED();
    STAMP(K3, 3);
  }
  {
    const int lane = fresh_lane(c);
    const int hoB = hbuf + (((16 + c.P) * 4 + c.sub * 2) * 56 + 14 * w) * 1024;
    f32x4 acc[2][4];
    g2_mma<4, 2>(c, lane, hf, w2f, acc, hoB, [&](auto S) {
      constexpr int s = S.v;
      if constexpr (LAYER == 0 && s == 13) { poll<true>(c, lane, CT_X); SCHED(); }
    });
    STAMP(K3, 4);
    g2_epi<LAYER == 1>(c, lane, acc, xr, 1, xnext + (16 + c.P) * 65536 + c.sub * 64, [&](int) {
      if (LAYER == 0) {
        if (!got<0>(32u)) wait_ge<true>(c, lane, CT_X, 32u);
        conv_load(c, lane, inA, xnext, WS_ST + 65536, 0);
        SCHED();
      }
    });
    STAMP(K3, 5);
  }
}

constexpr int KG_RS = 528, KG_X = 16 * KG_RS, KG_CH = 64 * KG_RS + 1296;
__global__ void __launch_bounds__(256, 1) main_kernel(const u16* x, const u16* lar, const u16* aim, const u16* cre,
                                                      const u16* cim, const u16* ldt, const u16* dsk, const u16* w1,
                                                      const u16* b1, const u16* w2, const u16* b2, char* ws, u16* out) {
  extern __shared__ __attribute__((aligned(16))) char smem[];
  const int t = threadIdx.x, lane = t & 63, b = blockIdx.x;
  const int w = __builtin_amdgcn_readfirstlane(t >> 6);
  const u32 junk = warm_code((b >> 3) * 4 + w, lane);
  STAMP(0, 0);
  Ctx c;
  c.rs = __builtin_amdgcn_make_buffer_rsrc(ws, 0, WS_END, 0x00020000);
  c.ct = (gu32*)(ws + WS_CT);
  c.smem = smem; c.ws = ws; c.out = out;
  c.lane0 = lane; c.w = w; c.b = b;
  c.P = (b & 7) * 2 + ((b >> 3) & 1);
  const int idx = b >> 4;
  const int sg = idx >> 1;
  c.eo = idx & 1; c.sub = idx >> 3; c.ct_ = idx & 7;
  c.ff0 = w < 3 ? 14 * sg + 6 * c.eo + 2 * w : 14 * sg + 12 + c.eo;
  c.hks = w < 3 ? 7 * sg + 3 * c.eo + w : 7 * sg + 6;
  c.f1o = w < 3 ? WS_W1P + c.ff0 * 16384 + 16384 : WS_ONE; c.f1s = w < 3 ? 4096 : 0;
  CIn inA, inB;
  {
    const int ch = w >> 1, d = 2 * b + ch, n = t & 127;
    const u16 r0 = ldt[d], r1 = lar[d * NST + n], r2 = aim[d * NST + n], r3 = cre[d * NST + n], r4 = cim[d * NST + n];
    const u16 r5 = cre[d * NST + (n ^ 64)], r6 = cim[d * NST + (n ^ 64)];
    const u32 gen = __hip_atomic_load((gu32*)(ws + WS_GEN), RLX_AGENT);
    u32x4 rv[8], rw[2];
    const bool early = !(b & 64);
    if (early) xpose_load(b & 63, x, rv);
    else if ((b & 63) < 56) pack_w1_load(b & 63, b >> 7, w1, rv);
    pack_w2_load(min(b, 223), w2, rw);
    const u32 r7 = *(const u32*)(dsk + 2 * b);
    u32 r8 = b1[min(c.ff0 * 16 + (lane & 15) + (lane >> 5) * 16, FF - 1)], r9 = b2[c.ct_ * 64 + w * 16 + (lane & 15)];
    SCHED();
    char* kg = smem + L_XA + ch * KG_CH;
    char* stg = smem + L_XA + 2 * KG_CH;
    const int i = lane & 15, g = lane >> 4;
    int ix[9];
#pragma unroll
    for (int e2 = 0; e2 < 9; ++e2) { ix[e2] = 2 * cidx(max(2047 - 8 * t - e2, 0)); asm volatile("" : "+v"(ix[e2])); }
    int wi[2][4];
    {
      const int m8 = -(i >> 3), s1 = -(w & 1);
      const int h1 = ((256 + 128 * (i >> 2) + 2 * (i & 3)) & ~m8) | ((128 * (i >> 1) + 4 * (i & 1)) & m8);
      const int hp = (((i + (i & 8) * 15) & ~s1) | (h1 & s1)) + 32 * g, hq = 1024 + 128 * i + 32 * g;
#pragma unroll
      for (int r = 0; r < 4; ++r) {
        wi[0][r] = 2 * cidx(hp + 8 * r); wi[1][r] = 2 * cidx(hq + 8 * r > 2048 ? 2064 : hq + 8 * r);
        asm volatile("" : "+v"(wi[0][r]), "+v"(wi[1][r]));
      }
    }
    if (t < 16) *(u32x4*)(smem + (t >> 2) * L_KC + 4096 + (t & 3) * 16) = u32x4{0, 0, 0, 0};
    if (t >= 64 && t < 128) *(u32x4*)(smem + L_U + ((t - 64) >> 5) * L_UC + (t & 31) * 16) = u32x4{0, 0, 0, 0};
    if (t == 128) *(u32*)(smem + L_SIG) = 0u;
    SCHED();
    STAMP(0, 1);
    const float dt = rbf(expf(bf2f(r0)));
    const float cr = bf2f(r3), ci = bf2f(r4), cr2 = bf2f(r5), ci2 = bf2f(r6);
    const float S = wave_sum(sqrt_(cr * cr + ci * ci) + sqrt_(cr2 * cr2 + ci2 * ci2)) * (2.002f * dt);
    int e = __builtin_amdgcn_frexp_expf(S);
    e = (S > 0.0f && S < 3.0e38f) ? max(-100, min(100, e)) : 14;
    const float scale = __builtin_amdgcn_ldexpf(2.0f, 14 - e);
    if (n == 0) *(float*)(smem + L_MISC + ch * 4) = __builtin_amdgcn_ldexpf(1.0f, e - 14);
    const float a2 = (-rbf(expf(bf2f(r1))) * dt) * 1.44269504088896f;
    const float br = (bf2f(r2) * dt) * 0.159154943091895f;
    {
      char* yp = kg + n * 4;
      char* xp = kg + KG_X + n * 4;
      const CM e128 = cexp_(a2, -br, 128.f), e8 = cexp_(a2, br, 8.f), e1 = cexp_(a2, -br, 1.f), e2 = cexp_(a2, -br, 2.f);
      const CM e4 = cexp_(a2, -br, 4.f);
      f32x2 f0 = {(cr * dt) * scale, -(ci * dt) * scale};
      f32x2 f = cmul(f0, cexp_(a2, -br, 512.f)), y = {1.f, 0.f};
      f32x2 g1 = cmul(f0, e128), g2 = cmul(g1, e128), g3 = cmul(g2, e128);
#define PUT(base, row, v) *(u32*)(base + (row) * KG_RS) = pack_h(v[0], v[1])
      auto chain = [&](auto K) {
        constexpr int k = K.v;
        PUT(yp, k, y);
        if (k < 15) y = cmul(y, e8);
        if (k < 4) { const f32x2 q4 = cmul(f, e4); PUT(xp, 24 + 2 * k, f); PUT(xp, 25 + 2 * k, q4); }
        else if (k < 13) PUT(xp, 28 + k, f);
        if (k < 12) f = cmul(f, e128);
        if (k < 8) { PUT(xp, k, f0); PUT(xp, 8 + k, g1); }
        if (k < 7) { f0 = cmul(f0, e1); g1 = cmul(g1, e1); }
        if (k >= 8 && k < 12) { PUT(xp, 8 + k, g2); PUT(xp, 12 + k, g3); }
        if (k >= 8 && k < 11) { g2 = cmul(g2, e2); g3 = cmul(g3, e2); }
      };
      c.tag = __builtin_amdgcn_readfirstlane(gen) * 0x9E3779B1u + 0x7F4A7C15u;
      sfor<0, 2>(chain);
      asm volatile("" : "+v"(y), "+v"(f), "+v"(f0), "+v"(g1));
      if (b < 64) xpose(b, rv, c.rs, stg);
      else if (early) row_stats(b & 63, rv, c.rs);
      asm volatile("" : "+v"(y), "+v"(f), "+v"(f0), "+v"(g1));
      sfor<2, 12>(chain);
      asm volatile("" : "+v"(y), "+v"(f));
      if (early) {
        WAIT_VM(0);
        __syncthreads();
        if (w == 0 && lane < 8)
          __hip_atomic_store((gu32*)(ws + WS_F0 + lane * 2048 + (b & 7) * 256 + ((b >> 3) & 7) * 8 + (b >> 7) * 4), c.tag, RLX_AGENT);
      }
      asm volatile("" : "+v"(y), "+v"(f));
      STAMP(0, 2);
      sfor<12, 16>(chain);
      STAMP(0, 3);
      if (!early && (b & 63) < 56) pack_w1(b & 63, b >> 7, rv, c.rs, stg);
#undef PUT
    }
    pack_w2_put(rw, smem + L_XB + 55552);
    __syncthreads();
    STAMP(0, 4);
    {
      const char* ya = kg + i * KG_RS + g * 16;
      const char* xa = kg + KG_X + (16 * (w & 1) + i) * KG_RS + g * 16;
      const char* xc = kg + KG_X + (32 + i) * KG_RS + g * 16;
      u32x4 fa[8], fb[8], fc[8];
#pragma unroll
      for (int ks = 0; ks < 8; ++ks) { fa[ks] = *(const u32x4*)(ya + ks * 64); fb[ks] = *(const u32x4*)(xa + ks * 64); }
#pragma unroll
      for (int ks = 0; ks < 8; ++ks) fc[ks] = *(const u32x4*)(xc + ks * 64);
      f32x4 ac0, ac1;
      SCHED();
      sfor<0, 8>([&](auto KS) { mma_hh<KS.v == 0>(ac0, fa[KS.v], fb[KS.v]); });
      sfor<0, 8>([&](auto KS) { mma_hh<KS.v == 0>(ac1, fa[KS.v], fc[KS.v]); });
      MFMA_DONE();
#pragma unroll
      for (int r = 0; r < 4; ++r) {
        *(u16*)(kg + 64 * KG_RS + wi[0][r]) = __builtin_bit_cast(u16, (_Float16)ac0[r]);
        *(u16*)(kg + 64 * KG_RS + wi[1][r]) = __builtin_bit_cast(u16, (_Float16)ac1[r]);
      }
    }
    if (!early) WAIT_VM(0);
    __syncthreads();
    if (w == 1 && !early && lane < 8)
      __hip_atomic_store((gu32*)(ws + WS_F1 + lane * 2048 + (b & 7) * 256 + ((b >> 3) & 7) * 8 + (b >> 7) * 4), c.tag, RLX_AGENT);
    if (w == 0) poll0(c, lane, WS_F0);
#pragma unroll
    for (int c2 = 0; c2 < 2; ++c2) {
      u32 v[9];
#pragma unroll
      for (int e2 = 0; e2 < 9; ++e2) v[e2] = *(const u16*)(smem + L_XA + c2 * KG_CH + 64 * KG_RS + ix[e2]);
      if (t == 255) v[8] = 0u;
      u32x4 oe, oo;
#pragma unroll
      for (int j = 0; j < 4; ++j) { oe[j] = v[2 * j] | (v[2 * j + 1] << 16); oo[j] = v[2 * j + 1] | (v[2 * j + 2] << 16); }
      *(u32x4*)(smem + c2 * 2 * L_KC + t * 16) = oe;
      *(u32x4*)(smem + c2 * 2 * L_KC + L_KC + t * 16) = oo;
    }
    const f32x2 ksc = *(const f32x2*)(smem + L_MISC);
    c.ks0 = __uint_as_float(__builtin_amdgcn_readfirstlane(__float_as_uint(ksc[0])));
    c.ks1 = __uint_as_float(__builtin_amdgcn_readfirstlane(__float_as_uint(ksc[1])));
    const u32 ds = __builtin_amdgcn_readfirstlane(r7);
    c.dsk0 = lo_bf(ds); c.dsk1 = hi_bf(ds);
    c.cs1 = lo_bf(r8); c.bb2 = lo_bf(r9);
    asm volatile("" :: "v"(junk));
    STAMP(0, 5);
    if (w == 0 && !got0<0>(c.tag)) wait0(c, lane, WS_F0);
    __syncthreads();
    conv_load(c, lane, inA, WS_X, WS_ST, 0);
    conv_load(c, lane, inB, WS_X, WS_ST, 1);
    pack_w2_out(b, c.rs, smem + L_XB + 55552);
    STAMP(0, 6);
  }
  u32x4 w1f[2][16], w2f[14][4];
  layer_body<0>(c, w1f, w2f, inA, inB);
  layer_body<1>(c, w1f, w2f, inA, inB);
}

extern "C" __global__ void code_end_marker() {}

extern "C" void kernel_launch(void* const* d_in, const int* in_sizes, int n_in, void* d_out, int out_size,
                              void* d_ws, size_t ws_size, hipStream_t stream) {
  (void)in_sizes; (void)n_in; (void)out_size; (void)ws_size;
  char* ws = (char*)d_ws + ((ws_size - (size_t)WS_END) & ~(size_t)(2 * MB - 1));
  (void)hipFuncSetAttribute((const void*)main_kernel, hipFuncAttributeMaxDynamicSharedMemorySize, MAIN_LDS);
  main_kernel<<<256, 256, MAIN_LDS, stream>>>((const u16*)d_in[0], (const u16*)d_in[1], (const u16*)d_in[2],
                                              (const u16*)d_in[3], (const u16*)d_in[4], (const u16*)d_in[5],
                                              (const u16*)d_in[6], (const u16*)d_in[8], (const u16*)d_in[9],
                                              (const u16*)d_in[10], (const u16*)d_in[11], ws, (u16*)d_out);
}
```

```cpp
#include <hip/hip_runtime.h>
#include <hip/hip_bf16.h>

typedef unsigned short u16;
typedef unsigned int u32;
typedef long long i64;
typedef __attribute__((ext_vector_type(4))) short s16x4;
typedef __attribute__((ext_vector_type(8))) __bf16 bf16x8;
typedef __attribute__((ext_vector_type(2))) __bf16 bf16x2;
typedef __attribute__((ext_vector_type(8))) _Float16 f16x8;
typedef __attribute__((ext_vector_type(2))) _Float16 f16x2;
typedef __attribute__((ext_vector_type(4))) float f32x4;
typedef __attribute__((ext_vector_type(2))) float f32x2;
typedef __attribute__((ext_vector_type(4))) unsigned u32x4;
typedef __attribute__((ext_vector_type(2))) unsigned u32x2;
typedef __attribute__((ext_vector_type(2))) long long i64x2;
typedef __attribute__((address_space(1))) unsigned gu32;
typedef __amdgpu_buffer_rsrc_t rsrc_t;
#define LAS __attribute__((address_space(3)))
#define DEVI __device__ __forceinline__
template <int V> struct IC { static constexpr int v = V; };
template <int I, int N, class F> __device__ __forceinline__ void sfor(F&& f) {
  if constexpr (I < N) { f(IC<I>{}); sfor<I + 1, N>(f); }
}
#define RLX_AGENT __ATOMIC_RELAXED, __HIP_MEMORY_SCOPE_AGENT

constexpr int SEQ = 2048, DM = 512, NST = 128, FF = 1792;
constexpr float LN_EPS = 1e-5f;
constexpr float FX_SCALE = 16777216.0f;
constexpr float FX_INV = 1.0f / 16777216.0f;
constexpr int SC1 = 16;

constexpr int MB = 1024 * 1024;
constexpr int WS_X = 0;
constexpr int WS_W1P = 12 * MB;
constexpr int WS_W2P = 14 * MB;
constexpr int WS_H = 16 * MB;
constexpr int WS_ST = 32 * MB;
constexpr int WS_S1 = 33 * MB;
constexpr int WS_GEN = 32 * MB + 196608;
constexpr int WS_F0 = WS_GEN + 256, WS_F1 = WS_F0 + 16384;
constexpr int WS_CT = 32 * MB + 262144;
constexpr int WS_ONE = 32 * MB + 393216;
constexpr int WS_DUMP = 32 * MB + 524288;
constexpr int WS_END = 34 * MB;
constexpr int CT_C = 0, CT_X = 128, CT_H = 256, CT_TMO = 288;
constexpr int SPIN_CAP = 1 << 16;

#define STAMP(k, i) do {} while (0)

DEVI float bf2f(u16 v) { return __uint_as_float((u32)v << 16); }
DEVI float lo_bf(u32 w) { return __uint_as_float(w << 16); }
DEVI float hi_bf(u32 w) { return __uint_as_float(w & 0xffff0000u); }
DEVI float rbf(float x) { return (float)(__bf16)x; }
DEVI u32 pack_bf(float a, float b) { f32x2 q = {a, b}; return __builtin_bit_cast(u32, __builtin_convertvector(q, bf16x2)); }
DEVI u32 pack_h(float a, float b) { f32x2 q = {a, b}; return __builtin_bit_cast(u32, __builtin_convertvector(q, f16x2)); }
DEVI float dot2bf(u32 a, u32 b, float c) {
  return __builtin_amdgcn_fdot2_f32_bf16(__builtin_bit_cast(bf16x2, a), __builtin_bit_cast(bf16x2, b), c, false);
}
DEVI f32x4 mfma_bf(u32x4 a, u32x4 b, f32x4 c) {
  return __builtin_amdgcn_mfma_f32_16x16x32_bf16(__builtin_bit_cast(bf16x8, a), __builtin_bit_cast(bf16x8, b), c, 0, 0, 0);
}
DEVI f32x4 mfma_h(u32x4 a, u32x4 b, f32x4 c) {
  return __builtin_amdgcn_mfma_f32_16x16x32_f16(__builtin_bit_cast(f16x8, a), __builtin_bit_cast(f16x8, b), c, 0, 0, 0);
}
DEVI u32x2 tr_read(const char* p) {
  return __builtin_bit_cast(u32x2, __builtin_amdgcn_ds_read_tr16_b64_v4i16((LAS s16x4*)p));
}
DEVI u32x4 cat2(u32x2 a, u32x2 b) { return __builtin_shufflevector(a, b, 0, 1, 2, 3); }
DEVI u32 lds_addr(const void* p) { return (u32)(size_t)(const LAS char*)p; }
#define WAIT_VM(n) asm volatile("s_waitcnt vmcnt(" #n ")" ::: "memory")
#define WAIT_VM_BAR(n) asm volatile("s_waitcnt vmcnt(" #n ")\n\ts_barrier" ::: "memory")
#define SCHED() __builtin_amdgcn_sched_barrier(0)
template <int CTRL> DEVI float dpp_add(float v) {
  return v + __uint_as_float(__builtin_amdgcn_update_dpp(0u, __float_as_uint(v), CTRL, 0xf, 0xf, true));
}
DEVI float sum_x16(float v) {
  auto r = __builtin_amdgcn_permlane16_swap(__float_as_uint(v), __float_as_uint(v), false, false);
  return __uint_as_float(r[0]) + __uint_as_float(r[1]);
}
DEVI float sum_x32(float v) {
  auto r = __builtin_amdgcn_permlane32_swap(__float_as_uint(v), __float_as_uint(v), false, false);
  return __uint_as_float(r[0]) + __uint_as_float(r[1]);
}
DEVI float wave_sum(float v) {
  v = dpp_add<0xB1>(v); v = dpp_add<0x4E>(v); v = dpp_add<0x141>(v); v = dpp_add<0x140>(v);
  return sum_x32(sum_x16(v));
}
DEVI float fx2f(u32 lo, u32 hi) { return fmaf((float)(int)hi, 4294967296.0f * FX_INV, (float)lo * FX_INV); }
DEVI i64 f2fx(float v) {
  const float a = v * (FX_SCALE / 4294967296.0f), h = __builtin_floorf(a);
  return __builtin_bit_cast(i64, u32x2{(u32)((a - h) * 4294967296.0f), (u32)(int)h});
}
DEVI float sel4(int i, float a, float b, float c, float d) {
  asm volatile("" : "+v"(a), "+v"(b), "+v"(c), "+v"(d));
  return (i & 2) ? ((i & 1) ? d : c) : ((i & 1) ? b : a);
}
DEVI int st_pos(int t) { return ((t >> 8) * 4 + (t & 3)) * 1024 + ((t >> 2) & 63) * 16; }
DEVI float rcp(float x) { return __builtin_amdgcn_rcpf(x); }
DEVI float sqrt_(float x) { return __builtin_amdgcn_sqrtf(x); }

DEVI u32 warm_code(int slice, int lane) {
  unsigned long long pc, pe;
  asm volatile("s_getpc_b64 %0\n\ts_getpc_b64 s[90:91]\n\ts_add_u32 s90, s90, code_end_marker@rel32@lo+4\n\t"
               "s_addc_u32 s91, s91, code_end_marker@rel32@hi+12\n\ts_mov_b64 %1, s[90:91]"
               : "=&s"(pc), "=s"(pe) :: "scc", "s90", "s91");
  const u32 off = min((u32)slice * 512u, (u32)(pe - pc) - 512u) + (lane & 7) * 64;
  u32 junk;
  asm volatile("s_nop 4\n\tglobal_load_dword %0, %1, %2" : "=v"(junk) : "v"(off), "s"(pc) : "memory");
  return junk;
}

DEVI int cidx(int l) {
  const int hb = (int)(pack_bf((float)l, 0.f) & 0xffffu) - (0x4300 - 128);
  return l < 128 ? l : hb;
}
struct CM { f32x2 e, f; };
DEVI f32x2 cmul(f32x2 a, const CM& m) { return a[1] * m.f + a[0] * m.e; }
DEVI CM cexp_(float a2, float br, float p) {
  const float e = __builtin_amdgcn_exp2f(a2 * p), f = __builtin_amdgcn_fractf(br * p);
  const float re = e * __builtin_amdgcn_cosf(f), im = e * __builtin_amdgcn_sinf(f);
  CM r; r.e = f32x2{re, im}; r.f = f32x2{-im, re};
  return r;
}
DEVI void st16(rsrc_t rs, int off, u32x4 v) { __builtin_amdgcn_raw_buffer_store_b128(v, rs, off, 0, SC1); }

DEVI void xpose(int b, const u32x4 (&v)[8], rsrc_t rs, char* smem) {
  const int t = threadIdx.x, lane = t & 63, w = t >> 6;
  constexpr int RS = 1024 + 32;
#pragma unroll
  for (int r = 0; r < 8; ++r) *(u32x4*)(smem + (w * 8 + r) * RS + lane * 16) = v[r];
  __syncthreads();
  const int g = lane >> 4, q = (lane & 15) >> 2, p = lane & 3;
#pragma unroll
  for (int cf = 0; cf < 8; ++cf) {
    const int c0 = (w * 8 + cf) * 16;
    const char* pa = smem + (8 * g + q) * RS + c0 * 2 + 8 * p;
    const u32x4 o = cat2(tr_read(pa), tr_read(pa + 4 * RS));
    st16(rs, WS_X + (b >> 1) * 65536 + (c0 + (lane & 15)) * 128 + (b & 1) * 64 + g * 16, o);
  }
}
DEVI void row_stats(int b, const u32x4 (&v)[8], rsrc_t rs) {
  const int t = threadIdx.x, lane = t & 63, w = t >> 6;
  const int t0 = b * 32;
  float a[16];
#pragma unroll
  for (int r = 0; r < 8; ++r) {
    float s = 0.f, ss = 0.f;
#pragma unroll
    for (int j = 0; j < 4; ++j) { s = dot2bf(v[r][j], 0x3f803f80u, s); ss = dot2bf(v[r][j], v[r][j], ss); }
    a[2 * r] = s; a[2 * r + 1] = ss;
  }
  const bool o1 = lane & 1, o2 = lane & 2;
#pragma unroll
  for (int k = 0; k < 8; ++k) {
    const float keep = o1 ? a[2 * k + 1] : a[2 * k], send = o1 ? a[2 * k] : a[2 * k + 1];
    a[k] = keep + __uint_as_float(__builtin_amdgcn_update_dpp(0u, __float_as_uint(send), 0xB1, 0xf, 0xf, true));
  }
#pragma unroll
  for (int k = 0; k < 4; ++k) {
    const float keep = o2 ? a[2 * k + 1] : a[2 * k], send = o2 ? a[2 * k] : a[2 * k + 1];
    a[k] = keep + __uint_as_float(__builtin_amdgcn_update_dpp(0u, __float_as_uint(send), 0x4E, 0xf, 0xf, true));
  }
#pragma unroll
  for (int k = 0; k < 4; ++k) a[k] = sum_x32(sum_x16(dpp_add<0x128>(dpp_add<0x124>(a[k]))));
  const i64 sv = lane < 16 ? f2fx(sel4(lane >> 2, a[0], a[1], a[2], a[3])) : 0;
  if (lane < 32)
    __builtin_amdgcn_raw_buffer_store_b64(__builtin_bit_cast(u32x2, sv), rs, (lane >> 4) * 65536 + st_pos(t0 + w * 8 + ((lane >> 1) & 7)) + (lane & 1) * 8, WS_ST, SC1);
  if (t < 5) __builtin_amdgcn_raw_buffer_store_b32(0u, rs, (b * 5 + t) * 256, WS_CT, SC1);
}

DEVI void pack_w1(int ks, int cg, const u32x4 (&v)[8], rsrc_t rs, char* smem) {
  const int t = threadIdx.x, lane = t & 63, w = t >> 6;
  constexpr int RS = 512 + 32;
#pragma unroll
  for (int it = 0; it < 4; ++it) *(u32x4*)(smem + (it * 8 + (t >> 5)) * RS + (t & 31) * 16) = v[it];
  __syncthreads();
  const int g = lane >> 4, q = (lane & 15) >> 2, p = lane & 3;
  float cs[4];
#pragma unroll
  for (int h = 0; h < 4; ++h) {
    const char* a = smem + (8 * g + q) * RS + (w * 4 + h) * 32 + 8 * p;
    const u32x4 o = cat2(tr_read(a), tr_read(a + 4 * RS));
    st16(rs, WS_W1P + (((cg * 16 + w * 4 + h) * 16 + ks) * 64 + lane) * 16, o);
    float s = 0.f;
#pragma unroll
    for (int j = 0; j < 4; ++j) s = dot2bf(o[j], 0x3f803f80u, s);
    cs[h] = sum_x32(sum_x16(s));
  }
  __builtin_amdgcn_raw_buffer_store_b32(__float_as_uint(sel4(g, cs[0], cs[1], cs[2], cs[3])), rs, (ks * FF + cg * 256 + t) * 4, WS_S1, SC1);
  if (ks == 0 && cg == 0) st16(rs, WS_ONE + t * 16, u32x4{0x3f803f80u, 0x3f803f80u, 0x3f803f80u, 0x3f803f80u});
}

constexpr int W2_RS = 256 + 32;
DEVI void pack_w2_put(const u32x4 (&v)[2], char* smem) {
  const int t = threadIdx.x;
#pragma unroll
  for (int r = 0; r < 2; ++r) *(u32x4*)(smem + (r * 16 + (t >> 4)) * W2_RS + (t & 15) * 16) = v[r];
}
DEVI void pack_w2_out(int b, rsrc_t rs, const char* smem) {
  const int t = threadIdx.x, lane = t & 63, w = t >> 6;
  const int g = lane >> 4, q = (lane & 15) >> 2, p = lane & 3;
#pragma unroll
  for (int cf = 0; cf < 2; ++cf) {
    const char* a = smem + (4 * g + q) * W2_RS + (w * 2 + cf) * 32 + 8 * p;
    const u32x4 o = cat2(tr_read(a), tr_read(a + 16 * W2_RS));
    st16(rs, b < 224 ? WS_W2P + ((((b & 3) * 8 + w * 2 + cf) * 56 + (b >> 2)) * 64 + lane) * 16 : 0x40000000, o);
  }
}

constexpr int L_KC = 4160;
constexpr int L_U = 4 * L_KC, L_UC = 4608;
constexpr int L_XA = 26112, L_XB = L_XA + 65536;
constexpr int L_MISC = L_XB + 65536;
constexpr int L_CST = L_MISC + 1024;
constexpr int L_SIG = L_CST + 4 * 256;
constexpr int MAIN_LDS = L_SIG + 256;
constexpr int L_RED = L_XB;

DEVI u32x4 ld128(rsrc_t rs, int v, int s) { return __builtin_amdgcn_raw_buffer_load_b128(rs, v, s, SC1); }
DEVI u32x2 ld64(rsrc_t rs, int v, int s) { return __builtin_amdgcn_raw_buffer_load_b64(rs, v, s, SC1); }
DEVI void st128(rsrc_t rs, int v, int s, u32x4 d) { __builtin_amdgcn_raw_buffer_store_b128(d, rs, v, s, SC1); }
DEVI void st64(rsrc_t rs, int v, int s, u32x2 d) { __builtin_amdgcn_raw_buffer_store_b64(d, rs, v, s, SC1); }
template <int OFF> DEVI u32x4 ld128a(rsrc_t rs, int v, int s) {
  u32x4 r;
  asm volatile("buffer_load_dwordx4 %0, %1, %2, %3 offen offset:%4 sc1" : "=a"(r) : "v"(v), "s"(rs), "s"(s), "i"(OFF) : "memory");
  return r;
}
template <int OFF> DEVI u32x4 ld128v(rsrc_t rs, int v, int s) {
  u32x4 r;
  asm volatile("buffer_load_dwordx4 %0, %1, %2, %3 offen offset:%4 sc1" : "=v"(r) : "v"(v), "s"(rs), "s"(s), "i"(OFF) : "memory");
  return r;
}
template <int OFF> DEVI void dma1k(rsrc_t rs, u32 lds_dst, int v, int soff) {
  asm volatile("s_mov_b32 m0, %0\n\ts_nop 0\n\tbuffer_load_dwordx4 %1, %2, %3 offen offset:%4 sc1 lds"
               :: "s"(lds_dst), "v"(v), "s"(rs), "s"(soff), "i"(OFF) : "memory");
}
#define MMA(NAME, OP, CA, CB)                                                                                   \
  template <bool Z> DEVI void NAME(f32x4& d, const u32x4& a, const u32x4& b) {                                  \
    if (Z) asm volatile(OP " %0, %1, %2, 0" : "=&v"(d) : CA(a), CB(b));                                         \
    else asm volatile(OP " %0, %1, %2, %0" : "+v"(d) : CA(a), CB(b));                                           \
  }
MMA(mma_vv, "v_mfma_f32_16x16x32_bf16", "v", "v")
MMA(mma_av, "v_mfma_f32_16x16x32_bf16", "a", "v")
MMA(mma_va, "v_mfma_f32_16x16x32_bf16", "v", "a")
MMA(mma_hh, "v_mfma_f32_16x16x32_f16", "v", "v")
#define MFMA_DONE() do { SCHED(); asm volatile("s_nop 7" ::: "memory"); SCHED(); } while (0)

struct Ctx {
  rsrc_t rs; gu32* ct; char* smem; char* ws; u16* out;
  int lane0, w, b, P, sub, ct_, ff0, hks, eo, f1o, f1s;
  float dsk0, dsk1, ks0, ks1, bb2, cs1;
  u32 tag;
};
DEVI int fresh_lane(const Ctx& c) { int l = c.lane0; asm volatile("" : "+v"(l)); return l; }
DEVI u32 sig_begin(const Ctx& c, int lane) {
  u32 old;
  asm volatile("ds_add_rtn_u32 %0, %1, %2" : "=v"(old) : "v"(lds_addr(c.smem) + L_SIG + lane * 4), "v"(1u) : "memory");
  return old;
}
template <bool ALL, bool BACK = false> DEVI void sig_end(const Ctx& c, int lane, u32 old, int line) {
  if (BACK) asm volatile("" : "+v"(old) :: "memory");
  else asm volatile("s_waitcnt lgkmcnt(0)" : "+v"(old) :: "memory");
  const bool last = (__builtin_amdgcn_readfirstlane(old) & 3u) == 3u;
  const int v = (last && lane < (ALL ? 8 : 1)) ? (ALL ? lane * 2048 : 0) : 0x40000000;
  asm volatile("s_nop 4\n\tbuffer_atomic_add %0, %1, %2, %3 offen" :: "v"(1u), "v"(v), "s"(c.rs),
               "s"(WS_CT + (line + (ALL ? (c.b >> 3) & 7 : 0)) * 256) : "memory");
}
template <bool ALL, int R = 0> DEVI void poll(const Ctx& c, int lane, int line) {
  const int v = ALL ? (lane & 7) * 256 : 0, so = WS_CT + (line + (ALL ? (c.b & 7) * 8 : 0)) * 256;
  if (R == 0) asm volatile("s_nop 4\n\tbuffer_load_dword v254, %0, %1, %2 offen sc1" :: "v"(v), "s"(c.rs), "s"(so) : "memory", "v254");
  else asm volatile("s_nop 4\n\tbuffer_load_dword v255, %0, %1, %2 offen sc1" :: "v"(v), "s"(c.rs), "s"(so) : "memory", "v255");
}
template <int YOUNGER, int R = 0> DEVI bool got(u32 want) {
  unsigned long long m;
  if (R == 0) asm volatile("s_waitcnt vmcnt(%2)\n\tv_cmp_le_u32_e64 %0, %1, v254" : "=s"(m) : "s"(want), "i"(YOUNGER) : "memory");
  else asm volatile("s_waitcnt vmcnt(%2)\n\tv_cmp_le_u32_e64 %0, %1, v255" : "=s"(m) : "s"(want), "i"(YOUNGER) : "memory");
  return m == ~0ull;
}
template <bool ALL> DEVI void wait_ge(const Ctx& c, int lane, int line, u32 want) {
  int sp = 0;
#pragma nounroll
  do {

    poll<ALL, 0>(c, lane, line);
    if (++sp > SPIN_CAP) { __hip_atomic_store(c.ct + CT_TMO * 64, 1u, RLX_AGENT); break; }
  } while (!got<0, 0>(want));
}

DEVI void poll0(const Ctx& c, int lane, int set) {
  asm volatile("s_nop 4\n\tbuffer_load_dwordx2 v[254:255], %0, %1, %2 offen sc1" :: "v"((lane & 7) * 256 + (lane >> 3) * 8), "s"(c.rs),
               "s"(set + (c.b & 7) * 2048) : "memory", "v254", "v255");
}
template <int YOUNGER> DEVI bool got0(u32 tag) {
  unsigned long long m, m2;
  asm volatile("s_waitcnt vmcnt(%3)\n\tv_cmp_eq_u32_e64 %0, %2, v254\n\tv_cmp_eq_u32_e64 %1, %2, v255"
               : "=&s"(m), "=s"(m2) : "s"(tag), "i"(YOUNGER) : "memory");
  return (m & m2) == ~0ull;
}
DEVI void wait0(const Ctx& c, int lane, int set) {
  int sp = 0;
#pragma nounroll
  do {
    poll0(c, lane, set);
    if (++sp > SPIN_CAP) { __hip_atomic_store(c.ct + CT_TMO * 64, 1u, RLX_AGENT); break; }
  } while (!got0<0>(c.tag));
}

struct CIn { u32x2 x0, x1; u32x4 s[4]; };
DEVI void conv_load(const Ctx& c, int lane, CIn& in, int xoff, int stoff, int h) {
  const int sx = xoff + (16 * h + 4 * c.w) * 65536 + c.b * 256;
  const int vx = (lane >> 4) * 65536 + (lane & 15) * 8;
  const int ss = stoff + (1024 * h + 256 * c.w) * 16;
#pragma unroll
  for (int j = 0; j < 4; ++j) in.s[j] = ld128(c.rs, lane * 16 + j * 1024, ss);
  in.x0 = ld64(c.rs, vx, sx);
  in.x1 = ld64(c.rs, vx + 128, sx);
}
DEVI void conv_ln(const Ctx& c, int lane, const CIn& in, int h, f32x4& base0, f32x4& base1) {
  constexpr float CH = 4294967296.0f * FX_INV / DM, CL = FX_INV / DM;
  f32x4 m, r;
#pragma unroll
  for (int j = 0; j < 4; ++j) {
    m[j] = fmaf((float)(int)in.s[j][1], CH, (float)in.s[j][0] * CL);
    r[j] = fmaf((float)(int)in.s[j][3], CH, fmaf((float)in.s[j][2], CL, LN_EPS));
  }
#pragma unroll
  for (int j = 0; j < 4; ++j) r[j] = __builtin_amdgcn_rsqf(fmaxf(fmaf(-m[j], m[j], r[j]), LN_EPS));
  const f32x4 rr = r, mr = -m * r;
  const f32x4 x0 = {lo_bf(in.x0[0]), hi_bf(in.x0[0]), lo_bf(in.x0[1]), hi_bf(in.x0[1])};
  const f32x4 x1 = {lo_bf(in.x1[0]), hi_bf(in.x1[0]), lo_bf(in.x1[1]), hi_bf(in.x1[1])};
  const f32x4 u0 = x0 * rr + mr, u1 = x1 * rr + mr;
  char* up = c.smem + L_U + 512 + (1024 * h + 256 * c.w + 4 * lane) * 2;
  *(u32x2*)up = u32x2{pack_h(u0[0], u0[1]), pack_h(u0[2], u0[3])};
  *(u32x2*)(up + L_UC) = u32x2{pack_h(u1[0], u1[1]), pack_h(u1[2], u1[3])};
  base0 = u0 * c.dsk0 + x0; base1 = u1 * c.dsk1 + x1;
}
struct KB { const char* p[4]; };
template <int MM, int LAM> DEVI u32x4 conv_k1(const KB& kb) {
  const u32* kp = (const u32*)(kb.p[(7 - LAM) >> 1] + 64 * (3 - MM) + 512 * ((7 - LAM) & 1));
  return u32x4{kp[0], kp[1], kp[2], kp[3]};
}
template <int MM, int TB> DEVI u32x4 conv_u1(const char* ub) { return *(const u32x4*)(ub + 64 * (3 - MM) + 512 * TB); }
template <int LO> DEVI void conv_ldk(const KB& kb, u32x4 (&af)[4]) {
  sfor<0, 4>([&](auto L) { af[L.v] = conv_k1<0, LO + L.v>(kb); });
}
constexpr int conv_rank(int DT, int LO, int gr, int lt) {
  int k = 0;
  for (int x = 0; x < lt; ++x) { const int tb = DT + (x & 3) - (LO + 4 * gr + (x >> 2)); k += (tb >= 0 && tb < 4); }
  return k;
}
template <int DT, int TB0, bool ZERO, class F>
DEVI void conv_mma(const char* ub, const KB& kb, f32x4 (&acc)[4], const u32x4 (&af0)[4], F&& hook) {
  constexpr int NG = DT ? 2 : 1, LO = DT ? 1 : 0;
  u32x4 af[2][4], bf[2][4];
  sfor<0, 4>([&](auto L) { af[0][L.v] = af0[L.v]; });
  sfor<0, 4>([&](auto T) { constexpr int tb = DT ? 3 - T.v : T.v; bf[0][tb] = conv_u1<0, TB0 + tb>(ub); });
  SCHED();
  sfor<0, 4 * NG>([&](auto ST) {
    constexpr int st = ST.v, mm = st / NG, gr = st % NG;
    constexpr bool more = st + 1 < 4 * NG;
    constexpr int mn = (st + 1) / NG, gn = (st + 1) % NG;
    constexpr bool nu = more && gr == NG - 1;
    sfor<0, 16>([&](auto LT) {
      constexpr int l = LT.v >> 2, T = LT.v & 3, tb = DT + T - (LO + 4 * gr + l);
      if constexpr (tb >= 0 && tb < 4) {
        constexpr int k = conv_rank(DT, LO, gr, LT.v);
        constexpr bool first = ZERO && st == 0 && (DT ? (l == T) : (l == 0));
        mma_hh<first>(acc[T], af[st & 1][l], bf[mm & 1][tb]);
        sfor<(gr == 1 ? 2 * k : k), (gr == 1 ? 2 * k + 2 : k + 1)>([&](auto J) {
          constexpr int j = J.v;
          if constexpr (nu && j < 8) {
            if constexpr (DT != 0) {
              if constexpr (j & 1) bf[mn & 1][3 - (j >> 1)] = conv_u1<mn, TB0 + 3 - (j >> 1)>(ub);
              else af[(st + 1) & 1][j >> 1] = conv_k1<mn, LO + (j >> 1)>(kb);
            } else {
              if constexpr (j >= 1 && j <= 4) bf[mn & 1][j - 1] = conv_u1<mn, TB0 + j - 1>(ub);
              else af[(st + 1) & 1][j ? j - 4 : 0] = conv_k1<mn, LO + (j ? j - 4 : 0)>(kb);
            }
          } else if constexpr (more && !nu && j < 3) af[(st + 1) & 1][j] = conv_k1<mn, LO + 4 * gn + j>(kb);
        });
        SCHED();
        hook(IC<mm * (DT ? 16 : 10) + gr * 10 + k>{});
      }
    });
  });
}
DEVI void conv_part(const Ctx& c, int lane, const f32x4 (&acc)[4]) {
  char* red = c.smem + L_RED;
#pragma unroll
  for (int T = 0; T < 4; ++T) *(f32x4*)(red + (c.w * 4 + T) * 1024 + (lane & 15) * 64 + (lane >> 4) * 16) = acc[T];
}
DEVI void conv_out(const Ctx& c, int lane, const f32x4& base0, const f32x4& base1, int xoff, int h) {
  const int sx = xoff + (16 * h + 4 * c.w) * 65536 + c.b * 256;
  const int vx = (lane >> 4) * 65536 + (lane & 15) * 8;
  const char* p = c.smem + L_RED + c.w * 1024 + lane * 16;
  const f32x4 a0 = *(const f32x4*)p, b0 = *(const f32x4*)(p + 4096), a1 = *(const f32x4*)(p + 8192), b1 = *(const f32x4*)(p + 12288);
  SCHED();
  const f32x4 y0 = (a0 + b0) * c.ks0 + base0, y1 = (a1 + b1) * c.ks1 + base1;
  st64(c.rs, vx, sx, u32x2{pack_bf(y0[0], y0[1]), pack_bf(y0[2], y0[3])});
  st64(c.rs, vx + 128, sx, u32x2{pack_bf(y1[0], y1[1]), pack_bf(y1[2], y1[3])});
}

template <int PC> DEVI void g1_dma1(const Ctx& c, int ve, int vo, int ldsx, int soff) {
  dma1k<(PC & 3) * 1024>(c.rs, lds_addr(c.smem) + ldsx + (4 * (PC >> 2) + c.w) * 4096, (PC & 1) ? vo : ve, soff + (4 * (PC >> 2) + c.w) * 4096);
}
template <bool ALL> DEVI void sig_bar(const Ctx& c, int lane, int line) {
  const int v = (c.w == 0 && lane < (ALL ? 8 : 1)) ? (ALL ? lane * 2048 : 0) : 0x40000000;
  asm volatile("s_nop 4\n\tbuffer_atomic_add %0, %1, %2, %3 offen" :: "v"(1u), "v"(v), "s"(c.rs),
               "s"(WS_CT + (line + (ALL ? (c.b >> 3) & 7 : 0)) * 256) : "memory");
}
template <class F, class G>
DEVI void g1_mma(const char* const (&xb)[4], const u32x4 (&w1f)[2][16], f32x4 (&acc)[2][4], f32x4& sa, F&& mid, G&& hook) {
  u32x2 xf[2][4][2];
#pragma unroll
  for (int i = 0; i < 4; ++i) { xf[0][i][0] = tr_read(xb[i]); xf[0][i][1] = tr_read(xb[i] + 512); }
  SCHED();
  sfor<0, 16>([&](auto KS) {
    constexpr int ks = KS.v;
    mid(KS);
    sfor<0, 4>([&](auto I) {
      constexpr int i = I.v;
      const u32x4 x = cat2(xf[ks & 1][i][0], xf[ks & 1][i][1]);
      mma_vv<ks == 0>(acc[0][i], w1f[0][ks], x);
      if constexpr (ks < 15) xf[(ks + 1) & 1][i][0] = tr_read(xb[i] + (ks + 1) * 4096);
      SCHED();
      if constexpr (ks < 8) mma_av<ks == 0>(acc[1][i], w1f[1][ks], x); else mma_vv<false>(acc[1][i], w1f[1][ks], x);
      if constexpr (ks < 15) xf[(ks + 1) & 1][i][1] = tr_read(xb[i] + (ks + 1) * 4096 + 512);
      SCHED();
      if constexpr (i == 0) { mma_vv<ks == 0>(sa, x, x); hook(IC<2 * ks>{}); SCHED(); }
      if constexpr (i == 2) { hook(IC<2 * ks + 1>{}); SCHED(); }
    });
  });
  MFMA_DONE();
}
template <class F>
DEVI void g1_epi(const Ctx& c, int lane, f32x4 (&acc)[2][4], const f32x4& sa, int hoff, F&& hook) {
  const int g = lane >> 4, cc = lane & 15, q = cc >> 2, p = lane & 3, w = c.w;
  float* sums = (float*)(c.smem + L_MISC);
  {
    const float d = sel4(p, sa[0], sa[1], sa[2], sa[3]);
    sums[q == g ? (w * 16 + cc) * 2 + 1 : 128 + lane] = d;
    const float r = sel4(g, acc[1][1][0], acc[1][2][0], acc[1][3][0], acc[1][0][0]);
    sums[w == 3 ? lane * 2 : 192 + lane] = r;
  }
  f32x4 s1v[2], b1v[2];
#pragma unroll
  for (int j = 0; j < 2; ++j) {
    s1v[j] = *(const f32x4*)(c.smem + L_CST + w * 256 + j * 128 + g * 16);
    b1v[j] = *(const f32x4*)(c.smem + L_CST + w * 256 + j * 128 + 64 + g * 16);
  }
  const int v16 = lane * 16 + (w < 3 ? 0 : 0x40000000), v8 = lane * 8 + (w < 3 ? 0x40000000 : c.eo * 512);
  __syncthreads();
  f32x2 k[4];
#pragma unroll
  for (int i = 0; i < 4; ++i) k[i] = *(const f32x2*)(sums + (((i + w) & 3) * 16 + cc) * 2);
#pragma unroll
  for (int i = 0; i < 4; ++i) {
    const float m = k[i][0] * (1.0f / DM);
    const float ve = fmaxf(fmaf(-m, m, fmaf(k[i][1], 1.0f / DM, LN_EPS)), LN_EPS);
    const float r = __builtin_amdgcn_rsqf(fmaf(ve, 1.0f + LN_EPS, -LN_EPS));
    k[i] = f32x2{r, -m * r};
  }
  SCHED();
  hook(IC<-1>{});
  SCHED();
  sfor<0, 2>([&](auto IH) {
    constexpr int ih = IH.v;
    f32x4 x[4], e[4];
#define STAGE(n) do { SCHED(); hook(IC<10 * ih + n>{}); SCHED(); } while (0)
#pragma unroll
    for (int n = 0; n < 4; ++n) x[n] = k[2 * ih + (n >> 1)][1] * s1v[n & 1] + b1v[n & 1];
#pragma unroll
    for (int n = 0; n < 4; ++n) x[n] = acc[n & 1][2 * ih + (n >> 1)] * k[2 * ih + (n >> 1)][0] + x[n];
    STAGE(0);
#pragma unroll
    for (int n = 0; n < 4; ++n) e[n] = x[n] * x[n];
#pragma unroll
    for (int n = 0; n < 4; ++n) e[n] = e[n] * (-0.0713548162726f * 1.44269504088896f) + (-1.59576912160573f * 1.44269504088896f);
    STAGE(1);
#pragma unroll
    for (int n = 0; n < 4; ++n) e[n] = e[n] * x[n];
    STAGE(2);
#pragma unroll
    for (int r = 0; r < 4; ++r) { e[0][r] = __builtin_amdgcn_exp2f(e[0][r]); e[1][r] = __builtin_amdgcn_exp2f(e[1][r]); }
    STAGE(3);
#pragma unroll
    for (int r = 0; r < 4; ++r) { e[2][r] = __builtin_amdgcn_exp2f(e[2][r]); e[3][r] = __builtin_amdgcn_exp2f(e[3][r]); }
    STAGE(4);
#pragma unroll
    for (int n = 0; n < 4; ++n) e[n] = e[n] + 1.0f;
    STAGE(5);
#pragma unroll
    for (int r = 0; r < 4; ++r) { e[0][r] = rcp(e[0][r]); e[1][r] = rcp(e[1][r]); }
    STAGE(6);
#pragma unroll
    for (int r = 0; r < 4; ++r) { e[2][r] = rcp(e[2][r]); e[3][r] = rcp(e[3][r]); }
    STAGE(7);
#pragma unroll
    for (int n = 0; n < 4; ++n) x[n] = x[n] * e[n];
    STAGE(8);
    u32x4 hv[2];
#pragma unroll
    for (int n = 0; n < 4; ++n) { hv[n >> 1][2 * (n & 1)] = pack_bf(x[n][0], x[n][1]); hv[n >> 1][2 * (n & 1) + 1] = pack_bf(x[n][2], x[n][3]); }
    STAGE(9);
    const int so0 = hoff + ((2 * ih + w) & 3) * 56 * 1024, so1 = hoff + ((2 * ih + 1 + w) & 3) * 56 * 1024;
    st128(c.rs, v16, so0, hv[0]); st128(c.rs, v16, so1, hv[1]);
    st64(c.rs, v8, so0, u32x2{hv[0][0], hv[0][1]}); st64(c.rs, v8, so1, u32x2{hv[1][0], hv[1][1]});
    SCHED();
    if constexpr (ih == 0) { hook(IC<-2>{}); SCHED(); }
#undef STAGE
  });
}

constexpr int PF = 10;
template <int N> DEVI void ld_w1(const Ctx& c, int v, u32x4 (&w1f)[2][16]) {
  constexpr int ks = N >> 1, j = N & 1;
  const int so = (j ? c.f1o + (ks >> 2) * c.f1s : WS_W1P + c.ff0 * 16384 + (ks >> 2) * 4096);
  if constexpr (j && ks < 8) w1f[j][ks] = ld128a<(ks & 3) * 1024>(c.rs, v, so);
  else w1f[j][ks] = ld128v<(ks & 3) * 1024>(c.rs, v, so);
}
template <int N> DEVI void ld_w2(const Ctx& c, int v, u32x4 (&w2f)[14][4]) {
  constexpr int s = N >> 2, j = N & 3;
  w2f[s][j] = ld128a<(s & 3) * 1024>(c.rs, v, WS_W2P + ((c.ct_ * 4 + ((c.w + j) & 3)) * 56 + 14 * c.w + (s & ~3)) * 1024);
}
template <int N> DEVI void g2_ldh1(const Ctx& c, int v16, u32x4 (&hf)[14][2], int ho) {
  constexpr int s = N >> 1, i = N & 1;
  if constexpr (s % 7 == 6)
    hf[s][i] = cat2(ld64(c.rs, (v16 >> 1) + (s & 3) * 1024, ho + i * 56 * 1024 + (s >> 2) * 4096),
                    ld64(c.rs, (v16 >> 1) + (s & 3) * 1024 + 512, ho + i * 56 * 1024 + (s >> 2) * 4096));
  else hf[s][i] = ld128(c.rs, v16 + (s & 3) * 1024, ho + i * 56 * 1024 + (s >> 2) * 4096);
}
template <int PRE, int RATE, class F>
DEVI void g2_mma(const Ctx& c, int lane, u32x4 (&hf)[14][2], const u32x4 (&w2f)[14][4], f32x4 (&acc)[2][4], int ho, F&& mid) {
  const int v16 = lane * 16;
  sfor<0, 14>([&](auto S) {
    constexpr int s = S.v;
    sfor<0, 8>([&](auto M) {
      constexpr int i = M.v >> 2, j = M.v & 3;
      mma_va<s == 0>(acc[i][j], hf[s][i], w2f[s][j]);
      constexpr int per = 4 / RATE, n = 2 * (PRE + RATE * s) + M.v / per;
      if constexpr (M.v % per == per - 1 && n < 28) { g2_ldh1<n>(c, v16, hf, ho); SCHED(); }
    });
    SCHED();
    mid(S);
  });
  MFMA_DONE();
}
template <bool LAST, class F>
DEVI void g2_epi(const Ctx& c, int lane, const f32x4 (&acc)[2][4], const u32x2 (&xr)[2], int h, int xdst, F&& mid) {
  const int g = lane >> 4, cc = lane & 15, q = cc >> 2, p = lane & 3, w = c.w, t = w * 64 + lane;
  char* red = c.smem + L_XA + h * 32768;
#pragma unroll
  for (int jj = 1; jj < 4; ++jj)
#pragma unroll
    for (int i = 0; i < 2; ++i) *(f32x4*)(red + ((((w + jj) & 3) * 3 + jj - 1) * 2 + i) * 1024 + lane * 16) = acc[i][jj];
  __syncthreads();
  char* xt = red + 24576;
  f32x4 pr[2][3];
#pragma unroll
  for (int i = 0; i < 2; ++i)
#pragma unroll
    for (int rk = 0; rk < 3; ++rk) pr[i][rk] = *(const f32x4*)(red + ((w * 3 + rk) * 2 + i) * 1024 + lane * 16);
  mid(0);
#pragma unroll
  for (int i = 0; i < 2; ++i) {
    const f32x4 xv = {lo_bf(xr[i][0]), hi_bf(xr[i][0]), lo_bf(xr[i][1]), hi_bf(xr[i][1])};
    const f32x4 o = ((acc[i][0] + (xv + c.bb2)) + pr[i][0]) + (pr[i][1] + pr[i][2]);
    const u32x2 r = {pack_bf(o[0], o[1]), pack_bf(o[2], o[3])};
    *(u32x2*)(xt + (w * 16 + cc) * 64 + ((i ^ (cc >> 3)) * 32) + g * 8) = r;
  }
  __syncthreads();
  const char* xq = xt + ((w >> 1) * 32 + 8 * g + q) * 64 + (((w & 1) ^ (g & 1)) * 32) + 8 * p;
  const u32x4 f0 = cat2(tr_read(xq), tr_read(xq + 256));
  const int tok = (16 * h + c.P) * 64 + c.sub * 32 + (w & 1) * 16 + cc;
  if (LAST) {
    *(u32x4*)(c.out + (size_t)tok * DM + c.ct_ * 64 + (w >> 1) * 32 + 8 * g) = f0;
  } else {
    const int row = t >> 2;
    const u32x4 v = *(const u32x4*)(xt + row * 64 + (((t & 3) ^ ((row >> 2) & 2)) * 16));
    st128(c.rs, (c.ct_ * 64 + row) * 128 + (t & 3) * 16, xdst, v);
    const char* xo = xq + ((w & 2) ? -2048 : 2048);
    const u32x4 f1 = cat2(tr_read(xo), tr_read(xo + 256));
    float s = 0.f, ss = 0.f, s2 = 0.f, ss2 = 0.f;
#pragma unroll
    for (int j = 0; j < 4; ++j) {
      s = dot2bf(f0[j], 0x3f803f80u, s); ss = dot2bf(f0[j], f0[j], ss);
      s2 = dot2bf(f1[j], 0x3f803f80u, s2); ss2 = dot2bf(f1[j], f1[j], ss2);
    }
    s = sum_x32(sum_x16(s + s2)); ss = sum_x32(sum_x16(ss + ss2));
    const u32 ao = t < 128 && lane < 32 ? WS_ST + 65536 + st_pos(tok) + g * 8 : WS_DUMP + (c.b * 256 + t) * 8;
    (void)__hip_atomic_fetch_add((unsigned long long*)(c.ws + ao), (unsigned long long)f2fx((g & 1) ? ss : s), RLX_AGENT);
  }
}

template <int LAYER>
DEVI void layer_body(const Ctx& c, u32x4 (&w1f)[2][16], u32x4 (&w2f)[14][4], CIn& inA, CIn& inB) {
  constexpr int xin = WS_X + LAYER * 4 * MB, xmid = xin + 2 * MB, xnext = xin + 4 * MB;
  constexpr int hbuf = WS_H + LAYER * 8 * MB, stin = WS_ST + LAYER * 65536;
  constexpr u32 ep = LAYER + 1;
  constexpr int K1 = 1 + 3 * LAYER, K2 = K1 + 1, K3 = K1 + 2;
  const int w = c.w;
  char* smem = c.smem;
  auto nop = [](auto) {};
  {
    const int lane = fresh_lane(c);
    const int g = lane >> 4, cc = lane & 15, mu0 = (w & 1) * 4, v16 = lane * 16;
    const char* ub = smem + L_U + (w >> 1) * L_UC + 512 + 2 * (16 * cc - 16 + 8 * g) - 64 * (mu0 + 3);
    KB kb;
#pragma unroll
    for (int j = 0; j < 4; ++j) {
      u32 a = lds_addr(smem) + (w >> 1) * 2 * L_KC + ((cc & 1) ? 0 : L_KC) + 2 * (2031 - cc + 8 * g - ((cc & 1) ? 0 : 1)) - 64 * (mu0 + 3 + 56) + 1024 * j;
      asm volatile("" : "+v"(a));
      kb.p[j] = (const char*)(const LAS char*)(size_t)a;
    }
    STAMP(K1, 1);
    f32x4 base0, base1;
    u32x4 a4[4];
    conv_ldk<0>(kb, a4);
    conv_ln(c, lane, inA, 0, base0, base1);
    if (!LAYER) poll0(c, lane, WS_F1);
    __syncthreads();
    STAMP(K1, 2);
    f32x4 acc[4];
    u32 o = 0;
    conv_mma<0, 0, true>(ub, kb, acc, a4, [&](auto N) {
      constexpr int n = N.v;
      if constexpr (LAYER && n == 19) { WAIT_VM(0); o = sig_begin(c, lane); SCHED(); }
      if constexpr (LAYER && n == 30) { sig_end<true, true>(c, lane, o, CT_X + 64); SCHED(); }
    });
    MFMA_DONE();
    conv_part(c, lane, acc);
    conv_ldk<1>(kb, a4);
    __syncthreads();
    float s1p[8];
    if (!LAYER) {
      if (!got0<0>(c.tag)) wait0(c, lane, WS_F1);
      asm volatile("" :: "v"(inB.x1));
      const int so1 = (min(c.ff0 * 16 + (lane & 15) + (lane >> 5) * 16, FF - 1) + (lane & 16) * (FF / 2)) * 4;
#pragma unroll
      for (int j = 0; j < 8; ++j)
        asm volatile("s_nop 4\n\tbuffer_load_dword %0, %1, %2, %3 offen sc1" : "=v"(s1p[j]) : "v"(so1), "s"(c.rs), "s"(WS_S1 + j * FF * 4) : "memory");
    }
    conv_out(c, lane, base0, base1, xmid, 0);
    STAMP(K1, 3);
    f32x4 acb[4];
    conv_mma<4, 0, true>(ub, kb, acb, a4, [&](auto N) {
      constexpr int n = N.v;
      if constexpr (!LAYER && n >= 48 && (n & 1)) { ld_w1<(n - 48) / 2>(c, v16, w1f); SCHED(); }
      if constexpr (n == 47) {
        WAIT_VM(0);
        o = sig_begin(c, lane);
        if (LAYER) poll<true>(c, lane, CT_X + 64);
        SCHED();
      }
      if constexpr (n == 58) { sig_end<true, true>(c, lane, o, CT_C); SCHED(); }
    });
    STAMP(K1, 4);
    if (!LAYER) {
#pragma unroll
      for (int j = 0; j < 8; ++j) asm volatile("" : "+v"(s1p[j]));
      const float sv = sum_x16(((s1p[0] + s1p[1]) + (s1p[2] + s1p[3])) + ((s1p[4] + s1p[5]) + (s1p[6] + s1p[7])));
      ((float*)(smem + L_CST))[w * 64 + lane] = (lane & 16) ? c.cs1 : sv;
    }
    if (LAYER) {
      if (!got<1>(32u)) wait_ge<true>(c, lane, CT_X + 64, 32u);
      conv_load(c, lane, inB, xin, stin, 1);
    }
    conv_ldk<0>(kb, a4);
    STAMP(K1, 5);
    if (!LAYER) sfor<8, 12>([&](auto N) { ld_w1<N.v>(c, v16, w1f); });
    conv_ln(c, lane, inB, 1, base0, base1);
    if (!LAYER) sfor<12, 14>([&](auto N) { ld_w1<N.v>(c, v16, w1f); });
    __syncthreads();
    conv_mma<0, 4, false>(ub, kb, acb, a4, [&](auto N) {
      constexpr int n = N.v;
      if constexpr (!LAYER && n % 3 == 1 && n < 36) { ld_w1<14 + n / 3>(c, v16, w1f); SCHED(); }
      if constexpr (n == 30) { poll<true>(c, lane, CT_C); SCHED(); }
    });
    MFMA_DONE();
    conv_part(c, lane, acb);
    __syncthreads();
    conv_out(c, lane, base0, base1, xmid, 1);
    STAMP(K1, 6);
    if (!got<(LAYER ? 2 : 4)>(32u * ep)) wait_ge<true>(c, lane, CT_C, 32u * ep);
    const int row = lane >> 3, sw = ((row >> 1) & 1) << 1;
    const int ve = row * 128 + ((lane & 7) ^ sw) * 16, vo = row * 128 + ((lane & 7) ^ (sw | 4)) * 16;
    if (!LAYER && c.b == 0 && w == 0) __hip_atomic_store((gu32*)(c.ws + WS_GEN), c.tag, RLX_AGENT);
    sfor<0, 8>([&](auto N) { g1_dma1<N.v>(c, ve, vo, L_XA, xmid + c.P * 65536); });
    WAIT_VM(8);
    o = sig_begin(c, lane);
    sig_end<true>(c, lane, o, CT_C + 64);
    STAMP(K1, 7);
  }
  u32x4 hf[14][2];
  u32x2 xr[2];
  {
    const int lane = fresh_lane(c);
    const int g = lane >> 4, cc = lane & 15, q = cc >> 2, p = lane & 3, v16 = lane * 16;
    const int sx = ((q >> 1) & 1) | ((g & 1) << 1);
    const char* xb[4];
#pragma unroll
    for (int i = 0; i < 4; ++i) xb[i] = smem + L_XA + (8 * g + q) * 128 + ((((i + w) & 3) ^ sx) * 32) + 8 * p;
    const int row = lane >> 3, sw = ((row >> 1) & 1) << 1;
    const int ve = row * 128 + ((lane & 7) ^ sw) * 16, vo = row * 128 + ((lane & 7) ^ (sw | 4)) * 16;
    sfor<8, 12>([&](auto N) { g1_dma1<N.v>(c, ve, vo, L_XA, xmid + c.P * 65536); });
    WAIT_VM_BAR(9);
    STAMP(K2, 0);
    f32x4 acc[2][4], sa;
    g1_mma(xb, w1f, acc, sa, [&](auto KS) {
      constexpr int ks = KS.v;
      if constexpr (ks == 3) { if (LAYER) WAIT_VM_BAR(8); else WAIT_VM_BAR(10); SCHED(); }
      if constexpr (ks == 7) { if (LAYER) WAIT_VM_BAR(0); else WAIT_VM_BAR(10); SCHED(); }
      if constexpr (ks == 13) { if (!LAYER) WAIT_VM(16); poll<true>(c, lane, CT_C + 64); SCHED(); }
    }, [&](auto N) {
      constexpr int n = N.v;
      if constexpr (n < 4) g1_dma1<12 + n>(c, ve, vo, L_XA, xmid + c.P * 65536);
      if constexpr (!LAYER && n >= 4 && n < 10) ld_w1<22 + n>(c, v16, w1f);
      if constexpr (!LAYER && n >= 10) ld_w2<n - 10>(c, v16, w2f);
    });
    STAMP(K2, 1);
    const int xo = xmid + (16 + c.P) * 65536;
    g1_epi(c, lane, acc, sa, hbuf + (c.P * 4 * 56 + c.hks) * 1024, [&](auto N) {
      constexpr int n = N.v;
      if constexpr (n == -1) { if (!got<(LAYER ? 0 : 6)>(32u * ep)) wait_ge<true>(c, lane, CT_C + 64, 32u * ep); }
      if constexpr (n >= 0 && n < 8) g1_dma1<n>(c, ve, vo, L_XB, xo);
      if constexpr (n >= 10 && n < 18) g1_dma1<n - 2>(c, ve, vo, L_XB, xo);
    });
    STAMP(K2, 2);
  }
  {
    const int lane = fresh_lane(c);
    const int g = lane >> 4, cc = lane & 15, q = cc >> 2, p = lane & 3, v16 = lane * 16;
    const int sx = ((q >> 1) & 1) | ((g & 1) << 1);
    const char* xb[4];
#pragma unroll
    for (int i = 0; i < 4; ++i) xb[i] = smem + L_XB + (8 * g + q) * 128 + ((((i + w) & 3) ^ sx) * 32) + 8 * p;
    WAIT_VM_BAR(16);
    STAMP(K2, 3);
    f32x4 acc[2][4], sa;
    g1_mma(xb, w1f, acc, sa, [&](auto KS) {
      constexpr int ks = KS.v;
      if constexpr (ks == 6) {
        if (LAYER) WAIT_VM_BAR(0); else WAIT_VM_BAR(12);
        sig_bar<false>(c, lane, CT_H + c.P);
        SCHED();
      }
      if constexpr (ks == 15) {
#pragma unroll
        for (int i = 0; i < 2; ++i) xr[i] = ld64(c.rs, (c.ct_ * 64 + w * 16 + cc) * 128 + i * 32 + g * 8, xmid + c.P * 65536 + c.sub * 64);
        SCHED();
      }
    }, [&](auto N) {
      constexpr int n = N.v;
      if constexpr (!LAYER) ld_w2<22 + n>(c, v16, w2f);
      if constexpr (!LAYER && n >= 30) ld_w2<24 + n>(c, v16, w2f);
    });
    STAMP(K2, 4);
    poll<false>(c, lane, CT_H + c.P);
    const int ho = hbuf + ((c.P * 4 + c.sub * 2) * 56 + 14 * w) * 1024;
    g1_epi(c, lane, acc, sa, hbuf + ((16 + c.P) * 4 * 56 + c.hks) * 1024, [&](auto N) {
      constexpr int n = N.v;
      if constexpr (n == -1) { if (!got<0>(16u * ep)) wait_ge<false>(c, lane, CT_H + c.P, 16u * ep); }
      if constexpr (n >= 0) g2_ldh1<n>(c, v16, hf, ho);
    });
    STAMP(K2, 5);
  }
  {
    const int lane = fresh_lane(c);
    const int g = lane >> 4, cc = lane & 15;
    const int ho = hbuf + ((c.P * 4 + c.sub * 2) * 56 + 14 * w) * 1024, hoB = ho + 64 * 56 * 1024;
    STAMP(K3, 0);
    f32x4 acc[2][4];
    u32 o = 0;
    g2_mma<10, 1>(c, lane, hf, w2f, acc, ho, [&](auto S) {
      constexpr int s = S.v;
      if constexpr (s == 6) { WAIT_VM(10); o = sig_begin(c, lane); SCHED(); }
      if constexpr (s == 7) { sig_end<false>(c, lane, o, CT_H + 16 + c.P); SCHED(); }
    });
    STAMP(K3, 1);
    poll<false>(c, lane, CT_H + 16 + c.P);
    g2_epi<LAYER == 1>(c, lane, acc, xr, 0, xnext + c.P * 65536 + c.sub * 64, nop);
    STAMP(K3, 2);
    if (!got<(LAYER ? 1 : 2)>(16u * ep)) wait_ge<false>(c, lane, CT_H + 16 + c.P, 16u * ep);
    sfor<0, 8>([&](auto N) { g2_ldh1<N.v>(c, lane * 16, hf, hoB); });
#pragma unroll
    for (int i = 0; i < 2; ++i) xr[i] = ld64(c.rs, (c.ct_ * 64 + w * 16 + cc) * 128 + i * 32 + g * 8, xmid + (16 + c.P) * 65536 + c.sub * 64);
    if (LAYER == 0) {
      WAIT_VM(10);
      o = sig_begin(c, lane);
      sig_end<true>(c, lane, o, CT_X);
    }
    SCHED();
    STAMP(K3, 3);
  }
  {
    const int lane = fresh_lane(c);
    const int hoB = hbuf + (((16 + c.P) * 4 + c.sub * 2) * 56 + 14 * w) * 1024;
    f32x4 acc[2][4];
    g2_mma<4, 2>(c, lane, hf, w2f, acc, hoB, [&](auto S) {
      constexpr int s = S.v;
      if constexpr (LAYER == 0 && s == 13) { poll<true>(c, lane, CT_X); SCHED(); }
    });
    STAMP(K3, 4);
    g2_epi<LAYER == 1>(c, lane, acc, xr, 1, xnext + (16 + c.P) * 65536 + c.sub * 64, [&](int) {
      if (LAYER == 0) {
        if (!got<0>(32u)) wait_ge<true>(c, lane, CT_X, 32u);
        conv_load(c, lane, inA, xnext, WS_ST + 65536, 0);
        SCHED();
      }
    });
    STAMP(K3, 5);
  }
}

constexpr int KG_RS = 528, KG_X = 16 * KG_RS, KG_CH = 64 * KG_RS + 1296;
__global__ void __launch_bounds__(256, 1) main_kernel(const u16* x, const u16* lar, const u16* aim, const u16* cre,
                                                      const u16* cim, const u16* ldt, const u16* dsk, const u16* w1,
                                                      const u16* b1, const u16* w2, const u16* b2, char* ws, u16* out) {
  extern __shared__ __attribute__((aligned(16))) char smem[];
  const int t = threadIdx.x, lane = t & 63, b = blockIdx.x;
  const int w = __builtin_amdgcn_readfirstlane(t >> 6);
  const u32 junk = warm_code((b >> 3) * 4 + w, lane);
  STAMP(0, 0);
  const int ch = w >> 1, d = 2 * b + ch, n = t & 127;
  const u32 po = (u32)(d * NST + n) * 2, po2 = po ^ 128u;
#define LD16(p, off) (*(const u16*)((const char*)(p) + (u32)(off)))
  const u16 r0 = ldt[d], r1 = LD16(lar, po), r2 = LD16(aim, po), r3 = LD16(cre, po), r4 = LD16(cim, po);
  const u16 r5 = LD16(cre, po2), r6 = LD16(cim, po2);
  const u32 gen = __hip_atomic_load((gu32*)(ws + WS_GEN), RLX_AGENT);
  const bool early = !(b & 64);
  const int pks = (b & 7) + (b >> 7) * 8, pcg = (b >> 3) & 7;
  u32x4 rv[8], rw[2];
  {
    const rsrc_t rr = __builtin_amdgcn_make_buffer_rsrc((void*)(early ? x : w1), 0, early ? SEQ * DM * 2 : DM * FF * 2, 0x00020000);
    const int vo = early ? w * 8192 + lane * 16 : ((b & 63) < 56 ? (t >> 5) * (FF * 2) + (t & 31) * 16 : 0x40000000);
    const int so = early ? (b & 63) * 32768 : pks * (32 * FF * 2) + min(pcg, 6) * 512, st = early ? 1024 : 8 * FF * 2;
#pragma unroll
    for (int j = 0; j < 8; ++j) rv[j] = __builtin_amdgcn_raw_buffer_load_b128(rr, (j < 4 || early) ? vo : 0x40000000, so + (j & 3) * st + (j >> 2) * (early ? 4096 : 0), 0);
    const rsrc_t r2w = __builtin_amdgcn_make_buffer_rsrc((void*)w2, 0, DM * FF * 2, 0x00020000);
#pragma unroll
    for (int j = 0; j < 2; ++j) rw[j] = __builtin_amdgcn_raw_buffer_load_b128(r2w, (t >> 4) * 1024 + (t & 15) * 16 + j * 16384, min(b >> 2, 55) * 32768 + (b & 3) * 256, 0);
  }
  CIn inA, inB;
  Ctx c;
  c.rs = __builtin_amdgcn_make_buffer_rsrc(ws, 0, WS_END, 0x00020000);
  c.ct = (gu32*)(ws + WS_CT);
  c.smem = smem; c.ws = ws; c.out = out;
  c.lane0 = lane; c.w = w; c.b = b;
  c.P = (b & 7) * 2 + ((b >> 3) & 1);
  const int idx = b >> 4;
  const int sg = idx >> 1, w3 = -(int)(w == 3);
  c.eo = idx & 1; c.sub = idx >> 3; c.ct_ = idx & 7;
  c.ff0 = 14 * sg + (((6 * c.eo + 2 * w) & ~w3) | ((12 + c.eo) & w3));
  c.hks = 7 * sg + (((3 * c.eo + w) & ~w3) | (6 & w3));
  c.f1o = w < 3 ? WS_W1P + c.ff0 * 16384 + 16384 : WS_ONE; c.f1s = w < 3 ? 4096 : 0;
  {
    const u32 r7 = *(const u32*)(dsk + 2 * b);
    u32 r8 = LD16(b1, min(c.ff0 * 16 + (lane & 15) + (lane >> 5) * 16, FF - 1) * 2), r9 = LD16(b2, (c.ct_ * 64 + w * 16 + (lane & 15)) * 2);
    SCHED();
    char* kg = smem + L_XA + ch * KG_CH;
    char* stg = smem + L_XA + 2 * KG_CH;
    const int i = lane & 15, g = lane >> 4;
    int ix[9];
#pragma unroll
    for (int e2 = 0; e2 < 9; ++e2) { ix[e2] = 2 * cidx(max(2047 - 8 * t - e2, 0)); asm volatile("" : "+v"(ix[e2])); }
    int wi[2][4];
    {
      const int m8 = -(i >> 3), s1 = -(w & 1);
      const int h1 = ((256 + 128 * (i >> 2) + 2 * (i & 3)) & ~m8) | ((128 * (i >> 1) + 4 * (i & 1)) & m8);
      const int hp = (((i + (i & 8) * 15) & ~s1) | (h1 & s1)) + 32 * g, hq = 1024 + 128 * i + 32 * g;
#pragma unroll
      for (int r = 0; r < 4; ++r) {
        wi[0][r] = 2 * cidx(hp + 8 * r); wi[1][r] = 2 * cidx(hq + 8 * r > 2048 ? 2064 : hq + 8 * r);
        asm volatile("" : "+v"(wi[0][r]), "+v"(wi[1][r]));
      }
    }
    if (t < 16) *(u32x4*)(smem + (t >> 2) * L_KC + 4096 + (t & 3) * 16) = u32x4{0, 0, 0, 0};
    if (t >= 64 && t < 128) *(u32x4*)(smem + L_U + ((t - 64) >> 5) * L_UC + (t & 31) * 16) = u32x4{0, 0, 0, 0};
    if (t == 128) *(u32*)(smem + L_SIG) = 0u;
    SCHED();
    STAMP(0, 1);
    const float dt = rbf(expf(bf2f(r0)));
    const float cr = bf2f(r3), ci = bf2f(r4), cr2 = bf2f(r5), ci2 = bf2f(r6);
    const float S = wave_sum(sqrt_(cr * cr + ci * ci) + sqrt_(cr2 * cr2 + ci2 * ci2)) * (2.002f * dt);
    int e = __builtin_amdgcn_frexp_expf(S);
    e = (S > 0.0f && S < 3.0e38f) ? max(-100, min(100, e)) : 14;
    const float scale = __builtin_amdgcn_ldexpf(2.0f, 14 - e);
    if (n == 0) *(float*)(smem + L_MISC + ch * 4) = __builtin_amdgcn_ldexpf(1.0f, e - 14);
    const float a2 = (-rbf(expf(bf2f(r1))) * dt) * 1.44269504088896f;
    const float br = (bf2f(r2) * dt) * 0.159154943091895f;
    {
      char* yp = kg + n * 4;
      char* xp = kg + KG_X + n * 4;
      const CM e128 = cexp_(a2, -br, 128.f), e8 = cexp_(a2, br, 8.f), e1 = cexp_(a2, -br, 1.f), e2 = cexp_(a2, -br, 2.f);
      const CM e4 = cexp_(a2, -br, 4.f);
      f32x2 f0 = {(cr * dt) * scale, -(ci * dt) * scale};
      f32x2 f = cmul(f0, cexp_(a2, -br, 512.f)), y = {1.f, 0.f};
      f32x2 g1 = cmul(f0, e128), g2 = cmul(g1, e128), g3 = cmul(g2, e128);
#define PUT(base, row, v) *(u32*)(base + (row) * KG_RS) = pack_h(v[0], v[1])
      auto chain = [&](auto K) {
        constexpr int k = K.v;
        PUT(yp, k, y);
        if (k < 15) y = cmul(y, e8);
        if (k < 4) { const f32x2 q4 = cmul(f, e4); PUT(xp, 24 + 2 * k, f); PUT(xp, 25 + 2 * k, q4); }
        else if (k < 13) PUT(xp, 28 + k, f);
        if (k < 12) f = cmul(f, e128);
        if (k < 8) { PUT(xp, k, f0); PUT(xp, 8 + k, g1); }
        if (k < 7) { f0 = cmul(f0, e1); g1 = cmul(g1, e1); }
        if (k >= 8 && k < 12) { PUT(xp, 8 + k, g2); PUT(xp, 12 + k, g3); }
        if (k >= 8 && k < 11) { g2 = cmul(g2, e2); g3 = cmul(g3, e2); }
      };
      c.tag = __builtin_amdgcn_readfirstlane(gen) * 0x9E3779B1u + 0x7F4A7C15u;
      sfor<0, 2>(chain);
      asm volatile("" : "+v"(y), "+v"(f), "+v"(f0), "+v"(g1));
      if (b < 64) xpose(b, rv, c.rs, stg);
      else if (early) row_stats(b & 63, rv, c.rs);
      asm volatile("" : "+v"(y), "+v"(f), "+v"(f0), "+v"(g1));
      sfor<2, 12>(chain);
      asm volatile("" : "+v"(y), "+v"(f));
      if (early) {
        WAIT_VM(0);
        __syncthreads();
        if (w == 0 && lane < 8)
          __hip_atomic_store((gu32*)(ws + WS_F0 + lane * 2048 + (b & 7) * 256 + ((b >> 3) & 7) * 8 + (b >> 7) * 4), c.tag, RLX_AGENT);
      }
      asm volatile("" : "+v"(y), "+v"(f));
      STAMP(0, 2);
      sfor<12, 16>(chain);
      STAMP(0, 3);
      if (!early && pcg < 7) pack_w1(pks, pcg, rv, c.rs, stg);
#undef PUT
    }
    pack_w2_put(rw, smem + L_XB + 55552);
    __syncthreads();
    STAMP(0, 4);
    {
      const char* ya = kg + i * KG_RS + g * 16;
      const char* xa = kg + KG_X + (16 * (w & 1) + i) * KG_RS + g * 16;
      const char* xc = kg + KG_X + (32 + i) * KG_RS + g * 16;
      u32x4 fa[8], fb[8], fc[8];
#pragma unroll
      for (int ks = 0; ks < 8; ++ks) { fa[ks] = *(const u32x4*)(ya + ks * 64); fb[ks] = *(const u32x4*)(xa + ks * 64); }
#pragma unroll
      for (int ks = 0; ks < 8; ++ks) fc[ks] = *(const u32x4*)(xc + ks * 64);
      f32x4 ac0, ac1;
      SCHED();
      sfor<0, 8>([&](auto KS) { mma_hh<KS.v == 0>(ac0, fa[KS.v], fb[KS.v]); });
      sfor<0, 8>([&](auto KS) { mma_hh<KS.v == 0>(ac1, fa[KS.v], fc[KS.v]); });
      MFMA_DONE();
#pragma unroll
      for (int r = 0; r < 4; ++r) {
        *(u16*)(kg + 64 * KG_RS + wi[0][r]) = __builtin_bit_cast(u16, (_Float16)ac0[r]);
        *(u16*)(kg + 64 * KG_RS + wi[1][r]) = __builtin_bit_cast(u16, (_Float16)ac1[r]);
      }
    }
    if (!early) WAIT_VM(0);
    __syncthreads();
    if (w == 1 && !early && lane < 8)
      __hip_atomic_store((gu32*)(ws + WS_F1 + lane * 2048 + (b & 7) * 256 + ((b >> 3) & 7) * 8 + (b >> 7) * 4), c.tag, RLX_AGENT);
    if (w == 0) poll0(c, lane, WS_F0);
#pragma unroll
    for (int c2 = 0; c2 < 2; ++c2) {
      u32 v[9];
#pragma unroll
      for (int e2 = 0; e2 < 9; ++e2) v[e2] = *(const u16*)(smem + L_XA + c2 * KG_CH + 64 * KG_RS + ix[e2]);
      if (t == 255) v[8] = 0u;
      u32x4 oe, oo;
#pragma unroll
      for (int j = 0; j < 4; ++j) { oe[j] = v[2 * j] | (v[2 * j + 1] << 16); oo[j] = v[2 * j + 1] | (v[2 * j + 2] << 16); }
      *(u32x4*)(smem + c2 * 2 * L_KC + t * 16) = oe;
      *(u32x4*)(smem + c2 * 2 * L_KC + L_KC + t * 16) = oo;
    }
    const f32x2 ksc = *(const f32x2*)(smem + L_MISC);
    c.ks0 = __uint_as_float(__builtin_amdgcn_readfirstlane(__float_as_uint(ksc[0])));
    c.ks1 = __uint_as_float(__builtin_amdgcn_readfirstlane(__float_as_uint(ksc[1])));
    const u32 ds = __builtin_amdgcn_readfirstlane(r7);
    c.dsk0 = lo_bf(ds); c.dsk1 = hi_bf(ds);
    c.cs1 = lo_bf(r8); c.bb2 = lo_bf(r9);
    asm volatile("" :: "v"(junk));
    STAMP(0, 5);
    if (w == 0 && !got0<0>(c.tag)) wait0(c, lane, WS_F0);
    __syncthreads();
    conv_load(c, lane, inA, WS_X, WS_ST, 0);
    conv_load(c, lane, inB, WS_X, WS_ST, 1);
    pack_w2_out(b, c.rs, smem + L_XB + 55552);
    STAMP(0, 6);
  }
  u32x4 w1f[2][16], w2f[14][4];
  layer_body<0>(c, w1f, w2f, inA, inB);
  layer_body<1>(c, w1f, w2f, inA, inB);
}

extern "C" __global__ void code_end_marker() {}

extern "C" void kernel_launch(void* const* d_in, const int* in_sizes, int n_in, void* d_out, int out_size,
                              void* d_ws, size_t ws_size, hipStream_t stream) {
  (void)in_sizes; (void)n_in; (void)out_size; (void)ws_size;
  char* ws = (char*)d_ws + ((ws_size - (size_t)WS_END) & ~(size_t)(2 * MB - 1));
  (void)hipFuncSetAttribute((const void*)main_kernel, hipFuncAttributeMaxDynamicSharedMemorySize, MAIN_LDS);
  main_kernel<<<256, 256, MAIN_LDS, stream>>>((const u16*)d_in[0], (const u16*)d_in[1], (const u16*)d_in[2],
                                              (const u16*)d_in[3], (const u16*)d_in[4], (const u16*)d_in[5],
                                              (const u16*)d_in[6], (const u16*)d_in[8], (const u16*)d_in[9],
                                              (const u16*)d_in[10], (const u16*)d_in[11], ws, (u16*)d_out);
}
```

```cpp
#include <hip/hip_runtime.h>
#include <hip/hip_bf16.h>

typedef unsigned short u16;
typedef unsigned int u32;
typedef long long i64;
typedef __attribute__((ext_vector_type(4))) short s16x4;
typedef __attribute__((ext_vector_type(8))) __bf16 bf16x8;
typedef __attribute__((ext_vector_type(2))) __bf16 bf16x2;
typedef __attribute__((ext_vector_type(8))) _Float16 f16x8;
typedef __attribute__((ext_vector_type(2))) _Float16 f16x2;
typedef __attribute__((ext_vector_type(4))) float f32x4;
typedef __attribute__((ext_vector_type(2))) float f32x2;
typedef __attribute__((ext_vector_type(4))) unsigned u32x4;
typedef __attribute__((ext_vector_type(2))) unsigned u32x2;
typedef __attribute__((ext_vector_type(2))) long long i64x2;
typedef __attribute__((address_space(1))) unsigned gu32;
typedef __amdgpu_buffer_rsrc_t rsrc_t;
#define LAS __attribute__((address_space(3)))
#define DEVI __device__ __forceinline__
template <int V> struct IC { static constexpr int v = V; };
template <int I, int N, class F> __device__ __forceinline__ void sfor(F&& f) {
  if constexpr (I < N) { f(IC<I>{}); sfor<I + 1, N>(f); }
}
#define RLX_AGENT __ATOMIC_RELAXED, __HIP_MEMORY_SCOPE_AGENT

constexpr int SEQ = 2048, DM = 512, NST = 128, FF = 1792;
constexpr float LN_EPS = 1e-5f;
constexpr float FX_SCALE = 16777216.0f;
constexpr float FX_INV = 1.0f / 16777216.0f;
constexpr int SC1 = 16;

constexpr int MB = 1024 * 1024;
constexpr int WS_X = 0;
constexpr int WS_W1P = 12 * MB;
constexpr int WS_W2P = 14 * MB;
constexpr int WS_H = 16 * MB;
constexpr int WS_ST = 32 * MB;
constexpr int WS_S1 = 33 * MB;
constexpr int WS_GEN = 32 * MB + 196608;
constexpr int WS_F0 = WS_GEN + 256, WS_F1 = WS_F0 + 16384;
constexpr int WS_CT = 32 * MB + 262144;
constexpr int WS_ONE = 32 * MB + 393216;
constexpr int WS_DUMP = 32 * MB + 524288;
constexpr int WS_END = 34 * MB;
constexpr int CT_C = 0, CT_X = 128, CT_H = 256, CT_TMO = 288;
constexpr int SPIN_CAP = 1 << 16;

#define STAMP(k, i) do {} while (0)

DEVI float bf2f(u16 v) { return __uint_as_float((u32)v << 16); }
DEVI float lo_bf(u32 w) { return __uint_as_float(w << 16); }
DEVI float hi_bf(u32 w) { return __uint_as_float(w & 0xffff0000u); }
DEVI float rbf(float x) { return (float)(__bf16)x; }
DEVI u32 pack_bf(float a, float b) { f32x2 q = {a, b}; return __builtin_bit_cast(u32, __builtin_convertvector(q, bf16x2)); }
DEVI u32 pack_h(float a, float b) { f32x2 q = {a, b}; return __builtin_bit_cast(u32, __builtin_convertvector(q, f16x2)); }
DEVI float dot2bf(u32 a, u32 b, float c) {
  return __builtin_amdgcn_fdot2_f32_bf16(__builtin_bit_cast(bf16x2, a), __builtin_bit_cast(bf16x2, b), c, false);
}
DEVI f32x4 mfma_bf(u32x4 a, u32x4 b, f32x4 c) {
  return __builtin_amdgcn_mfma_f32_16x16x32_bf16(__builtin_bit_cast(bf16x8, a), __builtin_bit_cast(bf16x8, b), c, 0, 0, 0);
}
DEVI f32x4 mfma_h(u32x4 a, u32x4 b, f32x4 c) {
  return __builtin_amdgcn_mfma_f32_16x16x32_f16(__builtin_bit_cast(f16x8, a), __builtin_bit_cast(f16x8, b), c, 0, 0, 0);
}
DEVI u32x2 tr_read(const char* p) {
  return __builtin_bit_cast(u32x2, __builtin_amdgcn_ds_read_tr16_b64_v4i16((LAS s16x4*)p));
}
DEVI u32x4 cat2(u32x2 a, u32x2 b) { return __builtin_shufflevector(a, b, 0, 1, 2, 3); }
DEVI u32 lds_addr(const void* p) { return (u32)(size_t)(const LAS char*)p; }
#define WAIT_VM(n) asm volatile("s_waitcnt vmcnt(" #n ")" ::: "memory")
#define WAIT_VM_BAR(n) asm volatile("s_waitcnt vmcnt(" #n ")\n\ts_barrier" ::: "memory")
#define SCHED() __builtin_amdgcn_sched_barrier(0)
template <int CTRL> DEVI float dpp_add(float v) {
  return v + __uint_as_float(__builtin_amdgcn_update_dpp(0u, __float_as_uint(v), CTRL, 0xf, 0xf, true));
}
DEVI float sum_x16(float v) {
  auto r = __builtin_amdgcn_permlane16_swap(__float_as_uint(v), __float_as_uint(v), false, false);
  return __uint_as_float(r[0]) + __uint_as_float(r[1]);
}
DEVI float sum_x32(float v) {
  auto r = __builtin_amdgcn_permlane32_swap(__float_as_uint(v), __float_as_uint(v), false, false);
  return __uint_as_float(r[0]) + __uint_as_float(r[1]);
}
DEVI float wave_sum(float v) {
  v = dpp_add<0xB1>(v); v = dpp_add<0x4E>(v); v = dpp_add<0x141>(v); v = dpp_add<0x140>(v);
  return sum_x32(sum_x16(v));
}
DEVI float fx2f(u32 lo, u32 hi) { return fmaf((float)(int)hi, 4294967296.0f * FX_INV, (float)lo * FX_INV); }
DEVI i64 f2fx(float v) {
  const float a = v * (FX_SCALE / 4294967296.0f), h = __builtin_floorf(a);
  return __builtin_bit_cast(i64, u32x2{(u32)((a - h) * 4294967296.0f), (u32)(int)h});
}
DEVI float sel4(int i, float a, float b, float c, float d) {
  asm volatile("" : "+v"(a), "+v"(b), "+v"(c), "+v"(d));
  return (i & 2) ? ((i & 1) ? d : c) : ((i & 1) ? b : a);
}
DEVI int st_pos(int t) { return ((t >> 8) * 4 + (t & 3)) * 1024 + ((t >> 2) & 63) * 16; }
DEVI float rcp(float x) { return __builtin_amdgcn_rcpf(x); }
DEVI float sqrt_(float x) { return __builtin_amdgcn_sqrtf(x); }

DEVI u32 warm_code(int slice, int lane) {
  unsigned long long pc, pe;
  asm volatile("s_getpc_b64 %0\n\ts_getpc_b64 s[90:91]\n\ts_add_u32 s90, s90, code_end_marker@rel32@lo+4\n\t"
               "s_addc_u32 s91, s91, code_end_marker@rel32@hi+12\n\ts_mov_b64 %1, s[90:91]"
               : "=&s"(pc), "=s"(pe) :: "scc", "s90", "s91");
  const u32 off = min((u32)slice * 512u, (u32)(pe - pc) - 512u) + (lane & 7) * 64;
  u32 junk;
  asm volatile("s_nop 4\n\tglobal_load_dword %0, %1, %2" : "=v"(junk) : "v"(off), "s"(pc) : "memory");
  return junk;
}

DEVI int cidx(int l) {
  const int hb = (int)(pack_bf((float)l, 0.f) & 0xffffu) - (0x4300 - 128);
  return l < 128 ? l : hb;
}
struct CM { f32x2 e, f; };
DEVI f32x2 cmul(f32x2 a, const CM& m) { return a[1] * m.f + a[0] * m.e; }
DEVI CM cexp_(float a2, float br, float p) {
  const float e = __builtin_amdgcn_exp2f(a2 * p), f = __builtin_amdgcn_fractf(br * p);
  const float re = e * __builtin_amdgcn_cosf(f), im = e * __builtin_amdgcn_sinf(f);
  CM r; r.e = f32x2{re, im}; r.f = f32x2{-im, re};
  return r;
}
DEVI void st16(rsrc_t rs, int off, u32x4 v) { __builtin_amdgcn_raw_buffer_store_b128(v, rs, off, 0, SC1); }

DEVI void xpose(int b, const u32x4 (&v)[8], rsrc_t rs, char* smem) {
  const int t = threadIdx.x, lane = t & 63, w = t >> 6;
  constexpr int RS = 1024 + 32;
#pragma unroll
  for (int r = 0; r < 8; ++r) *(u32x4*)(smem + (w * 8 + r) * RS + lane * 16) = v[r];
  __syncthreads();
  const int g = lane >> 4, q = (lane & 15) >> 2, p = lane & 3;
#pragma unroll
  for (int cf = 0; cf < 8; ++cf) {
    const int c0 = (w * 8 + cf) * 16;
    const char* pa = smem + (8 * g + q) * RS + c0 * 2 + 8 * p;
    const u32x4 o = cat2(tr_read(pa), tr_read(pa + 4 * RS));
    st16(rs, WS_X + (b >> 1) * 65536 + (c0 + (lane & 15)) * 128 + (b & 1) * 64 + g * 16, o);
  }
}
DEVI void row_stats(int b, const u32x4 (&v)[8], rsrc_t rs) {
  const int t = threadIdx.x, lane = t & 63, w = t >> 6;
  const int t0 = b * 32;
  float a[16];
#pragma unroll
  for (int r = 0; r < 8; ++r) {
    float s = 0.f, ss = 0.f;
#pragma unroll
    for (int j = 0; j < 4; ++j) { s = dot2bf(v[r][j], 0x3f803f80u, s); ss = dot2bf(v[r][j], v[r][j], ss); }
    a[2 * r] = s; a[2 * r + 1] = ss;
  }
  const bool o1 = lane & 1, o2 = lane & 2;
#pragma unroll
  for (int k = 0; k < 8; ++k) {
    const float keep = o1 ? a[2 * k + 1] : a[2 * k], send = o1 ? a[2 * k] : a[2 * k + 1];
    a[k] = keep + __uint_as_float(__builtin_amdgcn_update_dpp(0u, __float_as_uint(send), 0xB1, 0xf, 0xf, true));
  }
#pragma unroll
  for (int k = 0; k < 4; ++k) {
    const float keep = o2 ? a[2 * k + 1] : a[2 * k], send = o2 ? a[2 * k] : a[2 * k + 1];
    a[k] = keep + __uint_as_float(__builtin_amdgcn_update_dpp(0u, __float_as_uint(send), 0x4E, 0xf, 0xf, true));
  }
#pragma unroll
  for (int k = 0; k < 4; ++k) a[k] = sum_x32(sum_x16(dpp_add<0x128>(dpp_add<0x124>(a[k]))));
  const i64 sv = lane < 16 ? f2fx(sel4(lane >> 2, a[0], a[1], a[2], a[3])) : 0;
  if (lane < 32)
    __builtin_amdgcn_raw_buffer_store_b64(__builtin_bit_cast(u32x2, sv), rs, (lane >> 4) * 65536 + st_pos(t0 + w * 8 + ((lane >> 1) & 7)) + (lane & 1) * 8, WS_ST, SC1);
  if (t < 5) __builtin_amdgcn_raw_buffer_store_b32(0u, rs, (b * 5 + t) * 256, WS_CT, SC1);
}

DEVI void pack_w1(int ks, int cg, const u32x4 (&v)[8], rsrc_t rs, char* smem) {
  const int t = threadIdx.x, lane = t & 63, w = t >> 6;
  constexpr int RS = 512 + 32;
#pragma unroll
  for (int it = 0; it < 4; ++it) *(u32x4*)(smem + (it * 8 + (t >> 5)) * RS + (t & 31) * 16) = v[it];
  __syncthreads();
  const int g = lane >> 4, q = (lane & 15) >> 2, p = lane & 3;
  float cs[4];
#pragma unroll
  for (int h = 0; h < 4; ++h) {
    const char* a = smem + (8 * g + q) * RS + (w * 4 + h) * 32 + 8 * p;
    const u32x4 o = cat2(tr_read(a), tr_read(a + 4 * RS));
    st16(rs, WS_W1P + (((cg * 16 + w * 4 + h) * 16 + ks) * 64 + lane) * 16, o);
    float s = 0.f;
#pragma unroll
    for (int j = 0; j < 4; ++j) s = dot2bf(o[j], 0x3f803f80u, s);
    cs[h] = sum_x32(sum_x16(s));
  }
  __builtin_amdgcn_raw_buffer_store_b32(__float_as_uint(sel4(g, cs[0], cs[1], cs[2], cs[3])), rs, (ks * FF + cg * 256 + t) * 4, WS_S1, SC1);
  if (ks == 0 && cg == 0) st16(rs, WS_ONE + t * 16, u32x4{0x3f803f80u, 0x3f803f80u, 0x3f803f80u, 0x3f803f80u});
}

constexpr int W2_RS = 256 + 32;
DEVI void pack_w2_put(const u32x4 (&v)[2], char* smem) {
  const int t = threadIdx.x;
#pragma unroll
  for (int r = 0; r < 2; ++r) *(u32x4*)(smem + (r * 16 + (t >> 4)) * W2_RS + (t & 15) * 16) = v[r];
}
DEVI void pack_w2_out(int b, rsrc_t rs, const char* smem) {
  const int t = threadIdx.x, lane = t & 63, w = t >> 6;
  const int g = lane >> 4, q = (lane & 15) >> 2, p = lane & 3;
#pragma unroll
  for (int cf = 0; cf < 2; ++cf) {
    const char* a = smem + (4 * g + q) * W2_RS + (w * 2 + cf) * 32 + 8 * p;
    const u32x4 o = cat2(tr_read(a), tr_read(a + 16 * W2_RS));
    st16(rs, b < 224 ? WS_W2P + ((((b & 3) * 8 + w * 2 + cf) * 56 + (b >> 2)) * 64 + lane) * 16 : 0x40000000, o);
  }
}

constexpr int L_KC = 4160;
constexpr int L_U = 4 * L_KC, L_UC = 4608;
constexpr int L_XA = 26112, L_XB = L_XA + 65536;
constexpr int L_MISC = L_XB + 65536;
constexpr int L_CST = L_MISC + 1024;
constexpr int L_SIG = L_CST + 4 * 256;
constexpr int MAIN_LDS = L_SIG + 256;
constexpr int L_RED = L_XB;

DEVI u32x4 ld128(rsrc_t rs, int v, int s) { return __builtin_amdgcn_raw_buffer_load_b128(rs, v, s, SC1); }
DEVI u32x2 ld64(rsrc_t rs, int v, int s) { return __builtin_amdgcn_raw_buffer_load_b64(rs, v, s, SC1); }
DEVI void st128(rsrc_t rs, int v, int s, u32x4 d) { __builtin_amdgcn_raw_buffer_store_b128(d, rs, v, s, SC1); }
DEVI void st64(rsrc_t rs, int v, int s, u32x2 d) { __builtin_amdgcn_raw_buffer_store_b64(d, rs, v, s, SC1); }
template <int OFF> DEVI u32x4 ld128a(rsrc_t rs, int v, int s) {
  u32x4 r;
  asm volatile("buffer_load_dwordx4 %0, %1, %2, %3 offen offset:%4 sc1" : "=a"(r) : "v"(v), "s"(rs), "s"(s), "i"(OFF) : "memory");
  return r;
}
template <int OFF> DEVI u32x4 ld128v(rsrc_t rs, int v, int s) {
  u32x4 r;
  asm volatile("buffer_load_dwordx4 %0, %1, %2, %3 offen offset:%4 sc1" : "=v"(r) : "v"(v), "s"(rs), "s"(s), "i"(OFF) : "memory");
  return r;
}
template <int OFF> DEVI void dma1k(rsrc_t rs, u32 lds_dst, int v, int soff) {
  asm volatile("s_mov_b32 m0, %0\n\ts_nop 0\n\tbuffer_load_dwordx4 %1, %2, %3 offen offset:%4 sc1 lds"
               :: "s"(lds_dst), "v"(v), "s"(rs), "s"(soff), "i"(OFF) : "memory");
}
#define MMA(NAME, OP, CA, CB)                                                                                   \
  template <bool Z> DEVI void NAME(f32x4& d, const u32x4& a, const u32x4& b) {                                  \
    if (Z) asm volatile(OP " %0, %1, %2, 0" : "=&v"(d) : CA(a), CB(b));                                         \
    else asm volatile(OP " %0, %1, %2, %0" : "+v"(d) : CA(a), CB(b));                                           \
  }
MMA(mma_vv, "v_mfma_f32_16x16x32_bf16", "v", "v")
MMA(mma_av, "v_mfma_f32_16x16x32_bf16", "a", "v")
MMA(mma_va, "v_mfma_f32_16x16x32_bf16", "v", "a")
MMA(mma_hh, "v_mfma_f32_16x16x32_f16", "v", "v")
#define MFMA_DONE() do { SCHED(); asm volatile("s_nop 7" ::: "memory"); SCHED(); } while (0)

struct Ctx {
  rsrc_t rs; gu32* ct; char* smem; char* ws; u16* out;
  int lane0, w, b, P, sub, ct_, ff0, hks, eo, f1o, f1s;
  float dsk0, dsk1, ks0, ks1, bb2, cs1;
  u32 tag;
};
DEVI int fresh_lane(const Ctx& c) { int l = c.lane0; asm volatile("" : "+v"(l)); return l; }
DEVI u32 sig_begin(const Ctx& c, int lane) {
  u32 old;
  asm volatile("ds_add_rtn_u32 %0, %1, %2" : "=v"(old) : "v"(lds_addr(c.smem) + L_SIG + lane * 4), "v"(1u) : "memory");
  return old;
}
template <bool ALL, bool BACK = false> DEVI void sig_end(const Ctx& c, int lane, u32 old, int line) {
  if (BACK) asm volatile("" : "+v"(old) :: "memory");
  else asm volatile("s_waitcnt lgkmcnt(0)" : "+v"(old) :: "memory");
  const bool last = (__builtin_amdgcn_readfirstlane(old) & 3u) == 3u;
  const int v = (last && lane < (ALL ? 8 : 1)) ? (ALL ? lane * 2048 : 0) : 0x40000000;
  asm volatile("s_nop 4\n\tbuffer_atomic_add %0, %1, %2, %3 offen" :: "v"(1u), "v"(v), "s"(c.rs),
               "s"(WS_CT + (line + (ALL ? (c.b >> 3) & 7 : 0)) * 256) : "memory");
}
template <bool ALL, int R = 0> DEVI void poll(const Ctx& c, int lane, int line) {
  const int v = ALL ? (lane & 7) * 256 : 0, so = WS_CT + (line + (ALL ? (c.b & 7) * 8 : 0)) * 256;
  if (R == 0) asm volatile("s_nop 4\n\tbuffer_load_dword v254, %0, %1, %2 offen sc1" :: "v"(v), "s"(c.rs), "s"(so) : "memory", "v254");
  else asm volatile("s_nop 4\n\tbuffer_load_dword v255, %0, %1, %2 offen sc1" :: "v"(v), "s"(c.rs), "s"(so) : "memory", "v255");
}
template <int YOUNGER, int R = 0> DEVI bool got(u32 want) {
  unsigned long long m;
  if (R == 0) asm volatile("s_waitcnt vmcnt(%2)\n\tv_cmp_le_u32_e64 %0, %1, v254" : "=s"(m) : "s"(want), "i"(YOUNGER) : "memory");
  else asm volatile("s_waitcnt vmcnt(%2)\n\tv_cmp_le_u32_e64 %0, %1, v255" : "=s"(m) : "s"(want), "i"(YOUNGER) : "memory");
  return m == ~0ull;
}
template <bool ALL> DEVI void wait_ge(const Ctx& c, int lane, int line, u32 want) {
  int sp = 0;
#pragma nounroll
  do {

    poll<ALL, 0>(c, lane, line);
    if (++sp > SPIN_CAP) { __hip_atomic_store(c.ct + CT_TMO * 64, 1u, RLX_AGENT); break; }
  } while (!got<0, 0>(want));
}

DEVI void poll0(const Ctx& c, int lane, int set) {
  asm volatile("s_nop 4\n\tbuffer_load_dwordx2 v[254:255], %0, %1, %2 offen sc1" :: "v"((lane & 7) * 256 + (lane >> 3) * 8), "s"(c.rs),
               "s"(set + (c.b & 7) * 2048) : "memory", "v254", "v255");
}
template <int YOUNGER> DEVI bool got0(u32 tag) {
  unsigned long long m, m2;
  asm volatile("s_waitcnt vmcnt(%3)\n\tv_cmp_eq_u32_e64 %0, %2, v254\n\tv_cmp_eq_u32_e64 %1, %2, v255"
               : "=&s"(m), "=s"(m2) : "s"(tag), "i"(YOUNGER) : "memory");
  return (m & m2) == ~0ull;
}
DEVI void wait0(const Ctx& c, int lane, int set) {
  int sp = 0;
#pragma nounroll
  do {
    poll0(c, lane, set);
    if (++sp > SPIN_CAP) { __hip_atomic_store(c.ct + CT_TMO * 64, 1u, RLX_AGENT); break; }
  } while (!got0<0>(c.tag));
}

struct CIn { u32x2 x0, x1; u32x4 s[4]; };
DEVI void conv_load(const Ctx& c, int lane, CIn& in, int xoff, int stoff, int h) {
  const int sx = xoff + (16 * h + 4 * c.w) * 65536 + c.b * 256;
  const int vx = (lane >> 4) * 65536 + (lane & 15) * 8;
  const int ss = stoff + (1024 * h + 256 * c.w) * 16;
#pragma unroll
  for (int j = 0; j < 4; ++j) in.s[j] = ld128(c.rs, lane * 16 + j * 1024, ss);
  in.x0 = ld64(c.rs, vx, sx);
  in.x1 = ld64(c.rs, vx + 128, sx);
}
DEVI void conv_ln(const Ctx& c, int lane, const CIn& in, int h, f32x4& base0, f32x4& base1) {
  constexpr float CH = 4294967296.0f * FX_INV / DM, CL = FX_INV / DM;
  f32x4 m, r;
#pragma unroll
  for (int j = 0; j < 4; ++j) {
    m[j] = fmaf((float)(int)in.s[j][1], CH, (float)in.s[j][0] * CL);
    r[j] = fmaf((float)(int)in.s[j][3], CH, fmaf((float)in.s[j][2], CL, LN_EPS));
  }
#pragma unroll
  for (int j = 0; j < 4; ++j) r[j] = __builtin_amdgcn_rsqf(fmaxf(fmaf(-m[j], m[j], r[j]), LN_EPS));
  const f32x4 rr = r, mr = -m * r;
  const f32x4 x0 = {lo_bf(in.x0[0]), hi_bf(in.x0[0]), lo_bf(in.x0[1]), hi_bf(in.x0[1])};
  const f32x4 x1 = {lo_bf(in.x1[0]), hi_bf(in.x1[0]), lo_bf(in.x1[1]), hi_bf(in.x1[1])};
  const f32x4 u0 = x0 * rr + mr, u1 = x1 * rr + mr;
  char* up = c.smem + L_U + 512 + (1024 * h + 256 * c.w + 4 * lane) * 2;
  *(u32x2*)up = u32x2{pack_h(u0[0], u0[1]), pack_h(u0[2], u0[3])};
  *(u32x2*)(up + L_UC) = u32x2{pack_h(u1[0], u1[1]), pack_h(u1[2], u1[3])};
  base0 = u0 * c.dsk0 + x0; base1 = u1 * c.dsk1 + x1;
}
struct KB { const char* p[4]; };
template <int MM, int LAM> DEVI u32x4 conv_k1(const KB& kb) {
  const u32* kp = (const u32*)(kb.p[(7 - LAM) >> 1] + 64 * (3 - MM) + 512 * ((7 - LAM) & 1));
  return u32x4{kp[0], kp[1], kp[2], kp[3]};
}
template <int MM, int TB> DEVI u32x4 conv_u1(const char* ub) { return *(const u32x4*)(ub + 64 * (3 - MM) + 512 * TB); }
template <int LO> DEVI void conv_ldk(const KB& kb, u32x4 (&af)[4]) {
  sfor<0, 4>([&](auto L) { af[L.v] = conv_k1<0, LO + L.v>(kb); });
}
constexpr int conv_rank(int DT, int LO, int gr, int lt) {
  int k = 0;
  for (int x = 0; x < lt; ++x) { const int tb = DT + (x & 3) - (LO + 4 * gr + (x >> 2)); k += (tb >= 0 && tb < 4); }
  return k;
}
template <int DT, int TB0, bool ZERO, class F>
DEVI void conv_mma(const char* ub, const KB& kb, f32x4 (&acc)[4], const u32x4 (&af0)[4], F&& hook) {
  constexpr int NG = DT ? 2 : 1, LO = DT ? 1 : 0;
  u32x4 af[2][4], bf[2][4];
  sfor<0, 4>([&](auto L) { af[0][L.v] = af0[L.v]; });
  sfor<0, 4>([&](auto T) { constexpr int tb = DT ? 3 - T.v : T.v; bf[0][tb] = conv_u1<0, TB0 + tb>(ub); });
  SCHED();
  sfor<0, 4 * NG>([&](auto ST) {
    constexpr int st = ST.v, mm = st / NG, gr = st % NG;
    constexpr bool more = st + 1 < 4 * NG;
    constexpr int mn = (st + 1) / NG, gn = (st + 1) % NG;
    constexpr bool nu = more && gr == NG - 1;
    sfor<0, 16>([&](auto LT) {
      constexpr int l = LT.v >> 2, T = LT.v & 3, tb = DT + T - (LO + 4 * gr + l);
      if constexpr (tb >= 0 && tb < 4) {
        constexpr int k = conv_rank(DT, LO, gr, LT.v);
        constexpr bool first = ZERO && st == 0 && (DT ? (l == T) : (l == 0));
        mma_hh<first>(acc[T], af[st & 1][l], bf[mm & 1][tb]);
        sfor<(gr == 1 ? 2 * k : k), (gr == 1 ? 2 * k + 2 : k + 1)>([&](auto J) {
          constexpr int j = J.v;
          if constexpr (nu && j < 8) {
            if constexpr (DT != 0) {
              if constexpr (j & 1) bf[mn & 1][3 - (j >> 1)] = conv_u1<mn, TB0 + 3 - (j >> 1)>(ub);
              else af[(st + 1) & 1][j >> 1] = conv_k1<mn, LO + (j >> 1)>(kb);
            } else {
              if constexpr (j >= 1 && j <= 4) bf[mn & 1][j - 1] = conv_u1<mn, TB0 + j - 1>(ub);
              else af[(st + 1) & 1][j ? j - 4 : 0] = conv_k1<mn, LO + (j ? j - 4 : 0)>(kb);
            }
          } else if constexpr (more && !nu && j < 3) af[(st + 1) & 1][j] = conv_k1<mn, LO + 4 * gn + j>(kb);
        });
        SCHED();
        hook(IC<mm * (DT ? 16 : 10) + gr * 10 + k>{});
      }
    });
  });
}
DEVI void conv_part(const Ctx& c, int lane, const f32x4 (&acc)[4]) {
  char* red = c.smem + L_RED;
#pragma unroll
  for (int T = 0; T < 4; ++T) *(f32x4*)(red + (c.w * 4 + T) * 1024 + (lane & 15) * 64 + (lane >> 4) * 16) = acc[T];
}
DEVI void conv_out(const Ctx& c, int lane, const f32x4& base0, const f32x4& base1, int xoff, int h) {
  const int sx = xoff + (16 * h + 4 * c.w) * 65536 + c.b * 256;
  const int vx = (lane >> 4) * 65536 + (lane & 15) * 8;
  const char* p = c.smem + L_RED + c.w * 1024 + lane * 16;
  const f32x4 a0 = *(const f32x4*)p, b0 = *(const f32x4*)(p + 4096), a1 = *(const f32x4*)(p + 8192), b1 = *(const f32x4*)(p + 12288);
  SCHED();
  const f32x4 y0 = (a0 + b0) * c.ks0 + base0, y1 = (a1 + b1) * c.ks1 + base1;
  st64(c.rs, vx, sx, u32x2{pack_bf(y0[0], y0[1]), pack_bf(y0[2], y0[3])});
  st64(c.rs, vx + 128, sx, u32x2{pack_bf(y1[0], y1[1]), pack_bf(y1[2], y1[3])});
}

template <int PC> DEVI void g1_dma1(const Ctx& c, int ve, int vo, int ldsx, int soff) {
  dma1k<(PC & 3) * 1024>(c.rs, lds_addr(c.smem) + ldsx + (4 * (PC >> 2) + c.w) * 4096, (PC & 1) ? vo : ve, soff + (4 * (PC >> 2) + c.w) * 4096);
}
template <bool ALL> DEVI void sig_bar(const Ctx& c, int lane, int line) {
  const int v = (c.w == 0 && lane < (ALL ? 8 : 1)) ? (ALL ? lane * 2048 : 0) : 0x40000000;
  asm volatile("s_nop 4\n\tbuffer_atomic_add %0, %1, %2, %3 offen" :: "v"(1u), "v"(v), "s"(c.rs),
               "s"(WS_CT + (line + (ALL ? (c.b >> 3) & 7 : 0)) * 256) : "memory");
}
template <class F, class G>
DEVI void g1_mma(const char* const (&xb)[4], const u32x4 (&w1f)[2][16], f32x4 (&acc)[2][4], f32x4& sa, F&& mid, G&& hook) {
  u32x2 xf[2][4][2];
#pragma unroll
  for (int i = 0; i < 4; ++i) { xf[0][i][0] = tr_read(xb[i]); xf[0][i][1] = tr_read(xb[i] + 512); }
  SCHED();
  sfor<0, 16>([&](auto KS) {
    constexpr int ks = KS.v;
    mid(KS);
    sfor<0, 4>([&](auto I) {
      constexpr int i = I.v;
      const u32x4 x = cat2(xf[ks & 1][i][0], xf[ks & 1][i][1]);
      mma_vv<ks == 0>(acc[0][i], w1f[0][ks], x);
      if constexpr (ks < 15) xf[(ks + 1) & 1][i][0] = tr_read(xb[i] + (ks + 1) * 4096);
      SCHED();
      if constexpr (ks < 8) mma_av<ks == 0>(acc[1][i], w1f[1][ks], x); else mma_vv<false>(acc[1][i], w1f[1][ks], x);
      if constexpr (ks < 15) xf[(ks + 1) & 1][i][1] = tr_read(xb[i] + (ks + 1) * 4096 + 512);
      SCHED();
      if constexpr (i == 0) { mma_vv<ks == 0>(sa, x, x); hook(IC<2 * ks>{}); SCHED(); }
      if constexpr (i == 2) { hook(IC<2 * ks + 1>{}); SCHED(); }
    });
  });
  MFMA_DONE();
}
template <class F>
DEVI void g1_epi(const Ctx& c, int lane, f32x4 (&acc)[2][4], const f32x4& sa, int hoff, F&& hook) {
  const int g = lane >> 4, cc = lane & 15, q = cc >> 2, p = lane & 3, w = c.w;
  float* sums = (float*)(c.smem + L_MISC);
  {
    const float d = sel4(p, sa[0], sa[1], sa[2], sa[3]);
    sums[q == g ? (w * 16 + cc) * 2 + 1 : 128 + lane] = d;
    const float r = sel4(g, acc[1][1][0], acc[1][2][0], acc[1][3][0], acc[1][0][0]);
    sums[w == 3 ? lane * 2 : 192 + lane] = r;
  }
  f32x4 s1v[2], b1v[2];
#pragma unroll
  for (int j = 0; j < 2; ++j) {
    s1v[j] = *(const f32x4*)(c.smem + L_CST + w * 256 + j * 128 + g * 16);
    b1v[j] = *(const f32x4*)(c.smem + L_CST + w * 256 + j * 128 + 64 + g * 16);
  }
  const int v16 = lane * 16 + (w < 3 ? 0 : 0x40000000), v8 = lane * 8 + (w < 3 ? 0x40000000 : c.eo * 512);
  __syncthreads();
  f32x2 k[4];
#pragma unroll
  for (int i = 0; i < 4; ++i) k[i] = *(const f32x2*)(sums + (((i + w) & 3) * 16 + cc) * 2);
#pragma unroll
  for (int i = 0; i < 4; ++i) {
    const float m = k[i][0] * (1.0f / DM);
    const float ve = fmaxf(fmaf(-m, m, fmaf(k[i][1], 1.0f / DM, LN_EPS)), LN_EPS);
    const float r = __builtin_amdgcn_rsqf(fmaf(ve, 1.0f + LN_EPS, -LN_EPS));
    k[i] = f32x2{r, -m * r};
  }
  SCHED();
  hook(IC<-1>{});
  SCHED();
  sfor<0, 2>([&](auto IH) {
    constexpr int ih = IH.v;
    f32x4 x[4], e[4];
#define STAGE(n) do { SCHED(); hook(IC<10 * ih + n>{}); SCHED(); } while (0)
#pragma unroll
    for (int n = 0; n < 4; ++n) x[n] = k[2 * ih + (n >> 1)][1] * s1v[n & 1] + b1v[n & 1];
#pragma unroll
    for (int n = 0; n < 4; ++n) x[n] = acc[n & 1][2 * ih + (n >> 1)] * k[2 * ih + (n >> 1)][0] + x[n];
    STAGE(0);
#pragma unroll
    for (int n = 0; n < 4; ++n) e[n] = x[n] * x[n];
#pragma unroll
    for (int n = 0; n < 4; ++n) e[n] = e[n] * (-0.0713548162726f * 1.44269504088896f) + (-1.59576912160573f * 1.44269504088896f);
    STAGE(1);
#pragma unroll
    for (int n = 0; n < 4; ++n) e[n] = e[n] * x[n];
    STAGE(2);
#pragma unroll
    for (int r = 0; r < 4; ++r) { e[0][r] = __builtin_amdgcn_exp2f(e[0][r]); e[1][r] = __builtin_amdgcn_exp2f(e[1][r]); }
    STAGE(3);
#pragma unroll
    for (int r = 0; r < 4; ++r) { e[2][r] = __builtin_amdgcn_exp2f(e[2][r]); e[3][r] = __builtin_amdgcn_exp2f(e[3][r]); }
    STAGE(4);
#pragma unroll
    for (int n = 0; n < 4; ++n) e[n] = e[n] + 1.0f;
    STAGE(5);
#pragma unroll
    for (int r = 0; r < 4; ++r) { e[0][r] = rcp(e[0][r]); e[1][r] = rcp(e[1][r]); }
    STAGE(6);
#pragma unroll
    for (int r = 0; r < 4; ++r) { e[2][r] = rcp(e[2][r]); e[3][r] = rcp(e[3][r]); }
    STAGE(7);
#pragma unroll
    for (int n = 0; n < 4; ++n) x[n] = x[n] * e[n];
    STAGE(8);
    u32x4 hv[2];
#pragma unroll
    for (int n = 0; n < 4; ++n) { hv[n >> 1][2 * (n & 1)] = pack_bf(x[n][0], x[n][1]); hv[n >> 1][2 * (n & 1) + 1] = pack_bf(x[n][2], x[n][3]); }
    STAGE(9);
    const int so0 = hoff + ((2 * ih + w) & 3) * 56 * 1024, so1 = hoff + ((2 * ih + 1 + w) & 3) * 56 * 1024;
    st128(c.rs, v16, so0, hv[0]); st128(c.rs, v16, so1, hv[1]);
    st64(c.rs, v8, so0, u32x2{hv[0][0], hv[0][1]}); st64(c.rs, v8, so1, u32x2{hv[1][0], hv[1][1]});
    SCHED();
    if constexpr (ih == 0) { hook(IC<-2>{}); SCHED(); }
#undef STAGE
  });
}

constexpr int PF = 10;
template <int N> DEVI void ld_w1(const Ctx& c, int v, u32x4 (&w1f)[2][16]) {
  constexpr int ks = N >> 1, j = N & 1;
  const int so = (j ? c.f1o + (ks >> 2) * c.f1s : WS_W1P + c.ff0 * 16384 + (ks >> 2) * 4096);
  if constexpr (j && ks < 8) w1f[j][ks] = ld128a<(ks & 3) * 1024>(c.rs, v, so);
  else w1f[j][ks] = ld128v<(ks & 3) * 1024>(c.rs, v, so);
}
template <int N> DEVI void ld_w2(const Ctx& c, int v, u32x4 (&w2f)[14][4]) {
  constexpr int s = N >> 2, j = N & 3;
  w2f[s][j] = ld128a<(s & 3) * 1024>(c.rs, v, WS_W2P + ((c.ct_ * 4 + ((c.w + j) & 3)) * 56 + 14 * c.w + (s & ~3)) * 1024);
}
template <int N> DEVI void g2_ldh1(const Ctx& c, int v16, u32x4 (&hf)[14][2], int ho) {
  constexpr int s = N >> 1, i = N & 1;
  if constexpr (s % 7 == 6)
    hf[s][i] = cat2(ld64(c.rs, (v16 >> 1) + (s & 3) * 1024, ho + i * 56 * 1024 + (s >> 2) * 4096),
                    ld64(c.rs, (v16 >> 1) + (s & 3) * 1024 + 512, ho + i * 56 * 1024 + (s >> 2) * 4096));
  else hf[s][i] = ld128(c.rs, v16 + (s & 3) * 1024, ho + i * 56 * 1024 + (s >> 2) * 4096);
}
template <int PRE, int RATE, class F>
DEVI void g2_mma(const Ctx& c, int lane, u32x4 (&hf)[14][2], const u32x4 (&w2f)[14][4], f32x4 (&acc)[2][4], int ho, F&& mid) {
  const int v16 = lane * 16;
  sfor<0, 14>([&](auto S) {
    constexpr int s = S.v;
    sfor<0, 8>([&](auto M) {
      constexpr int i = M.v >> 2, j = M.v & 3;
      mma_va<s == 0>(acc[i][j], hf[s][i], w2f[s][j]);
      constexpr int per = 4 / RATE, n = 2 * (PRE + RATE * s) + M.v / per;
      if constexpr (M.v % per == per - 1 && n < 28) { g2_ldh1<n>(c, v16, hf, ho); SCHED(); }
    });
    SCHED();
    mid(S);
  });
  MFMA_DONE();
}
template <bool LAST, class F>
DEVI void g2_epi(const Ctx& c, int lane, const f32x4 (&acc)[2][4], const u32x2 (&xr)[2], int h, int xdst, F&& mid) {
  const int g = lane >> 4, cc = lane & 15, q = cc >> 2, p = lane & 3, w = c.w, t = w * 64 + lane;
  char* red = c.smem + L_XA + h * 32768;
#pragma unroll
  for (int jj = 1; jj < 4; ++jj)
#pragma unroll
    for (int i = 0; i < 2; ++i) *(f32x4*)(red + ((((w + jj) & 3) * 3 + jj - 1) * 2 + i) * 1024 + lane * 16) = acc[i][jj];
  __syncthreads();
  char* xt = red + 24576;
  f32x4 pr[2][3];
#pragma unroll
  for (int i = 0; i < 2; ++i)
#pragma unroll
    for (int rk = 0; rk < 3; ++rk) pr[i][rk] = *(const f32x4*)(red + ((w * 3 + rk) * 2 + i) * 1024 + lane * 16);
  mid(0);
#pragma unroll
  for (int i = 0; i < 2; ++i) {
    const f32x4 xv = {lo_bf(xr[i][0]), hi_bf(xr[i][0]), lo_bf(xr[i][1]), hi_bf(xr[i][1])};
    const f32x4 o = ((acc[i][0] + (xv + c.bb2)) + pr[i][0]) + (pr[i][1] + pr[i][2]);
    const u32x2 r = {pack_bf(o[0], o[1]), pack_bf(o[2], o[3])};
    *(u32x2*)(xt + (w * 16 + cc) * 64 + ((i ^ (cc >> 3)) * 32) + g * 8) = r;
  }
  __syncthreads();
  const char* xq = xt + ((w >> 1) * 32 + 8 * g + q) * 64 + (((w & 1) ^ (g & 1)) * 32) + 8 * p;
  const u32x4 f0 = cat2(tr_read(xq), tr_read(xq + 256));
  const int tok = (16 * h + c.P) * 64 + c.sub * 32 + (w & 1) * 16 + cc;
  if (LAST) {
    *(u32x4*)(c.out + (size_t)tok * DM + c.ct_ * 64 + (w >> 1) * 32 + 8 * g) = f0;
  } else {
    const int row = t >> 2;
    const u32x4 v = *(const u32x4*)(xt + row * 64 + (((t & 3) ^ ((row >> 2) & 2)) * 16));
    st128(c.rs, (c.ct_ * 64 + row) * 128 + (t & 3) * 16, xdst, v);
    const char* xo = xq + ((w & 2) ? -2048 : 2048);
    const u32x4 f1 = cat2(tr_read(xo), tr_read(xo + 256));
    float s = 0.f, ss = 0.f, s2 = 0.f, ss2 = 0.f;
#pragma unroll
    for (int j = 0; j < 4; ++j) {
      s = dot2bf(f0[j], 0x3f803f80u, s); ss = dot2bf(f0[j], f0[j], ss);
      s2 = dot2bf(f1[j], 0x3f803f80u, s2); ss2 = dot2bf(f1[j], f1[j], ss2);
    }
    s = sum_x32(sum_x16(s + s2)); ss = sum_x32(sum_x16(ss + ss2));
    const u32 ao = t < 128 && lane < 32 ? WS_ST + 65536 + st_pos(tok) + g * 8 : WS_DUMP + (c.b * 256 + t) * 8;
    (void)__hip_atomic_fetch_add((unsigned long long*)(c.ws + ao), (unsigned long long)f2fx((g & 1) ? ss : s), RLX_AGENT);
  }
}

template <int LAYER>
DEVI void layer_body(const Ctx& c, u32x4 (&w1f)[2][16], u32x4 (&w2f)[14][4], CIn& inA, CIn& inB) {
  constexpr int xin = WS_X + LAYER * 4 * MB, xmid = xin + 2 * MB, xnext = xin + 4 * MB;
  constexpr int hbuf = WS_H + LAYER * 8 * MB, stin = WS_ST + LAYER * 65536;
  constexpr u32 ep = LAYER + 1;
  constexpr int K1 = 1 + 3 * LAYER, K2 = K1 + 1, K3 = K1 + 2;
  const int w = c.w;
  char* smem = c.smem;
  auto nop = [](auto) {};
  {
    const int lane = fresh_lane(c);
    const int g = lane >> 4, cc = lane & 15, mu0 = (w & 1) * 4, v16 = lane * 16;
    const char* ub = smem + L_U + (w >> 1) * L_UC + 512 + 2 * (16 * cc - 16 + 8 * g) - 64 * (mu0 + 3);
    KB kb;
#pragma unroll
    for (int j = 0; j < 4; ++j) {
      u32 a = lds_addr(smem) + (w >> 1) * 2 * L_KC + ((cc & 1) ? 0 : L_KC) + 2 * (2031 - cc + 8 * g - ((cc & 1) ? 0 : 1)) - 64 * (mu0 + 3 + 56) + 1024 * j;
      asm volatile("" : "+v"(a));
      kb.p[j] = (const char*)(const LAS char*)(size_t)a;
    }
    STAMP(K1, 1);
    f32x4 base0, base1;
    u32x4 a4[4];
    conv_ldk<0>(kb, a4);
    conv_ln(c, lane, inA, 0, base0, base1);
    if (!LAYER) poll0(c, lane, WS_F1);
    __syncthreads();
    STAMP(K1, 2);
    f32x4 acc[4];
    u32 o = 0;
    conv_mma<0, 0, true>(ub, kb, acc, a4, [&](auto N) {
      constexpr int n = N.v;
      if constexpr (LAYER && n == 19) { WAIT_VM(0); o = sig_begin(c, lane); SCHED(); }
      if constexpr (LAYER && n == 30) { sig_end<true, true>(c, lane, o, CT_X + 64); SCHED(); }
    });
    MFMA_DONE();
    conv_part(c, lane, acc);
    conv_ldk<1>(kb, a4);
    __syncthreads();
    float s1p[8];
    if (!LAYER) {
      if (!got0<0>(c.tag)) wait0(c, lane, WS_F1);
      asm volatile("" :: "v"(inB.x1));
      const int so1 = (min(c.ff0 * 16 + (lane & 15) + (lane >> 5) * 16, FF - 1) + (lane & 16) * (FF / 2)) * 4;
#pragma unroll
      for (int j = 0; j < 8; ++j)
        asm volatile("s_nop 4\n\tbuffer_load_dword %0, %1, %2, %3 offen sc1" : "=v"(s1p[j]) : "v"(so1), "s"(c.rs), "s"(WS_S1 + j * FF * 4) : "memory");
    }
    conv_out(c, lane, base0, base1, xmid, 0);
    STAMP(K1, 3);
    f32x4 acb[4];
    conv_mma<4, 0, true>(ub, kb, acb, a4, [&](auto N) {
      constexpr int n = N.v;
      if constexpr (!LAYER && n >= 48 && (n & 1)) { ld_w1<(n - 48) / 2>(c, v16, w1f); SCHED(); }
      if constexpr (n == 47) {
        WAIT_VM(0);
        o = sig_begin(c, lane);
        if (LAYER) poll<true>(c, lane, CT_X + 64);
        SCHED();
      }
      if constexpr (n == 58) { sig_end<true, true>(c, lane, o, CT_C); SCHED(); }
    });
    STAMP(K1, 4);
    if (!LAYER) {
#pragma unroll
      for (int j = 0; j < 8; ++j) asm volatile("" : "+v"(s1p[j]));
      const float sv = sum_x16(((s1p[0] + s1p[1]) + (s1p[2] + s1p[3])) + ((s1p[4] + s1p[5]) + (s1p[6] + s1p[7])));
      ((float*)(smem + L_CST))[w * 64 + lane] = (lane & 16) ? c.cs1 : sv;
    }
    if (LAYER) {
      if (!got<1>(32u)) wait_ge<true>(c, lane, CT_X + 64, 32u);
      conv_load(c, lane, inB, xin, stin, 1);
    }
    conv_ldk<0>(kb, a4);
    STAMP(K1, 5);
    if (!LAYER) sfor<8, 12>([&](auto N) { ld_w1<N.v>(c, v16, w1f); });
    conv_ln(c, lane, inB, 1, base0, base1);
    if (!LAYER) sfor<12, 14>([&](auto N) { ld_w1<N.v>(c, v16, w1f); });
    __syncthreads();
    conv_mma<0, 4, false>(ub, kb, acb, a4, [&](auto N) {
      constexpr int n = N.v;
      if constexpr (!LAYER && n % 3 == 1 && n < 36) { ld_w1<14 + n / 3>(c, v16, w1f); SCHED(); }
      if constexpr (n == 30) { poll<true>(c, lane, CT_C); SCHED(); }
    });
    MFMA_DONE();
    conv_part(c, lane, acb);
    __syncthreads();
    conv_out(c, lane, base0, base1, xmid, 1);
    STAMP(K1, 6);
    if (!got<(LAYER ? 2 : 4)>(32u * ep)) wait_ge<true>(c, lane, CT_C, 32u * ep);
    const int row = lane >> 3, sw = ((row >> 1) & 1) << 1;
    const int ve = row * 128 + ((lane & 7) ^ sw) * 16, vo = row * 128 + ((lane & 7) ^ (sw | 4)) * 16;
    if (!LAYER && c.b == 0 && w == 0) __hip_atomic_store((gu32*)(c.ws + WS_GEN), c.tag, RLX_AGENT);
    sfor<0, 8>([&](auto N) { g1_dma1<N.v>(c, ve, vo, L_XA, xmid + c.P * 65536); });
    WAIT_VM(8);
    o = sig_begin(c, lane);
    sig_end<true>(c, lane, o, CT_C + 64);
    STAMP(K1, 7);
  }
  u32x4 hf[14][2];
  u32x2 xr[2];
  {
    const int lane = fresh_lane(c);
    const int g = lane >> 4, cc = lane & 15, q = cc >> 2, p = lane & 3, v16 = lane * 16;
    const int sx = ((q >> 1) & 1) | ((g & 1) << 1);
    const char* xb[4];
#pragma unroll
    for (int i = 0; i < 4; ++i) xb[i] = smem + L_XA + (8 * g + q) * 128 + ((((i + w) & 3) ^ sx) * 32) + 8 * p;
    const int row = lane >> 3, sw = ((row >> 1) & 1) << 1;
    const int ve = row * 128 + ((lane & 7) ^ sw) * 16, vo = row * 128 + ((lane & 7) ^ (sw | 4)) * 16;
    sfor<8, 12>([&](auto N) { g1_dma1<N.v>(c, ve, vo, L_XA, xmid + c.P * 65536); });
    WAIT_VM_BAR(9);
    STAMP(K2, 0);
    f32x4 acc[2][4], sa;
    g1_mma(xb, w1f, acc, sa, [&](auto KS) {
      constexpr int ks = KS.v;
      if constexpr (ks == 3) { if (LAYER) WAIT_VM_BAR(8); else WAIT_VM_BAR(10); SCHED(); }
      if constexpr (ks == 7) { if (LAYER) WAIT_VM_BAR(0); else WAIT_VM_BAR(10); SCHED(); }
      if constexpr (ks == 13) { if (!LAYER) WAIT_VM(16); poll<true>(c, lane, CT_C + 64); SCHED(); }
    }, [&](auto N) {
      constexpr int n = N.v;
      if constexpr (n < 4) g1_dma1<12 + n>(c, ve, vo, L_XA, xmid + c.P * 65536);
      if constexpr (!LAYER && n >= 4 && n < 10) ld_w1<22 + n>(c, v16, w1f);
      if constexpr (!LAYER && n >= 10) ld_w2<n - 10>(c, v16, w2f);
    });
    STAMP(K2, 1);
    const int xo = xmid + (16 + c.P) * 65536;
    g1_epi(c, lane, acc, sa, hbuf + (c.P * 4 * 56 + c.hks) * 1024, [&](auto N) {
      constexpr int n = N.v;
      if constexpr (n == -1) { if (!got<(LAYER ? 0 : 6)>(32u * ep)) wait_ge<true>(c, lane, CT_C + 64, 32u * ep); }
      if constexpr (n >= 0 && n < 8) g1_dma1<n>(c, ve, vo, L_XB, xo);
      if constexpr (n >= 10 && n < 18) g1_dma1<n - 2>(c, ve, vo, L_XB, xo);
    });
    STAMP(K2, 2);
  }
  {
    const int lane = fresh_lane(c);
    const int g = lane >> 4, cc = lane & 15, q = cc >> 2, p = lane & 3, v16 = lane * 16;
    const int sx = ((q >> 1) & 1) | ((g & 1) << 1);
    const char* xb[4];
#pragma unroll
    for (int i = 0; i < 4; ++i) xb[i] = smem + L_XB + (8 * g + q) * 128 + ((((i + w) & 3) ^ sx) * 32) + 8 * p;
    WAIT_VM_BAR(16);
    STAMP(K2, 3);
    f32x4 acc[2][4], sa;
    g1_mma(xb, w1f, acc, sa, [&](auto KS) {
      constexpr int ks = KS.v;
      if constexpr (ks == 6) {
        if (LAYER) WAIT_VM_BAR(0); else WAIT_VM_BAR(12);
        sig_bar<false>(c, lane, CT_H + c.P);
        SCHED();
      }
      if constexpr (ks == 15) {
#pragma unroll
        for (int i = 0; i < 2; ++i) xr[i] = ld64(c.rs, (c.ct_ * 64 + w * 16 + cc) * 128 + i * 32 + g * 8, xmid + c.P * 65536 + c.sub * 64);
        SCHED();
      }
    }, [&](auto N) {
      constexpr int n = N.v;
      if constexpr (!LAYER) ld_w2<22 + n>(c, v16, w2f);
      if constexpr (!LAYER && n >= 30) ld_w2<24 + n>(c, v16, w2f);
    });
    STAMP(K2, 4);
    poll<false>(c, lane, CT_H + c.P);
    const int ho = hbuf + ((c.P * 4 + c.sub * 2) * 56 + 14 * w) * 1024;
    g1_epi(c, lane, acc, sa, hbuf + ((16 + c.P) * 4 * 56 + c.hks) * 1024, [&](auto N) {
      constexpr int n = N.v;
      if constexpr (n == -1) { if (!got<0>(16u * ep)) wait_ge<false>(c, lane, CT_H + c.P, 16u * ep); }
      if constexpr (n >= 0) g2_ldh1<n>(c, v16, hf, ho);
    });
    STAMP(K2, 5);
  }
  {
    const int lane = fresh_lane(c);
    const int g = lane >> 4, cc = lane & 15;
    const int ho = hbuf + ((c.P * 4 + c.sub * 2) * 56 + 14 * w) * 1024, hoB = ho + 64 * 56 * 1024;
    STAMP(K3, 0);
    f32x4 acc[2][4];
    u32 o = 0;
    g2_mma<10, 1>(c, lane, hf, w2f, acc, ho, [&](auto S) {
      constexpr int s = S.v;
      if constexpr (s == 6) { WAIT_VM(10); o = sig_begin(c, lane); SCHED(); }
      if constexpr (s == 7) { sig_end<false>(c, lane, o, CT_H + 16 + c.P); SCHED(); }
    });
    STAMP(K3, 1);
    poll<false>(c, lane, CT_H + 16 + c.P);
    g2_epi<LAYER == 1>(c, lane, acc, xr, 0, xnext + c.P * 65536 + c.sub * 64, nop);
    STAMP(K3, 2);
    if (!got<(LAYER ? 1 : 2)>(16u * ep)) wait_ge<false>(c, lane, CT_H + 16 + c.P, 16u * ep);
    sfor<0, 8>([&](auto N) { g2_ldh1<N.v>(c, lane * 16, hf, hoB); });
#pragma unroll
    for (int i = 0; i < 2; ++i) xr[i] = ld64(c.rs, (c.ct_ * 64 + w * 16 + cc) * 128 + i * 32 + g * 8, xmid + (16 + c.P) * 65536 + c.sub * 64);
    if (LAYER == 0) {
      WAIT_VM(10);
      o = sig_begin(c, lane);
      sig_end<true>(c, lane, o, CT_X);
    }
    SCHED();
    STAMP(K3, 3);
  }
  {
    const int lane = fresh_lane(c);
    const int hoB = hbuf + (((16 + c.P) * 4 + c.sub * 2) * 56 + 14 * w) * 1024;
    f32x4 acc[2][4];
    g2_mma<4, 2>(c, lane, hf, w2f, acc, hoB, [&](auto S) {
      constexpr int s = S.v;
      if constexpr (LAYER == 0 && s == 13) { poll<true>(c, lane, CT_X); SCHED(); }
    });
    STAMP(K3, 4);
    g2_epi<LAYER == 1>(c, lane, acc, xr, 1, xnext + (16 + c.P) * 65536 + c.sub * 64, [&](int) {
      if (LAYER == 0) {
        if (!got<0>(32u)) wait_ge<true>(c, lane, CT_X, 32u);
        conv_load(c, lane, inA, xnext, WS_ST + 65536, 0);
        SCHED();
      }
    });
    STAMP(K3, 5);
  }
}

constexpr int KG_RS = 528, KG_X = 16 * KG_RS, KG_CH = 64 * KG_RS + 1296;
__global__ void __launch_bounds__(256, 1) main_kernel(const u16* x, const u16* lar, const u16* aim, const u16* cre,
                                                      const u16* cim, const u16* ldt, const u16* dsk, const u16* w1,
                                                      const u16* b1, const u16* w2, const u16* b2, char* ws, u16* out) {
  extern __shared__ __attribute__((aligned(16))) char smem[];
  const int t = threadIdx.x, lane = t & 63, b = blockIdx.x;
  const int w = __builtin_amdgcn_readfirstlane(t >> 6);
  const u32 junk = warm_code((b >> 3) * 4 + w, lane);
  u32 junk2;
  {
    const char* const* ka = (const char* const*)__builtin_amdgcn_kernarg_segment_ptr();
    const char* pp = ka[((b >> 3) * 4 + w) % 13];
    asm volatile("s_nop 4\n\tglobal_load_dword %0, %1, %2" : "=v"(junk2) : "v"(pp == ws ? (lane & 15) * (2 * MB) + (lane >> 5) * MB : 0), "s"(pp) : "memory");
  }
  STAMP(0, 0);
  const int ch = w >> 1, d = 2 * b + ch, n = t & 127;
  const u32 po = (u32)(d * NST + n) * 2, po2 = po ^ 128u;
#define LD16(p, off) (*(const u16*)((const char*)(p) + (u32)(off)))
  const u16 r0 = ldt[d], r1 = LD16(lar, po), r2 = LD16(aim, po), r3 = LD16(cre, po), r4 = LD16(cim, po);
  const u16 r5 = LD16(cre, po2), r6 = LD16(cim, po2);
  const u32 gen = __hip_atomic_load((gu32*)(ws + WS_GEN), RLX_AGENT);
  const bool early = !(b & 64);
  const int pks = (b & 7) + (b >> 7) * 8, pcg = (b >> 3) & 7;
  u32x4 rv[8], rw[2];
  {
    const rsrc_t rr = __builtin_amdgcn_make_buffer_rsrc((void*)(early ? x : w1), 0, early ? SEQ * DM * 2 : DM * FF * 2, 0x00020000);
    const int vo = early ? w * 8192 + lane * 16 : ((b & 63) < 56 ? (t >> 5) * (FF * 2) + (t & 31) * 16 : 0x40000000);
    const int so = early ? (b & 63) * 32768 : pks * (32 * FF * 2) + min(pcg, 6) * 512, st = early ? 1024 : 8 * FF * 2;
#pragma unroll
    for (int j = 0; j < 8; ++j) rv[j] = __builtin_amdgcn_raw_buffer_load_b128(rr, (j < 4 || early) ? vo : 0x40000000, so + (j & 3) * st + (j >> 2) * (early ? 4096 : 0), 0);
    const rsrc_t r2w = __builtin_amdgcn_make_buffer_rsrc((void*)w2, 0, DM * FF * 2, 0x00020000);
#pragma unroll
    for (int j = 0; j < 2; ++j) rw[j] = __builtin_amdgcn_raw_buffer_load_b128(r2w, (t >> 4) * 1024 + (t & 15) * 16 + j * 16384, min(b >> 2, 55) * 32768 + (b & 3) * 256, 0);
  }
  CIn inA, inB;
  Ctx c;
  c.rs = __builtin_amdgcn_make_buffer_rsrc(ws, 0, WS_END, 0x00020000);
  c.ct = (gu32*)(ws + WS_CT);
  c.smem = smem; c.ws = ws; c.out = out;
  c.lane0 = lane; c.w = w; c.b = b;
  c.P = (b & 7) * 2 + ((b >> 3) & 1);
  const int idx = b >> 4;
  const int sg = idx >> 1, w3 = -(int)(w == 3);
  c.eo = idx & 1; c.sub = idx >> 3; c.ct_ = idx & 7;
  c.ff0 = 14 * sg + (((6 * c.eo + 2 * w) & ~w3) | ((12 + c.eo) & w3));
  c.hks = 7 * sg + (((3 * c.eo + w) & ~w3) | (6 & w3));
  c.f1o = w < 3 ? WS_W1P + c.ff0 * 16384 + 16384 : WS_ONE; c.f1s = w < 3 ? 4096 : 0;
  {
    const u32 r7 = *(const u32*)(dsk + 2 * b);
    u32 r8 = LD16(b1, min(c.ff0 * 16 + (lane & 15) + (lane >> 5) * 16, FF - 1) * 2), r9 = LD16(b2, (c.ct_ * 64 + w * 16 + (lane & 15)) * 2);
    SCHED();
    char* kg = smem + L_XA + ch * KG_CH;
    char* stg = smem + L_XA + 2 * KG_CH;
    const int i = lane & 15, g = lane >> 4;
    int ix[9];
#pragma unroll
    for (int e2 = 0; e2 < 9; ++e2) { ix[e2] = 2 * cidx(max(2047 - 8 * t - e2, 0)); asm volatile("" : "+v"(ix[e2])); }
    int wi[2][4];
    {
      const int m8 = -(i >> 3), s1 = -(w & 1);
      const int h1 = ((256 + 128 * (i >> 2) + 2 * (i & 3)) & ~m8) | ((128 * (i >> 1) + 4 * (i & 1)) & m8);
      const int hp = (((i + (i & 8) * 15) & ~s1) | (h1 & s1)) + 32 * g, hq = 1024 + 128 * i + 32 * g;
#pragma unroll
      for (int r = 0; r < 4; ++r) {
        wi[0][r] = 2 * cidx(hp + 8 * r); wi[1][r] = 2 * cidx(hq + 8 * r > 2048 ? 2064 : hq + 8 * r);
        asm volatile("" : "+v"(wi[0][r]), "+v"(wi[1][r]));
      }
    }
    if (t < 16) *(u32x4*)(smem + (t >> 2) * L_KC + 4096 + (t & 3) * 16) = u32x4{0, 0, 0, 0};
    if (t >= 64 && t < 128) *(u32x4*)(smem + L_U + ((t - 64) >> 5) * L_UC + (t & 31) * 16) = u32x4{0, 0, 0, 0};
    if (t == 128) *(u32*)(smem + L_SIG) = 0u;
    SCHED();
    STAMP(0, 1);
    const float dt = rbf(expf(bf2f(r0)));
    const float cr = bf2f(r3), ci = bf2f(r4), cr2 = bf2f(r5), ci2 = bf2f(r6);
    const float S = wave_sum(sqrt_(cr * cr + ci * ci) + sqrt_(cr2 * cr2 + ci2 * ci2)) * (2.002f * dt);
    int e = __builtin_amdgcn_frexp_expf(S);
    e = (S > 0.0f && S < 3.0e38f) ? max(-100, min(100, e)) : 14;
    const float scale = __builtin_amdgcn_ldexpf(2.0f, 14 - e);
    if (n == 0) *(float*)(smem + L_MISC + ch * 4) = __builtin_amdgcn_ldexpf(1.0f, e - 14);
    const float a2 = (-rbf(expf(bf2f(r1))) * dt) * 1.44269504088896f;
    const float br = (bf2f(r2) * dt) * 0.159154943091895f;
    {
      char* yp = kg + n * 4;
      char* xp = kg + KG_X + n * 4;
      const CM e128 = cexp_(a2, -br, 128.f), e8 = cexp_(a2, br, 8.f), e1 = cexp_(a2, -br, 1.f), e2 = cexp_(a2, -br, 2.f);
      const CM e4 = cexp_(a2, -br, 4.f);
      f32x2 f0 = {(cr * dt) * scale, -(ci * dt) * scale};
      f32x2 f = cmul(f0, cexp_(a2, -br, 512.f)), y = {1.f, 0.f};
      f32x2 g1 = cmul(f0, e128), g2 = cmul(g1, e128), g3 = cmul(g2, e128);
#define PUT(base, row, v) *(u32*)(base + (row) * KG_RS) = pack_h(v[0], v[1])
      auto chain = [&](auto K) {
        constexpr int k = K.v;
        PUT(yp, k, y);
        if (k < 15) y = cmul(y, e8);
        if (k < 4) { const f32x2 q4 = cmul(f, e4); PUT(xp, 24 + 2 * k, f); PUT(xp, 25 + 2 * k, q4); }
        else if (k < 13) PUT(xp, 28 + k, f);
        if (k < 12) f = cmul(f, e128);
        if (k < 8) { PUT(xp, k, f0); PUT(xp, 8 + k, g1); }
        if (k < 7) { f0 = cmul(f0, e1); g1 = cmul(g1, e1); }
        if (k >= 8 && k < 12) { PUT(xp, 8 + k, g2); PUT(xp, 12 + k, g3); }
        if (k >= 8 && k < 11) { g2 = cmul(g2, e2); g3 = cmul(g3, e2); }
      };
      c.tag = __builtin_amdgcn_readfirstlane(gen) * 0x9E3779B1u + 0x7F4A7C15u;
      sfor<0, 2>(chain);
      asm volatile("" : "+v"(y), "+v"(f), "+v"(f0), "+v"(g1));
      if (b < 64) xpose(b, rv, c.rs, stg);
      else if (early) row_stats(b & 63, rv, c.rs);
      asm volatile("" : "+v"(y), "+v"(f), "+v"(f0), "+v"(g1));
      sfor<2, 12>(chain);
      asm volatile("" : "+v"(y), "+v"(f));
      if (early) {
        WAIT_VM(0);
        __syncthreads();
        if (w == 0 && lane < 8)
          __hip_atomic_store((gu32*)(ws + WS_F0 + lane * 2048 + (b & 7) * 256 + ((b >> 3) & 7) * 8 + (b >> 7) * 4), c.tag, RLX_AGENT);
      }
      asm volatile("" : "+v"(y), "+v"(f));
      STAMP(0, 2);
      sfor<12, 16>(chain);
      STAMP(0, 3);
      if (!early && pcg < 7) pack_w1(pks, pcg, rv, c.rs, stg);
#undef PUT
    }
    pack_w2_put(rw, smem + L_XB + 55552);
    __syncthreads();
    STAMP(0, 4);
    {
      const char* ya = kg + i * KG_RS + g * 16;
      const char* xa = kg + KG_X + (16 * (w & 1) + i) * KG_RS + g * 16;
      const char* xc = kg + KG_X + (32 + i) * KG_RS + g * 16;
      u32x4 fa[8], fb[8], fc[8];
#pragma unroll
      for (int ks = 0; ks < 8; ++ks) { fa[ks] = *(const u32x4*)(ya + ks * 64); fb[ks] = *(const u32x4*)(xa + ks * 64); }
#pragma unroll
      for (int ks = 0; ks < 8; ++ks) fc[ks] = *(const u32x4*)(xc + ks * 64);
      f32x4 ac0, ac1;
      SCHED();
      sfor<0, 8>([&](auto KS) { mma_hh<KS.v == 0>(ac0, fa[KS.v], fb[KS.v]); });
      sfor<0, 8>([&](auto KS) { mma_hh<KS.v == 0>(ac1, fa[KS.v], fc[KS.v]); });
      MFMA_DONE();
#pragma unroll
      for (int r = 0; r < 4; ++r) {
        *(u16*)(kg + 64 * KG_RS + wi[0][r]) = __builtin_bit_cast(u16, (_Float16)ac0[r]);
        *(u16*)(kg + 64 * KG_RS + wi[1][r]) = __builtin_bit_cast(u16, (_Float16)ac1[r]);
      }
    }
    if (!early) WAIT_VM(0);
    __syncthreads();
    if (w == 1 && !early && lane < 8)
      __hip_atomic_store((gu32*)(ws + WS_F1 + lane * 2048 + (b & 7) * 256 + ((b >> 3) & 7) * 8 + (b >> 7) * 4), c.tag, RLX_AGENT);
    if (w == 0) poll0(c, lane, WS_F0);
#pragma unroll
    for (int c2 = 0; c2 < 2; ++c2) {
      u32 v[9];
#pragma unroll
      for (int e2 = 0; e2 < 9; ++e2) v[e2] = *(const u16*)(smem + L_XA + c2 * KG_CH + 64 * KG_RS + ix[e2]);
      if (t == 255) v[8] = 0u;
      u32x4 oe, oo;
#pragma unroll
      for (int j = 0; j < 4; ++j) { oe[j] = v[2 * j] | (v[2 * j + 1] << 16); oo[j] = v[2 * j + 1] | (v[2 * j + 2] << 16); }
      *(u32x4*)(smem + c2 * 2 * L_KC + t * 16) = oe;
      *(u32x4*)(smem + c2 * 2 * L_KC + L_KC + t * 16) = oo;
    }
    const f32x2 ksc = *(const f32x2*)(smem + L_MISC);
    c.ks0 = __uint_as_float(__builtin_amdgcn_readfirstlane(__float_as_uint(ksc[0])));
    c.ks1 = __uint_as_float(__builtin_amdgcn_readfirstlane(__float_as_uint(ksc[1])));
    const u32 ds = __builtin_amdgcn_readfirstlane(r7);
    c.dsk0 = lo_bf(ds); c.dsk1 = hi_bf(ds);
    c.cs1 = lo_bf(r8); c.bb2 = lo_bf(r9);
    asm volatile("" :: "v"(junk), "v"(junk2));
    STAMP(0, 5);
    if (w == 0 && !got0<0>(c.tag)) wait0(c, lane, WS_F0);
    __syncthreads();
    conv_load(c, lane, inA, WS_X, WS_ST, 0);
    conv_load(c, lane, inB, WS_X, WS_ST, 1);
    pack_w2_out(b, c.rs, smem + L_XB + 55552);
    STAMP(0, 6);
  }
  u32x4 w1f[2][16], w2f[14][4];
  layer_body<0>(c, w1f, w2f, inA, inB);
  layer_body<1>(c, w1f, w2f, inA, inB);
}

extern "C" __global__ void code_end_marker() {}

extern "C" void kernel_launch(void* const* d_in, const int* in_sizes, int n_in, void* d_out, int out_size,
                              void* d_ws, size_t ws_size, hipStream_t stream) {
  (void)in_sizes; (void)n_in; (void)out_size; (void)ws_size;
  char* ws = (char*)d_ws + ((ws_size - (size_t)WS_END) & ~(size_t)(2 * MB - 1));
  (void)hipFuncSetAttribute((const void*)main_kernel, hipFuncAttributeMaxDynamicSharedMemorySize, MAIN_LDS);
  main_kernel<<<256, 256, MAIN_LDS, stream>>>((const u16*)d_in[0], (const u16*)d_in[1], (const u16*)d_in[2],
                                              (const u16*)d_in[3], (const u16*)d_in[4], (const u16*)d_in[5],
                                              (const u16*)d_in[6], (const u16*)d_in[8], (const u16*)d_in[9],
                                              (const u16*)d_in[10], (const u16*)d_in[11], ws, (u16*)d_out);
}
```

```cpp
#include <hip/hip_runtime.h>
#include <hip/hip_bf16.h>

typedef unsigned short u16;
typedef unsigned int u32;
typedef long long i64;
typedef __attribute__((ext_vector_type(4))) short s16x4;
typedef __attribute__((ext_vector_type(8))) __bf16 bf16x8;
typedef __attribute__((ext_vector_type(2))) __bf16 bf16x2;
typedef __attribute__((ext_vector_type(8))) _Float16 f16x8;
typedef __attribute__((ext_vector_type(2))) _Float16 f16x2;
typedef __attribute__((ext_vector_type(4))) float f32x4;
typedef __attribute__((ext_vector_type(2))) float f32x2;
typedef __attribute__((ext_vector_type(4))) unsigned u32x4;
typedef __attribute__((ext_vector_type(2))) unsigned u32x2;
typedef __attribute__((ext_vector_type(2))) long long i64x2;
typedef __attribute__((address_space(1))) unsigned gu32;
typedef __amdgpu_buffer_rsrc_t rsrc_t;
#define LAS __attribute__((address_space(3)))
#define DEVI __device__ __forceinline__
template <int V> struct IC { static constexpr int v = V; };
template <int I, int N, class F> __device__ __forceinline__ void sfor(F&& f) {
  if constexpr (I < N) { f(IC<I>{}); sfor<I + 1, N>(f); }
}
#define RLX_AGENT __ATOMIC_RELAXED, __HIP_MEMORY_SCOPE_AGENT

constexpr int SEQ = 2048, DM = 512, NST = 128, FF = 1792;
constexpr float LN_EPS = 1e-5f;
constexpr float FX_SCALE = 16777216.0f;
constexpr float FX_INV = 1.0f / 16777216.0f;
constexpr int SC1 = 16;

constexpr int MB = 1024 * 1024;
constexpr int WS_X = 0;
constexpr int WS_W1P = 12 * MB;
constexpr int WS_W2P = 14 * MB;
constexpr int WS_H = 16 * MB;
constexpr int WS_ST = 32 * MB;
constexpr int WS_S1 = 33 * MB;
constexpr int WS_GEN = 32 * MB + 196608;
constexpr int WS_F0 = WS_GEN + 256, WS_F1 = WS_F0 + 16384;
constexpr int WS_CT = 32 * MB + 262144;
constexpr int WS_ONE = 32 * MB + 393216;
constexpr int WS_DUMP = 32 * MB + 524288;
constexpr int WS_END = 34 * MB;
constexpr int CT_C = 0, CT_X = 128, CT_H = 256, CT_TMO = 288;
constexpr int SPIN_CAP = 1 << 16;

#define STAMP(k, i) do {} while (0)

DEVI float bf2f(u16 v) { return __uint_as_float((u32)v << 16); }
DEVI float lo_bf(u32 w) { return __uint_as_float(w << 16); }
DEVI float hi_bf(u32 w) { return __uint_as_float(w & 0xffff0000u); }
DEVI float rbf(float x) { return (float)(__bf16)x; }
DEVI u32 pack_bf(float a, float b) { f32x2 q = {a, b}; return __builtin_bit_cast(u32, __builtin_convertvector(q, bf16x2)); }
DEVI u32 pack_h(float a, float b) { f32x2 q = {a, b}; return __builtin_bit_cast(u32, __builtin_convertvector(q, f16x2)); }
DEVI float dot2bf(u32 a, u32 b, float c) {
  return __builtin_amdgcn_fdot2_f32_bf16(__builtin_bit_cast(bf16x2, a), __builtin_bit_cast(bf16x2, b), c, false);
}
DEVI f32x4 mfma_bf(u32x4 a, u32x4 b, f32x4 c) {
  return __builtin_amdgcn_mfma_f32_16x16x32_bf16(__builtin_bit_cast(bf16x8, a), __builtin_bit_cast(bf16x8, b), c, 0, 0, 0);
}
DEVI f32x4 mfma_h(u32x4 a, u32x4 b, f32x4 c) {
  return __builtin_amdgcn_mfma_f32_16x16x32_f16(__builtin_bit_cast(f16x8, a), __builtin_bit_cast(f16x8, b), c, 0, 0, 0);
}
DEVI u32x2 tr_read(const char* p) {
  return __builtin_bit_cast(u32x2, __builtin_amdgcn_ds_read_tr16_b64_v4i16((LAS s16x4*)p));
}
DEVI u32x4 cat2(u32x2 a, u32x2 b) { return __builtin_shufflevector(a, b, 0, 1, 2, 3); }
DEVI u32 lds_addr(const void* p) { return (u32)(size_t)(const LAS char*)p; }
#define WAIT_VM(n) asm volatile("s_waitcnt vmcnt(" #n ")" ::: "memory")
#define WAIT_VM_BAR(n) asm volatile("s_waitcnt vmcnt(" #n ")\n\ts_barrier" ::: "memory")
#define SCHED() __builtin_amdgcn_sched_barrier(0)
template <int CTRL> DEVI float dpp_add(float v) {
  return v + __uint_as_float(__builtin_amdgcn_update_dpp(0u, __float_as_uint(v), CTRL, 0xf, 0xf, true));
}
DEVI float sum_x16(float v) {
  auto r = __builtin_amdgcn_permlane16_swap(__float_as_uint(v), __float_as_uint(v), false, false);
  return __uint_as_float(r[0]) + __uint_as_float(r[1]);
}
DEVI float sum_x32(float v) {
  auto r = __builtin_amdgcn_permlane32_swap(__float_as_uint(v), __float_as_uint(v), false, false);
  return __uint_as_float(r[0]) + __uint_as_float(r[1]);
}
DEVI float wave_sum(float v) {
  v = dpp_add<0xB1>(v); v = dpp_add<0x4E>(v); v = dpp_add<0x141>(v); v = dpp_add<0x140>(v);
  return sum_x32(sum_x16(v));
}
DEVI float fx2f(u32 lo, u32 hi) { return fmaf((float)(int)hi, 4294967296.0f * FX_INV, (float)lo * FX_INV); }
DEVI i64 f2fx(float v) {
  const float a = v * (FX_SCALE / 4294967296.0f), h = __builtin_floorf(a);
  return __builtin_bit_cast(i64, u32x2{(u32)((a - h) * 4294967296.0f), (u32)(int)h});
}
DEVI float sel4(int i, float a, float b, float c, float d) {
  asm volatile("" : "+v"(a), "+v"(b), "+v"(c), "+v"(d));
  return (i & 2) ? ((i & 1) ? d : c) : ((i & 1) ? b : a);
}
DEVI int st_pos(int t) { return ((t >> 8) * 4 + (t & 3)) * 1024 + ((t >> 2) & 63) * 16; }
DEVI float rcp(float x) { return __builtin_amdgcn_rcpf(x); }
DEVI float sqrt_(float x) { return __builtin_amdgcn_sqrtf(x); }

DEVI u32 warm_code(int slice, int lane) {
  unsigned long long pc, pe;
  asm volatile("s_getpc_b64 %0\n\ts_getpc_b64 s[90:91]\n\ts_add_u32 s90, s90, code_end_marker@rel32@lo+4\n\t"
               "s_addc_u32 s91, s91, code_end_marker@rel32@hi+12\n\ts_mov_b64 %1, s[90:91]"
               : "=&s"(pc), "=s"(pe) :: "scc", "s90", "s91");
  const u32 off = min((u32)slice * 512u, (u32)(pe - pc) - 512u) + (lane & 7) * 64;
  u32 junk;
  asm volatile("s_nop 4\n\tglobal_load_dword %0, %1, %2" : "=v"(junk) : "v"(off), "s"(pc) : "memory");
  return junk;
}

DEVI int cidx(int l) {
  const int hb = (int)(pack_bf((float)l, 0.f) & 0xffffu) - (0x4300 - 128);
  return l < 128 ? l : hb;
}
struct CM { f32x2 e, f; };
DEVI f32x2 cmul(f32x2 a, const CM& m) { return a[1] * m.f + a[0] * m.e; }
DEVI CM cexp_(float a2, float br, float p) {
  const float e = __builtin_amdgcn_exp2f(a2 * p), f = __builtin_amdgcn_fractf(br * p);
  const float re = e * __builtin_amdgcn_cosf(f), im = e * __builtin_amdgcn_sinf(f);
  CM r; r.e = f32x2{re, im}; r.f = f32x2{-im, re};
  return r;
}
DEVI void st16(rsrc_t rs, int off, u32x4 v) { __builtin_amdgcn_raw_buffer_store_b128(v, rs, off, 0, SC1); }

DEVI void xpose(int b, const u32x4 (&v)[8], rsrc_t rs, char* smem) {
  const int t = threadIdx.x, lane = t & 63, w = t >> 6;
  constexpr int RS = 1024 + 32;
#pragma unroll
  for (int r = 0; r < 8; ++r) *(u32x4*)(smem + (w * 8 + r) * RS + lane * 16) = v[r];
  __syncthreads();
  const int g = lane >> 4, q = (lane & 15) >> 2, p = lane & 3;
#pragma unroll
  for (int cf = 0; cf < 8; ++cf) {
    const int c0 = (w * 8 + cf) * 16;
    const char* pa = smem + (8 * g + q) * RS + c0 * 2 + 8 * p;
    const u32x4 o = cat2(tr_read(pa), tr_read(pa + 4 * RS));
    st16(rs, WS_X + b * 32768 + (c0 + (lane & 15)) * 64 + g * 16, o);
  }
}
DEVI void row_stats(int b, const u32x4 (&v)[8], rsrc_t rs) {
  const int t = threadIdx.x, lane = t & 63, w = t >> 6;
  const int t0 = b * 32;
  float a[16];
#pragma unroll
  for (int r = 0; r < 8; ++r) {
    float s = 0.f, ss = 0.f;
#pragma unroll
    for (int j = 0; j < 4; ++j) { s = dot2bf(v[r][j], 0x3f803f80u, s); ss = dot2bf(v[r][j], v[r][j], ss); }
    a[2 * r] = s; a[2 * r + 1] = ss;
  }
  const bool o1 = lane & 1, o2 = lane & 2;
#pragma unroll
  for (int k = 0; k < 8; ++k) {
    const float keep = o1 ? a[2 * k + 1] : a[2 * k], send = o1 ? a[2 * k] : a[2 * k + 1];
    a[k] = keep + __uint_as_float(__builtin_amdgcn_update_dpp(0u, __float_as_uint(send), 0xB1, 0xf, 0xf, true));
  }
#pragma unroll
  for (int k = 0; k < 4; ++k) {
    const float keep = o2 ? a[2 * k + 1] : a[2 * k], send = o2 ? a[2 * k] : a[2 * k + 1];
    a[k] = keep + __uint_as_float(__builtin_amdgcn_update_dpp(0u, __float_as_uint(send), 0x4E, 0xf, 0xf, true));
  }
#pragma unroll
  for (int k = 0; k < 4; ++k) a[k] = sum_x32(sum_x16(dpp_add<0x128>(dpp_add<0x124>(a[k]))));
  const i64 sv = lane < 16 ? f2fx(sel4(lane >> 2, a[0], a[1], a[2], a[3])) : 0;
  if (lane < 32)
    __builtin_amdgcn_raw_buffer_store_b64(__builtin_bit_cast(u32x2, sv), rs, (lane >> 4) * 65536 + st_pos(t0 + w * 8 + ((lane >> 1) & 7)) + (lane & 1) * 8, WS_ST, SC1);
  if (t < 80) st16(rs, WS_CT + b * 1280 + t * 16, u32x4{0, 0, 0, 0});
}

DEVI void pack_w1(int ks, int cg, const u32x4 (&v)[8], rsrc_t rs, char* smem) {
  const int t = threadIdx.x, lane = t & 63, w = t >> 6;
  constexpr int RS = 512 + 32;
#pragma unroll
  for (int it = 0; it < 4; ++it) *(u32x4*)(smem + (it * 8 + (t >> 5)) * RS + (t & 31) * 16) = v[it];
  __syncthreads();
  const int g = lane >> 4, q = (lane & 15) >> 2, p = lane & 3;
  float cs[4];
#pragma unroll
  for (int h = 0; h < 4; ++h) {
    const char* a = smem + (8 * g + q) * RS + (w * 4 + h) * 32 + 8 * p;
    const u32x4 o = cat2(tr_read(a), tr_read(a + 4 * RS));
    st16(rs, WS_W1P + (((cg * 16 + w * 4 + h) * 16 + ks) * 64 + lane) * 16, o);
    float s = 0.f;
#pragma unroll
    for (int j = 0; j < 4; ++j) s = dot2bf(o[j], 0x3f803f80u, s);
    cs[h] = sum_x32(sum_x16(s));
  }
  __builtin_amdgcn_raw_buffer_store_b32(__float_as_uint(sel4(g, cs[0], cs[1], cs[2], cs[3])), rs, (ks * FF + cg * 256 + t) * 4, WS_S1, SC1);
  if (ks == 0 && cg == 0) st16(rs, WS_ONE + t * 16, u32x4{0x3f803f80u, 0x3f803f80u, 0x3f803f80u, 0x3f803f80u});
}

constexpr int W2_RS = 256 + 32;
DEVI void pack_w2_put(const u32x4 (&v)[2], char* smem) {
  const int t = threadIdx.x;
#pragma unroll
  for (int r = 0; r < 2; ++r) *(u32x4*)(smem + (r * 16 + (t >> 4)) * W2_RS + (t & 15) * 16) = v[r];
}
DEVI void pack_w2_out(int b, rsrc_t rs, const char* smem) {
  const int t = threadIdx.x, lane = t & 63, w = t >> 6;
  const int g = lane >> 4, q = (lane & 15) >> 2, p = lane & 3;
#pragma unroll
  for (int cf = 0; cf < 2; ++cf) {
    const char* a = smem + (4 * g + q) * W2_RS + (w * 2 + cf) * 32 + 8 * p;
    const u32x4 o = cat2(tr_read(a), tr_read(a + 16 * W2_RS));
    st16(rs, b < 224 ? WS_W2P + ((((b & 3) * 8 + w * 2 + cf) * 56 + (b >> 2)) * 64 + lane) * 16 : 0x40000000, o);
  }
}

constexpr int L_KC = 4160;
constexpr int L_U = 4 * L_KC, L_UC = 4608;
constexpr int L_XA = 26112, L_XB = L_XA + 65536;
constexpr int L_MISC = L_XB + 65536;
constexpr int L_CST = L_MISC + 1024;
constexpr int L_SIG = L_CST + 4 * 256;
constexpr int MAIN_LDS = L_SIG + 256;
constexpr int L_RED = L_XB;

DEVI u32x4 ld128(rsrc_t rs, int v, int s) { return __builtin_amdgcn_raw_buffer_load_b128(rs, v, s, SC1); }
DEVI u32x2 ld64(rsrc_t rs, int v, int s) { return __builtin_amdgcn_raw_buffer_load_b64(rs, v, s, SC1); }
DEVI void st128(rsrc_t rs, int v, int s, u32x4 d) { __builtin_amdgcn_raw_buffer_store_b128(d, rs, v, s, SC1); }
DEVI void st64(rsrc_t rs, int v, int s, u32x2 d) { __builtin_amdgcn_raw_buffer_store_b64(d, rs, v, s, SC1); }
template <int OFF> DEVI u32x4 ld128a(rsrc_t rs, int v, int s) {
  u32x4 r;
  asm volatile("buffer_load_dwordx4 %0, %1, %2, %3 offen offset:%4 sc1" : "=a"(r) : "v"(v), "s"(rs), "s"(s), "i"(OFF) : "memory");
  return r;
}
template <int OFF> DEVI u32x4 ld128v(rsrc_t rs, int v, int s) {
  u32x4 r;
  asm volatile("buffer_load_dwordx4 %0, %1, %2, %3 offen offset:%4 sc1" : "=v"(r) : "v"(v), "s"(rs), "s"(s), "i"(OFF) : "memory");
  return r;
}
template <int OFF> DEVI void dma1k(rsrc_t rs, u32 lds_dst, int v, int soff) {
  asm volatile("s_mov_b32 m0, %0\n\ts_nop 0\n\tbuffer_load_dwordx4 %1, %2, %3 offen offset:%4 sc1 lds"
               :: "s"(lds_dst), "v"(v), "s"(rs), "s"(soff), "i"(OFF) : "memory");
}
#define MMA(NAME, OP, CA, CB)                                                                                   \
  template <bool Z> DEVI void NAME(f32x4& d, const u32x4& a, const u32x4& b) {                                  \
    if (Z) asm volatile(OP " %0, %1, %2, 0" : "=&v"(d) : CA(a), CB(b));                                         \
    else asm volatile(OP " %0, %1, %2, %0" : "+v"(d) : CA(a), CB(b));                                           \
  }
MMA(mma_vv, "v_mfma_f32_16x16x32_bf16", "v", "v")
MMA(mma_av, "v_mfma_f32_16x16x32_bf16", "a", "v")
MMA(mma_va, "v_mfma_f32_16x16x32_bf16", "v", "a")
MMA(mma_hh, "v_mfma_f32_16x16x32_f16", "v", "v")
#define MFMA_DONE() do { SCHED(); asm volatile("s_nop 7" ::: "memory"); SCHED(); } while (0)

struct Ctx {
  rsrc_t rs; gu32* ct; char* smem; char* ws; u16* out;
  int lane0, w, b, P, sub, ct_, ff0, hks, eo, f1o, f1s;
  float dsk0, dsk1, ks0, ks1, bb2, cs1;
  u32 tag;
};
DEVI int fresh_lane(const Ctx& c) { int l = c.lane0; asm volatile("" : "+v"(l)); return l; }
DEVI u32 sig_begin(const Ctx& c, int lane) {
  u32 old;
  asm volatile("ds_add_rtn_u32 %0, %1, %2" : "=v"(old) : "v"(lds_addr(c.smem) + L_SIG + lane * 4), "v"(1u) : "memory");
  return old;
}
template <bool ALL, bool BACK = false> DEVI void sig_end(const Ctx& c, int lane, u32 old, int line) {
  if (BACK) asm volatile("" : "+v"(old) :: "memory");
  else asm volatile("s_waitcnt lgkmcnt(0)" : "+v"(old) :: "memory");
  const bool last = (__builtin_amdgcn_readfirstlane(old) & 3u) == 3u;
  const int v = (last && lane < (ALL ? 8 : 1)) ? (ALL ? lane * 2048 : 0) : 0x40000000;
  asm volatile("s_nop 4\n\tbuffer_atomic_add %0, %1, %2, %3 offen" :: "v"(1u), "v"(v), "s"(c.rs),
               "s"(WS_CT + (line + (ALL ? (c.b >> 3) & 7 : 0)) * 256) : "memory");
}
template <bool ALL, int R = 0> DEVI void poll(const Ctx& c, int lane, int line) {
  const int v = ALL ? (lane & 7) * 256 : 0, so = WS_CT + (line + (ALL ? (c.b & 7) * 8 : 0)) * 256;
  if (R == 0) asm volatile("s_nop 4\n\tbuffer_load_dword v254, %0, %1, %2 offen sc1" :: "v"(v), "s"(c.rs), "s"(so) : "memory", "v254");
  else asm volatile("s_nop 4\n\tbuffer_load_dword v255, %0, %1, %2 offen sc1" :: "v"(v), "s"(c.rs), "s"(so) : "memory", "v255");
}
template <int YOUNGER, int R = 0> DEVI bool got(u32 want) {
  unsigned long long m;
  if (R == 0) asm volatile("s_waitcnt vmcnt(%2)\n\tv_cmp_le_u32_e64 %0, %1, v254" : "=s"(m) : "s"(want), "i"(YOUNGER) : "memory");
  else asm volatile("s_waitcnt vmcnt(%2)\n\tv_cmp_le_u32_e64 %0, %1, v255" : "=s"(m) : "s"(want), "i"(YOUNGER) : "memory");
  return m == ~0ull;
}
template <bool ALL> DEVI void wait_ge(const Ctx& c, int lane, int line, u32 want) {
  int sp = 0;
#pragma nounroll
  do {

    poll<ALL, 0>(c, lane, line);
    if (++sp > SPIN_CAP) { __hip_atomic_store(c.ct + CT_TMO * 64, 1u, RLX_AGENT); break; }
  } while (!got<0, 0>(want));
}

DEVI void poll0(const Ctx& c, int lane, int set) {
  asm volatile("s_nop 4\n\tbuffer_load_dwordx2 v[254:255], %0, %1, %2 offen sc1" :: "v"((lane & 7) * 256 + (lane >> 3) * 8), "s"(c.rs),
               "s"(set + (c.b & 7) * 2048) : "memory", "v254", "v255");
}
template <int YOUNGER> DEVI bool got0(u32 tag) {
  unsigned long long m, m2;
  asm volatile("s_waitcnt vmcnt(%3)\n\tv_cmp_eq_u32_e64 %0, %2, v254\n\tv_cmp_eq_u32_e64 %1, %2, v255"
               : "=&s"(m), "=s"(m2) : "s"(tag), "i"(YOUNGER) : "memory");
  return (m & m2) == ~0ull;
}
DEVI void wait0(const Ctx& c, int lane, int set) {
  int sp = 0;
#pragma nounroll
  do {
    poll0(c, lane, set);
    if (++sp > SPIN_CAP) { __hip_atomic_store(c.ct + CT_TMO * 64, 1u, RLX_AGENT); break; }
  } while (!got0<0>(c.tag));
}

struct CIn { u32x2 x0, x1; u32x4 s[4]; };
DEVI void conv_load(const Ctx& c, int lane, CIn& in, int xoff, int stoff, int h) {
  const int sx = xoff + (32 * h + 8 * c.w) * 32768 + c.b * 128;
  const int vx = (lane >> 3) * 32768 + (lane & 7) * 8;
  const int ss = stoff + (1024 * h + 256 * c.w) * 16;
#pragma unroll
  for (int j = 0; j < 4; ++j) in.s[j] = ld128(c.rs, lane * 16 + j * 1024, ss);
  in.x0 = ld64(c.rs, vx, sx);
  in.x1 = ld64(c.rs, vx + 64, sx);
}
DEVI void conv_ln(const Ctx& c, int lane, const CIn& in, int h, f32x4& base0, f32x4& base1) {
  constexpr float CH = 4294967296.0f * FX_INV / DM, CL = FX_INV / DM;
  f32x4 m, r;
#pragma unroll
  for (int j = 0; j < 4; ++j) {
    m[j] = fmaf((float)(int)in.s[j][1], CH, (float)in.s[j][0] * CL);
    r[j] = fmaf((float)(int)in.s[j][3], CH, fmaf((float)in.s[j][2], CL, LN_EPS));
  }
#pragma unroll
  for (int j = 0; j < 4; ++j) r[j] = __builtin_amdgcn_rsqf(fmaxf(fmaf(-m[j], m[j], r[j]), LN_EPS));
  const f32x4 rr = r, mr = -m * r;
  const f32x4 x0 = {lo_bf(in.x0[0]), hi_bf(in.x0[0]), lo_bf(in.x0[1]), hi_bf(in.x0[1])};
  const f32x4 x1 = {lo_bf(in.x1[0]), hi_bf(in.x1[0]), lo_bf(in.x1[1]), hi_bf(in.x1[1])};
  const f32x4 u0 = x0 * rr + mr, u1 = x1 * rr + mr;
  char* up = c.smem + L_U + 512 + (1024 * h + 256 * c.w + 4 * lane) * 2;
  *(u32x2*)up = u32x2{pack_h(u0[0], u0[1]), pack_h(u0[2], u0[3])};
  *(u32x2*)(up + L_UC) = u32x2{pack_h(u1[0], u1[1]), pack_h(u1[2], u1[3])};
  base0 = u0 * c.dsk0 + x0; base1 = u1 * c.dsk1 + x1;
}
struct KB { const char* p[4]; };
template <int MM, int LAM> DEVI u32x4 conv_k1(const KB& kb) {
  const u32* kp = (const u32*)(kb.p[(7 - LAM) >> 1] + 64 * (3 - MM) + 512 * ((7 - LAM) & 1));
  return u32x4{kp[0], kp[1], kp[2], kp[3]};
}
template <int MM, int TB> DEVI u32x4 conv_u1(const char* ub) { return *(const u32x4*)(ub + 64 * (3 - MM) + 512 * TB); }
template <int LO> DEVI void conv_ldk(const KB& kb, u32x4 (&af)[4]) {
  sfor<0, 4>([&](auto L) { af[L.v] = conv_k1<0, LO + L.v>(kb); });
}
constexpr int conv_rank(int DT, int LO, int gr, int lt) {
  int k = 0;
  for (int x = 0; x < lt; ++x) { const int tb = DT + (x & 3) - (LO + 4 * gr + (x >> 2)); k += (tb >= 0 && tb < 4); }
  return k;
}
template <int DT, int TB0, bool ZERO, class F>
DEVI void conv_mma(const char* ub, const KB& kb, f32x4 (&acc)[4], const u32x4 (&af0)[4], F&& hook) {
  constexpr int NG = DT ? 2 : 1, LO = DT ? 1 : 0;
  u32x4 af[2][4], bf[2][4];
  sfor<0, 4>([&](auto L) { af[0][L.v] = af0[L.v]; });
  sfor<0, 4>([&](auto T) { constexpr int tb = DT ? 3 - T.v : T.v; bf[0][tb] = conv_u1<0, TB0 + tb>(ub); });
  SCHED();
  sfor<0, 4 * NG>([&](auto ST) {
    constexpr int st = ST.v, mm = st / NG, gr = st % NG;
    constexpr bool more = st + 1 < 4 * NG;
    constexpr int mn = (st + 1) / NG, gn = (st + 1) % NG;
    constexpr bool nu = more && gr == NG - 1;
    sfor<0, 16>([&](auto LT) {
      constexpr int l = LT.v >> 2, T = LT.v & 3, tb = DT + T - (LO + 4 * gr + l);
      if constexpr (tb >= 0 && tb < 4) {
        constexpr int k = conv_rank(DT, LO, gr, LT.v);
        constexpr bool first = ZERO && st == 0 && (DT ? (l == T) : (l == 0));
        mma_hh<first>(acc[T], af[st & 1][l], bf[mm & 1][tb]);
        sfor<(gr == 1 ? 2 * k : k), (gr == 1 ? 2 * k + 2 : k + 1)>([&](auto J) {
          constexpr int j = J.v;
          if constexpr (nu && j < 8) {
            if constexpr (DT != 0) {
              if constexpr (j & 1) bf[mn & 1][3 - (j >> 1)] = conv_u1<mn, TB0 + 3 - (j >> 1)>(ub);
              else af[(st + 1) & 1][j >> 1] = conv_k1<mn, LO + (j >> 1)>(kb);
            } else {
              if constexpr (j >= 1 && j <= 4) bf[mn & 1][j - 1] = conv_u1<mn, TB0 + j - 1>(ub);
              else af[(st + 1) & 1][j ? j - 4 : 0] = conv_k1<mn, LO + (j ? j - 4 : 0)>(kb);
            }
          } else if constexpr (more && !nu && j < 3) af[(st + 1) & 1][j] = conv_k1<mn, LO + 4 * gn + j>(kb);
        });
        SCHED();
        hook(IC<mm * (DT ? 16 : 10) + gr * 10 + k>{});
      }
    });
  });
}
DEVI void conv_part(const Ctx& c, int lane, const f32x4 (&acc)[4]) {
  char* red = c.smem + L_RED;
#pragma unroll
  for (int T = 0; T < 4; ++T) *(f32x4*)(red + (c.w * 4 + T) * 1024 + (lane & 15) * 64 + (lane >> 4) * 16) = acc[T];
}
DEVI void conv_out(const Ctx& c, int lane, const f32x4& base0, const f32x4& base1, int xoff, int h) {
  const int sx = xoff + (16 * h + 4 * c.w) * 65536 + c.b * 256;
  const int vx = (lane >> 4) * 65536 + (lane & 15) * 8;
  const char* p = c.smem + L_RED + c.w * 1024 + lane * 16;
  const f32x4 a0 = *(const f32x4*)p, b0 = *(const f32x4*)(p + 4096), a1 = *(const f32x4*)(p + 8192), b1 = *(const f32x4*)(p + 12288);
  SCHED();
  const f32x4 y0 = (a0 + b0) * c.ks0 + base0, y1 = (a1 + b1) * c.ks1 + base1;
  st64(c.rs, vx, sx, u32x2{pack_bf(y0[0], y0[1]), pack_bf(y0[2], y0[3])});
  st64(c.rs, vx + 128, sx, u32x2{pack_bf(y1[0], y1[1]), pack_bf(y1[2], y1[3])});
}

template <int PC> DEVI void g1_dma1(const Ctx& c, int ve, int vo, int ldsx, int soff) {
  dma1k<(PC & 3) * 1024>(c.rs, lds_addr(c.smem) + ldsx + (4 * (PC >> 2) + c.w) * 4096, (PC & 1) ? vo : ve, soff + (4 * (PC >> 2) + c.w) * 4096);
}
template <bool ALL> DEVI void sig_bar(const Ctx& c, int lane, int line) {
  const int v = (c.w == 0 && lane < (ALL ? 8 : 1)) ? (ALL ? lane * 2048 : 0) : 0x40000000;
  asm volatile("s_nop 4\n\tbuffer_atomic_add %0, %1, %2, %3 offen" :: "v"(1u), "v"(v), "s"(c.rs),
               "s"(WS_CT + (line + (ALL ? (c.b >> 3) & 7 : 0)) * 256) : "memory");
}
template <class F, class G>
DEVI void g1_mma(const char* const (&xb)[4], const u32x4 (&w1f)[2][16], f32x4 (&acc)[2][4], f32x4& sa, F&& mid, G&& hook) {
  u32x2 xf[2][4][2];
#pragma unroll
  for (int i = 0; i < 4; ++i) { xf[0][i][0] = tr_read(xb[i]); xf[0][i][1] = tr_read(xb[i] + 512); }
  SCHED();
  sfor<0, 16>([&](auto KS) {
    constexpr int ks = KS.v;
    mid(KS);
    sfor<0, 4>([&](auto I) {
      constexpr int i = I.v;
      const u32x4 x = cat2(xf[ks & 1][i][0], xf[ks & 1][i][1]);
      mma_vv<ks == 0>(acc[0][i], w1f[0][ks], x);
      if constexpr (ks < 15) xf[(ks + 1) & 1][i][0] = tr_read(xb[i] + (ks + 1) * 4096);
      SCHED();
      if constexpr (ks < 8) mma_av<ks == 0>(acc[1][i], w1f[1][ks], x); else mma_vv<false>(acc[1][i], w1f[1][ks], x);
      if constexpr (ks < 15) xf[(ks + 1) & 1][i][1] = tr_read(xb[i] + (ks + 1) * 4096 + 512);
      SCHED();
      if constexpr (i == 0) { mma_vv<ks == 0>(sa, x, x); hook(IC<2 * ks>{}); SCHED(); }
      if constexpr (i == 2) { hook(IC<2 * ks + 1>{}); SCHED(); }
    });
  });
  MFMA_DONE();
}
template <class F>
DEVI void g1_epi(const Ctx& c, int lane, f32x4 (&acc)[2][4], const f32x4& sa, int hoff, F&& hook) {
  const int g = lane >> 4, cc = lane & 15, q = cc >> 2, p = lane & 3, w = c.w;
  float* sums = (float*)(c.smem + L_MISC);
  {
    const float d = sel4(p, sa[0], sa[1], sa[2], sa[3]);
    sums[q == g ? (w * 16 + cc) * 2 + 1 : 128 + lane] = d;
    const float r = sel4(g, acc[1][1][0], acc[1][2][0], acc[1][3][0], acc[1][0][0]);
    sums[w == 3 ? lane * 2 : 192 + lane] = r;
  }
  f32x4 s1v[2], b1v[2];
#pragma unroll
  for (int j = 0; j < 2; ++j) {
    s1v[j] = *(const f32x4*)(c.smem + L_CST + w * 256 + j * 128 + g * 16);
    b1v[j] = *(const f32x4*)(c.smem + L_CST + w * 256 + j * 128 + 64 + g * 16);
  }
  const int v16 = lane * 16 + (w < 3 ? 0 : 0x40000000), v8 = lane * 8 + (w < 3 ? 0x40000000 : c.eo * 512);
  __syncthreads();
  f32x2 k[4];
#pragma unroll
  for (int i = 0; i < 4; ++i) k[i] = *(const f32x2*)(sums + (((i + w) & 3) * 16 + cc) * 2);
#pragma unroll
  for (int i = 0; i < 4; ++i) {
    const float m = k[i][0] * (1.0f / DM);
    const float ve = fmaxf(fmaf(-m, m, fmaf(k[i][1], 1.0f / DM, LN_EPS)), LN_EPS);
    const float r = __builtin_amdgcn_rsqf(fmaf(ve, 1.0f + LN_EPS, -LN_EPS));
    k[i] = f32x2{r, -m * r};
  }
  SCHED();
  hook(IC<-1>{});
  SCHED();
  sfor<0, 2>([&](auto IH) {
    constexpr int ih = IH.v;
    f32x4 x[4], e[4];
#define STAGE(n) do { SCHED(); hook(IC<10 * ih + n>{}); SCHED(); } while (0)
#pragma unroll
    for (int n = 0; n < 4; ++n) x[n] = k[2 * ih + (n >> 1)][1] * s1v[n & 1] + b1v[n & 1];
#pragma unroll
    for (int n = 0; n < 4; ++n) x[n] = acc[n & 1][2 * ih + (n >> 1)] * k[2 * ih + (n >> 1)][0] + x[n];
    STAGE(0);
#pragma unroll
    for (int n = 0; n < 4; ++n) e[n] = x[n] * x[n];
#pragma unroll
    for (int n = 0; n < 4; ++n) e[n] = e[n] * (-0.0713548162726f * 1.44269504088896f) + (-1.59576912160573f * 1.44269504088896f);
    STAGE(1);
#pragma unroll
    for (int n = 0; n < 4; ++n) e[n] = e[n] * x[n];
    STAGE(2);
#pragma unroll
    for (int r = 0; r < 4; ++r) { e[0][r] = __builtin_amdgcn_exp2f(e[0][r]); e[1][r] = __builtin_amdgcn_exp2f(e[1][r]); }
    STAGE(3);
#pragma unroll
    for (int r = 0; r < 4; ++r) { e[2][r] = __builtin_amdgcn_exp2f(e[2][r]); e[3][r] = __builtin_amdgcn_exp2f(e[3][r]); }
    STAGE(4);
#pragma unroll
    for (int n = 0; n < 4; ++n) e[n] = e[n] + 1.0f;
    STAGE(5);
#pragma unroll
    for (int r = 0; r < 4; ++r) { e[0][r] = rcp(e[0][r]); e[1][r] = rcp(e[1][r]); }
    STAGE(6);
#pragma unroll
    for (int r = 0; r < 4; ++r) { e[2][r] = rcp(e[2][r]); e[3][r] = rcp(e[3][r]); }
    STAGE(7);
#pragma unroll
    for (int n = 0; n < 4; ++n) x[n] = x[n] * e[n];
    STAGE(8);
    u32x4 hv[2];
#pragma unroll
    for (int n = 0; n < 4; ++n) { hv[n >> 1][2 * (n & 1)] = pack_bf(x[n][0], x[n][1]); hv[n >> 1][2 * (n & 1) + 1] = pack_bf(x[n][2], x[n][3]); }
    STAGE(9);
    const int so0 = hoff + ((2 * ih + w) & 3) * 56 * 1024, so1 = hoff + ((2 * ih + 1 + w) & 3) * 56 * 1024;
    st128(c.rs, v16, so0, hv[0]); st128(c.rs, v16, so1, hv[1]);
    st64(c.rs, v8, so0, u32x2{hv[0][0], hv[0][1]}); st64(c.rs, v8, so1, u32x2{hv[1][0], hv[1][1]});
    SCHED();
    if constexpr (ih == 0) { hook(IC<-2>{}); SCHED(); }
#undef STAGE
  });
}

constexpr int PF = 10;
template <int N> DEVI void ld_w1(const Ctx& c, int v, u32x4 (&w1f)[2][16]) {
  constexpr int ks = N >> 1, j = N & 1;
  const int so = (j ? c.f1o + (ks >> 2) * c.f1s : WS_W1P + c.ff0 * 16384 + (ks >> 2) * 4096);
  if constexpr (j && ks < 8) w1f[j][ks] = ld128a<(ks & 3) * 1024>(c.rs, v, so);
  else w1f[j][ks] = ld128v<(ks & 3) * 1024>(c.rs, v, so);
}
template <int N> DEVI void ld_w2(const Ctx& c, int v, u32x4 (&w2f)[14][4]) {
  constexpr int s = N >> 2, j = N & 3;
  w2f[s][j] = ld128a<(s & 3) * 1024>(c.rs, v, WS_W2P + ((c.ct_ * 4 + ((c.w + j) & 3)) * 56 + 14 * c.w + (s & ~3)) * 1024);
}
template <int N> DEVI void g2_ldh1(const Ctx& c, int v16, u32x4 (&hf)[14][2], int ho) {
  constexpr int s = N >> 1, i = N & 1;
  if constexpr (s % 7 == 6)
    hf[s][i] = cat2(ld64(c.rs, (v16 >> 1) + (s & 3) * 1024, ho + i * 56 * 1024 + (s >> 2) * 4096),
                    ld64(c.rs, (v16 >> 1) + (s & 3) * 1024 + 512, ho + i * 56 * 1024 + (s >> 2) * 4096));
  else hf[s][i] = ld128(c.rs, v16 + (s & 3) * 1024, ho + i * 56 * 1024 + (s >> 2) * 4096);
}
template <int PRE, int RATE, class F>
DEVI void g2_mma(const Ctx& c, int lane, u32x4 (&hf)[14][2], const u32x4 (&w2f)[14][4], f32x4 (&acc)[2][4], int ho, F&& mid) {
  const int v16 = lane * 16;
  sfor<0, 14>([&](auto S) {
    constexpr int s = S.v;
    sfor<0, 8>([&](auto M) {
      constexpr int i = M.v >> 2, j = M.v & 3;
      mma_va<s == 0>(acc[i][j], hf[s][i], w2f[s][j]);
      constexpr int per = 4 / RATE, n = 2 * (PRE + RATE * s) + M.v / per;
      if constexpr (M.v % per == per - 1 && n < 28) { g2_ldh1<n>(c, v16, hf, ho); SCHED(); }
    });
    SCHED();
    mid(S);
  });
  MFMA_DONE();
}
template <bool LAST, class F>
DEVI void g2_epi(const Ctx& c, int lane, const f32x4 (&acc)[2][4], const u32x2 (&xr)[2], int h, int xdst, F&& mid) {
  const int g = lane >> 4, cc = lane & 15, q = cc >> 2, p = lane & 3, w = c.w, t = w * 64 + lane;
  char* red = c.smem + L_XA + h * 32768;
#pragma unroll
  for (int jj = 1; jj < 4; ++jj)
#pragma unroll
    for (int i = 0; i < 2; ++i) *(f32x4*)(red + ((((w + jj) & 3) * 3 + jj - 1) * 2 + i) * 1024 + lane * 16) = acc[i][jj];
  __syncthreads();
  char* xt = red + 24576;
  f32x4 pr[2][3];
#pragma unroll
  for (int i = 0; i < 2; ++i)
#pragma unroll
    for (int rk = 0; rk < 3; ++rk) pr[i][rk] = *(const f32x4*)(red + ((w * 3 + rk) * 2 + i) * 1024 + lane * 16);
  mid(0);
#pragma unroll
  for (int i = 0; i < 2; ++i) {
    const f32x4 xv = {lo_bf(xr[i][0]), hi_bf(xr[i][0]), lo_bf(xr[i][1]), hi_bf(xr[i][1])};
    const f32x4 o = ((acc[i][0] + (xv + c.bb2)) + pr[i][0]) + (pr[i][1] + pr[i][2]);
    const u32x2 r = {pack_bf(o[0], o[1]), pack_bf(o[2], o[3])};
    *(u32x2*)(xt + (w * 16 + cc) * 64 + ((i ^ (cc >> 3)) * 32) + g * 8) = r;
  }
  __syncthreads();
  const char* xq = xt + ((w >> 1) * 32 + 8 * g + q) * 64 + (((w & 1) ^ (g & 1)) * 32) + 8 * p;
  const u32x4 f0 = cat2(tr_read(xq), tr_read(xq + 256));
  const int tok = (16 * h + c.P) * 64 + c.sub * 32 + (w & 1) * 16 + cc;
  if (LAST) {
    *(u32x4*)(c.out + (size_t)tok * DM + c.ct_ * 64 + (w >> 1) * 32 + 8 * g) = f0;
  } else {
    const int row = t >> 2;
    const u32x4 v = *(const u32x4*)(xt + row * 64 + (((t & 3) ^ ((row >> 2) & 2)) * 16));
    st128(c.rs, c.ct_ * 4096 + t * 16, xdst, v);
    const char* xo = xq + ((w & 2) ? -2048 : 2048);
    const u32x4 f1 = cat2(tr_read(xo), tr_read(xo + 256));
    float s = 0.f, ss = 0.f, s2 = 0.f, ss2 = 0.f;
#pragma unroll
    for (int j = 0; j < 4; ++j) {
      s = dot2bf(f0[j], 0x3f803f80u, s); ss = dot2bf(f0[j], f0[j], ss);
      s2 = dot2bf(f1[j], 0x3f803f80u, s2); ss2 = dot2bf(f1[j], f1[j], ss2);
    }
    s = sum_x32(sum_x16(s + s2)); ss = sum_x32(sum_x16(ss + ss2));
    const u32 ao = t < 128 && lane < 32 ? WS_ST + 65536 + st_pos(tok) + g * 8 : WS_DUMP + (c.b * 256 + t) * 8;
    (void)__hip_atomic_fetch_add((unsigned long long*)(c.ws + ao), (unsigned long long)f2fx((g & 1) ? ss : s), RLX_AGENT);
  }
}

template <int LAYER>
DEVI void layer_body(const Ctx& c, u32x4 (&w1f)[2][16], u32x4 (&w2f)[14][4], CIn& inA, CIn& inB) {
  constexpr int xin = WS_X + LAYER * 4 * MB, xmid = xin + 2 * MB, xnext = xin + 4 * MB;
  constexpr int hbuf = WS_H + LAYER * 8 * MB, stin = WS_ST + LAYER * 65536;
  constexpr u32 ep = LAYER + 1;
  constexpr int K1 = 1 + 3 * LAYER, K2 = K1 + 1, K3 = K1 + 2;
  const int w = c.w;
  char* smem = c.smem;
  auto nop = [](auto) {};
  {
    const int lane = fresh_lane(c);
    const int g = lane >> 4, cc = lane & 15, mu0 = (w & 1) * 4, v16 = lane * 16;
    const char* ub = smem + L_U + (w >> 1) * L_UC + 512 + 2 * (16 * cc - 16 + 8 * g) - 64 * (mu0 + 3);
    KB kb;
#pragma unroll
    for (int j = 0; j < 4; ++j) {
      u32 a = lds_addr(smem) + (w >> 1) * 2 * L_KC + ((cc & 1) ? 0 : L_KC) + 2 * (2031 - cc + 8 * g - ((cc & 1) ? 0 : 1)) - 64 * (mu0 + 3 + 56) + 1024 * j;
      asm volatile("" : "+v"(a));
      kb.p[j] = (const char*)(const LAS char*)(size_t)a;
    }
    STAMP(K1, 1);
    f32x4 base0, base1;
    u32x4 a4[4];
    conv_ldk<0>(kb, a4);
    conv_ln(c, lane, inA, 0, base0, base1);
    if (!LAYER) poll0(c, lane, WS_F1);
    __syncthreads();
    STAMP(K1, 2);
    f32x4 acc[4];
    u32 o = 0;
    conv_mma<0, 0, true>(ub, kb, acc, a4, [&](auto N) {
      constexpr int n = N.v;
      if constexpr (LAYER && n == 19) { WAIT_VM(0); o = sig_begin(c, lane); SCHED(); }
      if constexpr (LAYER && n == 30) { sig_end<true, true>(c, lane, o, CT_X + 64); SCHED(); }
    });
    MFMA_DONE();
    conv_part(c, lane, acc);
    conv_ldk<1>(kb, a4);
    __syncthreads();
    float s1p[8];
    if (!LAYER) {
      if (!got0<0>(c.tag)) wait0(c, lane, WS_F1);
      asm volatile("" :: "v"(inB.x1));
      const int so1 = (min(c.ff0 * 16 + (lane & 15) + (lane >> 5) * 16, FF - 1) + (lane & 16) * (FF / 2)) * 4;
#pragma unroll
      for (int j = 0; j < 8; ++j)
        asm volatile("s_nop 4\n\tbuffer_load_dword %0, %1, %2, %3 offen sc1" : "=v"(s1p[j]) : "v"(so1), "s"(c.rs), "s"(WS_S1 + j * FF * 4) : "memory");
    }
    conv_out(c, lane, base0, base1, xmid, 0);
    STAMP(K1, 3);
    f32x4 acb[4];
    conv_mma<4, 0, true>(ub, kb, acb, a4, [&](auto N) {
      constexpr int n = N.v;
      if constexpr (!LAYER && n >= 48 && (n & 1)) { ld_w1<(n - 48) / 2>(c, v16, w1f); SCHED(); }
      if constexpr (n == 47) {
        WAIT_VM(0);
        o = sig_begin(c, lane);
        if (LAYER) poll<true>(c, lane, CT_X + 64);
        SCHED();
      }
      if constexpr (n == 58) { sig_end<true, true>(c, lane, o, CT_C); SCHED(); }
    });
    STAMP(K1, 4);
    if (!LAYER) {
#pragma unroll
      for (int j = 0; j < 8; ++j) asm volatile("" : "+v"(s1p[j]));
      const float sv = sum_x16(((s1p[0] + s1p[1]) + (s1p[2] + s1p[3])) + ((s1p[4] + s1p[5]) + (s1p[6] + s1p[7])));
      ((float*)(smem + L_CST))[w * 64 + lane] = (lane & 16) ? c.cs1 : sv;
    }
    if (LAYER) {
      if (!got<1>(32u)) wait_ge<true>(c, lane, CT_X + 64, 32u);
      conv_load(c, lane, inB, xin, stin, 1);
    }
    conv_ldk<0>(kb, a4);
    STAMP(K1, 5);
    if (!LAYER) sfor<8, 12>([&](auto N) { ld_w1<N.v>(c, v16, w1f); });
    conv_ln(c, lane, inB, 1, base0, base1);
    if (!LAYER) sfor<12, 14>([&](auto N) { ld_w1<N.v>(c, v16, w1f); });
    __syncthreads();
    conv_mma<0, 4, false>(ub, kb, acb, a4, [&](auto N) {
      constexpr int n = N.v;
      if constexpr (!LAYER && n % 3 == 1 && n < 36) { ld_w1<14 + n / 3>(c, v16, w1f); SCHED(); }
      if constexpr (n == 30) { poll<true>(c, lane, CT_C); SCHED(); }
    });
    MFMA_DONE();
    conv_part(c, lane, acb);
    __syncthreads();
    conv_out(c, lane, base0, base1, xmid, 1);
    STAMP(K1, 6);
    if (!got<(LAYER ? 2 : 4)>(32u * ep)) wait_ge<true>(c, lane, CT_C, 32u * ep);
    const int row = lane >> 3, sw = ((row >> 1) & 1) << 1;
    const int ve = row * 128 + ((lane & 7) ^ sw) * 16, vo = row * 128 + ((lane & 7) ^ (sw | 4)) * 16;
    if (!LAYER && c.b == 0 && w == 0) __hip_atomic_store((gu32*)(c.ws + WS_GEN), c.tag, RLX_AGENT);
    sfor<0, 8>([&](auto N) { g1_dma1<N.v>(c, ve, vo, L_XA, xmid + c.P * 65536); });
    WAIT_VM(8);
    o = sig_begin(c, lane);
    sig_end<true>(c, lane, o, CT_C + 64);
    STAMP(K1, 7);
  }
  u32x4 hf[14][2];
  u32x2 xr[2];
  {
    const int lane = fresh_lane(c);
    const int g = lane >> 4, cc = lane & 15, q = cc >> 2, p = lane & 3, v16 = lane * 16;
    const int sx = ((q >> 1) & 1) | ((g & 1) << 1);
    const char* xb[4];
#pragma unroll
    for (int i = 0; i < 4; ++i) xb[i] = smem + L_XA + (8 * g + q) * 128 + ((((i + w) & 3) ^ sx) * 32) + 8 * p;
    const int row = lane >> 3, sw = ((row >> 1) & 1) << 1;
    const int ve = row * 128 + ((lane & 7) ^ sw) * 16, vo = row * 128 + ((lane & 7) ^ (sw | 4)) * 16;
    sfor<8, 12>([&](auto N) { g1_dma1<N.v>(c, ve, vo, L_XA, xmid + c.P * 65536); });
    WAIT_VM_BAR(9);
    STAMP(K2, 0);
    f32x4 acc[2][4], sa;
    g1_mma(xb, w1f, acc, sa, [&](auto KS) {
      constexpr int ks = KS.v;
      if constexpr (ks == 3) { if (LAYER) WAIT_VM_BAR(8); else WAIT_VM_BAR(10); SCHED(); }
      if constexpr (ks == 7) { if (LAYER) WAIT_VM_BAR(0); else WAIT_VM_BAR(10); SCHED(); }
      if constexpr (ks == 13) { if (!LAYER) WAIT_VM(16); poll<true>(c, lane, CT_C + 64); SCHED(); }
    }, [&](auto N) {
      constexpr int n = N.v;
      if constexpr (n < 4) g1_dma1<12 + n>(c, ve, vo, L_XA, xmid + c.P * 65536);
      if constexpr (!LAYER && n >= 4 && n < 10) ld_w1<22 + n>(c, v16, w1f);
      if constexpr (!LAYER && n >= 10) ld_w2<n - 10>(c, v16, w2f);
    });
    STAMP(K2, 1);
    const int xo = xmid + (16 + c.P) * 65536;
    g1_epi(c, lane, acc, sa, hbuf + (c.P * 4 * 56 + c.hks) * 1024, [&](auto N) {
      constexpr int n = N.v;
      if constexpr (n == -1) { if (!got<(LAYER ? 0 : 6)>(32u * ep)) wait_ge<true>(c, lane, CT_C + 64, 32u * ep); }
      if constexpr (n >= 0 && n < 8) g1_dma1<n>(c, ve, vo, L_XB, xo);
      if constexpr (n >= 10 && n < 18) g1_dma1<n - 2>(c, ve, vo, L_XB, xo);
    });
    STAMP(K2, 2);
  }
  {
    const int lane = fresh_lane(c);
    const int g = lane >> 4, cc = lane & 15, q = cc >> 2, p = lane & 3, v16 = lane * 16;
    const int sx = ((q >> 1) & 1) | ((g & 1) << 1);
    const char* xb[4];
#pragma unroll
    for (int i = 0; i < 4; ++i) xb[i] = smem + L_XB + (8 * g + q) * 128 + ((((i + w) & 3) ^ sx) * 32) + 8 * p;
    WAIT_VM_BAR(16);
    STAMP(K2, 3);
    f32x4 acc[2][4], sa;
    g1_mma(xb, w1f, acc, sa, [&](auto KS) {
      constexpr int ks = KS.v;
      if constexpr (ks == 6) {
        if (LAYER) WAIT_VM_BAR(0); else WAIT_VM_BAR(12);
        sig_bar<false>(c, lane, CT_H + c.P);
        SCHED();
      }
      if constexpr (ks == 15) {
#pragma unroll
        for (int i = 0; i < 2; ++i) xr[i] = ld64(c.rs, (c.ct_ * 64 + w * 16 + cc) * 128 + i * 32 + g * 8, xmid + c.P * 65536 + c.sub * 64);
        SCHED();
      }
    }, [&](auto N) {
      constexpr int n = N.v;
      if constexpr (!LAYER) ld_w2<22 + n>(c, v16, w2f);
      if constexpr (!LAYER && n >= 30) ld_w2<24 + n>(c, v16, w2f);
    });
    STAMP(K2, 4);
    poll<false>(c, lane, CT_H + c.P);
    const int ho = hbuf + ((c.P * 4 + c.sub * 2) * 56 + 14 * w) * 1024;
    g1_epi(c, lane, acc, sa, hbuf + ((16 + c.P) * 4 * 56 + c.hks) * 1024, [&](auto N) {
      constexpr int n = N.v;
      if constexpr (n == -1) { if (!got<0>(16u * ep)) wait_ge<false>(c, lane, CT_H + c.P, 16u * ep); }
      if constexpr (n >= 0) g2_ldh1<n>(c, v16, hf, ho);
    });
    STAMP(K2, 5);
  }
  {
    const int lane = fresh_lane(c);
    const int g = lane >> 4, cc = lane & 15;
    const int ho = hbuf + ((c.P * 4 + c.sub * 2) * 56 + 14 * w) * 1024, hoB = ho + 64 * 56 * 1024;
    STAMP(K3, 0);
    f32x4 acc[2][4];
    u32 o = 0;
    g2_mma<10, 1>(c, lane, hf, w2f, acc, ho, [&](auto S) {
      constexpr int s = S.v;
      if constexpr (s == 6) { WAIT_VM(10); o = sig_begin(c, lane); SCHED(); }
      if constexpr (s == 7) { sig_end<false>(c, lane, o, CT_H + 16 + c.P); SCHED(); }
    });
    STAMP(K3, 1);
    poll<false>(c, lane, CT_H + 16 + c.P);
    g2_epi<LAYER == 1>(c, lane, acc, xr, 0, xnext + c.P * 65536 + c.sub * 32768, nop);
    STAMP(K3, 2);
    if (!got<(LAYER ? 1 : 2)>(16u * ep)) wait_ge<false>(c, lane, CT_H + 16 + c.P, 16u * ep);
    sfor<0, 8>([&](auto N) { g2_ldh1<N.v>(c, lane * 16, hf, hoB); });
#pragma unroll
    for (int i = 0; i < 2; ++i) xr[i] = ld64(c.rs, (c.ct_ * 64 + w * 16 + cc) * 128 + i * 32 + g * 8, xmid + (16 + c.P) * 65536 + c.sub * 64);
    if (LAYER == 0) {
      WAIT_VM(10);
      o = sig_begin(c, lane);
      sig_end<true>(c, lane, o, CT_X);
    }
    SCHED();
    STAMP(K3, 3);
  }
  {
    const int lane = fresh_lane(c);
    const int hoB = hbuf + (((16 + c.P) * 4 + c.sub * 2) * 56 + 14 * w) * 1024;
    f32x4 acc[2][4];
    g2_mma<4, 2>(c, lane, hf, w2f, acc, hoB, [&](auto S) {
      constexpr int s = S.v;
      if constexpr (LAYER == 0 && s == 13) { poll<true>(c, lane, CT_X); SCHED(); }
    });
    STAMP(K3, 4);
    g2_epi<LAYER == 1>(c, lane, acc, xr, 1, xnext + (16 + c.P) * 65536 + c.sub * 32768, [&](int) {
      if (LAYER == 0) {
        if (!got<0>(32u)) wait_ge<true>(c, lane, CT_X, 32u);
        conv_load(c, lane, inA, xnext, WS_ST + 65536, 0);
        SCHED();
      }
    });
    STAMP(K3, 5);
  }
}

constexpr int KG_RS = 528, KG_X = 16 * KG_RS, KG_CH = 64 * KG_RS + 1296;
__global__ void __launch_bounds__(256, 1) main_kernel(const u16* x, const u16* lar, const u16* aim, const u16* cre,
                                                      const u16* cim, const u16* ldt, const u16* dsk, const u16* w1,
                                                      const u16* b1, const u16* w2, const u16* b2, char* ws, u16* out) {
  extern __shared__ __attribute__((aligned(16))) char smem[];
  const int t = threadIdx.x, lane = t & 63, b = blockIdx.x;
  const int w = __builtin_amdgcn_readfirstlane(t >> 6);
  const u32 junk = warm_code((b >> 3) * 4 + w, lane);
  STAMP(0, 0);
  const int ch = w >> 1, d = 2 * b + ch, n = t & 127;
  const u32 po = (u32)(d * NST + n) * 2, po2 = po ^ 128u;
#define LD16(p, off) (*(const u16*)((const char*)(p) + (u32)(off)))
  const u16 r0 = ldt[d], r1 = LD16(lar, po), r2 = LD16(aim, po), r3 = LD16(cre, po), r4 = LD16(cim, po);
  const u16 r5 = LD16(cre, po2), r6 = LD16(cim, po2);
  const u32 gen = __hip_atomic_load((gu32*)(ws + WS_GEN), RLX_AGENT);
  const bool early = !(b & 64);
  const int pks = (b & 7) + (b >> 7) * 8, pcg = (b >> 3) & 7;
  u32x4 rv[8], rw[2];
  {
    const rsrc_t rr = __builtin_amdgcn_make_buffer_rsrc((void*)(early ? x : w1), 0, early ? SEQ * DM * 2 : DM * FF * 2, 0x00020000);
    const int vo = early ? w * 8192 + lane * 16 : ((b & 63) < 56 ? (t >> 5) * (FF * 2) + (t & 31) * 16 : 0x40000000);
    const int so = early ? (b & 63) * 32768 : pks * (32 * FF * 2) + min(pcg, 6) * 512, st = early ? 1024 : 8 * FF * 2;
#pragma unroll
    for (int j = 0; j < 8; ++j) rv[j] = __builtin_amdgcn_raw_buffer_load_b128(rr, (j < 4 || early) ? vo : 0x40000000, so + (j & 3) * st + (j >> 2) * (early ? 4096 : 0), 0);
    const rsrc_t r2w = __builtin_amdgcn_make_buffer_rsrc((void*)w2, 0, DM * FF * 2, 0x00020000);
#pragma unroll
    for (int j = 0; j < 2; ++j) rw[j] = __builtin_amdgcn_raw_buffer_load_b128(r2w, (t >> 4) * 1024 + (t & 15) * 16 + j * 16384, min(b >> 2, 55) * 32768 + (b & 3) * 256, 0);
  }
  CIn inA, inB;
  Ctx c;
  c.rs = __builtin_amdgcn_make_buffer_rsrc(ws, 0, WS_END, 0x00020000);
  c.ct = (gu32*)(ws + WS_CT);
  c.smem = smem; c.ws = ws; c.out = out;
  c.lane0 = lane; c.w = w; c.b = b;
  c.P = (b & 7) * 2 + ((b >> 3) & 1);
  const int idx = b >> 4;
  const int sg = idx >> 1, w3 = -(int)(w == 3);
  c.eo = idx & 1; c.sub = idx >> 3; c.ct_ = idx & 7;
  c.ff0 = 14 * sg + (((6 * c.eo + 2 * w) & ~w3) | ((12 + c.eo) & w3));
  c.hks = 7 * sg + (((3 * c.eo + w) & ~w3) | (6 & w3));
  c.f1o = w < 3 ? WS_W1P + c.ff0 * 16384 + 16384 : WS_ONE; c.f1s = w < 3 ? 4096 : 0;
  {
    const u32 r7 = *(const u32*)(dsk + 2 * b);
    u32 r8 = LD16(b1, min(c.ff0 * 16 + (lane & 15) + (lane >> 5) * 16, FF - 1) * 2), r9 = LD16(b2, (c.ct_ * 64 + w * 16 + (lane & 15)) * 2);
    SCHED();
    char* kg = smem + L_XA + ch * KG_CH;
    char* stg = smem + L_XA + 2 * KG_CH;
    const int i = lane & 15, g = lane >> 4;
    int ix[9];
#pragma unroll
    for (int e2 = 0; e2 < 9; ++e2) { ix[e2] = 2 * cidx(max(2047 - 8 * t - e2, 0)); asm volatile("" : "+v"(ix[e2])); }
    int wi[2][4];
    {
      const int m8 = -(i >> 3), s1 = -(w & 1);
      const int h1 = ((256 + 128 * (i >> 2) + 2 * (i & 3)) & ~m8) | ((128 * (i >> 1) + 4 * (i & 1)) & m8);
      const int hp = (((i + (i & 8) * 15) & ~s1) | (h1 & s1)) + 32 * g, hq = 1024 + 128 * i + 32 * g;
#pragma unroll
      for (int r = 0; r < 4; ++r) {
        wi[0][r] = 2 * cidx(hp + 8 * r); wi[1][r] = 2 * cidx(hq + 8 * r > 2048 ? 2064 : hq + 8 * r);
        asm volatile("" : "+v"(wi[0][r]), "+v"(wi[1][r]));
      }
    }
    if (t < 16) *(u32x4*)(smem + (t >> 2) * L_KC + 4096 + (t & 3) * 16) = u32x4{0, 0, 0, 0};
    if (t >= 64 && t < 128) *(u32x4*)(smem + L_U + ((t - 64) >> 5) * L_UC + (t & 31) * 16) = u32x4{0, 0, 0, 0};
    if (t == 128) *(u32*)(smem + L_SIG) = 0u;
    st64(c.rs, t * 8, WS_DUMP + b * 2048, u32x2{0, 0});
    if (w == 2) poll0(c, lane, WS_F1);
    SCHED();
    STAMP(0, 1);
    const float dt = rbf(expf(bf2f(r0)));
    const float cr = bf2f(r3), ci = bf2f(r4), cr2 = bf2f(r5), ci2 = bf2f(r6);
    const float S = wave_sum(sqrt_(cr * cr + ci * ci) + sqrt_(cr2 * cr2 + ci2 * ci2)) * (2.002f * dt);
    int e = __builtin_amdgcn_frexp_expf(S);
    e = (S > 0.0f && S < 3.0e38f) ? max(-100, min(100, e)) : 14;
    const float scale = __builtin_amdgcn_ldexpf(2.0f, 14 - e);
    if (n == 0) *(float*)(smem + L_MISC + ch * 4) = __builtin_amdgcn_ldexpf(1.0f, e - 14);
    const float a2 = (-rbf(expf(bf2f(r1))) * dt) * 1.44269504088896f;
    const float br = (bf2f(r2) * dt) * 0.159154943091895f;
    {
      char* yp = kg + n * 4;
      char* xp = kg + KG_X + n * 4;
      const CM e128 = cexp_(a2, -br, 128.f), e8 = cexp_(a2, br, 8.f), e1 = cexp_(a2, -br, 1.f), e2 = cexp_(a2, -br, 2.f);
      const CM e4 = cexp_(a2, -br, 4.f);
      f32x2 f0 = {(cr * dt) * scale, -(ci * dt) * scale};
      f32x2 f = cmul(f0, cexp_(a2, -br, 512.f)), y = {1.f, 0.f};
      f32x2 g1 = cmul(f0, e128), g2 = cmul(g1, e128), g3 = cmul(g2, e128);
#define PUT(base, row, v) *(u32*)(base + (row) * KG_RS) = pack_h(v[0], v[1])
      auto chain = [&](auto K) {
        constexpr int k = K.v;
        PUT(yp, k, y);
        if (k < 15) y = cmul(y, e8);
        if (k < 4) { const f32x2 q4 = cmul(f, e4); PUT(xp, 24 + 2 * k, f); PUT(xp, 25 + 2 * k, q4); }
        else if (k < 13) PUT(xp, 28 + k, f);
        if (k < 12) f = cmul(f, e128);
        if (k < 8) { PUT(xp, k, f0); PUT(xp, 8 + k, g1); }
        if (k < 7) { f0 = cmul(f0, e1); g1 = cmul(g1, e1); }
        if (k >= 8 && k < 12) { PUT(xp, 8 + k, g2); PUT(xp, 12 + k, g3); }
        if (k >= 8 && k < 11) { g2 = cmul(g2, e2); g3 = cmul(g3, e2); }
      };
      c.tag = __builtin_amdgcn_readfirstlane(gen) * 0x9E3779B1u + 0x7F4A7C15u;
      sfor<0, 2>(chain);
      asm volatile("" : "+v"(y), "+v"(f), "+v"(f0), "+v"(g1));
      if (b < 64) xpose(b, rv, c.rs, stg);
      else if (early) row_stats(b & 63, rv, c.rs);
      asm volatile("" : "+v"(y), "+v"(f), "+v"(f0), "+v"(g1));
      sfor<2, 12>(chain);
      asm volatile("" : "+v"(y), "+v"(f));
      if (early) {
        WAIT_VM(0);
        __syncthreads();
        if (w == 0 && lane < 8)
          __hip_atomic_store((gu32*)(ws + WS_F0 + lane * 2048 + (b & 7) * 256 + ((b >> 3) & 7) * 8 + (b >> 7) * 4), c.tag, RLX_AGENT);
      }
      asm volatile("" : "+v"(y), "+v"(f));
      STAMP(0, 2);
      sfor<12, 16>(chain);
      STAMP(0, 3);
      if (!early && pcg < 7) pack_w1(pks, pcg, rv, c.rs, stg);
#undef PUT
    }
    pack_w2_put(rw, smem + L_XB + 55552);
    __syncthreads();
    STAMP(0, 4);
    {
      const char* ya = kg + i * KG_RS + g * 16;
      const char* xa = kg + KG_X + (16 * (w & 1) + i) * KG_RS + g * 16;
      const char* xc = kg + KG_X + (32 + i) * KG_RS + g * 16;
      u32x4 fa[8], fb[8], fc[8];
#pragma unroll
      for (int ks = 0; ks < 8; ++ks) { fa[ks] = *(const u32x4*)(ya + ks * 64); fb[ks] = *(const u32x4*)(xa + ks * 64); }
#pragma unroll
      for (int ks = 0; ks < 8; ++ks) fc[ks] = *(const u32x4*)(xc + ks * 64);
      f32x4 ac0, ac1;
      SCHED();
      sfor<0, 8>([&](auto KS) { mma_hh<KS.v == 0>(ac0, fa[KS.v], fb[KS.v]); });
      sfor<0, 8>([&](auto KS) { mma_hh<KS.v == 0>(ac1, fa[KS.v], fc[KS.v]); });
      MFMA_DONE();
#pragma unroll
      for (int r = 0; r < 4; ++r) {
        *(u16*)(kg + 64 * KG_RS + wi[0][r]) = __builtin_bit_cast(u16, (_Float16)ac0[r]);
        *(u16*)(kg + 64 * KG_RS + wi[1][r]) = __builtin_bit_cast(u16, (_Float16)ac1[r]);
      }
    }
    if (!early) WAIT_VM(0);
    __syncthreads();
    if (w == 1 && !early && lane < 8)
      __hip_atomic_store((gu32*)(ws + WS_F1 + lane * 2048 + (b & 7) * 256 + ((b >> 3) & 7) * 8 + (b >> 7) * 4), c.tag, RLX_AGENT);
    if (w == 0) poll0(c, lane, WS_F0);
#pragma unroll
    for (int c2 = 0; c2 < 2; ++c2) {
      u32 v[9];
#pragma unroll
      for (int e2 = 0; e2 < 9; ++e2) v[e2] = *(const u16*)(smem + L_XA + c2 * KG_CH + 64 * KG_RS + ix[e2]);
      if (t == 255) v[8] = 0u;
      u32x4 oe, oo;
#pragma unroll
      for (int j = 0; j < 4; ++j) { oe[j] = v[2 * j] | (v[2 * j + 1] << 16); oo[j] = v[2 * j + 1] | (v[2 * j + 2] << 16); }
      *(u32x4*)(smem + c2 * 2 * L_KC + t * 16) = oe;
      *(u32x4*)(smem + c2 * 2 * L_KC + L_KC + t * 16) = oo;
    }
    const f32x2 ksc = *(const f32x2*)(smem + L_MISC);
    c.ks0 = __uint_as_float(__builtin_amdgcn_readfirstlane(__float_as_uint(ksc[0])));
    c.ks1 = __uint_as_float(__builtin_amdgcn_readfirstlane(__float_as_uint(ksc[1])));
    const u32 ds = __builtin_amdgcn_readfirstlane(r7);
    c.dsk0 = lo_bf(ds); c.dsk1 = hi_bf(ds);
    c.cs1 = lo_bf(r8); c.bb2 = lo_bf(r9);
    asm volatile("" :: "v"(junk));
    STAMP(0, 5);
    if (w == 0 && !got0<0>(c.tag)) wait0(c, lane, WS_F0);
    __syncthreads();
    conv_load(c, lane, inA, WS_X, WS_ST, 0);
    conv_load(c, lane, inB, WS_X, WS_ST, 1);
    pack_w2_out(b, c.rs, smem + L_XB + 55552);
    STAMP(0, 6);
  }
  u32x4 w1f[2][16], w2f[14][4];
  layer_body<0>(c, w1f, w2f, inA, inB);
  layer_body<1>(c, w1f, w2f, inA, inB);
}

extern "C" __global__ void code_end_marker() {}

extern "C" void kernel_launch(void* const* d_in, const int* in_sizes, int n_in, void* d_out, int out_size,
                              void* d_ws, size_t ws_size, hipStream_t stream) {
  (void)in_sizes; (void)n_in; (void)out_size; (void)ws_size;
  char* ws = (char*)d_ws + ((ws_size - (size_t)WS_END) & ~(size_t)(2 * MB - 1));
  (void)hipFuncSetAttribute((const void*)main_kernel, hipFuncAttributeMaxDynamicSharedMemorySize, MAIN_LDS);
  main_kernel<<<256, 256, MAIN_LDS, stream>>>((const u16*)d_in[0], (const u16*)d_in[1], (const u16*)d_in[2],
                                              (const u16*)d_in[3], (const u16*)d_in[4], (const u16*)d_in[5],
                                              (const u16*)d_in[6], (const u16*)d_in[8], (const u16*)d_in[9],
                                              (const u16*)d_in[10], (const u16*)d_in[11], ws, (u16*)d_out);
}
```

```cpp
#include <hip/hip_runtime.h>
#include <hip/hip_bf16.h>

typedef unsigned short u16;
typedef unsigned int u32;
typedef long long i64;
typedef __attribute__((ext_vector_type(4))) short s16x4;
typedef __attribute__((ext_vector_type(8))) __bf16 bf16x8;
typedef __attribute__((ext_vector_type(2))) __bf16 bf16x2;
typedef __attribute__((ext_vector_type(8))) _Float16 f16x8;
typedef __attribute__((ext_vector_type(2))) _Float16 f16x2;
typedef __attribute__((ext_vector_type(4))) float f32x4;
typedef __attribute__((ext_vector_type(2))) float f32x2;
typedef __attribute__((ext_vector_type(4))) unsigned u32x4;
typedef __attribute__((ext_vector_type(2))) unsigned u32x2;
typedef __attribute__((ext_vector_type(2))) long long i64x2;
typedef __attribute__((address_space(1))) unsigned gu32;
typedef __amdgpu_buffer_rsrc_t rsrc_t;
#define LAS __attribute__((address_space(3)))
#define DEVI __device__ __forceinline__
template <int V> struct IC { static constexpr int v = V; };
template <int I, int N, class F> __device__ __forceinline__ void sfor(F&& f) {
  if constexpr (I < N) { f(IC<I>{}); sfor<I + 1, N>(f); }
}
#define RLX_AGENT __ATOMIC_RELAXED, __HIP_MEMORY_SCOPE_AGENT

constexpr int SEQ = 2048, DM = 512, NST = 128, FF = 1792;
constexpr float LN_EPS = 1e-5f;
constexpr float FX_SCALE = 16777216.0f;
constexpr float FX_INV = 1.0f / 16777216.0f;
constexpr int SC1 = 16;

constexpr int MB = 1024 * 1024;
constexpr int WS_X = 0;
constexpr int WS_W1P = 12 * MB;
constexpr int WS_W2P = 14 * MB;
constexpr int WS_H = 16 * MB;
constexpr int WS_ST = 32 * MB;
constexpr int WS_S1 = 33 * MB;
constexpr int WS_GEN = 32 * MB + 196608;
constexpr int WS_F0 = WS_GEN + 256, WS_F1 = WS_F0 + 16384;
constexpr int WS_CT = 32 * MB + 262144;
constexpr int WS_ONE = 32 * MB + 393216;
constexpr int WS_DUMP = 32 * MB + 524288;
constexpr int WS_END = 34 * MB;
constexpr int CT_C = 0, CT_X = 128, CT_H = 256, CT_TMO = 288;
constexpr int SPIN_CAP = 1 << 16;

#define STAMP(k, i) do {} while (0)

DEVI float bf2f(u16 v) { return __uint_as_float((u32)v << 16); }
DEVI float lo_bf(u32 w) { return __uint_as_float(w << 16); }
DEVI float hi_bf(u32 w) { return __uint_as_float(w & 0xffff0000u); }
DEVI float rbf(float x) { return (float)(__bf16)x; }
DEVI u32 pack_bf(float a, float b) { f32x2 q = {a, b}; return __builtin_bit_cast(u32, __builtin_convertvector(q, bf16x2)); }
DEVI u32 pack_h(float a, float b) { f32x2 q = {a, b}; return __builtin_bit_cast(u32, __builtin_convertvector(q, f16x2)); }
DEVI float dot2bf(u32 a, u32 b, float c) {
  return __builtin_amdgcn_fdot2_f32_bf16(__builtin_bit_cast(bf16x2, a), __builtin_bit_cast(bf16x2, b), c, false);
}
DEVI f32x4 mfma_bf(u32x4 a, u32x4 b, f32x4 c) {
  return __builtin_amdgcn_mfma_f32_16x16x32_bf16(__builtin_bit_cast(bf16x8, a), __builtin_bit_cast(bf16x8, b), c, 0, 0, 0);
}
DEVI f32x4 mfma_h(u32x4 a, u32x4 b, f32x4 c) {
  return __builtin_amdgcn_mfma_f32_16x16x32_f16(__builtin_bit_cast(f16x8, a), __builtin_bit_cast(f16x8, b), c, 0, 0, 0);
}
DEVI u32x2 tr_read(const char* p) {
  return __builtin_bit_cast(u32x2, __builtin_amdgcn_ds_read_tr16_b64_v4i16((LAS s16x4*)p));
}
DEVI u32x4 cat2(u32x2 a, u32x2 b) { return __builtin_shufflevector(a, b, 0, 1, 2, 3); }
DEVI u32 lds_addr(const void* p) { return (u32)(size_t)(const LAS char*)p; }
#define WAIT_VM(n) asm volatile("s_waitcnt vmcnt(" #n ")" ::: "memory")
#define WAIT_VM_BAR(n) asm volatile("s_waitcnt vmcnt(" #n ")\n\ts_barrier" ::: "memory")
#define SCHED() __builtin_amdgcn_sched_barrier(0)
template <int CTRL> DEVI float dpp_add(float v) {
  return v + __uint_as_float(__builtin_amdgcn_update_dpp(0u, __float_as_uint(v), CTRL, 0xf, 0xf, true));
}
DEVI float sum_x16(float v) {
  auto r = __builtin_amdgcn_permlane16_swap(__float_as_uint(v), __float_as_uint(v), false, false);
  return __uint_as_float(r[0]) + __uint_as_float(r[1]);
}
DEVI float sum_x32(float v) {
  auto r = __builtin_amdgcn_permlane32_swap(__float_as_uint(v), __float_as_uint(v), false, false);
  return __uint_as_float(r[0]) + __uint_as_float(r[1]);
}
DEVI float wave_sum(float v) {
  v = dpp_add<0xB1>(v); v = dpp_add<0x4E>(v); v = dpp_add<0x141>(v); v = dpp_add<0x140>(v);
  return sum_x32(sum_x16(v));
}
DEVI float fx2f(u32 lo, u32 hi) { return fmaf((float)(int)hi, 4294967296.0f * FX_INV, (float)lo * FX_INV); }
DEVI i64 f2fx(float v) {
  const float a = v * (FX_SCALE / 4294967296.0f), h = __builtin_floorf(a);
  return __builtin_bit_cast(i64, u32x2{(u32)((a - h) * 4294967296.0f), (u32)(int)h});
}
DEVI float sel4(int i, float a, float b, float c, float d) {
  asm volatile("" : "+v"(a), "+v"(b), "+v"(c), "+v"(d));
  return (i & 2) ? ((i & 1) ? d : c) : ((i & 1) ? b : a);
}
DEVI int st_pos(int t) { return ((t >> 8) * 4 + (t & 3)) * 1024 + ((t >> 2) & 63) * 16; }
DEVI float rcp(float x) { return __builtin_amdgcn_rcpf(x); }
DEVI float sqrt_(float x) { return __builtin_amdgcn_sqrtf(x); }

DEVI u32 warm_code(int slice, int lane) {
  unsigned long long pc, pe;
  asm volatile("s_getpc_b64 %0\n\ts_getpc_b64 s[90:91]\n\ts_add_u32 s90, s90, code_end_marker@rel32@lo+4\n\t"
               "s_addc_u32 s91, s91, code_end_marker@rel32@hi+12\n\ts_mov_b64 %1, s[90:91]"
               : "=&s"(pc), "=s"(pe) :: "scc", "s90", "s91");
  const u32 off = min((u32)slice * 512u, (u32)(pe - pc) - 512u) + (lane & 7) * 64;
  u32 junk;
  asm volatile("s_nop 4\n\tglobal_load_dword %0, %1, %2" : "=v"(junk) : "v"(off), "s"(pc) : "memory");
  return junk;
}

DEVI int cidx(int l) {
  const int hb = (int)(pack_bf((float)l, 0.f) & 0xffffu) - (0x4300 - 128);
  return l < 128 ? l : hb;
}
struct CM { f32x2 e, f; };
DEVI f32x2 cmul(f32x2 a, const CM& m) { return a[1] * m.f + a[0] * m.e; }
DEVI CM cexp_(float a2, float br, float p) {
  const float e = __builtin_amdgcn_exp2f(a2 * p), f = __builtin_amdgcn_fractf(br * p);
  const float re = e * __builtin_amdgcn_cosf(f), im = e * __builtin_amdgcn_sinf(f);
  CM r; r.e = f32x2{re, im}; r.f = f32x2{-im, re};
  return r;
}
DEVI void st16(rsrc_t rs, int off, u32x4 v) { __builtin_amdgcn_raw_buffer_store_b128(v, rs, off, 0, SC1); }

DEVI void xpose(int b, const u32x4 (&v)[8], rsrc_t rs, char* smem) {
  const int t = threadIdx.x, lane = t & 63, w = t >> 6;
  constexpr int RS = 1024 + 32;
#pragma unroll
  for (int r = 0; r < 8; ++r) *(u32x4*)(smem + (w + 4 * r) * RS + lane * 16) = v[r];
  __syncthreads();
  const int g = lane >> 4, q = (lane & 15) >> 2, p = lane & 3;
#pragma unroll
  for (int cf = 0; cf < 8; ++cf) {
    const int c0 = (w * 8 + cf) * 16;
    const char* pa = smem + (8 * g + q) * RS + c0 * 2 + 8 * p;
    const u32x4 o = cat2(tr_read(pa), tr_read(pa + 4 * RS));
    st16(rs, WS_X + b * 32768 + (c0 + (lane & 15)) * 64 + g * 16, o);
  }
}
DEVI void row_stats(int b, const u32x4 (&v)[8], rsrc_t rs) {
  const int t = threadIdx.x, lane = t & 63, w = t >> 6;
  const int t0 = b * 32;
  float a[16];
#pragma unroll
  for (int r = 0; r < 8; ++r) {
    float s = 0.f, ss = 0.f;
#pragma unroll
    for (int j = 0; j < 4; ++j) { s = dot2bf(v[r][j], 0x3f803f80u, s); ss = dot2bf(v[r][j], v[r][j], ss); }
    a[2 * r] = s; a[2 * r + 1] = ss;
  }
  const bool o1 = lane & 1, o2 = lane & 2;
#pragma unroll
  for (int k = 0; k < 8; ++k) {
    const float keep = o1 ? a[2 * k + 1] : a[2 * k], send = o1 ? a[2 * k] : a[2 * k + 1];
    a[k] = keep + __uint_as_float(__builtin_amdgcn_update_dpp(0u, __float_as_uint(send), 0xB1, 0xf, 0xf, true));
  }
#pragma unroll
  for (int k = 0; k < 4; ++k) {
    const float keep = o2 ? a[2 * k + 1] : a[2 * k], send = o2 ? a[2 * k] : a[2 * k + 1];
    a[k] = keep + __uint_as_float(__builtin_amdgcn_update_dpp(0u, __float_as_uint(send), 0x4E, 0xf, 0xf, true));
  }
#pragma unroll
  for (int k = 0; k < 4; ++k) a[k] = sum_x32(sum_x16(dpp_add<0x128>(dpp_add<0x124>(a[k]))));
  const i64 sv = f2fx(sel4(lane >> 2, a[0], a[1], a[2], a[3]));
  if (lane < 16)
    __builtin_amdgcn_raw_buffer_store_b64(__builtin_bit_cast(u32x2, sv), rs, st_pos(t0 + w) + lane * 8, WS_ST, SC1);
  if (t >= 96 && t < 128) st16(rs, WS_ST + 65536 + b * 512 + (t - 96) * 16, u32x4{0, 0, 0, 0});
  if (t < 80) st16(rs, WS_CT + b * 1280 + t * 16, u32x4{0, 0, 0, 0});
}

DEVI void pack_w1(int ks, int cg, const u32x4 (&v)[8], rsrc_t rs, char* smem) {
  const int t = threadIdx.x, lane = t & 63, w = t >> 6;
  constexpr int RS = 512 + 32;
#pragma unroll
  for (int it = 0; it < 4; ++it) *(u32x4*)(smem + (it * 8 + (t >> 5)) * RS + (t & 31) * 16) = v[it];
  __syncthreads();
  const int g = lane >> 4, q = (lane & 15) >> 2, p = lane & 3;
  float cs[4];
#pragma unroll
  for (int h = 0; h < 4; ++h) {
    const char* a = smem + (8 * g + q) * RS + (w * 4 + h) * 32 + 8 * p;
    const u32x4 o = cat2(tr_read(a), tr_read(a + 4 * RS));
    st16(rs, WS_W1P + (((cg * 16 + w * 4 + h) * 16 + ks) * 64 + lane) * 16, o);
    float s = 0.f;
#pragma unroll
    for (int j = 0; j < 4; ++j) s = dot2bf(o[j], 0x3f803f80u, s);
    cs[h] = sum_x32(sum_x16(s));
  }
  __builtin_amdgcn_raw_buffer_store_b32(__float_as_uint(sel4(g, cs[0], cs[1], cs[2], cs[3])), rs, (ks * FF + cg * 256 + t) * 4, WS_S1, SC1);
  if (ks == 0 && cg == 0) st16(rs, WS_ONE + t * 16, u32x4{0x3f803f80u, 0x3f803f80u, 0x3f803f80u, 0x3f803f80u});
}

constexpr int W2_RS = 256 + 32;
DEVI void pack_w2_put(const u32x4 (&v)[2], char* smem) {
  const int t = threadIdx.x;
#pragma unroll
  for (int r = 0; r < 2; ++r) *(u32x4*)(smem + (r * 16 + (t >> 4)) * W2_RS + (t & 15) * 16) = v[r];
}
DEVI void pack_w2_out(int b, rsrc_t rs, const char* smem) {
  const int t = threadIdx.x, lane = t & 63, w = t >> 6;
  const int g = lane >> 4, q = (lane & 15) >> 2, p = lane & 3;
#pragma unroll
  for (int cf = 0; cf < 2; ++cf) {
    const char* a = smem + (4 * g + q) * W2_RS + (w * 2 + cf) * 32 + 8 * p;
    const u32x4 o = cat2(tr_read(a), tr_read(a + 16 * W2_RS));
    st16(rs, b < 224 ? WS_W2P + ((((b & 3) * 8 + w * 2 + cf) * 56 + (b >> 2)) * 64 + lane) * 16 : 0x40000000, o);
  }
}

constexpr int L_KC = 4160;
constexpr int L_U = 4 * L_KC, L_UC = 4608;
constexpr int L_XA = 26112, L_XB = L_XA + 65536;
constexpr int L_MISC = L_XB + 65536;
constexpr int L_CST = L_MISC + 1024;
constexpr int L_SIG = L_CST + 4 * 256;
constexpr int MAIN_LDS = L_SIG + 256;
constexpr int L_RED = L_XB;

DEVI u32x4 ld128(rsrc_t rs, int v, int s) { return __builtin_amdgcn_raw_buffer_load_b128(rs, v, s, SC1); }
DEVI u32x2 ld64(rsrc_t rs, int v, int s) { return __builtin_amdgcn_raw_buffer_load_b64(rs, v, s, SC1); }
DEVI void st128(rsrc_t rs, int v, int s, u32x4 d) { __builtin_amdgcn_raw_buffer_store_b128(d, rs, v, s, SC1); }
DEVI void st64(rsrc_t rs, int v, int s, u32x2 d) { __builtin_amdgcn_raw_buffer_store_b64(d, rs, v, s, SC1); }
template <int OFF> DEVI u32x4 ld128a(rsrc_t rs, int v, int s) {
  u32x4 r;
  asm volatile("buffer_load_dwordx4 %0, %1, %2, %3 offen offset:%4 sc1" : "=a"(r) : "v"(v), "s"(rs), "s"(s), "i"(OFF) : "memory");
  return r;
}
template <int OFF> DEVI u32x4 ld128v(rsrc_t rs, int v, int s) {
  u32x4 r;
  asm volatile("buffer_load_dwordx4 %0, %1, %2, %3 offen offset:%4 sc1" : "=v"(r) : "v"(v), "s"(rs), "s"(s), "i"(OFF) : "memory");
  return r;
}
template <int OFF> DEVI void dma1k(rsrc_t rs, u32 lds_dst, int v, int soff) {
  asm volatile("s_mov_b32 m0, %0\n\ts_nop 0\n\tbuffer_load_dwordx4 %1, %2, %3 offen offset:%4 sc1 lds"
               :: "s"(lds_dst), "v"(v), "s"(rs), "s"(soff), "i"(OFF) : "memory");
}
#define MMA(NAME, OP, CA, CB)                                                                                   \
  template <bool Z> DEVI void NAME(f32x4& d, const u32x4& a, const u32x4& b) {                                  \
    if (Z) asm volatile(OP " %0, %1, %2, 0" : "=&v"(d) : CA(a), CB(b));                                         \
    else asm volatile(OP " %0, %1, %2, %0" : "+v"(d) : CA(a), CB(b));                                           \
  }
MMA(mma_vv, "v_mfma_f32_16x16x32_bf16", "v", "v")
MMA(mma_av, "v_mfma_f32_16x16x32_bf16", "a", "v")
MMA(mma_va, "v_mfma_f32_16x16x32_bf16", "v", "a")
MMA(mma_hh, "v_mfma_f32_16x16x32_f16", "v", "v")
#define MFMA_DONE() do { SCHED(); asm volatile("s_nop 7" ::: "memory"); SCHED(); } while (0)

struct Ctx {
  rsrc_t rs; gu32* ct; char* smem; char* ws; u16* out;
  int lane0, w, b, P, sub, ct_, ff0, hks, eo, f1o, f1s;
  float dsk0, dsk1, ks0, ks1, bb2, cs1;
  u32 tag;
};
DEVI int fresh_lane(const Ctx& c) { int l = c.lane0; asm volatile("" : "+v"(l)); return l; }
DEVI u32 sig_begin(const Ctx& c, int lane) {
  u32 old;
  asm volatile("ds_add_rtn_u32 %0, %1, %2" : "=v"(old) : "v"(lds_addr(c.smem) + L_SIG + lane * 4), "v"(1u) : "memory");
  return old;
}
template <bool ALL, bool BACK = false> DEVI void sig_end(const Ctx& c, int lane, u32 old, int line) {
  if (BACK) asm volatile("" : "+v"(old) :: "memory");
  else asm volatile("s_waitcnt lgkmcnt(0)" : "+v"(old) :: "memory");
  const bool last = (__builtin_amdgcn_readfirstlane(old) & 3u) == 3u;
  const int v = (last && lane < (ALL ? 8 : 1)) ? (ALL ? lane * 2048 : 0) : 0x40000000;
  asm volatile("s_nop 4\n\tbuffer_atomic_add %0, %1, %2, %3 offen" :: "v"(1u), "v"(v), "s"(c.rs),
               "s"(WS_CT + (line + (ALL ? (c.b >> 3) & 7 : 0)) * 256) : "memory");
}
template <bool ALL, int R = 0> DEVI void poll(const Ctx& c, int lane, int line) {
  const int v = ALL ? (lane & 7) * 256 : 0, so = WS_CT + (line + (ALL ? (c.b & 7) * 8 : 0)) * 256;
  if (R == 0) asm volatile("s_nop 4\n\tbuffer_load_dword v254, %0, %1, %2 offen sc1" :: "v"(v), "s"(c.rs), "s"(so) : "memory", "v254");
  else asm volatile("s_nop 4\n\tbuffer_load_dword v255, %0, %1, %2 offen sc1" :: "v"(v), "s"(c.rs), "s"(so) : "memory", "v255");
}
template <int YOUNGER, int R = 0> DEVI bool got(u32 want) {
  unsigned long long m;
  if (R == 0) asm volatile("s_waitcnt vmcnt(%2)\n\tv_cmp_le_u32_e64 %0, %1, v254" : "=s"(m) : "s"(want), "i"(YOUNGER) : "memory");
  else asm volatile("s_waitcnt vmcnt(%2)\n\tv_cmp_le_u32_e64 %0, %1, v255" : "=s"(m) : "s"(want), "i"(YOUNGER) : "memory");
  return m == ~0ull;
}
template <bool ALL> DEVI void wait_ge(const Ctx& c, int lane, int line, u32 want) {
  int sp = 0;
#pragma nounroll
  do {

    poll<ALL, 0>(c, lane, line);
    if (++sp > SPIN_CAP) { __hip_atomic_store(c.ct + CT_TMO * 64, 1u, RLX_AGENT); break; }
  } while (!got<0, 0>(want));
}

DEVI void poll0(const Ctx& c, int lane, int set) {
  asm volatile("s_nop 4\n\tbuffer_load_dwordx2 v[254:255], %0, %1, %2 offen sc1" :: "v"((lane & 7) * 256 + (lane >> 3) * 8), "s"(c.rs),
               "s"(set + (c.b & 7) * 2048) : "memory", "v254", "v255");
}
template <int YOUNGER> DEVI bool got0(u32 tag) {
  unsigned long long m, m2;
  asm volatile("s_waitcnt vmcnt(%3)\n\tv_cmp_eq_u32_e64 %0, %2, v254\n\tv_cmp_eq_u32_e64 %1, %2, v255"
               : "=&s"(m), "=s"(m2) : "s"(tag), "i"(YOUNGER) : "memory");
  return (m & m2) == ~0ull;
}
DEVI void wait0(const Ctx& c, int lane, int set) {
  int sp = 0;
#pragma nounroll
  do {
    poll0(c, lane, set);
    if (++sp > SPIN_CAP) { __hip_atomic_store(c.ct + CT_TMO * 64, 1u, RLX_AGENT); break; }
  } while (!got0<0>(c.tag));
}

struct CIn { u32x2 x0, x1; u32x4 s[4]; };
DEVI void conv_load(const Ctx& c, int lane, CIn& in, int xoff, int stoff, int h) {
  const int sx = xoff + (32 * h + 8 * c.w) * 32768 + c.b * 128;
  const int vx = (lane >> 3) * 32768 + (lane & 7) * 8;
  const int ss = stoff + (1024 * h + 256 * c.w) * 16;
#pragma unroll
  for (int j = 0; j < 4; ++j) in.s[j] = ld128(c.rs, lane * 16 + j * 1024, ss);
  in.x0 = ld64(c.rs, vx, sx);
  in.x1 = ld64(c.rs, vx + 64, sx);
}
DEVI void conv_ln(const Ctx& c, int lane, const CIn& in, int h, f32x4& base0, f32x4& base1) {
  constexpr float CH = 4294967296.0f * FX_INV / DM, CL = FX_INV / DM;
  f32x4 m, r;
#pragma unroll
  for (int j = 0; j < 4; ++j) {
    m[j] = fmaf((float)(int)in.s[j][1], CH, (float)in.s[j][0] * CL);
    r[j] = fmaf((float)(int)in.s[j][3], CH, fmaf((float)in.s[j][2], CL, LN_EPS));
  }
#pragma unroll
  for (int j = 0; j < 4; ++j) r[j] = __builtin_amdgcn_rsqf(fmaxf(fmaf(-m[j], m[j], r[j]), LN_EPS));
  const f32x4 rr = r, mr = -m * r;
  const f32x4 x0 = {lo_bf(in.x0[0]), hi_bf(in.x0[0]), lo_bf(in.x0[1]), hi_bf(in.x0[1])};
  const f32x4 x1 = {lo_bf(in.x1[0]), hi_bf(in.x1[0]), lo_bf(in.x1[1]), hi_bf(in.x1[1])};
  const f32x4 u0 = x0 * rr + mr, u1 = x1 * rr + mr;
  char* up = c.smem + L_U + 512 + (1024 * h + 256 * c.w + 4 * lane) * 2;
  *(u32x2*)up = u32x2{pack_h(u0[0], u0[1]), pack_h(u0[2], u0[3])};
  *(u32x2*)(up + L_UC) = u32x2{pack_h(u1[0], u1[1]), pack_h(u1[2], u1[3])};
  base0 = u0 * c.dsk0 + x0; base1 = u1 * c.dsk1 + x1;
}
struct KB { const char* p[4]; };
template <int MM, int LAM> DEVI u32x4 conv_k1(const KB& kb) {
  const u32* kp = (const u32*)(kb.p[(7 - LAM) >> 1] + 64 * (3 - MM) + 512 * ((7 - LAM) & 1));
  return u32x4{kp[0], kp[1], kp[2], kp[3]};
}
template <int MM, int TB> DEVI u32x4 conv_u1(const char* ub) { return *(const u32x4*)(ub + 64 * (3 - MM) + 512 * TB); }
template <int LO> DEVI void conv_ldk(const KB& kb, u32x4 (&af)[4]) {
  sfor<0, 4>([&](auto L) { af[L.v] = conv_k1<0, LO + L.v>(kb); });
}
constexpr int conv_rank(int DT, int LO, int gr, int lt) {
  int k = 0;
  for (int x = 0; x < lt; ++x) { const int tb = DT + (x & 3) - (LO + 4 * gr + (x >> 2)); k += (tb >= 0 && tb < 4); }
  return k;
}
template <int DT, int TB0, bool ZERO, class F>
DEVI void conv_mma(const char* ub, const KB& kb, f32x4 (&acc)[4], const u32x4 (&af0)[4], F&& hook) {
  constexpr int NG = DT ? 2 : 1, LO = DT ? 1 : 0;
  u32x4 af[2][4], bf[2][4];
  sfor<0, 4>([&](auto L) { af[0][L.v] = af0[L.v]; });
  sfor<0, 4>([&](auto T) { constexpr int tb = DT ? 3 - T.v : T.v; bf[0][tb] = conv_u1<0, TB0 + tb>(ub); });
  SCHED();
  sfor<0, 4 * NG>([&](auto ST) {
    constexpr int st = ST.v, mm = st / NG, gr = st % NG;
    constexpr bool more = st + 1 < 4 * NG;
    constexpr int mn = (st + 1) / NG, gn = (st + 1) % NG;
    constexpr bool nu = more && gr == NG - 1;
    sfor<0, 16>([&](auto LT) {
      constexpr int l = LT.v >> 2, T = LT.v & 3, tb = DT + T - (LO + 4 * gr + l);
      if constexpr (tb >= 0 && tb < 4) {
        constexpr int k = conv_rank(DT, LO, gr, LT.v);
        constexpr bool first = ZERO && st == 0 && (DT ? (l == T) : (l == 0));
        mma_hh<first>(acc[T], af[st & 1][l], bf[mm & 1][tb]);
        sfor<(gr == 1 ? 2 * k : k), (gr == 1 ? 2 * k + 2 : k + 1)>([&](auto J) {
          constexpr int j = J.v;
          if constexpr (nu && j < 8) {
            if constexpr (DT != 0) {
              if constexpr (j & 1) bf[mn & 1][3 - (j >> 1)] = conv_u1<mn, TB0 + 3 - (j >> 1)>(ub);
              else af[(st + 1) & 1][j >> 1] = conv_k1<mn, LO + (j >> 1)>(kb);
            } else {
              if constexpr (j >= 1 && j <= 4) bf[mn & 1][j - 1] = conv_u1<mn, TB0 + j - 1>(ub);
              else af[(st + 1) & 1][j ? j - 4 : 0] = conv_k1<mn, LO + (j ? j - 4 : 0)>(kb);
            }
          } else if constexpr (more && !nu && j < 3) af[(st + 1) & 1][j] = conv_k1<mn, LO + 4 * gn + j>(kb);
        });
        SCHED();
        hook(IC<mm * (DT ? 16 : 10) + gr * 10 + k>{});
      }
    });
  });
}
DEVI void conv_part(const Ctx& c, int lane, const f32x4 (&acc)[4]) {
  char* red = c.smem + L_RED;
#pragma unroll
  for (int T = 0; T < 4; ++T) *(f32x4*)(red + (c.w * 4 + T) * 1024 + (lane & 15) * 64 + (lane >> 4) * 16) = acc[T];
}
DEVI void conv_out(const Ctx& c, int lane, const f32x4& base0, const f32x4& base1, int xoff, int h) {
  const int sx = xoff + (16 * h + 4 * c.w) * 65536 + c.b * 256;
  const int vx = (lane >> 4) * 65536 + (lane & 15) * 8;
  const char* p = c.smem + L_RED + c.w * 1024 + lane * 16;
  const f32x4 a0 = *(const f32x4*)p, b0 = *(const f32x4*)(p + 4096), a1 = *(const f32x4*)(p + 8192), b1 = *(const f32x4*)(p + 12288);
  SCHED();
  const f32x4 y0 = (a0 + b0) * c.ks0 + base0, y1 = (a1 + b1) * c.ks1 + base1;
  st64(c.rs, vx, sx, u32x2{pack_bf(y0[0], y0[1]), pack_bf(y0[2], y0[3])});
  st64(c.rs, vx + 128, sx, u32x2{pack_bf(y1[0], y1[1]), pack_bf(y1[2], y1[3])});
}

template <int PC> DEVI void g1_dma1(const Ctx& c, int ve, int vo, int ldsx, int soff) {
  dma1k<(PC & 3) * 1024>(c.rs, lds_addr(c.smem) + ldsx + (4 * (PC >> 2) + c.w) * 4096, (PC & 1) ? vo : ve, soff + (4 * (PC >> 2) + c.w) * 4096);
}
template <bool ALL> DEVI void sig_bar(const Ctx& c, int lane, int line) {
  const int v = (c.w == 0 && lane < (ALL ? 8 : 1)) ? (ALL ? lane * 2048 : 0) : 0x40000000;
  asm volatile("s_nop 4\n\tbuffer_atomic_add %0, %1, %2, %3 offen" :: "v"(1u), "v"(v), "s"(c.rs),
               "s"(WS_CT + (line + (ALL ? (c.b >> 3) & 7 : 0)) * 256) : "memory");
}
template <class F, class G>
DEVI void g1_mma(const char* const (&xb)[4], const u32x4 (&w1f)[2][16], f32x4 (&acc)[2][4], f32x4& sa, F&& mid, G&& hook) {
  u32x2 xf[2][4][2];
#pragma unroll
  for (int i = 0; i < 4; ++i) { xf[0][i][0] = tr_read(xb[i]); xf[0][i][1] = tr_read(xb[i] + 512); }
  SCHED();
  sfor<0, 16>([&](auto KS) {
    constexpr int ks = KS.v;
    mid(KS);
    sfor<0, 4>([&](auto I) {
      constexpr int i = I.v;
      const u32x4 x = cat2(xf[ks & 1][i][0], xf[ks & 1][i][1]);
      mma_vv<ks == 0>(acc[0][i], w1f[0][ks], x);
      if constexpr (ks < 15) xf[(ks + 1) & 1][i][0] = tr_read(xb[i] + (ks + 1) * 4096);
      SCHED();
      if constexpr (ks < 8) mma_av<ks == 0>(acc[1][i], w1f[1][ks], x); else mma_vv<false>(acc[1][i], w1f[1][ks], x);
      if constexpr (ks < 15) xf[(ks + 1) & 1][i][1] = tr_read(xb[i] + (ks + 1) * 4096 + 512);
      SCHED();
      if constexpr (i == 0) { mma_vv<ks == 0>(sa, x, x); hook(IC<2 * ks>{}); SCHED(); }
      if constexpr (i == 2) { hook(IC<2 * ks + 1>{}); SCHED(); }
    });
  });
  MFMA_DONE();
}
template <class F>
DEVI void g1_epi(const Ctx& c, int lane, f32x4 (&acc)[2][4], const f32x4& sa, int hoff, F&& hook) {
  const int g = lane >> 4, cc = lane & 15, q = cc >> 2, p = lane & 3, w = c.w;
  float* sums = (float*)(c.smem + L_MISC);
  {
    const float d = sel4(p, sa[0], sa[1], sa[2], sa[3]);
    sums[q == g ? (w * 16 + cc) * 2 + 1 : 128 + lane] = d;
    const float r = sel4(g, acc[1][1][0], acc[1][2][0], acc[1][3][0], acc[1][0][0]);
    sums[w == 3 ? lane * 2 : 192 + lane] = r;
  }
  f32x4 s1v[2], b1v[2];
#pragma unroll
  for (int j = 0; j < 2; ++j) {
    s1v[j] = *(const f32x4*)(c.smem + L_CST + w * 256 + j * 128 + g * 16);
    b1v[j] = *(const f32x4*)(c.smem + L_CST + w * 256 + j * 128 + 64 + g * 16);
  }
  const int v16 = lane * 16 + (w < 3 ? 0 : 0x40000000), v8 = lane * 8 + (w < 3 ? 0x40000000 : c.eo * 512);
  __syncthreads();
  f32x2 k[4];
#pragma unroll
  for (int i = 0; i < 4; ++i) k[i] = *(const f32x2*)(sums + (((i + w) & 3) * 16 + cc) * 2);
#pragma unroll
  for (int i = 0; i < 4; ++i) {
    const float m = k[i][0] * (1.0f / DM);
    const float ve = fmaxf(fmaf(-m, m, fmaf(k[i][1], 1.0f / DM, LN_EPS)), LN_EPS);
    const float r = __builtin_amdgcn_rsqf(fmaf(ve, 1.0f + LN_EPS, -LN_EPS));
    k[i] = f32x2{r, -m * r};
  }
  SCHED();
  hook(IC<-1>{});
  SCHED();
  sfor<0, 2>([&](auto IH) {
    constexpr int ih = IH.v;
    f32x4 x[4], e[4];
#define STAGE(n) do { SCHED(); hook(IC<10 * ih + n>{}); SCHED(); } while (0)
#pragma unroll
    for (int n = 0; n < 4; ++n) x[n] = k[2 * ih + (n >> 1)][1] * s1v[n & 1] + b1v[n & 1];
#pragma unroll
    for (int n = 0; n < 4; ++n) x[n] = acc[n & 1][2 * ih + (n >> 1)] * k[2 * ih + (n >> 1)][0] + x[n];
    STAGE(0);
#pragma unroll
    for (int n = 0; n < 4; ++n) e[n] = x[n] * x[n];
#pragma unroll
    for (int n = 0; n < 4; ++n) e[n] = e[n] * (-0.0713548162726f * 1.44269504088896f) + (-1.59576912160573f * 1.44269504088896f);
    STAGE(1);
#pragma unroll
    for (int n = 0; n < 4; ++n) e[n] = e[n] * x[n];
    STAGE(2);
#pragma unroll
    for (int r = 0; r < 4; ++r) { e[0][r] = __builtin_amdgcn_exp2f(e[0][r]); e[1][r] = __builtin_amdgcn_exp2f(e[1][r]); }
    STAGE(3);
#pragma unroll
    for (int r = 0; r < 4; ++r) { e[2][r] = __builtin_amdgcn_exp2f(e[2][r]); e[3][r] = __builtin_amdgcn_exp2f(e[3][r]); }
    STAGE(4);
#pragma unroll
    for (int n = 0; n < 4; ++n) e[n] = e[n] + 1.0f;
    STAGE(5);
#pragma unroll
    for (int r = 0; r < 4; ++r) { e[0][r] = rcp(e[0][r]); e[1][r] = rcp(e[1][r]); }
    STAGE(6);
#pragma unroll
    for (int r = 0; r < 4; ++r) { e[2][r] = rcp(e[2][r]); e[3][r] = rcp(e[3][r]); }
    STAGE(7);
#pragma unroll
    for (int n = 0; n < 4; ++n) x[n] = x[n] * e[n];
    STAGE(8);
    u32x4 hv[2];
#pragma unroll
    for (int n = 0; n < 4; ++n) { hv[n >> 1][2 * (n & 1)] = pack_bf(x[n][0], x[n][1]); hv[n >> 1][2 * (n & 1) + 1] = pack_bf(x[n][2], x[n][3]); }
    STAGE(9);
    const int so0 = hoff + ((2 * ih + w) & 3) * 56 * 1024, so1 = hoff + ((2 * ih + 1 + w) & 3) * 56 * 1024;
    st128(c.rs, v16, so0, hv[0]); st128(c.rs, v16, so1, hv[1]);
    st64(c.rs, v8, so0, u32x2{hv[0][0], hv[0][1]}); st64(c.rs, v8, so1, u32x2{hv[1][0], hv[1][1]});
    SCHED();
    if constexpr (ih == 0) { hook(IC<-2>{}); SCHED(); }
#undef STAGE
  });
}

constexpr int PF = 10;
template <int N> DEVI void ld_w1(const Ctx& c, int v, u32x4 (&w1f)[2][16]) {
  constexpr int ks = N >> 1, j = N & 1;
  const int so = (j ? c.f1o + (ks >> 2) * c.f1s : WS_W1P + c.ff0 * 16384 + (ks >> 2) * 4096);
  if constexpr (j && ks < 8) w1f[j][ks] = ld128a<(ks & 3) * 1024>(c.rs, v, so);
  else w1f[j][ks] = ld128v<(ks & 3) * 1024>(c.rs, v, so);
}
template <int N> DEVI void ld_w2(const Ctx& c, int v, u32x4 (&w2f)[14][4]) {
  constexpr int s = N >> 2, j = N & 3;
  w2f[s][j] = ld128a<(s & 3) * 1024>(c.rs, v, WS_W2P + ((c.ct_ * 4 + ((c.w + j) & 3)) * 56 + 14 * c.w + (s & ~3)) * 1024);
}
template <int N> DEVI void g2_ldh1(const Ctx& c, int v16, u32x4 (&hf)[14][2], int ho) {
  constexpr int s = N >> 1, i = N & 1;
  if constexpr (s % 7 == 6)
    hf[s][i] = cat2(ld64(c.rs, (v16 >> 1) + (s & 3) * 1024, ho + i * 56 * 1024 + (s >> 2) * 4096),
                    ld64(c.rs, (v16 >> 1) + (s & 3) * 1024 + 512, ho + i * 56 * 1024 + (s >> 2) * 4096));
  else hf[s][i] = ld128(c.rs, v16 + (s & 3) * 1024, ho + i * 56 * 1024 + (s >> 2) * 4096);
}
template <int PRE, int RATE, class F>
DEVI void g2_mma(const Ctx& c, int lane, u32x4 (&hf)[14][2], const u32x4 (&w2f)[14][4], f32x4 (&acc)[2][4], int ho, F&& mid) {
  const int v16 = lane * 16;
  sfor<0, 14>([&](auto S) {
    constexpr int s = S.v;
    sfor<0, 8>([&](auto M) {
      constexpr int i = M.v >> 2, j = M.v & 3;
      mma_va<s == 0>(acc[i][j], hf[s][i], w2f[s][j]);
      constexpr int per = 4 / RATE, n = 2 * (PRE + RATE * s) + M.v / per;
      if constexpr (M.v % per == per - 1 && n < 28) { g2_ldh1<n>(c, v16, hf, ho); SCHED(); }
    });
    SCHED();
    mid(S);
  });
  MFMA_DONE();
}
template <bool LAST, class F>
DEVI void g2_epi(const Ctx& c, int lane, const f32x4 (&acc)[2][4], const u32x2 (&xr)[2], int h, int xdst, F&& mid) {
  const int g = lane >> 4, cc = lane & 15, q = cc >> 2, p = lane & 3, w = c.w, t = w * 64 + lane;
  char* red = c.smem + L_XA + h * 32768;
#pragma unroll
  for (int jj = 1; jj < 4; ++jj)
#pragma unroll
    for (int i = 0; i < 2; ++i) *(f32x4*)(red + ((((w + jj) & 3) * 3 + jj - 1) * 2 + i) * 1024 + lane * 16) = acc[i][jj];
  __syncthreads();
  char* xt = red + 24576;
  f32x4 pr[2][3];
#pragma unroll
  for (int i = 0; i < 2; ++i)
#pragma unroll
    for (int rk = 0; rk < 3; ++rk) pr[i][rk] = *(const f32x4*)(red + ((w * 3 + rk) * 2 + i) * 1024 + lane * 16);
  mid(0);
#pragma unroll
  for (int i = 0; i < 2; ++i) {
    const f32x4 xv = {lo_bf(xr[i][0]), hi_bf(xr[i][0]), lo_bf(xr[i][1]), hi_bf(xr[i][1])};
    const f32x4 o = ((acc[i][0] + (xv + c.bb2)) + pr[i][0]) + (pr[i][1] + pr[i][2]);
    const u32x2 r = {pack_bf(o[0], o[1]), pack_bf(o[2], o[3])};
    *(u32x2*)(xt + (w * 16 + cc) * 64 + ((i ^ (cc >> 3)) * 32) + g * 8) = r;
  }
  __syncthreads();
  const char* xq = xt + ((w >> 1) * 32 + 8 * g + q) * 64 + (((w & 1) ^ (g & 1)) * 32) + 8 * p;
  const u32x4 f0 = cat2(tr_read(xq), tr_read(xq + 256));
  const int tok = (16 * h + c.P) * 64 + c.sub * 32 + (w & 1) * 16 + cc;
  if (LAST) {
    *(u32x4*)(c.out + (size_t)tok * DM + c.ct_ * 64 + (w >> 1) * 32 + 8 * g) = f0;
  } else {
    const int row = t >> 2;
    const u32x4 v = *(const u32x4*)(xt + row * 64 + (((t & 3) ^ ((row >> 2) & 2)) * 16));
    st128(c.rs, c.ct_ * 4096 + t * 16, xdst, v);
    const char* xo = xq + ((w & 2) ? -2048 : 2048);
    const u32x4 f1 = cat2(tr_read(xo), tr_read(xo + 256));
    float s = 0.f, ss = 0.f, s2 = 0.f, ss2 = 0.f;
#pragma unroll
    for (int j = 0; j < 4; ++j) {
      s = dot2bf(f0[j], 0x3f803f80u, s); ss = dot2bf(f0[j], f0[j], ss);
      s2 = dot2bf(f1[j], 0x3f803f80u, s2); ss2 = dot2bf(f1[j], f1[j], ss2);
    }
    s = sum_x32(sum_x16(s + s2)); ss = sum_x32(sum_x16(ss + ss2));
    const u32 ao = t < 128 && lane < 32 ? WS_ST + 65536 + st_pos(tok) + g * 8 : WS_DUMP + (c.b * 256 + t) * 8;
    (void)__hip_atomic_fetch_add((unsigned long long*)(c.ws + ao), (unsigned long long)f2fx((g & 1) ? ss : s), RLX_AGENT);
  }
}

template <int LAYER>
DEVI void layer_body(const Ctx& c, u32x4 (&w1f)[2][16], u32x4 (&w2f)[14][4], CIn& inA, CIn& inB) {
  constexpr int xin = WS_X + LAYER * 4 * MB, xmid = xin + 2 * MB, xnext = xin + 4 * MB;
  constexpr int hbuf = WS_H + LAYER * 8 * MB, stin = WS_ST + LAYER * 65536;
  constexpr u32 ep = LAYER + 1;
  constexpr int K1 = 1 + 3 * LAYER, K2 = K1 + 1, K3 = K1 + 2;
  const int w = c.w;
  char* smem = c.smem;
  auto nop = [](auto) {};
  {
    const int lane = fresh_lane(c);
    const int g = lane >> 4, cc = lane & 15, mu0 = (w & 1) * 4, v16 = lane * 16;
    const char* ub = smem + L_U + (w >> 1) * L_UC + 512 + 2 * (16 * cc - 16 + 8 * g) - 64 * (mu0 + 3);
    KB kb;
#pragma unroll
    for (int j = 0; j < 4; ++j) {
      u32 a = lds_addr(smem) + (w >> 1) * 2 * L_KC + ((cc & 1) ? 0 : L_KC) + 2 * (2031 - cc + 8 * g - ((cc & 1) ? 0 : 1)) - 64 * (mu0 + 3 + 56) + 1024 * j;
      asm volatile("" : "+v"(a));
      kb.p[j] = (const char*)(const LAS char*)(size_t)a;
    }
    STAMP(K1, 1);
    f32x4 base0, base1;
    u32x4 a4[4];
    conv_ldk<0>(kb, a4);
    conv_ln(c, lane, inA, 0, base0, base1);
    if (!LAYER) poll0(c, lane, WS_F1);
    __syncthreads();
    STAMP(K1, 2);
    f32x4 acc[4];
    u32 o = 0;
    conv_mma<0, 0, true>(ub, kb, acc, a4, [&](auto N) {
      constexpr int n = N.v;
      if constexpr (LAYER && n == 19) { WAIT_VM(0); o = sig_begin(c, lane); SCHED(); }
      if constexpr (LAYER && n == 30) { sig_end<true, true>(c, lane, o, CT_X + 64); SCHED(); }
    });
    MFMA_DONE();
    conv_part(c, lane, acc);
    conv_ldk<1>(kb, a4);
    __syncthreads();
    float s1p[8];
    if (!LAYER) {
      if (!got0<0>(c.tag)) wait0(c, lane, WS_F1);
      asm volatile("" :: "v"(inB.x1));
      const int so1 = (min(c.ff0 * 16 + (lane & 15) + (lane >> 5) * 16, FF - 1) + (lane & 16) * (FF / 2)) * 4;
#pragma unroll
      for (int j = 0; j < 8; ++j)
        asm volatile("s_nop 4\n\tbuffer_load_dword %0, %1, %2, %3 offen sc1" : "=v"(s1p[j]) : "v"(so1), "s"(c.rs), "s"(WS_S1 + j * FF * 4) : "memory");
    }
    conv_out(c, lane, base0, base1, xmid, 0);
    STAMP(K1, 3);
    f32x4 acb[4];
    conv_mma<4, 0, true>(ub, kb, acb, a4, [&](auto N) {
      constexpr int n = N.v;
      if constexpr (!LAYER && n >= 48 && (n & 1)) { ld_w1<(n - 48) / 2>(c, v16, w1f); SCHED(); }
      if constexpr (n == 47) {
        WAIT_VM(0);
        o = sig_begin(c, lane);
        if (LAYER) poll<true>(c, lane, CT_X + 64);
        SCHED();
      }
      if constexpr (n == 58) { sig_end<true, true>(c, lane, o, CT_C); SCHED(); }
    });
    STAMP(K1, 4);
    if (!LAYER) {
#pragma unroll
      for (int j = 0; j < 8; ++j) asm volatile("" : "+v"(s1p[j]));
      const float sv = sum_x16(((s1p[0] + s1p[1]) + (s1p[2] + s1p[3])) + ((s1p[4] + s1p[5]) + (s1p[6] + s1p[7])));
      ((float*)(smem + L_CST))[w * 64 + lane] = (lane & 16) ? c.cs1 : sv;
    }
    if (LAYER) {
      if (!got<1>(32u)) wait_ge<true>(c, lane, CT_X + 64, 32u);
      conv_load(c, lane, inB, xin, stin, 1);
    }
    conv_ldk<0>(kb, a4);
    STAMP(K1, 5);
    if (!LAYER) sfor<8, 12>([&](auto N) { ld_w1<N.v>(c, v16, w1f); });
    conv_ln(c, lane, inB, 1, base0, base1);
    if (!LAYER) sfor<12, 14>([&](auto N) { ld_w1<N.v>(c, v16, w1f); });
    __syncthreads();
    conv_mma<0, 4, false>(ub, kb, acb, a4, [&](auto N) {
      constexpr int n = N.v;
      if constexpr (!LAYER && n % 3 == 1 && n < 36) { ld_w1<14 + n / 3>(c, v16, w1f); SCHED(); }
      if constexpr (n == 30) { poll<true>(c, lane, CT_C); SCHED(); }
    });
    MFMA_DONE();
    conv_part(c, lane, acb);
    __syncthreads();
    conv_out(c, lane, base0, base1, xmid, 1);
    STAMP(K1, 6);
    if (!got<(LAYER ? 2 : 4)>(32u * ep)) wait_ge<true>(c, lane, CT_C, 32u * ep);
    const int row = lane >> 3, sw = ((row >> 1) & 1) << 1;
    const int ve = row * 128 + ((lane & 7) ^ sw) * 16, vo = row * 128 + ((lane & 7) ^ (sw | 4)) * 16;
    if (!LAYER && c.b == 0 && w == 0) __hip_atomic_store((gu32*)(c.ws + WS_GEN), c.tag, RLX_AGENT);
    sfor<0, 8>([&](auto N) { g1_dma1<N.v>(c, ve, vo, L_XA, xmid + c.P * 65536); });
    WAIT_VM(8);
    o = sig_begin(c, lane);
    sig_end<true>(c, lane, o, CT_C + 64);
    STAMP(K1, 7);
  }
  u32x4 hf[14][2];
  u32x2 xr[2];
  {
    const int lane = fresh_lane(c);
    const int g = lane >> 4, cc = lane & 15, q = cc >> 2, p = lane & 3, v16 = lane * 16;
    const int sx = ((q >> 1) & 1) | ((g & 1) << 1);
    const char* xb[4];
#pragma unroll
    for (int i = 0; i < 4; ++i) xb[i] = smem + L_XA + (8 * g + q) * 128 + ((((i + w) & 3) ^ sx) * 32) + 8 * p;
    const int row = lane >> 3, sw = ((row >> 1) & 1) << 1;
    const int ve = row * 128 + ((lane & 7) ^ sw) * 16, vo = row * 128 + ((lane & 7) ^ (sw | 4)) * 16;
    sfor<8, 12>([&](auto N) { g1_dma1<N.v>(c, ve, vo, L_XA, xmid + c.P * 65536); });
    WAIT_VM_BAR(9);
    STAMP(K2, 0);
    f32x4 acc[2][4], sa;
    g1_mma(xb, w1f, acc, sa, [&](auto KS) {
      constexpr int ks = KS.v;
      if constexpr (ks == 3) { if (LAYER) WAIT_VM_BAR(8); else WAIT_VM_BAR(10); SCHED(); }
      if constexpr (ks == 7) { if (LAYER) WAIT_VM_BAR(0); else WAIT_VM_BAR(10); SCHED(); }
      if constexpr (ks == 13) { if (!LAYER) WAIT_VM(16); poll<true>(c, lane, CT_C + 64); SCHED(); }
    }, [&](auto N) {
      constexpr int n = N.v;
      if constexpr (n < 4) g1_dma1<12 + n>(c, ve, vo, L_XA, xmid + c.P * 65536);
      if constexpr (!LAYER && n >= 4 && n < 10) ld_w1<22 + n>(c, v16, w1f);
      if constexpr (!LAYER && n >= 10) ld_w2<n - 10>(c, v16, w2f);
    });
    STAMP(K2, 1);
    const int xo = xmid + (16 + c.P) * 65536;
    g1_epi(c, lane, acc, sa, hbuf + (c.P * 4 * 56 + c.hks) * 1024, [&](auto N) {
      constexpr int n = N.v;
      if constexpr (n == -1) { if (!got<(LAYER ? 0 : 6)>(32u * ep)) wait_ge<true>(c, lane, CT_C + 64, 32u * ep); }
      if constexpr (n >= 0 && n < 8) g1_dma1<n>(c, ve, vo, L_XB, xo);
      if constexpr (n >= 10 && n < 18) g1_dma1<n - 2>(c, ve, vo, L_XB, xo);
    });
    STAMP(K2, 2);
  }
  {
    const int lane = fresh_lane(c);
    const int g = lane >> 4, cc = lane & 15, q = cc >> 2, p = lane & 3, v16 = lane * 16;
    const int sx = ((q >> 1) & 1) | ((g & 1) << 1);
    const char* xb[4];
#pragma unroll
    for (int i = 0; i < 4; ++i) xb[i] = smem + L_XB + (8 * g + q) * 128 + ((((i + w) & 3) ^ sx) * 32) + 8 * p;
    WAIT_VM_BAR(16);
    STAMP(K2, 3);
    f32x4 acc[2][4], sa;
    g1_mma(xb, w1f, acc, sa, [&](auto KS) {
      constexpr int ks = KS.v;
      if constexpr (ks == 5) {
        if (LAYER) WAIT_VM_BAR(0); else WAIT_VM_BAR(10);
        sig_bar<false>(c, lane, CT_H + c.P);
        SCHED();
      }
      if constexpr (ks == 15) {
#pragma unroll
        for (int i = 0; i < 2; ++i) xr[i] = ld64(c.rs, (c.ct_ * 64 + w * 16 + cc) * 128 + i * 32 + g * 8, xmid + c.P * 65536 + c.sub * 64);
        SCHED();
      }
    }, [&](auto N) {
      constexpr int n = N.v;
      if constexpr (!LAYER) ld_w2<22 + n>(c, v16, w2f);
      if constexpr (!LAYER && n >= 30) ld_w2<24 + n>(c, v16, w2f);
    });
    STAMP(K2, 4);
    poll<false>(c, lane, CT_H + c.P);
    const int ho = hbuf + ((c.P * 4 + c.sub * 2) * 56 + 14 * w) * 1024;
    g1_epi(c, lane, acc, sa, hbuf + ((16 + c.P) * 4 * 56 + c.hks) * 1024, [&](auto N) {
      constexpr int n = N.v;
      if constexpr (n == -1) { if (!got<0>(16u * ep)) wait_ge<false>(c, lane, CT_H + c.P, 16u * ep); }
      if constexpr (n >= 0) g2_ldh1<n>(c, v16, hf, ho);
    });
    STAMP(K2, 5);
  }
  {
    const int lane = fresh_lane(c);
    const int g = lane >> 4, cc = lane & 15;
    const int ho = hbuf + ((c.P * 4 + c.sub * 2) * 56 + 14 * w) * 1024, hoB = ho + 64 * 56 * 1024;
    STAMP(K3, 0);
    f32x4 acc[2][4];
    u32 o = 0;
    g2_mma<10, 1>(c, lane, hf, w2f, acc, ho, [&](auto S) {
      constexpr int s = S.v;
      if constexpr (s == 5) { WAIT_VM(10); o = sig_begin(c, lane); SCHED(); }
      if constexpr (s == 6) { sig_end<false>(c, lane, o, CT_H + 16 + c.P); SCHED(); }
    });
    STAMP(K3, 1);
    poll<false>(c, lane, CT_H + 16 + c.P);
    g2_epi<LAYER == 1>(c, lane, acc, xr, 0, xnext + c.P * 65536 + c.sub * 32768, nop);
    STAMP(K3, 2);
    if (!got<(LAYER ? 1 : 2)>(16u * ep)) wait_ge<false>(c, lane, CT_H + 16 + c.P, 16u * ep);
    sfor<0, 8>([&](auto N) { g2_ldh1<N.v>(c, lane * 16, hf, hoB); });
#pragma unroll
    for (int i = 0; i < 2; ++i) xr[i] = ld64(c.rs, (c.ct_ * 64 + w * 16 + cc) * 128 + i * 32 + g * 8, xmid + (16 + c.P) * 65536 + c.sub * 64);
    if (LAYER == 0) {
      WAIT_VM(10);
      o = sig_begin(c, lane);
      sig_end<true>(c, lane, o, CT_X);
    }
    SCHED();
    STAMP(K3, 3);
  }
  {
    const int lane = fresh_lane(c);
    const int hoB = hbuf + (((16 + c.P) * 4 + c.sub * 2) * 56 + 14 * w) * 1024;
    f32x4 acc[2][4];
    g2_mma<4, 2>(c, lane, hf, w2f, acc, hoB, [&](auto S) {
      constexpr int s = S.v;
      if constexpr (LAYER == 0 && s == 13) { poll<true>(c, lane, CT_X); SCHED(); }
    });
    STAMP(K3, 4);
    g2_epi<LAYER == 1>(c, lane, acc, xr, 1, xnext + (16 + c.P) * 65536 + c.sub * 32768, [&](int) {
      if (LAYER == 0) {
        if (!got<0>(32u)) wait_ge<true>(c, lane, CT_X, 32u);
        conv_load(c, lane, inA, xnext, WS_ST + 65536, 0);
        SCHED();
      }
    });
    STAMP(K3, 5);
  }
}

constexpr int KG_RS = 528, KG_X = 16 * KG_RS, KG_CH = 64 * KG_RS + 1296;
__global__ void __launch_bounds__(256, 1) main_kernel(const u16* x, const u16* lar, const u16* aim, const u16* cre,
                                                      const u16* cim, const u16* ldt, const u16* dsk, const u16* w1,
                                                      const u16* b1, const u16* w2, const u16* b2, char* ws, u16* out) {
  extern __shared__ __attribute__((aligned(16))) char smem[];
  const int t = threadIdx.x, lane = t & 63, b = blockIdx.x;
  const int w = __builtin_amdgcn_readfirstlane(t >> 6);
  const u32 junk = warm_code((b >> 3) * 4 + w, lane);
  STAMP(0, 0);
  const int ch = w >> 1, d = 2 * b + ch, n = t & 127;
  const u32 po = (u32)(d * NST + n) * 2, po2 = po ^ 128u;
#define LD16(p, off) (*(const u16*)((const char*)(p) + (u32)(off)))
  const u16 r0 = ldt[d], r1 = LD16(lar, po), r2 = LD16(aim, po), r3 = LD16(cre, po), r4 = LD16(cim, po);
  const u16 r5 = LD16(cre, po2), r6 = LD16(cim, po2);
  const u32 gen = __hip_atomic_load((gu32*)(ws + WS_GEN), RLX_AGENT);
  const bool early = !(b & 64);
  const int pks = (b & 7) + (b >> 7) * 8, pcg = (b >> 3) & 7;
  u32x4 rv[8], rw[2];
  {
    const rsrc_t rr = __builtin_amdgcn_make_buffer_rsrc((void*)(early ? x : w1), 0, early ? SEQ * DM * 2 : DM * FF * 2, 0x00020000);
    const int vo = early ? w * 1024 + lane * 16 : ((b & 63) < 56 ? (t >> 5) * (FF * 2) + (t & 31) * 16 : 0x40000000);
    const int so = early ? (b & 63) * 32768 : pks * (32 * FF * 2) + min(pcg, 6) * 512, st = early ? 4096 : 8 * FF * 2;
#pragma unroll
    for (int j = 0; j < 8; ++j) rv[j] = __builtin_amdgcn_raw_buffer_load_b128(rr, (j < 4 || early) ? vo : 0x40000000, so + (j & 3) * st + (j >> 2) * (early ? 16384 : 0), 0);
    const rsrc_t r2w = __builtin_amdgcn_make_buffer_rsrc((void*)w2, 0, DM * FF * 2, 0x00020000);
#pragma unroll
    for (int j = 0; j < 2; ++j) rw[j] = __builtin_amdgcn_raw_buffer_load_b128(r2w, (t >> 4) * 1024 + (t & 15) * 16 + j * 16384, min(b >> 2, 55) * 32768 + (b & 3) * 256, 0);
  }
  CIn inA, inB;
  Ctx c;
  c.rs = __builtin_amdgcn_make_buffer_rsrc(ws, 0, WS_END, 0x00020000);
  c.ct = (gu32*)(ws + WS_CT);
  c.smem = smem; c.ws = ws; c.out = out;
  c.lane0 = lane; c.w = w; c.b = b;
  c.P = (b & 7) * 2 + ((b >> 3) & 1);
  const int idx = b >> 4;
  const int sg = idx >> 1, w3 = -(int)(w == 3);
  c.eo = idx & 1; c.sub = idx >> 3; c.ct_ = idx & 7;
  c.ff0 = 14 * sg + (((6 * c.eo + 2 * w) & ~w3) | ((12 + c.eo) & w3));
  c.hks = 7 * sg + (((3 * c.eo + w) & ~w3) | (6 & w3));
  c.f1o = w < 3 ? WS_W1P + c.ff0 * 16384 + 16384 : WS_ONE; c.f1s = w < 3 ? 4096 : 0;
  {
    const u32 r7 = *(const u32*)(dsk + 2 * b);
    u32 r8 = LD16(b1, min(c.ff0 * 16 + (lane & 15) + (lane >> 5) * 16, FF - 1) * 2), r9 = LD16(b2, (c.ct_ * 64 + w * 16 + (lane & 15)) * 2);
    SCHED();
    char* kg = smem + L_XA + ch * KG_CH;
    char* stg = smem + L_XA + 2 * KG_CH;
    const int i = lane & 15, g = lane >> 4;
    int ix[9];
#pragma unroll
    for (int e2 = 0; e2 < 9; ++e2) { ix[e2] = 2 * cidx(max(2047 - 8 * t - e2, 0)); asm volatile("" : "+v"(ix[e2])); }
    int wi[2][4];
    {
      const int m8 = -(i >> 3), s1 = -(w & 1);
      const int h1 = ((256 + 128 * (i >> 2) + 2 * (i & 3)) & ~m8) | ((128 * (i >> 1) + 4 * (i & 1)) & m8);
      const int hp = (((i + (i & 8) * 15) & ~s1) | (h1 & s1)) + 32 * g, hq = 1024 + 128 * i + 32 * g;
#pragma unroll
      for (int r = 0; r < 4; ++r) {
        wi[0][r] = 2 * cidx(hp + 8 * r); wi[1][r] = 2 * cidx(hq + 8 * r > 2048 ? 2064 : hq + 8 * r);
        asm volatile("" : "+v"(wi[0][r]), "+v"(wi[1][r]));
      }
    }
    if (t < 16) *(u32x4*)(smem + (t >> 2) * L_KC + 4096 + (t & 3) * 16) = u32x4{0, 0, 0, 0};
    if (t >= 64 && t < 128) *(u32x4*)(smem + L_U + ((t - 64) >> 5) * L_UC + (t & 31) * 16) = u32x4{0, 0, 0, 0};
    if (t == 128) *(u32*)(smem + L_SIG) = 0u;
    st64(c.rs, t * 8, WS_DUMP + b * 2048, u32x2{0, 0});
    if (w >= 2) poll0(c, lane, w == 2 ? WS_F1 : WS_F0);
    SCHED();
    STAMP(0, 1);
    const float dt = rbf(expf(bf2f(r0)));
    const float cr = bf2f(r3), ci = bf2f(r4), cr2 = bf2f(r5), ci2 = bf2f(r6);
    const float S = wave_sum(sqrt_(cr * cr + ci * ci) + sqrt_(cr2 * cr2 + ci2 * ci2)) * (2.002f * dt);
    int e = __builtin_amdgcn_frexp_expf(S);
    e = (S > 0.0f && S < 3.0e38f) ? max(-100, min(100, e)) : 14;
    const float scale = __builtin_amdgcn_ldexpf(2.0f, 14 - e);
    if (n == 0) *(float*)(smem + L_MISC + ch * 4) = __builtin_amdgcn_ldexpf(1.0f, e - 14);
    const float a2 = (-rbf(expf(bf2f(r1))) * dt) * 1.44269504088896f;
    const float br = (bf2f(r2) * dt) * 0.159154943091895f;
    {
      char* yp = kg + n * 4;
      char* xp = kg + KG_X + n * 4;
      const CM e128 = cexp_(a2, -br, 128.f), e8 = cexp_(a2, br, 8.f), e1 = cexp_(a2, -br, 1.f), e2 = cexp_(a2, -br, 2.f);
      const CM e4 = cexp_(a2, -br, 4.f);
      f32x2 f0 = {(cr * dt) * scale, -(ci * dt) * scale};
      f32x2 f = cmul(f0, cexp_(a2, -br, 512.f)), y = {1.f, 0.f};
      f32x2 g1 = cmul(f0, e128), g2 = cmul(g1, e128), g3 = cmul(g2, e128);
#define PUT(base, row, v) *(u32*)(base + (row) * KG_RS) = pack_h(v[0], v[1])
      auto chain = [&](auto K) {
        constexpr int k = K.v;
        PUT(yp, k, y);
        if (k < 15) y = cmul(y, e8);
        if (k < 4) { const f32x2 q4 = cmul(f, e4); PUT(xp, 24 + 2 * k, f); PUT(xp, 25 + 2 * k, q4); }
        else if (k < 13) PUT(xp, 28 + k, f);
        if (k < 12) f = cmul(f, e128);
        if (k < 8) { PUT(xp, k, f0); PUT(xp, 8 + k, g1); }
        if (k < 7) { f0 = cmul(f0, e1); g1 = cmul(g1, e1); }
        if (k >= 8 && k < 12) { PUT(xp, 8 + k, g2); PUT(xp, 12 + k, g3); }
        if (k >= 8 && k < 11) { g2 = cmul(g2, e2); g3 = cmul(g3, e2); }
      };
      c.tag = __builtin_amdgcn_readfirstlane(gen) * 0x9E3779B1u + 0x7F4A7C15u;
      sfor<0, 2>(chain);
      asm volatile("" : "+v"(y), "+v"(f), "+v"(f0), "+v"(g1));
      if (b < 64) xpose(b, rv, c.rs, stg);
      else if (early) row_stats(b & 63, rv, c.rs);
      asm volatile("" : "+v"(y), "+v"(f), "+v"(f0), "+v"(g1));
      sfor<2, 12>(chain);
      asm volatile("" : "+v"(y), "+v"(f));
      if (early) {
        WAIT_VM(0);
        __syncthreads();
        if (w == 0 && lane < 8)
          __hip_atomic_store((gu32*)(ws + WS_F0 + lane * 2048 + (b & 7) * 256 + ((b >> 3) & 7) * 8 + (b >> 7) * 4), c.tag, RLX_AGENT);
      }
      asm volatile("" : "+v"(y), "+v"(f));
      STAMP(0, 2);
      sfor<12, 16>(chain);
      STAMP(0, 3);
      if (!early && pcg < 7) pack_w1(pks, pcg, rv, c.rs, stg);
#undef PUT
    }
    pack_w2_put(rw, smem + L_XB + 55552);
    __syncthreads();
    STAMP(0, 4);
    {
      const char* ya = kg + i * KG_RS + g * 16;
      const char* xa = kg + KG_X + (16 * (w & 1) + i) * KG_RS + g * 16;
      const char* xc = kg + KG_X + (32 + i) * KG_RS + g * 16;
      u32x4 fa[8], fb[8], fc[8];
#pragma unroll
      for (int ks = 0; ks < 8; ++ks) { fa[ks] = *(const u32x4*)(ya + ks * 64); fb[ks] = *(const u32x4*)(xa + ks * 64); }
#pragma unroll
      for (int ks = 0; ks < 8; ++ks) fc[ks] = *(const u32x4*)(xc + ks * 64);
      f32x4 ac0, ac1;
      SCHED();
      sfor<0, 8>([&](auto KS) { mma_hh<KS.v == 0>(ac0, fa[KS.v], fb[KS.v]); });
      sfor<0, 8>([&](auto KS) { mma_hh<KS.v == 0>(ac1, fa[KS.v], fc[KS.v]); });
      MFMA_DONE();
#pragma unroll
      for (int r = 0; r < 4; ++r) {
        *(u16*)(kg + 64 * KG_RS + wi[0][r]) = __builtin_bit_cast(u16, (_Float16)ac0[r]);
        *(u16*)(kg + 64 * KG_RS + wi[1][r]) = __builtin_bit_cast(u16, (_Float16)ac1[r]);
      }
    }
    if (!early) WAIT_VM(0);
    __syncthreads();
    if (w == 1 && !early && lane < 8)
      __hip_atomic_store((gu32*)(ws + WS_F1 + lane * 2048 + (b & 7) * 256 + ((b >> 3) & 7) * 8 + (b >> 7) * 4), c.tag, RLX_AGENT);
    if (w == 0) poll0(c, lane, WS_F0);
#pragma unroll
    for (int c2 = 0; c2 < 2; ++c2) {
      u32 v[9];
#pragma unroll
      for (int e2 = 0; e2 < 9; ++e2) v[e2] = *(const u16*)(smem + L_XA + c2 * KG_CH + 64 * KG_RS + ix[e2]);
      if (t == 255) v[8] = 0u;
      u32x4 oe, oo;
#pragma unroll
      for (int j = 0; j < 4; ++j) { oe[j] = v[2 * j] | (v[2 * j + 1] << 16); oo[j] = v[2 * j + 1] | (v[2 * j + 2] << 16); }
      *(u32x4*)(smem + c2 * 2 * L_KC + t * 16) = oe;
      *(u32x4*)(smem + c2 * 2 * L_KC + L_KC + t * 16) = oo;
    }
    const f32x2 ksc = *(const f32x2*)(smem + L_MISC);
    c.ks0 = __uint_as_float(__builtin_amdgcn_readfirstlane(__float_as_uint(ksc[0])));
    c.ks1 = __uint_as_float(__builtin_amdgcn_readfirstlane(__float_as_uint(ksc[1])));
    const u32 ds = __builtin_amdgcn_readfirstlane(r7);
    c.dsk0 = lo_bf(ds); c.dsk1 = hi_bf(ds);
    c.cs1 = lo_bf(r8); c.bb2 = lo_bf(r9);
    asm volatile("" :: "v"(junk));
    STAMP(0, 5);
    if (w == 0 && !got0<0>(c.tag)) wait0(c, lane, WS_F0);
    __syncthreads();
    conv_load(c, lane, inA, WS_X, WS_ST, 0);
    conv_load(c, lane, inB, WS_X, WS_ST, 1);
    pack_w2_out(b, c.rs, smem + L_XB + 55552);
    STAMP(0, 6);
  }
  u32x4 w1f[2][16], w2f[14][4];
  layer_body<0>(c, w1f, w2f, inA, inB);
  layer_body<1>(c, w1f, w2f, inA, inB);
}

extern "C" __global__ void code_end_marker() {}

extern "C" void kernel_launch(void* const* d_in, const int* in_sizes, int n_in, void* d_out, int out_size,
                              void* d_ws, size_t ws_size, hipStream_t stream) {
  (void)in_sizes; (void)n_in; (void)out_size; (void)ws_size;
  char* ws = (char*)d_ws + ((ws_size - (size_t)WS_END) & ~(size_t)(2 * MB - 1));
  (void)hipFuncSetAttribute((const void*)main_kernel, hipFuncAttributeMaxDynamicSharedMemorySize, MAIN_LDS);
  main_kernel<<<256, 256, MAIN_LDS, stream>>>((const u16*)d_in[0], (const u16*)d_in[1], (const u16*)d_in[2],
                                              (const u16*)d_in[3], (const u16*)d_in[4], (const u16*)d_in[5],
                                              (const u16*)d_in[6], (const u16*)d_in[8], (const u16*)d_in[9],
                                              (const u16*)d_in[10], (const u16*)d_in[11], ws, (u16*)d_out);
}
```

```cpp
#include <hip/hip_runtime.h>
#include <hip/hip_bf16.h>

typedef unsigned short u16;
typedef unsigned int u32;
typedef long long i64;
typedef __attribute__((ext_vector_type(4))) short s16x4;
typedef __attribute__((ext_vector_type(8))) __bf16 bf16x8;
typedef __attribute__((ext_vector_type(2))) __bf16 bf16x2;
typedef __attribute__((ext_vector_type(8))) _Float16 f16x8;
typedef __attribute__((ext_vector_type(2))) _Float16 f16x2;
typedef __attribute__((ext_vector_type(4))) float f32x4;
typedef __attribute__((ext_vector_type(2))) float f32x2;
typedef __attribute__((ext_vector_type(4))) unsigned u32x4;
typedef __attribute__((ext_vector_type(2))) unsigned u32x2;
typedef __attribute__((ext_vector_type(2))) long long i64x2;
typedef __attribute__((address_space(1))) unsigned gu32;
typedef __amdgpu_buffer_rsrc_t rsrc_t;
#define LAS __attribute__((address_space(3)))
#define DEVI __device__ __forceinline__
template <int V> struct IC { static constexpr int v = V; };
template <int I, int N, class F> __device__ __forceinline__ void sfor(F&& f) {
  if constexpr (I < N) { f(IC<I>{}); sfor<I + 1, N>(f); }
}
#define RLX_AGENT __ATOMIC_RELAXED, __HIP_MEMORY_SCOPE_AGENT

constexpr int SEQ = 2048, DM = 512, NST = 128, FF = 1792;
constexpr float LN_EPS = 1e-5f;
constexpr float FX_SCALE = 16777216.0f;
constexpr float FX_INV = 1.0f / 16777216.0f;
constexpr int SC1 = 16;

constexpr int MB = 1024 * 1024;
constexpr int WS_X = 0;
constexpr int WS_W1P = 12 * MB;
constexpr int WS_W2P = 14 * MB;
constexpr int WS_H = 16 * MB;
constexpr int WS_ST = 32 * MB;
constexpr int WS_S1 = 33 * MB;
constexpr int WS_GEN = 32 * MB + 196608;
constexpr int WS_F0 = WS_GEN + 256, WS_F1 = WS_F0 + 16384;
constexpr int WS_CT = 32 * MB + 262144;
constexpr int WS_ONE = 32 * MB + 393216;
constexpr int WS_DUMP = 32 * MB + 524288;
constexpr int WS_END = 34 * MB;
constexpr int CT_C = 0, CT_X = 128, CT_H = 256, CT_TMO = 288;
constexpr int SPIN_CAP = 1 << 16;


DEVI float bf2f(u16 v) { return __uint_as_float((u32)v << 16); }
DEVI float lo_bf(u32 w) { return __uint_as_float(w << 16); }
DEVI float hi_bf(u32 w) { return __uint_as_float(w & 0xffff0000u); }
DEVI float rbf(float x) { return (float)(__bf16)x; }
DEVI u32 pack_bf(float a, float b) { f32x2 q = {a, b}; return __builtin_bit_cast(u32, __builtin_convertvector(q, bf16x2)); }
DEVI u32 pack_h(float a, float b) { f32x2 q = {a, b}; return __builtin_bit_cast(u32, __builtin_convertvector(q, f16x2)); }
DEVI float dot2bf(u32 a, u32 b, float c) {
  return __builtin_amdgcn_fdot2_f32_bf16(__builtin_bit_cast(bf16x2, a), __builtin_bit_cast(bf16x2, b), c, false);
}
DEVI f32x4 mfma_bf(u32x4 a, u32x4 b, f32x4 c) {
  return __builtin_amdgcn_mfma_f32_16x16x32_bf16(__builtin_bit_cast(bf16x8, a), __builtin_bit_cast(bf16x8, b), c, 0, 0, 0);
}
DEVI f32x4 mfma_h(u32x4 a, u32x4 b, f32x4 c) {
  return __builtin_amdgcn_mfma_f32_16x16x32_f16(__builtin_bit_cast(f16x8, a), __builtin_bit_cast(f16x8, b), c, 0, 0, 0);
}
DEVI u32x2 tr_read(const char* p) {
  return __builtin_bit_cast(u32x2, __builtin_amdgcn_ds_read_tr16_b64_v4i16((LAS s16x4*)p));
}
DEVI u32x4 cat2(u32x2 a, u32x2 b) { return __builtin_shufflevector(a, b, 0, 1, 2, 3); }
DEVI u32 lds_addr(const void* p) { return (u32)(size_t)(const LAS char*)p; }
#define WAIT_VM(n) asm volatile("s_waitcnt vmcnt(" #n ")" ::: "memory")
#define WAIT_VM_BAR(n) asm volatile("s_waitcnt vmcnt(" #n ")\n\ts_barrier" ::: "memory")
#define SCHED() __builtin_amdgcn_sched_barrier(0)
template <int CTRL> DEVI float dpp_add(float v) {
  return v + __uint_as_float(__builtin_amdgcn_update_dpp(0u, __float_as_uint(v), CTRL, 0xf, 0xf, true));
}
DEVI float sum_x16(float v) {
  auto r = __builtin_amdgcn_permlane16_swap(__float_as_uint(v), __float_as_uint(v), false, false);
  return __uint_as_float(r[0]) + __uint_as_float(r[1]);
}
DEVI float sum_x32(float v) {
  auto r = __builtin_amdgcn_permlane32_swap(__float_as_uint(v), __float_as_uint(v), false, false);
  return __uint_as_float(r[0]) + __uint_as_float(r[1]);
}
DEVI float wave_sum(float v) {
  v = dpp_add<0xB1>(v); v = dpp_add<0x4E>(v); v = dpp_add<0x141>(v); v = dpp_add<0x140>(v);
  return sum_x32(sum_x16(v));
}
DEVI float fx2f(u32 lo, u32 hi) { return fmaf((float)(int)hi, 4294967296.0f * FX_INV, (float)lo * FX_INV); }
DEVI i64 f2fx(float v) {
  const float a = v * (FX_SCALE / 4294967296.0f), h = __builtin_floorf(a);
  return __builtin_bit_cast(i64, u32x2{(u32)((a - h) * 4294967296.0f), (u32)(int)h});
}
DEVI float sel4(int i, float a, float b, float c, float d) {
  asm volatile("" : "+v"(a), "+v"(b), "+v"(c), "+v"(d));
  return (i & 2) ? ((i & 1) ? d : c) : ((i & 1) ? b : a);
}
DEVI int st_pos(int t) { return ((t >> 8) * 4 + (t & 3)) * 1024 + ((t >> 2) & 63) * 16; }
DEVI float rcp(float x) { return __builtin_amdgcn_rcpf(x); }
DEVI float sqrt_(float x) { return __builtin_amdgcn_sqrtf(x); }

DEVI u32 warm_code(int slice, int lane) {
  unsigned long long pc, pe;
  asm volatile("s_getpc_b64 %0\n\ts_getpc_b64 s[90:91]\n\ts_add_u32 s90, s90, code_end_marker@rel32@lo+4\n\t"
               "s_addc_u32 s91, s91, code_end_marker@rel32@hi+12\n\ts_mov_b64 %1, s[90:91]"
               : "=&s"(pc), "=s"(pe) :: "scc", "s90", "s91");
  const u32 off = min((u32)slice * 512u, (u32)(pe - pc) - 512u) + (lane & 7) * 64;
  u32 junk;
  asm volatile("s_nop 4\n\tglobal_load_dword %0, %1, %2" : "=v"(junk) : "v"(off), "s"(pc) : "memory");
  return junk;
}

DEVI int cidx(int l) {
  const int hb = (int)(pack_bf((float)l, 0.f) & 0xffffu) - (0x4300 - 128);
  return l < 128 ? l : hb;
}
struct CM { f32x2 e, f; };
DEVI f32x2 cmul(f32x2 a, const CM& m) { return a[1] * m.f + a[0] * m.e; }
DEVI CM cexp_(float a2, float br, float p) {
  const float e = __builtin_amdgcn_exp2f(a2 * p), f = __builtin_amdgcn_fractf(br * p);
  const float re = e * __builtin_amdgcn_cosf(f), im = e * __builtin_amdgcn_sinf(f);
  CM r; r.e = f32x2{re, im}; r.f = f32x2{-im, re};
  return r;
}
DEVI void st16(rsrc_t rs, int off, u32x4 v) { __builtin_amdgcn_raw_buffer_store_b128(v, rs, off, 0, SC1); }

DEVI void xpose(int b, const u32x4 (&v)[8], rsrc_t rs, char* smem) {
  const int t = threadIdx.x, lane = t & 63, w = t >> 6;
  constexpr int RS = 1024 + 32;
#pragma unroll
  for (int r = 0; r < 8; ++r) *(u32x4*)(smem + (w + 4 * r) * RS + lane * 16) = v[r];
  __syncthreads();
  const int g = lane >> 4, q = (lane & 15) >> 2, p = lane & 3;
#pragma unroll
  for (int cf = 0; cf < 8; ++cf) {
    const int c0 = (w * 8 + cf) * 16;
    const char* pa = smem + (8 * g + q) * RS + c0 * 2 + 8 * p;
    const u32x4 o = cat2(tr_read(pa), tr_read(pa + 4 * RS));
    st16(rs, WS_X + b * 32768 + (c0 + (lane & 15)) * 64 + g * 16, o);
  }
}
DEVI void row_stats(int b, const u32x4 (&v)[8], rsrc_t rs) {
  const int t = threadIdx.x, lane = t & 63, w = t >> 6;
  const int t0 = b * 32;
  float a[16];
#pragma unroll
  for (int r = 0; r < 8; ++r) {
    float s = 0.f, ss = 0.f;
#pragma unroll
    for (int j = 0; j < 4; ++j) { s = dot2bf(v[r][j], 0x3f803f80u, s); ss = dot2bf(v[r][j], v[r][j], ss); }
    a[2 * r] = s; a[2 * r + 1] = ss;
  }
  const bool o1 = lane & 1, o2 = lane & 2;
#pragma unroll
  for (int k = 0; k < 8; ++k) {
    const float keep = o1 ? a[2 * k + 1] : a[2 * k], send = o1 ? a[2 * k] : a[2 * k + 1];
    a[k] = keep + __uint_as_float(__builtin_amdgcn_update_dpp(0u, __float_as_uint(send), 0xB1, 0xf, 0xf, true));
  }
#pragma unroll
  for (int k = 0; k < 4; ++k) {
    const float keep = o2 ? a[2 * k + 1] : a[2 * k], send = o2 ? a[2 * k] : a[2 * k + 1];
    a[k] = keep + __uint_as_float(__builtin_amdgcn_update_dpp(0u, __float_as_uint(send), 0x4E, 0xf, 0xf, true));
  }
#pragma unroll
  for (int k = 0; k < 4; ++k) a[k] = sum_x32(sum_x16(dpp_add<0x128>(dpp_add<0x124>(a[k]))));
  const i64 sv = f2fx(sel4(lane >> 2, a[0], a[1], a[2], a[3]));
  if (lane < 16)
    __builtin_amdgcn_raw_buffer_store_b64(__builtin_bit_cast(u32x2, sv), rs, st_pos(t0 + w) + lane * 8, WS_ST, SC1);
  if (t >= 96 && t < 128) st16(rs, WS_ST + 65536 + b * 512 + (t - 96) * 16, u32x4{0, 0, 0, 0});
  if (t < 80) st16(rs, WS_CT + b * 1280 + t * 16, u32x4{0, 0, 0, 0});
}

DEVI void pack_w1(int ks, int cg, const u32x4 (&v)[8], rsrc_t rs, char* smem) {
  const int t = threadIdx.x, lane = t & 63, w = t >> 6;
  constexpr int RS = 512 + 32;
#pragma unroll
  for (int it = 0; it < 4; ++it) *(u32x4*)(smem + (it * 8 + (t >> 5)) * RS + (t & 31) * 16) = v[it];
  __syncthreads();
  const int g = lane >> 4, q = (lane & 15) >> 2, p = lane & 3;
  float cs[4];
#pragma unroll
  for (int h = 0; h < 4; ++h) {
    const char* a = smem + (8 * g + q) * RS + (w * 4 + h) * 32 + 8 * p;
    const u32x4 o = cat2(tr_read(a), tr_read(a + 4 * RS));
    st16(rs, WS_W1P + (((cg * 16 + w * 4 + h) * 16 + ks) * 64 + lane) * 16, o);
    float s = 0.f;
#pragma unroll
    for (int j = 0; j < 4; ++j) s = dot2bf(o[j], 0x3f803f80u, s);
    cs[h] = sum_x32(sum_x16(s));
  }
  __builtin_amdgcn_raw_buffer_store_b32(__float_as_uint(sel4(g, cs[0], cs[1], cs[2], cs[3])), rs, (ks * FF + cg * 256 + t) * 4, WS_S1, SC1);
  if (ks == 0 && cg == 0) st16(rs, WS_ONE + t * 16, u32x4{0x3f803f80u, 0x3f803f80u, 0x3f803f80u, 0x3f803f80u});
}

constexpr int W2_RS = 256 + 32;
DEVI void pack_w2_put(const u32x4 (&v)[2], char* smem) {
  const int t = threadIdx.x;
#pragma unroll
  for (int r = 0; r < 2; ++r) *(u32x4*)(smem + (r * 16 + (t >> 4)) * W2_RS + (t & 15) * 16) = v[r];
}
DEVI void pack_w2_out(int b, rsrc_t rs, const char* smem) {
  const int t = threadIdx.x, lane = t & 63, w = t >> 6;
  const int g = lane >> 4, q = (lane & 15) >> 2, p = lane & 3;
#pragma unroll
  for (int cf = 0; cf < 2; ++cf) {
    const char* a = smem + (4 * g + q) * W2_RS + (w * 2 + cf) * 32 + 8 * p;
    const u32x4 o = cat2(tr_read(a), tr_read(a + 16 * W2_RS));
    st16(rs, b < 224 ? WS_W2P + ((((b & 3) * 8 + w * 2 + cf) * 56 + (b >> 2)) * 64 + lane) * 16 : 0x40000000, o);
  }
}

constexpr int L_KC = 4160;
constexpr int L_U = 4 * L_KC, L_UC = 4608;
constexpr int L_XA = 26112, L_XB = L_XA + 65536;
constexpr int L_MISC = L_XB + 65536;
constexpr int L_CST = L_MISC + 1024;
constexpr int L_SIG = L_CST + 4 * 256;
constexpr int MAIN_LDS = L_SIG + 256;
constexpr int L_RED = L_XB;

DEVI u32x4 ld128(rsrc_t rs, int v, int s) { return __builtin_amdgcn_raw_buffer_load_b128(rs, v, s, SC1); }
DEVI u32x2 ld64(rsrc_t rs, int v, int s) { return __builtin_amdgcn_raw_buffer_load_b64(rs, v, s, SC1); }
DEVI void st128(rsrc_t rs, int v, int s, u32x4 d) { __builtin_amdgcn_raw_buffer_store_b128(d, rs, v, s, SC1); }
DEVI void st64(rsrc_t rs, int v, int s, u32x2 d) { __builtin_amdgcn_raw_buffer_store_b64(d, rs, v, s, SC1); }
template <int OFF> DEVI u32x4 ld128a(rsrc_t rs, int v, int s) {
  u32x4 r;
  asm volatile("buffer_load_dwordx4 %0, %1, %2, %3 offen offset:%4 sc1" : "=a"(r) : "v"(v), "s"(rs), "s"(s), "i"(OFF) : "memory");
  return r;
}
template <int OFF> DEVI u32x4 ld128v(rsrc_t rs, int v, int s) {
  u32x4 r;
  asm volatile("buffer_load_dwordx4 %0, %1, %2, %3 offen offset:%4 sc1" : "=v"(r) : "v"(v), "s"(rs), "s"(s), "i"(OFF) : "memory");
  return r;
}
template <int OFF> DEVI void dma1k(rsrc_t rs, u32 lds_dst, int v, int soff) {
  asm volatile("s_mov_b32 m0, %0\n\ts_nop 0\n\tbuffer_load_dwordx4 %1, %2, %3 offen offset:%4 sc1 lds"
               :: "s"(lds_dst), "v"(v), "s"(rs), "s"(soff), "i"(OFF) : "memory");
}
#define MMA(NAME, OP, CA, CB)                                                                                   \
  template <bool Z> DEVI void NAME(f32x4& d, const u32x4& a, const u32x4& b) {                                  \
    if (Z) asm volatile(OP " %0, %1, %2, 0" : "=&v"(d) : CA(a), CB(b));                                         \
    else asm volatile(OP " %0, %1, %2, %0" : "+v"(d) : CA(a), CB(b));                                           \
  }
MMA(mma_vv, "v_mfma_f32_16x16x32_bf16", "v", "v")
MMA(mma_av, "v_mfma_f32_16x16x32_bf16", "a", "v")
MMA(mma_va, "v_mfma_f32_16x16x32_bf16", "v", "a")
MMA(mma_hh, "v_mfma_f32_16x16x32_f16", "v", "v")
#define MFMA_DONE() do { SCHED(); asm volatile("s_nop 7" ::: "memory"); SCHED(); } while (0)

struct Ctx {
  rsrc_t rs; gu32* ct; char* smem; char* ws; u16* out;
  int lane0, w, b, P, sub, ct_, ff0, hks, eo, f1o, f1s;
  float dsk0, dsk1, ks0, ks1, bb2, cs1;
  u32 tag;
};
DEVI int fresh_lane(const Ctx& c) { int l = c.lane0; asm volatile("" : "+v"(l)); return l; }
DEVI u32 sig_begin(const Ctx& c, int lane) {
  u32 old;
  asm volatile("ds_add_rtn_u32 %0, %1, %2" : "=v"(old) : "v"(lds_addr(c.smem) + L_SIG + lane * 4), "v"(1u) : "memory");
  return old;
}
template <bool ALL, bool BACK = false> DEVI void sig_end(const Ctx& c, int lane, u32 old, int line) {
  if (BACK) asm volatile("" : "+v"(old) :: "memory");
  else asm volatile("s_waitcnt lgkmcnt(0)" : "+v"(old) :: "memory");
  const bool last = (__builtin_amdgcn_readfirstlane(old) & 3u) == 3u;
  const int v = (last && lane < (ALL ? 8 : 1)) ? (ALL ? lane * 2048 : 0) : 0x40000000;
  asm volatile("s_nop 4\n\tbuffer_atomic_add %0, %1, %2, %3 offen" :: "v"(1u), "v"(v), "s"(c.rs),
               "s"(WS_CT + (line + (ALL ? (c.b >> 3) & 7 : 0)) * 256) : "memory");
}
template <bool ALL, int R = 0> DEVI void poll(const Ctx& c, int lane, int line) {
  const int v = ALL ? (lane & 7) * 256 : 0, so = WS_CT + (line + (ALL ? (c.b & 7) * 8 : 0)) * 256;
  if (R == 0) asm volatile("s_nop 4\n\tbuffer_load_dword v254, %0, %1, %2 offen sc1" :: "v"(v), "s"(c.rs), "s"(so) : "memory", "v254");
  else asm volatile("s_nop 4\n\tbuffer_load_dword v255, %0, %1, %2 offen sc1" :: "v"(v), "s"(c.rs), "s"(so) : "memory", "v255");
}
template <int YOUNGER, int R = 0> DEVI bool got(u32 want) {
  unsigned long long m;
  if (R == 0) asm volatile("s_waitcnt vmcnt(%2)\n\tv_cmp_le_u32_e64 %0, %1, v254" : "=s"(m) : "s"(want), "i"(YOUNGER) : "memory");
  else asm volatile("s_waitcnt vmcnt(%2)\n\tv_cmp_le_u32_e64 %0, %1, v255" : "=s"(m) : "s"(want), "i"(YOUNGER) : "memory");
  return m == ~0ull;
}
template <bool ALL> DEVI void wait_ge(const Ctx& c, int lane, int line, u32 want) {
  int sp = 0;
#pragma nounroll
  do {

    poll<ALL, 0>(c, lane, line);
    if (++sp > SPIN_CAP) { __hip_atomic_store(c.ct + CT_TMO * 64, 1u, RLX_AGENT); break; }
  } while (!got<0, 0>(want));
}

DEVI void poll0(const Ctx& c, int lane, int set) {
  asm volatile("s_nop 4\n\tbuffer_load_dwordx2 v[254:255], %0, %1, %2 offen sc1" :: "v"((lane & 7) * 256 + (lane >> 3) * 8), "s"(c.rs),
               "s"(set + (c.b & 7) * 2048) : "memory", "v254", "v255");
}
template <int YOUNGER> DEVI bool got0(u32 tag) {
  unsigned long long m, m2;
  asm volatile("s_waitcnt vmcnt(%3)\n\tv_cmp_eq_u32_e64 %0, %2, v254\n\tv_cmp_eq_u32_e64 %1, %2, v255"
               : "=&s"(m), "=s"(m2) : "s"(tag), "i"(YOUNGER) : "memory");
  return (m & m2) == ~0ull;
}
DEVI void wait0(const Ctx& c, int lane, int set) {
  int sp = 0;
#pragma nounroll
  do {
    poll0(c, lane, set);
    if (++sp > SPIN_CAP) { __hip_atomic_store(c.ct + CT_TMO * 64, 1u, RLX_AGENT); break; }
  } while (!got0<0>(c.tag));
}

struct CIn { u32x2 x0, x1; u32x4 s[4]; };
DEVI void conv_load(const Ctx& c, int lane, CIn& in, int xoff, int stoff, int h) {
  const int sx = xoff + (32 * h + 8 * c.w) * 32768 + c.b * 128;
  const int vx = (lane >> 3) * 32768 + (lane & 7) * 8;
  const int ss = stoff + (1024 * h + 256 * c.w) * 16;
#pragma unroll
  for (int j = 0; j < 4; ++j) in.s[j] = ld128(c.rs, lane * 16 + j * 1024, ss);
  in.x0 = ld64(c.rs, vx, sx);
  in.x1 = ld64(c.rs, vx + 64, sx);
}
DEVI void conv_ln(const Ctx& c, int lane, const CIn& in, int h, f32x4& base0, f32x4& base1) {
  constexpr float CH = 4294967296.0f * FX_INV / DM, CL = FX_INV / DM;
  f32x4 m, r;
#pragma unroll
  for (int j = 0; j < 4; ++j) {
    m[j] = fmaf((float)(int)in.s[j][1], CH, (float)in.s[j][0] * CL);
    r[j] = fmaf((float)(int)in.s[j][3], CH, fmaf((float)in.s[j][2], CL, LN_EPS));
  }
#pragma unroll
  for (int j = 0; j < 4; ++j) r[j] = __builtin_amdgcn_rsqf(fmaxf(fmaf(-m[j], m[j], r[j]), LN_EPS));
  const f32x4 rr = r, mr = -m * r;
  const f32x4 x0 = {lo_bf(in.x0[0]), hi_bf(in.x0[0]), lo_bf(in.x0[1]), hi_bf(in.x0[1])};
  const f32x4 x1 = {lo_bf(in.x1[0]), hi_bf(in.x1[0]), lo_bf(in.x1[1]), hi_bf(in.x1[1])};
  const f32x4 u0 = x0 * rr + mr, u1 = x1 * rr + mr;
  char* up = c.smem + L_U + 512 + (1024 * h + 256 * c.w + 4 * lane) * 2;
  *(u32x2*)up = u32x2{pack_h(u0[0], u0[1]), pack_h(u0[2], u0[3])};
  *(u32x2*)(up + L_UC) = u32x2{pack_h(u1[0], u1[1]), pack_h(u1[2], u1[3])};
  base0 = u0 * c.dsk0 + x0; base1 = u1 * c.dsk1 + x1;
}
struct KB { const char* p[4]; };
template <int MM, int LAM> DEVI u32x4 conv_k1(const KB& kb) {
  const u32* kp = (const u32*)(kb.p[(7 - LAM) >> 1] + 64 * (3 - MM) + 512 * ((7 - LAM) & 1));
  return u32x4{kp[0], kp[1], kp[2], kp[3]};
}
template <int MM, int TB> DEVI u32x4 conv_u1(const char* ub) { return *(const u32x4*)(ub + 64 * (3 - MM) + 512 * TB); }
template <int LO> DEVI void conv_ldk(const KB& kb, u32x4 (&af)[4]) {
  sfor<0, 4>([&](auto L) { af[L.v] = conv_k1<0, LO + L.v>(kb); });
}
constexpr int conv_rank(int DT, int LO, int gr, int lt) {
  int k = 0;
  for (int x = 0; x < lt; ++x) { const int tb = DT + (x & 3) - (LO + 4 * gr + (x >> 2)); k += (tb >= 0 && tb < 4); }
  return k;
}
template <int DT, int TB0, bool ZERO, class F>
DEVI void conv_mma(const char* ub, const KB& kb, f32x4 (&acc)[4], const u32x4 (&af0)[4], F&& hook) {
  constexpr int NG = DT ? 2 : 1, LO = DT ? 1 : 0;
  u32x4 af[2][4], bf[2][4];
  sfor<0, 4>([&](auto L) { af[0][L.v] = af0[L.v]; });
  sfor<0, 4>([&](auto T) { constexpr int tb = DT ? 3 - T.v : T.v; bf[0][tb] = conv_u1<0, TB0 + tb>(ub); });
  SCHED();
  sfor<0, 4 * NG>([&](auto ST) {
    constexpr int st = ST.v, mm = st / NG, gr = st % NG;
    constexpr bool more = st + 1 < 4 * NG;
    constexpr int mn = (st + 1) / NG, gn = (st + 1) % NG;
    constexpr bool nu = more && gr == NG - 1;
    sfor<0, 16>([&](auto LT) {
      constexpr int l = LT.v >> 2, T = LT.v & 3, tb = DT + T - (LO + 4 * gr + l);
      if constexpr (tb >= 0 && tb < 4) {
        constexpr int k = conv_rank(DT, LO, gr, LT.v);
        constexpr bool first = ZERO && st == 0 && (DT ? (l == T) : (l == 0));
        mma_hh<first>(acc[T], af[st & 1][l], bf[mm & 1][tb]);
        sfor<(gr == 1 ? 2 * k : k), (gr == 1 ? 2 * k + 2 : k + 1)>([&](auto J) {
          constexpr int j = J.v;
          if constexpr (nu && j < 8) {
            if constexpr (DT != 0) {
              if constexpr (j & 1) bf[mn & 1][3 - (j >> 1)] = conv_u1<mn, TB0 + 3 - (j >> 1)>(ub);
              else af[(st + 1) & 1][j >> 1] = conv_k1<mn, LO + (j >> 1)>(kb);
            } else {
              if constexpr (j >= 1 && j <= 4) bf[mn & 1][j - 1] = conv_u1<mn, TB0 + j - 1>(ub);
              else af[(st + 1) & 1][j ? j - 4 : 0] = conv_k1<mn, LO + (j ? j - 4 : 0)>(kb);
            }
          } else if constexpr (more && !nu && j < 3) af[(st + 1) & 1][j] = conv_k1<mn, LO + 4 * gn + j>(kb);
        });
        SCHED();
        hook(IC<mm * (DT ? 16 : 10) + gr * 10 + k>{});
      }
    });
  });
}
DEVI void conv_part(const Ctx& c, int lane, const f32x4 (&acc)[4]) {
  char* red = c.smem + L_RED;
#pragma unroll
  for (int T = 0; T < 4; ++T) *(f32x4*)(red + (c.w * 4 + T) * 1024 + (lane & 15) * 64 + (lane >> 4) * 16) = acc[T];
}
DEVI void conv_out(const Ctx& c, int lane, const f32x4& base0, const f32x4& base1, int xoff, int h) {
  const int sx = xoff + (16 * h + 4 * c.w) * 65536 + c.b * 256;
  const int vx = (lane >> 4) * 65536 + (lane & 15) * 8;
  const char* p = c.smem + L_RED + c.w * 1024 + lane * 16;
  const f32x4 a0 = *(const f32x4*)p, b0 = *(const f32x4*)(p + 4096), a1 = *(const f32x4*)(p + 8192), b1 = *(const f32x4*)(p + 12288);
  SCHED();
  const f32x4 y0 = (a0 + b0) * c.ks0 + base0, y1 = (a1 + b1) * c.ks1 + base1;
  st64(c.rs, vx, sx, u32x2{pack_bf(y0[0], y0[1]), pack_bf(y0[2], y0[3])});
  st64(c.rs, vx + 128, sx, u32x2{pack_bf(y1[0], y1[1]), pack_bf(y1[2], y1[3])});
}

template <int PC> DEVI void g1_dma1(const Ctx& c, int ve, int vo, int ldsx, int soff) {
  dma1k<(PC & 3) * 1024>(c.rs, lds_addr(c.smem) + ldsx + (4 * (PC >> 2) + c.w) * 4096, (PC & 1) ? vo : ve, soff + (4 * (PC >> 2) + c.w) * 4096);
}
template <bool ALL> DEVI void sig_bar(const Ctx& c, int lane, int line) {
  const int v = (c.w == 0 && lane < (ALL ? 8 : 1)) ? (ALL ? lane * 2048 : 0) : 0x40000000;
  asm volatile("s_nop 4\n\tbuffer_atomic_add %0, %1, %2, %3 offen" :: "v"(1u), "v"(v), "s"(c.rs),
               "s"(WS_CT + (line + (ALL ? (c.b >> 3) & 7 : 0)) * 256) : "memory");
}
template <class F, class G>
DEVI void g1_mma(const char* const (&xb)[4], const u32x4 (&w1f)[2][16], f32x4 (&acc)[2][4], f32x4& sa, F&& mid, G&& hook) {
  u32x2 xf[2][4][2];
#pragma unroll
  for (int i = 0; i < 4; ++i) { xf[0][i][0] = tr_read(xb[i]); xf[0][i][1] = tr_read(xb[i] + 512); }
  SCHED();
  sfor<0, 16>([&](auto KS) {
    constexpr int ks = KS.v;
    mid(KS);
    sfor<0, 4>([&](auto I) {
      constexpr int i = I.v;
      const u32x4 x = cat2(xf[ks & 1][i][0], xf[ks & 1][i][1]);
      mma_vv<ks == 0>(acc[0][i], w1f[0][ks], x);
      if constexpr (ks < 15) xf[(ks + 1) & 1][i][0] = tr_read(xb[i] + (ks + 1) * 4096);
      SCHED();
      if constexpr (ks < 8) mma_av<ks == 0>(acc[1][i], w1f[1][ks], x); else mma_vv<false>(acc[1][i], w1f[1][ks], x);
      if constexpr (ks < 15) xf[(ks + 1) & 1][i][1] = tr_read(xb[i] + (ks + 1) * 4096 + 512);
      SCHED();
      if constexpr (i == 0) { mma_vv<ks == 0>(sa, x, x); hook(IC<2 * ks>{}); SCHED(); }
      if constexpr (i == 2) { hook(IC<2 * ks + 1>{}); SCHED(); }
    });
  });
  MFMA_DONE();
}
template <class F>
DEVI void g1_epi(const Ctx& c, int lane, f32x4 (&acc)[2][4], const f32x4& sa, int hoff, F&& hook) {
  const int g = lane >> 4, cc = lane & 15, q = cc >> 2, p = lane & 3, w = c.w;
  float* sums = (float*)(c.smem + L_MISC);
  {
    const float d = sel4(p, sa[0], sa[1], sa[2], sa[3]);
    sums[q == g ? (w * 16 + cc) * 2 + 1 : 128 + lane] = d;
    const float r = sel4(g, acc[1][1][0], acc[1][2][0], acc[1][3][0], acc[1][0][0]);
    sums[w == 3 ? lane * 2 : 192 + lane] = r;
  }
  f32x4 s1v[2], b1v[2];
#pragma unroll
  for (int j = 0; j < 2; ++j) {
    s1v[j] = *(const f32x4*)(c.smem + L_CST + w * 256 + j * 128 + g * 16);
    b1v[j] = *(const f32x4*)(c.smem + L_CST + w * 256 + j * 128 + 64 + g * 16);
  }
  const int v16 = lane * 16 + (w < 3 ? 0 : 0x40000000), v8 = lane * 8 + (w < 3 ? 0x40000000 : c.eo * 512);
  __syncthreads();
  f32x2 k[4];
#pragma unroll
  for (int i = 0; i < 4; ++i) k[i] = *(const f32x2*)(sums + (((i + w) & 3) * 16 + cc) * 2);
#pragma unroll
  for (int i = 0; i < 4; ++i) {
    const float m = k[i][0] * (1.0f / DM);
    const float ve = fmaxf(fmaf(-m, m, fmaf(k[i][1], 1.0f / DM, LN_EPS)), LN_EPS);
    const float r = __builtin_amdgcn_rsqf(fmaf(ve, 1.0f + LN_EPS, -LN_EPS));
    k[i] = f32x2{r, -m * r};
  }
  SCHED();
  hook(IC<-1>{});
  SCHED();
  sfor<0, 2>([&](auto IH) {
    constexpr int ih = IH.v;
    f32x4 x[4], e[4];
#define STAGE(n) do { SCHED(); hook(IC<10 * ih + n>{}); SCHED(); } while (0)
#pragma unroll
    for (int n = 0; n < 4; ++n) x[n] = k[2 * ih + (n >> 1)][1] * s1v[n & 1] + b1v[n & 1];
#pragma unroll
    for (int n = 0; n < 4; ++n) x[n] = acc[n & 1][2 * ih + (n >> 1)] * k[2 * ih + (n >> 1)][0] + x[n];
    STAGE(0);
#pragma unroll
    for (int n = 0; n < 4; ++n) e[n] = x[n] * x[n];
#pragma unroll
    for (int n = 0; n < 4; ++n) e[n] = e[n] * (-0.0713548162726f * 1.44269504088896f) + (-1.59576912160573f * 1.44269504088896f);
    STAGE(1);
#pragma unroll
    for (int n = 0; n < 4; ++n) e[n] = e[n] * x[n];
    STAGE(2);
#pragma unroll
    for (int r = 0; r < 4; ++r) { e[0][r] = __builtin_amdgcn_exp2f(e[0][r]); e[1][r] = __builtin_amdgcn_exp2f(e[1][r]); }
    STAGE(3);
#pragma unroll
    for (int r = 0; r < 4; ++r) { e[2][r] = __builtin_amdgcn_exp2f(e[2][r]); e[3][r] = __builtin_amdgcn_exp2f(e[3][r]); }
    STAGE(4);
#pragma unroll
    for (int n = 0; n < 4; ++n) e[n] = e[n] + 1.0f;
    STAGE(5);
#pragma unroll
    for (int r = 0; r < 4; ++r) { e[0][r] = rcp(e[0][r]); e[1][r] = rcp(e[1][r]); }
    STAGE(6);
#pragma unroll
    for (int r = 0; r < 4; ++r) { e[2][r] = rcp(e[2][r]); e[3][r] = rcp(e[3][r]); }
    STAGE(7);
#pragma unroll
    for (int n = 0; n < 4; ++n) x[n] = x[n] * e[n];
    STAGE(8);
    u32x4 hv[2];
#pragma unroll
    for (int n = 0; n < 4; ++n) { hv[n >> 1][2 * (n & 1)] = pack_bf(x[n][0], x[n][1]); hv[n >> 1][2 * (n & 1) + 1] = pack_bf(x[n][2], x[n][3]); }
    STAGE(9);
    const int so0 = hoff + ((2 * ih + w) & 3) * 56 * 1024, so1 = hoff + ((2 * ih + 1 + w) & 3) * 56 * 1024;
    st128(c.rs, v16, so0, hv[0]); st128(c.rs, v16, so1, hv[1]);
    st64(c.rs, v8, so0, u32x2{hv[0][0], hv[0][1]}); st64(c.rs, v8, so1, u32x2{hv[1][0], hv[1][1]});
    SCHED();
    if constexpr (ih == 0) { hook(IC<-2>{}); SCHED(); }
#undef STAGE
  });
}

constexpr int PF = 10;
template <int N> DEVI void ld_w1(const Ctx& c, int v, u32x4 (&w1f)[2][16]) {
  constexpr int ks = N >> 1, j = N & 1;
  const int so = (j ? c.f1o + (ks >> 2) * c.f1s : WS_W1P + c.ff0 * 16384 + (ks >> 2) * 4096);
  if constexpr (j && ks < 8) w1f[j][ks] = ld128a<(ks & 3) * 1024>(c.rs, v, so);
  else w1f[j][ks] = ld128v<(ks & 3) * 1024>(c.rs, v, so);
}
template <int N> DEVI void ld_w2(const Ctx& c, int v, u32x4 (&w2f)[14][4]) {
  constexpr int s = N >> 2, j = N & 3;
  w2f[s][j] = ld128a<(s & 3) * 1024>(c.rs, v, WS_W2P + ((c.ct_ * 4 + ((c.w + j) & 3)) * 56 + 14 * c.w + (s & ~3)) * 1024);
}
template <int N> DEVI void g2_ldh1(const Ctx& c, int v16, u32x4 (&hf)[14][2], int ho) {
  constexpr int s = N >> 1, i = N & 1;
  if constexpr (s % 7 == 6)
    hf[s][i] = cat2(ld64(c.rs, (v16 >> 1) + (s & 3) * 1024, ho + i * 56 * 1024 + (s >> 2) * 4096),
                    ld64(c.rs, (v16 >> 1) + (s & 3) * 1024 + 512, ho + i * 56 * 1024 + (s >> 2) * 4096));
  else hf[s][i] = ld128(c.rs, v16 + (s & 3) * 1024, ho + i * 56 * 1024 + (s >> 2) * 4096);
}
template <int PRE, int RATE, class F>
DEVI void g2_mma(const Ctx& c, int lane, u32x4 (&hf)[14][2], const u32x4 (&w2f)[14][4], f32x4 (&acc)[2][4], int ho, F&& mid) {
  const int v16 = lane * 16;
  sfor<0, 14>([&](auto S) {
    constexpr int s = S.v;
    sfor<0, 8>([&](auto M) {
      constexpr int i = M.v >> 2, j = M.v & 3;
      mma_va<s == 0>(acc[i][j], hf[s][i], w2f[s][j]);
      constexpr int per = 4 / RATE, n = 2 * (PRE + RATE * s) + M.v / per;
      if constexpr (M.v % per == per - 1 && n < 28) { g2_ldh1<n>(c, v16, hf, ho); SCHED(); }
    });
    SCHED();
    mid(S);
  });
  MFMA_DONE();
}
template <bool LAST, class F>
DEVI void g2_epi(const Ctx& c, int lane, const f32x4 (&acc)[2][4], const u32x2 (&xr)[2], int h, int xdst, F&& mid) {
  const int g = lane >> 4, cc = lane & 15, q = cc >> 2, p = lane & 3, w = c.w, t = w * 64 + lane;
  char* red = c.smem + L_XA + h * 32768;
#pragma unroll
  for (int jj = 1; jj < 4; ++jj)
#pragma unroll
    for (int i = 0; i < 2; ++i) *(f32x4*)(red + ((((w + jj) & 3) * 3 + jj - 1) * 2 + i) * 1024 + lane * 16) = acc[i][jj];
  __syncthreads();
  char* xt = red + 24576;
  f32x4 pr[2][3];
#pragma unroll
  for (int i = 0; i < 2; ++i)
#pragma unroll
    for (int rk = 0; rk < 3; ++rk) pr[i][rk] = *(const f32x4*)(red + ((w * 3 + rk) * 2 + i) * 1024 + lane * 16);
  mid(0);
#pragma unroll
  for (int i = 0; i < 2; ++i) {
    const f32x4 xv = {lo_bf(xr[i][0]), hi_bf(xr[i][0]), lo_bf(xr[i][1]), hi_bf(xr[i][1])};
    const f32x4 o = ((acc[i][0] + (xv + c.bb2)) + pr[i][0]) + (pr[i][1] + pr[i][2]);
    const u32x2 r = {pack_bf(o[0], o[1]), pack_bf(o[2], o[3])};
    *(u32x2*)(xt + (w * 16 + cc) * 64 + ((i ^ (cc >> 3)) * 32) + g * 8) = r;
  }
  __syncthreads();
  const char* xq = xt + ((w >> 1) * 32 + 8 * g + q) * 64 + (((w & 1) ^ (g & 1)) * 32) + 8 * p;
  const u32x4 f0 = cat2(tr_read(xq), tr_read(xq + 256));
  const int tok = (16 * h + c.P) * 64 + c.sub * 32 + (w & 1) * 16 + cc;
  if (LAST) {
    *(u32x4*)(c.out + (size_t)tok * DM + c.ct_ * 64 + (w >> 1) * 32 + 8 * g) = f0;
  } else {
    const int row = t >> 2;
    const u32x4 v = *(const u32x4*)(xt + row * 64 + (((t & 3) ^ ((row >> 2) & 2)) * 16));
    st128(c.rs, c.ct_ * 4096 + t * 16, xdst, v);
    const char* xo = xq + ((w & 2) ? -2048 : 2048);
    const u32x4 f1 = cat2(tr_read(xo), tr_read(xo + 256));
    float s = 0.f, ss = 0.f, s2 = 0.f, ss2 = 0.f;
#pragma unroll
    for (int j = 0; j < 4; ++j) {
      s = dot2bf(f0[j], 0x3f803f80u, s); ss = dot2bf(f0[j], f0[j], ss);
      s2 = dot2bf(f1[j], 0x3f803f80u, s2); ss2 = dot2bf(f1[j], f1[j], ss2);
    }
    s = sum_x32(sum_x16(s + s2)); ss = sum_x32(sum_x16(ss + ss2));
    const u32 ao = t < 128 && lane < 32 ? WS_ST + 65536 + st_pos(tok) + g * 8 : WS_DUMP + (c.b * 256 + t) * 8;
    (void)__hip_atomic_fetch_add((unsigned long long*)(c.ws + ao), (unsigned long long)f2fx((g & 1) ? ss : s), RLX_AGENT);
  }
}

template <int LAYER>
DEVI void layer_body(const Ctx& c, u32x4 (&w1f)[2][16], u32x4 (&w2f)[14][4], CIn& inA, CIn& inB) {
  constexpr int xin = WS_X + LAYER * 4 * MB, xmid = xin + 2 * MB, xnext = xin + 4 * MB;
  constexpr int hbuf = WS_H + LAYER * 8 * MB, stin = WS_ST + LAYER * 65536;
  constexpr u32 ep = LAYER + 1;
  const int w = c.w;
  char* smem = c.smem;
  auto nop = [](auto) {};
  {
    const int lane = fresh_lane(c);
    const int g = lane >> 4, cc = lane & 15, mu0 = (w & 1) * 4, v16 = lane * 16;
    const char* ub = smem + L_U + (w >> 1) * L_UC + 512 + 2 * (16 * cc - 16 + 8 * g) - 64 * (mu0 + 3);
    KB kb;
#pragma unroll
    for (int j = 0; j < 4; ++j) {
      u32 a = lds_addr(smem) + (w >> 1) * 2 * L_KC + ((cc & 1) ? 0 : L_KC) + 2 * (2031 - cc + 8 * g - ((cc & 1) ? 0 : 1)) - 64 * (mu0 + 3 + 56) + 1024 * j;
      asm volatile("" : "+v"(a));
      kb.p[j] = (const char*)(const LAS char*)(size_t)a;
    }
    f32x4 base0, base1;
    u32x4 a4[4];
    conv_ldk<0>(kb, a4);
    conv_ln(c, lane, inA, 0, base0, base1);
    if (!LAYER) poll0(c, lane, WS_F1);
    __syncthreads();
    f32x4 acc[4];
    u32 o = 0;
    conv_mma<0, 0, true>(ub, kb, acc, a4, [&](auto N) {
      constexpr int n = N.v;
      if constexpr (LAYER && n == 19) { WAIT_VM(0); o = sig_begin(c, lane); SCHED(); }
      if constexpr (LAYER && n == 30) { sig_end<true, true>(c, lane, o, CT_X + 64); SCHED(); }
    });
    MFMA_DONE();
    conv_part(c, lane, acc);
    conv_ldk<1>(kb, a4);
    __syncthreads();
    float s1p[8];
    if (!LAYER) {
      if (!got0<0>(c.tag)) wait0(c, lane, WS_F1);
      asm volatile("" :: "v"(inB.x1));
      const int so1 = (min(c.ff0 * 16 + (lane & 15) + (lane >> 5) * 16, FF - 1) + (lane & 16) * (FF / 2)) * 4;
#pragma unroll
      for (int j = 0; j < 8; ++j)
        asm volatile("s_nop 4\n\tbuffer_load_dword %0, %1, %2, %3 offen sc1" : "=v"(s1p[j]) : "v"(so1), "s"(c.rs), "s"(WS_S1 + j * FF * 4) : "memory");
    }
    conv_out(c, lane, base0, base1, xmid, 0);
    f32x4 acb[4];
    conv_mma<4, 0, true>(ub, kb, acb, a4, [&](auto N) {
      constexpr int n = N.v;
      if constexpr (!LAYER && n >= 48 && (n & 1)) { ld_w1<(n - 48) / 2>(c, v16, w1f); SCHED(); }
      if constexpr (n == 47) {
        WAIT_VM(0);
        o = sig_begin(c, lane);
        if (LAYER) poll<true>(c, lane, CT_X + 64);
        SCHED();
      }
      if constexpr (n == 58) { sig_end<true, true>(c, lane, o, CT_C); SCHED(); }
    });
    if (!LAYER) {
#pragma unroll
      for (int j = 0; j < 8; ++j) asm volatile("" : "+v"(s1p[j]));
      const float sv = sum_x16(((s1p[0] + s1p[1]) + (s1p[2] + s1p[3])) + ((s1p[4] + s1p[5]) + (s1p[6] + s1p[7])));
      ((float*)(smem + L_CST))[w * 64 + lane] = (lane & 16) ? c.cs1 : sv;
    }
    if (LAYER) {
      if (!got<1>(32u)) wait_ge<true>(c, lane, CT_X + 64, 32u);
      conv_load(c, lane, inB, xin, stin, 1);
    }
    conv_ldk<0>(kb, a4);
    if (!LAYER) sfor<8, 12>([&](auto N) { ld_w1<N.v>(c, v16, w1f); });
    conv_ln(c, lane, inB, 1, base0, base1);
    if (!LAYER) sfor<12, 14>([&](auto N) { ld_w1<N.v>(c, v16, w1f); });
    __syncthreads();
    conv_mma<0, 4, false>(ub, kb, acb, a4, [&](auto N) {
      constexpr int n = N.v;
      if constexpr (!LAYER && n % 3 == 1 && n < 36) { ld_w1<14 + n / 3>(c, v16, w1f); SCHED(); }
      if constexpr (n == 30) { poll<true>(c, lane, CT_C); SCHED(); }
    });
    MFMA_DONE();
    conv_part(c, lane, acb);
    __syncthreads();
    conv_out(c, lane, base0, base1, xmid, 1);
    if (!got<(LAYER ? 2 : 4)>(32u * ep)) wait_ge<true>(c, lane, CT_C, 32u * ep);
    const int row = lane >> 3, sw = ((row >> 1) & 1) << 1;
    const int ve = row * 128 + ((lane & 7) ^ sw) * 16, vo = row * 128 + ((lane & 7) ^ (sw | 4)) * 16;
    if (!LAYER && c.b == 0 && w == 0) __hip_atomic_store((gu32*)(c.ws + WS_GEN), c.tag, RLX_AGENT);
    sfor<0, 8>([&](auto N) { g1_dma1<N.v>(c, ve, vo, L_XA, xmid + c.P * 65536); });
    WAIT_VM(8);
    o = sig_begin(c, lane);
    sig_end<true>(c, lane, o, CT_C + 64);
  }
  u32x4 hf[14][2];
  u32x2 xr[2];
  {
    const int lane = fresh_lane(c);
    const int g = lane >> 4, cc = lane & 15, q = cc >> 2, p = lane & 3, v16 = lane * 16;
    const int sx = ((q >> 1) & 1) | ((g & 1) << 1);
    const char* xb[4];
#pragma unroll
    for (int i = 0; i < 4; ++i) xb[i] = smem + L_XA + (8 * g + q) * 128 + ((((i + w) & 3) ^ sx) * 32) + 8 * p;
    const int row = lane >> 3, sw = ((row >> 1) & 1) << 1;
    const int ve = row * 128 + ((lane & 7) ^ sw) * 16, vo = row * 128 + ((lane & 7) ^ (sw | 4)) * 16;
    sfor<8, 12>([&](auto N) { g1_dma1<N.v>(c, ve, vo, L_XA, xmid + c.P * 65536); });
    WAIT_VM_BAR(9);
    f32x4 acc[2][4], sa;
    g1_mma(xb, w1f, acc, sa, [&](auto KS) {
      constexpr int ks = KS.v;
      if constexpr (ks == 3) { if (LAYER) WAIT_VM_BAR(8); else WAIT_VM_BAR(10); SCHED(); }
      if constexpr (ks == 7) { if (LAYER) WAIT_VM_BAR(0); else WAIT_VM_BAR(10); SCHED(); }
      if constexpr (ks == 13) { if (!LAYER) WAIT_VM(16); poll<true>(c, lane, CT_C + 64); SCHED(); }
    }, [&](auto N) {
      constexpr int n = N.v;
      if constexpr (n < 4) g1_dma1<12 + n>(c, ve, vo, L_XA, xmid + c.P * 65536);
      if constexpr (!LAYER && n >= 4 && n < 10) ld_w1<22 + n>(c, v16, w1f);
      if constexpr (!LAYER && n >= 10) ld_w2<n - 10>(c, v16, w2f);
    });
    const int xo = xmid + (16 + c.P) * 65536;
    g1_epi(c, lane, acc, sa, hbuf + (c.P * 4 * 56 + c.hks) * 1024, [&](auto N) {
      constexpr int n = N.v;
      if constexpr (n == -1) { if (!got<(LAYER ? 0 : 6)>(32u * ep)) wait_ge<true>(c, lane, CT_C + 64, 32u * ep); }
      if constexpr (n >= 0 && n < 8) g1_dma1<n>(c, ve, vo, L_XB, xo);
      if constexpr (n >= 10 && n < 18) g1_dma1<n - 2>(c, ve, vo, L_XB, xo);
    });
  }
  {
    const int lane = fresh_lane(c);
    const int g = lane >> 4, cc = lane & 15, q = cc >> 2, p = lane & 3, v16 = lane * 16;
    const int sx = ((q >> 1) & 1) | ((g & 1) << 1);
    const char* xb[4];
#pragma unroll
    for (int i = 0; i < 4; ++i) xb[i] = smem + L_XB + (8 * g + q) * 128 + ((((i + w) & 3) ^ sx) * 32) + 8 * p;
    WAIT_VM_BAR(16);
    f32x4 acc[2][4], sa;
    g1_mma(xb, w1f, acc, sa, [&](auto KS) {
      constexpr int ks = KS.v;
      if constexpr (ks == 5) {
        if (LAYER) WAIT_VM_BAR(0); else WAIT_VM_BAR(10);
        sig_bar<false>(c, lane, CT_H + c.P);
        SCHED();
      }
      if constexpr (ks == 15) {
#pragma unroll
        for (int i = 0; i < 2; ++i) xr[i] = ld64(c.rs, (c.ct_ * 64 + w * 16 + cc) * 128 + i * 32 + g * 8, xmid + c.P * 65536 + c.sub * 64);
        SCHED();
      }
    }, [&](auto N) {
      constexpr int n = N.v;
      if constexpr (!LAYER) ld_w2<22 + n>(c, v16, w2f);
      if constexpr (!LAYER && n >= 30) ld_w2<24 + n>(c, v16, w2f);
    });
    poll<false>(c, lane, CT_H + c.P);
    const int ho = hbuf + ((c.P * 4 + c.sub * 2) * 56 + 14 * w) * 1024;
    g1_epi(c, lane, acc, sa, hbuf + ((16 + c.P) * 4 * 56 + c.hks) * 1024, [&](auto N) {
      constexpr int n = N.v;
      if constexpr (n == -1) { if (!got<0>(16u * ep)) wait_ge<false>(c, lane, CT_H + c.P, 16u * ep); }
      if constexpr (n >= 0) g2_ldh1<n>(c, v16, hf, ho);
    });
  }
  {
    const int lane = fresh_lane(c);
    const int g = lane >> 4, cc = lane & 15;
    const int ho = hbuf + ((c.P * 4 + c.sub * 2) * 56 + 14 * w) * 1024, hoB = ho + 64 * 56 * 1024;
    f32x4 acc[2][4];
    u32 o = 0;
    g2_mma<10, 1>(c, lane, hf, w2f, acc, ho, [&](auto S) {
      constexpr int s = S.v;
      if constexpr (s == 5) { WAIT_VM(10); o = sig_begin(c, lane); SCHED(); }
      if constexpr (s == 6) { sig_end<false>(c, lane, o, CT_H + 16 + c.P); SCHED(); }
    });
    poll<false>(c, lane, CT_H + 16 + c.P);
    g2_epi<LAYER == 1>(c, lane, acc, xr, 0, xnext + c.P * 65536 + c.sub * 32768, nop);
    if (!got<(LAYER ? 1 : 2)>(16u * ep)) wait_ge<false>(c, lane, CT_H + 16 + c.P, 16u * ep);
    sfor<0, 8>([&](auto N) { g2_ldh1<N.v>(c, lane * 16, hf, hoB); });
#pragma unroll
    for (int i = 0; i < 2; ++i) xr[i] = ld64(c.rs, (c.ct_ * 64 + w * 16 + cc) * 128 + i * 32 + g * 8, xmid + (16 + c.P) * 65536 + c.sub * 64);
    if (LAYER == 0) {
      WAIT_VM(10);
      o = sig_begin(c, lane);
      sig_end<true>(c, lane, o, CT_X);
    }
    SCHED();
  }
  {
    const int lane = fresh_lane(c);
    const int hoB = hbuf + (((16 + c.P) * 4 + c.sub * 2) * 56 + 14 * w) * 1024;
    f32x4 acc[2][4];
    g2_mma<4, 2>(c, lane, hf, w2f, acc, hoB, [&](auto S) {
      constexpr int s = S.v;
      if constexpr (LAYER == 0 && s == 13) { poll<true>(c, lane, CT_X); SCHED(); }
    });
    g2_epi<LAYER == 1>(c, lane, acc, xr, 1, xnext + (16 + c.P) * 65536 + c.sub * 32768, [&](int) {
      if (LAYER == 0) {
        if (!got<0>(32u)) wait_ge<true>(c, lane, CT_X, 32u);
        conv_load(c, lane, inA, xnext, WS_ST + 65536, 0);
        SCHED();
      }
    });
  }
}

constexpr int KG_RS = 528, KG_X = 16 * KG_RS, KG_CH = 64 * KG_RS + 1296;
__global__ void __launch_bounds__(256, 1) main_kernel(const u16* x, const u16* lar, const u16* aim, const u16* cre,
                                                      const u16* cim, const u16* ldt, const u16* dsk, const u16* w1,
                                                      const u16* b1, const u16* w2, const u16* b2, char* ws, u16* out) {
  extern __shared__ __attribute__((aligned(16))) char smem[];
  const int t = threadIdx.x, lane = t & 63, b = blockIdx.x;
  const int w = __builtin_amdgcn_readfirstlane(t >> 6);
  const u32 junk = warm_code((b >> 3) * 4 + w, lane);
  const int ch = w >> 1, d = 2 * b + ch, n = t & 127;
  const u32 po = (u32)(d * NST + n) * 2, po2 = po ^ 128u;
#define LD16(p, off) (*(const u16*)((const char*)(p) + (u32)(off)))
  const u16 r0 = ldt[d], r1 = LD16(lar, po), r2 = LD16(aim, po), r3 = LD16(cre, po), r4 = LD16(cim, po);
  const u16 r5 = LD16(cre, po2), r6 = LD16(cim, po2);
  const u32 gen = __hip_atomic_load((gu32*)(ws + WS_GEN), RLX_AGENT);
  const bool early = !(b & 64);
  const int pks = (b & 7) + (b >> 7) * 8, pcg = (b >> 3) & 7;
  u32x4 rv[8], rw[2];
  {
    const rsrc_t rr = __builtin_amdgcn_make_buffer_rsrc((void*)(early ? x : w1), 0, early ? SEQ * DM * 2 : DM * FF * 2, 0x00020000);
    const int vo = early ? w * 1024 + lane * 16 : ((b & 63) < 56 ? (t >> 5) * (FF * 2) + (t & 31) * 16 : 0x40000000);
    const int so = early ? (b & 63) * 32768 : pks * (32 * FF * 2) + min(pcg, 6) * 512, st = early ? 4096 : 8 * FF * 2;
#pragma unroll
    for (int j = 0; j < 8; ++j) rv[j] = __builtin_amdgcn_raw_buffer_load_b128(rr, (j < 4 || early) ? vo : 0x40000000, so + (j & 3) * st + (j >> 2) * (early ? 16384 : 0), 0);
    const rsrc_t r2w = __builtin_amdgcn_make_buffer_rsrc((void*)w2, 0, DM * FF * 2, 0x00020000);
#pragma unroll
    for (int j = 0; j < 2; ++j) rw[j] = __builtin_amdgcn_raw_buffer_load_b128(r2w, (t >> 4) * 1024 + (t & 15) * 16 + j * 16384, min(b >> 2, 55) * 32768 + (b & 3) * 256, 0);
  }
  CIn inA, inB;
  Ctx c;
  c.rs = __builtin_amdgcn_make_buffer_rsrc(ws, 0, WS_END, 0x00020000);
  c.ct = (gu32*)(ws + WS_CT);
  c.smem = smem; c.ws = ws; c.out = out;
  c.lane0 = lane; c.w = w; c.b = b;
  c.P = (b & 7) * 2 + ((b >> 3) & 1);
  const int idx = b >> 4;
  const int sg = idx >> 1, w3 = -(int)(w == 3);
  c.eo = idx & 1; c.sub = idx >> 3; c.ct_ = idx & 7;
  c.ff0 = 14 * sg + (((6 * c.eo + 2 * w) & ~w3) | ((12 + c.eo) & w3));
  c.hks = 7 * sg + (((3 * c.eo + w) & ~w3) | (6 & w3));
  c.f1o = w < 3 ? WS_W1P + c.ff0 * 16384 + 16384 : WS_ONE; c.f1s = w < 3 ? 4096 : 0;
  {
    const u32 r7 = *(const u32*)(dsk + 2 * b);
    u32 r8 = LD16(b1, min(c.ff0 * 16 + (lane & 15) + (lane >> 5) * 16, FF - 1) * 2), r9 = LD16(b2, (c.ct_ * 64 + w * 16 + (lane & 15)) * 2);
    SCHED();
    char* kg = smem + L_XA + ch * KG_CH;
    char* stg = smem + L_XA + 2 * KG_CH;
    const int i = lane & 15, g = lane >> 4;
    int ix[9];
#pragma unroll
    for (int e2 = 0; e2 < 9; ++e2) { ix[e2] = 2 * cidx(max(2047 - 8 * t - e2, 0)); asm volatile("" : "+v"(ix[e2])); }
    int wi[2][4];
    {
      const int m8 = -(i >> 3), s1 = -(w & 1);
      const int h1 = ((256 + 128 * (i >> 2) + 2 * (i & 3)) & ~m8) | ((128 * (i >> 1) + 4 * (i & 1)) & m8);
      const int hp = (((i + (i & 8) * 15) & ~s1) | (h1 & s1)) + 32 * g, hq = 1024 + 128 * i + 32 * g;
#pragma unroll
      for (int r = 0; r < 4; ++r) {
        wi[0][r] = 2 * cidx(hp + 8 * r); wi[1][r] = 2 * cidx(hq + 8 * r > 2048 ? 2064 : hq + 8 * r);
        asm volatile("" : "+v"(wi[0][r]), "+v"(wi[1][r]));
      }
    }
    if (t < 16) *(u32x4*)(smem + (t >> 2) * L_KC + 4096 + (t & 3) * 16) = u32x4{0, 0, 0, 0};
    if (t >= 64 && t < 128) *(u32x4*)(smem + L_U + ((t - 64) >> 5) * L_UC + (t & 31) * 16) = u32x4{0, 0, 0, 0};
    if (t == 128) *(u32*)(smem + L_SIG) = 0u;
    st64(c.rs, t * 8, WS_DUMP + b * 2048, u32x2{0, 0});
    if (w >= 2) poll0(c, lane, w == 2 ? WS_F1 : WS_F0);
    SCHED();
    const float dt = rbf(expf(bf2f(r0)));
    const float cr = bf2f(r3), ci = bf2f(r4), cr2 = bf2f(r5), ci2 = bf2f(r6);
    const float S = wave_sum(sqrt_(cr * cr + ci * ci) + sqrt_(cr2 * cr2 + ci2 * ci2)) * (2.002f * dt);
    int e = __builtin_amdgcn_frexp_expf(S);
    e = (S > 0.0f && S < 3.0e38f) ? max(-100, min(100, e)) : 14;
    const float scale = __builtin_amdgcn_ldexpf(2.0f, 14 - e);
    if (n == 0) *(float*)(smem + L_MISC + ch * 4) = __builtin_amdgcn_ldexpf(1.0f, e - 14);
    const float a2 = (-rbf(expf(bf2f(r1))) * dt) * 1.44269504088896f;
    const float br = (bf2f(r2) * dt) * 0.159154943091895f;
    {
      char* yp = kg + n * 4;
      char* xp = kg + KG_X + n * 4;
      const CM e128 = cexp_(a2, -br, 128.f), e8 = cexp_(a2, br, 8.f), e1 = cexp_(a2, -br, 1.f), e2 = cexp_(a2, -br, 2.f);
      const CM e4 = cexp_(a2, -br, 4.f);
      f32x2 f0 = {(cr * dt) * scale, -(ci * dt) * scale};
      f32x2 f = cmul(f0, cexp_(a2, -br, 512.f)), y = {1.f, 0.f};
      f32x2 g1 = cmul(f0, e128), g2 = cmul(g1, e128), g3 = cmul(g2, e128);
#define PUT(base, row, v) *(u32*)(base + (row) * KG_RS) = pack_h(v[0], v[1])
      auto chain = [&](auto K) {
        constexpr int k = K.v;
        PUT(yp, k, y);
        if (k < 15) y = cmul(y, e8);
        if (k < 4) { const f32x2 q4 = cmul(f, e4); PUT(xp, 24 + 2 * k, f); PUT(xp, 25 + 2 * k, q4); }
        else if (k < 13) PUT(xp, 28 + k, f);
        if (k < 12) f = cmul(f, e128);
        if (k < 8) { PUT(xp, k, f0); PUT(xp, 8 + k, g1); }
        if (k < 7) { f0 = cmul(f0, e1); g1 = cmul(g1, e1); }
        if (k >= 8 && k < 12) { PUT(xp, 8 + k, g2); PUT(xp, 12 + k, g3); }
        if (k >= 8 && k < 11) { g2 = cmul(g2, e2); g3 = cmul(g3, e2); }
      };
      c.tag = __builtin_amdgcn_readfirstlane(gen) * 0x9E3779B1u + 0x7F4A7C15u;
      sfor<0, 2>(chain);
      asm volatile("" : "+v"(y), "+v"(f), "+v"(f0), "+v"(g1));
      if (b < 64) xpose(b, rv, c.rs, stg);
      else if (early) row_stats(b & 63, rv, c.rs);
      asm volatile("" : "+v"(y), "+v"(f), "+v"(f0), "+v"(g1));
      sfor<2, 12>(chain);
      asm volatile("" : "+v"(y), "+v"(f));
      if (early) {
        WAIT_VM(0);
        __syncthreads();
        if (w == 0 && lane < 8)
          __hip_atomic_store((gu32*)(ws + WS_F0 + lane * 2048 + (b & 7) * 256 + ((b >> 3) & 7) * 8 + (b >> 7) * 4), c.tag, RLX_AGENT);
      }
      asm volatile("" : "+v"(y), "+v"(f));
      sfor<12, 16>(chain);
      if (!early && pcg < 7) pack_w1(pks, pcg, rv, c.rs, stg);
#undef PUT
    }
    pack_w2_put(rw, smem + L_XB + 55552);
    __syncthreads();
    {
      const char* ya = kg + i * KG_RS + g * 16;
      const char* xa = kg + KG_X + (16 * (w & 1) + i) * KG_RS + g * 16;
      const char* xc = kg + KG_X + (32 + i) * KG_RS + g * 16;
      u32x4 fa[8], fb[8], fc[8];
#pragma unroll
      for (int ks = 0; ks < 8; ++ks) { fa[ks] = *(const u32x4*)(ya + ks * 64); fb[ks] = *(const u32x4*)(xa + ks * 64); }
#pragma unroll
      for (int ks = 0; ks < 8; ++ks) fc[ks] = *(const u32x4*)(xc + ks * 64);
      f32x4 ac0, ac1;
      SCHED();
      sfor<0, 8>([&](auto KS) { mma_hh<KS.v == 0>(ac0, fa[KS.v], fb[KS.v]); });
      sfor<0, 8>([&](auto KS) { mma_hh<KS.v == 0>(ac1, fa[KS.v], fc[KS.v]); });
      MFMA_DONE();
#pragma unroll
      for (int r = 0; r < 4; ++r) {
        *(u16*)(kg + 64 * KG_RS + wi[0][r]) = __builtin_bit_cast(u16, (_Float16)ac0[r]);
        *(u16*)(kg + 64 * KG_RS + wi[1][r]) = __builtin_bit_cast(u16, (_Float16)ac1[r]);
      }
    }
    if (!early) WAIT_VM(0);
    __syncthreads();
    if (w == 1 && !early && lane < 8)
      __hip_atomic_store((gu32*)(ws + WS_F1 + lane * 2048 + (b & 7) * 256 + ((b >> 3) & 7) * 8 + (b >> 7) * 4), c.tag, RLX_AGENT);
    if (w == 0) poll0(c, lane, WS_F0);
#pragma unroll
    for (int c2 = 0; c2 < 2; ++c2) {
      u32 v[9];
#pragma unroll
      for (int e2 = 0; e2 < 9; ++e2) v[e2] = *(const u16*)(smem + L_XA + c2 * KG_CH + 64 * KG_RS + ix[e2]);
      if (t == 255) v[8] = 0u;
      u32x4 oe, oo;
#pragma unroll
      for (int j = 0; j < 4; ++j) { oe[j] = v[2 * j] | (v[2 * j + 1] << 16); oo[j] = v[2 * j + 1] | (v[2 * j + 2] << 16); }
      *(u32x4*)(smem + c2 * 2 * L_KC + t * 16) = oe;
      *(u32x4*)(smem + c2 * 2 * L_KC + L_KC + t * 16) = oo;
    }
    const f32x2 ksc = *(const f32x2*)(smem + L_MISC);
    c.ks0 = __uint_as_float(__builtin_amdgcn_readfirstlane(__float_as_uint(ksc[0])));
    c.ks1 = __uint_as_float(__builtin_amdgcn_readfirstlane(__float_as_uint(ksc[1])));
    const u32 ds = __builtin_amdgcn_readfirstlane(r7);
    c.dsk0 = lo_bf(ds); c.dsk1 = hi_bf(ds);
    c.cs1 = lo_bf(r8); c.bb2 = lo_bf(r9);
    asm volatile("" :: "v"(junk));
    if (w == 0 && !got0<0>(c.tag)) wait0(c, lane, WS_F0);
    __syncthreads();
    conv_load(c, lane, inA, WS_X, WS_ST, 0);
    conv_load(c, lane, inB, WS_X, WS_ST, 1);
    pack_w2_out(b, c.rs, smem + L_XB + 55552);
  }
  u32x4 w1f[2][16], w2f[14][4];
  layer_body<0>(c, w1f, w2f, inA, inB);
  layer_body<1>(c, w1f, w2f, inA, inB);
}

extern "C" __global__ void code_end_marker() {}

extern "C" void kernel_launch(void* const* d_in, const int* in_sizes, int n_in, void* d_out, int out_size,
                              void* d_ws, size_t ws_size, hipStream_t stream) {
  (void)in_sizes; (void)n_in; (void)out_size; (void)ws_size;
  char* ws = (char*)d_ws + ((ws_size - (size_t)WS_END) & ~(size_t)(2 * MB - 1));
  (void)hipFuncSetAttribute((const void*)main_kernel, hipFuncAttributeMaxDynamicSharedMemorySize, MAIN_LDS);
  main_kernel<<<256, 256, MAIN_LDS, stream>>>((const u16*)d_in[0], (const u16*)d_in[1], (const u16*)d_in[2],
                                              (const u16*)d_in[3], (const u16*)d_in[4], (const u16*)d_in[5],
                                              (const u16*)d_in[6], (const u16*)d_in[8], (const u16*)d_in[9],
                                              (const u16*)d_in[10], (const u16*)d_in[11], ws, (u16*)d_out);
}
```

```cpp
#include <hip/hip_runtime.h>
#include <hip/hip_bf16.h>

typedef unsigned short u16;
typedef unsigned int u32;
typedef long long i64;
typedef __attribute__((ext_vector_type(4))) short s16x4;
typedef __attribute__((ext_vector_type(2))) __bf16 bf16x2;
typedef __attribute__((ext_vector_type(2))) _Float16 f16x2;
typedef __attribute__((ext_vector_type(4))) float f32x4;
typedef __attribute__((ext_vector_type(2))) float f32x2;
typedef __attribute__((ext_vector_type(4))) unsigned u32x4;
typedef __attribute__((ext_vector_type(2))) unsigned u32x2;
typedef __attribute__((address_space(1))) unsigned gu32;
typedef __amdgpu_buffer_rsrc_t rsrc_t;
#define LAS __attribute__((address_space(3)))
#define DEVI __device__ __forceinline__
template <int V> struct IC { static constexpr int v = V; };
template <int I, int N, class F> __device__ __forceinline__ void sfor(F&& f) {
  if constexpr (I < N) { f(IC<I>{}); sfor<I + 1, N>(f); }
}
#define RLX_AGENT __ATOMIC_RELAXED, __HIP_MEMORY_SCOPE_AGENT

constexpr int SEQ = 2048, DM = 512, NST = 128, FF = 1792;
constexpr float LN_EPS = 1e-5f;
constexpr float FX_SCALE = 16777216.0f;
constexpr float FX_INV = 1.0f / 16777216.0f;
constexpr int SC1 = 16;

constexpr int MB = 1024 * 1024;
constexpr int WS_X = 0;
constexpr int WS_W1P = 12 * MB;
constexpr int WS_W2P = 14 * MB;
constexpr int WS_H = 16 * MB;
constexpr int WS_ST = 32 * MB;
constexpr int WS_S1 = 33 * MB;
constexpr int WS_GEN = 32 * MB + 196608;
constexpr int WS_F0 = WS_GEN + 256, WS_F1 = WS_F0 + 16384;
constexpr int WS_CT = 32 * MB + 262144;
constexpr int WS_ONE = 32 * MB + 393216;
constexpr int WS_DUMP = 32 * MB + 524288;
constexpr int WS_END = 34 * MB;
constexpr int CT_C = 0, CT_X = 128, CT_H = 256, CT_TMO = 288;
constexpr int SPIN_CAP = 1 << 16;

DEVI float bf2f(u16 v) { return __uint_as_float((u32)v << 16); }
DEVI float lo_bf(u32 w) { return __uint_as_float(w << 16); }
DEVI float hi_bf(u32 w) { return __uint_as_float(w & 0xffff0000u); }
DEVI float rbf(float x) { return (float)(__bf16)x; }
DEVI u32 pack_bf(float a, float b) { f32x2 q = {a, b}; return __builtin_bit_cast(u32, __builtin_convertvector(q, bf16x2)); }
DEVI u32 pack_h(float a, float b) { f32x2 q = {a, b}; return __builtin_bit_cast(u32, __builtin_convertvector(q, f16x2)); }
DEVI float dot2bf(u32 a, u32 b, float c) {
  return __builtin_amdgcn_fdot2_f32_bf16(__builtin_bit_cast(bf16x2, a), __builtin_bit_cast(bf16x2, b), c, false);
}
DEVI u32x2 tr_read(const char* p) {
  return __builtin_bit_cast(u32x2, __builtin_amdgcn_ds_read_tr16_b64_v4i16((LAS s16x4*)p));
}
DEVI u32x4 cat2(u32x2 a, u32x2 b) { return __builtin_shufflevector(a, b, 0, 1, 2, 3); }
DEVI u32 lds_addr(const void* p) { return (u32)(size_t)(const LAS char*)p; }
#define WAIT_VM(n) asm volatile("s_waitcnt vmcnt(" #n ")" ::: "memory")
#define WAIT_VM_BAR(n) asm volatile("s_waitcnt vmcnt(" #n ")\n\ts_barrier" ::: "memory")
#define SCHED() __builtin_amdgcn_sched_barrier(0)
template <int CTRL> DEVI float dpp_add(float v) {
  return v + __uint_as_float(__builtin_amdgcn_update_dpp(0u, __float_as_uint(v), CTRL, 0xf, 0xf, true));
}
DEVI float sum_x16(float v) {
  auto r = __builtin_amdgcn_permlane16_swap(__float_as_uint(v), __float_as_uint(v), false, false);
  return __uint_as_float(r[0]) + __uint_as_float(r[1]);
}
DEVI float sum_x32(float v) {
  auto r = __builtin_amdgcn_permlane32_swap(__float_as_uint(v), __float_as_uint(v), false, false);
  return __uint_as_float(r[0]) + __uint_as_float(r[1]);
}
DEVI float wave_sum(float v) {
  v = dpp_add<0xB1>(v); v = dpp_add<0x4E>(v); v = dpp_add<0x141>(v); v = dpp_add<0x140>(v);
  return sum_x32(sum_x16(v));
}
DEVI i64 f2fx(float v) {
  const float a = v * (FX_SCALE / 4294967296.0f), h = __builtin_floorf(a);
  return __builtin_bit_cast(i64, u32x2{(u32)((a - h) * 4294967296.0f), (u32)(int)h});
}
DEVI float sel4(int i, float a, float b, float c, float d) {
  asm volatile("" : "+v"(a), "+v"(b), "+v"(c), "+v"(d));
  return (i & 2) ? ((i & 1) ? d : c) : ((i & 1) ? b : a);
}
DEVI int st_pos(int t) { return ((t >> 8) * 4 + (t & 3)) * 1024 + ((t >> 2) & 63) * 16; }
DEVI float rcp(float x) { return __builtin_amdgcn_rcpf(x); }
DEVI float sqrt_(float x) { return __builtin_amdgcn_sqrtf(x); }

DEVI u32 warm_code(int slice, int lane) {
  unsigned long long pc, pe;
  asm volatile("s_getpc_b64 %0\n\ts_getpc_b64 s[90:91]\n\ts_add_u32 s90, s90, code_end_marker@rel32@lo+4\n\t"
               "s_addc_u32 s91, s91, code_end_marker@rel32@hi+12\n\ts_mov_b64 %1, s[90:91]"
               : "=&s"(pc), "=s"(pe) :: "scc", "s90", "s91");
  const u32 off = min((u32)slice * 512u, (u32)(pe - pc) - 512u) + (lane & 7) * 64;
  u32 junk;
  asm volatile("s_nop 4\n\tglobal_load_dword %0, %1, %2" : "=v"(junk) : "v"(off), "s"(pc) : "memory");
  return junk;
}

DEVI int cidx(int l) {
  const int hb = (int)(pack_bf((float)l, 0.f) & 0xffffu) - (0x4300 - 128);
  return l < 128 ? l : hb;
}
struct CM { f32x2 e, f; };
DEVI f32x2 cmul(f32x2 a, const CM& m) { return a[1] * m.f + a[0] * m.e; }
DEVI CM cexp_(float a2, float br, float p) {
  const float e = __builtin_amdgcn_exp2f(a2 * p), f = __builtin_amdgcn_fractf(br * p);
  const float re = e * __builtin_amdgcn_cosf(f), im = e * __builtin_amdgcn_sinf(f);
  CM r; r.e = f32x2{re, im}; r.f = f32x2{-im, re};
  return r;
}
DEVI void st16(rsrc_t rs, int off, u32x4 v) { __builtin_amdgcn_raw_buffer_store_b128(v, rs, off, 0, SC1); }

DEVI void xpose(int b, const u32x4 (&v)[8], rsrc_t rs, char* smem) {
  const int t = threadIdx.x, lane = t & 63, w = t >> 6;
  constexpr int RS = 1024 + 32;
#pragma unroll
  for (int r = 0; r < 8; ++r) *(u32x4*)(smem + (w + 4 * r) * RS + lane * 16) = v[r];
  __syncthreads();
  const int g = lane >> 4, q = (lane & 15) >> 2, p = lane & 3;
#pragma unroll
  for (int cf = 0; cf < 8; ++cf) {
    const int c0 = (w * 8 + cf) * 16;
    const char* pa = smem + (8 * g + q) * RS + c0 * 2 + 8 * p;
    const u32x4 o = cat2(tr_read(pa), tr_read(pa + 4 * RS));
    st16(rs, WS_X + b * 32768 + (c0 + (lane & 15)) * 64 + g * 16, o);
  }
}
DEVI void row_stats(int b, const u32x4 (&v)[8], rsrc_t rs) {
  const int t = threadIdx.x, lane = t & 63, w = t >> 6;
  const int t0 = b * 32;
  float a[16];
#pragma unroll
  for (int r = 0; r < 8; ++r) {
    float s = 0.f, ss = 0.f;
#pragma unroll
    for (int j = 0; j < 4; ++j) { s = dot2bf(v[r][j], 0x3f803f80u, s); ss = dot2bf(v[r][j], v[r][j], ss); }
    a[2 * r] = s; a[2 * r + 1] = ss;
  }
  const bool o1 = lane & 1, o2 = lane & 2;
#pragma unroll
  for (int k = 0; k < 8; ++k) {
    const float keep = o1 ? a[2 * k + 1] : a[2 * k], send = o1 ? a[2 * k] : a[2 * k + 1];
    a[k] = keep + __uint_as_float(__builtin_amdgcn_update_dpp(0u, __float_as_uint(send), 0xB1, 0xf, 0xf, true));
  }
#pragma unroll
  for (int k = 0; k < 4; ++k) {
    const float keep = o2 ? a[2 * k + 1] : a[2 * k], send = o2 ? a[2 * k] : a[2 * k + 1];
    a[k] = keep + __uint_as_float(__builtin_amdgcn_update_dpp(0u, __float_as_uint(send), 0x4E, 0xf, 0xf, true));
  }
#pragma unroll
  for (int k = 0; k < 4; ++k) a[k] = sum_x32(sum_x16(dpp_add<0x128>(dpp_add<0x124>(a[k]))));
  const i64 sv = f2fx(sel4(lane >> 2, a[0], a[1], a[2], a[3]));
  if (lane < 16)
    __builtin_amdgcn_raw_buffer_store_b64(__builtin_bit_cast(u32x2, sv), rs, st_pos(t0 + w) + lane * 8, WS_ST, SC1);
  if (t >= 96 && t < 128) st16(rs, WS_ST + 65536 + b * 512 + (t - 96) * 16, u32x4{0, 0, 0, 0});
  if (t < 80) st16(rs, WS_CT + b * 1280 + t * 16, u32x4{0, 0, 0, 0});
}

DEVI void pack_w1(int ks, int cg, const u32x4 (&v)[8], rsrc_t rs, char* smem) {
  const int t = threadIdx.x, lane = t & 63, w = t >> 6;
  constexpr int RS = 512 + 32;
#pragma unroll
  for (int it = 0; it < 4; ++it) *(u32x4*)(smem + (it * 8 + (t >> 5)) * RS + (t & 31) * 16) = v[it];
  __syncthreads();
  const int g = lane >> 4, q = (lane & 15) >> 2, p = lane & 3;
  float cs[4];
#pragma unroll
  for (int h = 0; h < 4; ++h) {
    const char* a = smem + (8 * g + q) * RS + (w * 4 + h) * 32 + 8 * p;
    const u32x4 o = cat2(tr_read(a), tr_read(a + 4 * RS));
    st16(rs, WS_W1P + (((cg * 16 + w * 4 + h) * 16 + ks) * 64 + lane) * 16, o);
    float s = 0.f;
#pragma unroll
    for (int j = 0; j < 4; ++j) s = dot2bf(o[j], 0x3f803f80u, s);
    cs[h] = sum_x32(sum_x16(s));
  }
  __builtin_amdgcn_raw_buffer_store_b32(__float_as_uint(sel4(g, cs[0], cs[1], cs[2], cs[3])), rs, (ks * FF + cg * 256 + t) * 4, WS_S1, SC1);
  if (ks == 0 && cg == 0) st16(rs, WS_ONE + t * 16, u32x4{0x3f803f80u, 0x3f803f80u, 0x3f803f80u, 0x3f803f80u});
}

constexpr int W2_RS = 256 + 32;
DEVI void pack_w2_put(const u32x4 (&v)[2], char* smem) {
  const int t = threadIdx.x;
#pragma unroll
  for (int r = 0; r < 2; ++r) *(u32x4*)(smem + (r * 16 + (t >> 4)) * W2_RS + (t & 15) * 16) = v[r];
}
DEVI void pack_w2_out(int b, rsrc_t rs, const char* smem) {
  const int t = threadIdx.x, lane = t & 63, w = t >> 6;
  const int g = lane >> 4, q = (lane & 15) >> 2, p = lane & 3;
#pragma unroll
  for (int cf = 0; cf < 2; ++cf) {
    const char* a = smem + (4 * g + q) * W2_RS + (w * 2 + cf) * 32 + 8 * p;
    const u32x4 o = cat2(tr_read(a), tr_read(a + 16 * W2_RS));
    st16(rs, b < 224 ? WS_W2P + ((((b & 3) * 8 + w * 2 + cf) * 56 + (b >> 2)) * 64 + lane) * 16 : 0x40000000, o);
  }
}

constexpr int L_KC = 4160;
constexpr int L_U = 4 * L_KC, L_UC = 4608;
constexpr int L_XA = 26112, L_XB = L_XA + 65536;
constexpr int L_MISC = L_XB + 65536;
constexpr int L_CST = L_MISC + 1024;
constexpr int L_SIG = L_CST + 4 * 256;
constexpr int MAIN_LDS = L_SIG + 256;
constexpr int L_RED = L_XB;

DEVI u32x4 ld128(rsrc_t rs, int v, int s) { return __builtin_amdgcn_raw_buffer_load_b128(rs, v, s, SC1); }
DEVI u32x2 ld64(rsrc_t rs, int v, int s) { return __builtin_amdgcn_raw_buffer_load_b64(rs, v, s, SC1); }
DEVI void st128(rsrc_t rs, int v, int s, u32x4 d) { __builtin_amdgcn_raw_buffer_store_b128(d, rs, v, s, SC1); }
DEVI void st64(rsrc_t rs, int v, int s, u32x2 d) { __builtin_amdgcn_raw_buffer_store_b64(d, rs, v, s, SC1); }
template <int OFF> DEVI u32x4 ld128a(rsrc_t rs, int v, int s) {
  u32x4 r;
  asm volatile("buffer_load_dwordx4 %0, %1, %2, %3 offen offset:%4 sc1" : "=a"(r) : "v"(v), "s"(rs), "s"(s), "i"(OFF) : "memory");
  return r;
}
template <int OFF> DEVI u32x4 ld128v(rsrc_t rs, int v, int s) {
  u32x4 r;
  asm volatile("buffer_load_dwordx4 %0, %1, %2, %3 offen offset:%4 sc1" : "=v"(r) : "v"(v), "s"(rs), "s"(s), "i"(OFF) : "memory");
  return r;
}
template <int OFF> DEVI void dma1k(rsrc_t rs, u32 lds_dst, int v, int soff) {
  asm volatile("s_mov_b32 m0, %0\n\ts_nop 0\n\tbuffer_load_dwordx4 %1, %2, %3 offen offset:%4 sc1 lds"
               :: "s"(lds_dst), "v"(v), "s"(rs), "s"(soff), "i"(OFF) : "memory");
}
#define MMA(NAME, OP, CA, CB)                                                                                   \
  template <bool Z> DEVI void NAME(f32x4& d, const u32x4& a, const u32x4& b) {                                  \
    if (Z) asm volatile(OP " %0, %1, %2, 0" : "=&v"(d) : CA(a), CB(b));                                         \
    else asm volatile(OP " %0, %1, %2, %0" : "+v"(d) : CA(a), CB(b));                                           \
  }
MMA(mma_vv, "v_mfma_f32_16x16x32_bf16", "v", "v")
MMA(mma_av, "v_mfma_f32_16x16x32_bf16", "a", "v")
MMA(mma_va, "v_mfma_f32_16x16x32_bf16", "v", "a")
MMA(mma_hh, "v_mfma_f32_16x16x32_f16", "v", "v")
#define MFMA_DONE() do { SCHED(); asm volatile("s_nop 7" ::: "memory"); SCHED(); } while (0)

struct Ctx {
  rsrc_t rs; gu32* ct; char* smem; char* ws; u16* out;
  int lane0, w, b, P, sub, ct_, ff0, hks, eo, f1o, f1s;
  float dsk0, dsk1, ks0, ks1, bb2, cs1;
  u32 tag;
};
DEVI int fresh_lane(const Ctx& c) { int l = c.lane0; asm volatile("" : "+v"(l)); return l; }
DEVI u32 sig_begin(const Ctx& c, int lane) {
  u32 old;
  asm volatile("ds_add_rtn_u32 %0, %1, %2" : "=v"(old) : "v"(lds_addr(c.smem) + L_SIG + lane * 4), "v"(1u) : "memory");
  return old;
}
template <bool ALL, bool BACK = false> DEVI void sig_end(const Ctx& c, int lane, u32 old, int line) {
  if (BACK) asm volatile("" : "+v"(old) :: "memory");
  else asm volatile("s_waitcnt lgkmcnt(0)" : "+v"(old) :: "memory");
  const bool last = (__builtin_amdgcn_readfirstlane(old) & 3u) == 3u;
  const int v = (last && lane < (ALL ? 8 : 1)) ? (ALL ? lane * 2048 : 0) : 0x40000000;
  asm volatile("s_nop 4\n\tbuffer_atomic_add %0, %1, %2, %3 offen" :: "v"(1u), "v"(v), "s"(c.rs),
               "s"(WS_CT + (line + (ALL ? (c.b >> 3) & 7 : 0)) * 256) : "memory");
}
template <bool ALL> DEVI void poll(const Ctx& c, int lane, int line) {
  const int v = ALL ? (lane & 7) * 256 : 0, so = WS_CT + (line + (ALL ? (c.b & 7) * 8 : 0)) * 256;
  asm volatile("s_nop 4\n\tbuffer_load_dword v254, %0, %1, %2 offen sc1" :: "v"(v), "s"(c.rs), "s"(so) : "memory", "v254");
}
template <int YOUNGER> DEVI bool got(u32 want) {
  unsigned long long m;
  asm volatile("s_waitcnt vmcnt(%2)\n\tv_cmp_le_u32_e64 %0, %1, v254" : "=s"(m) : "s"(want), "i"(YOUNGER) : "memory");
  return m == ~0ull;
}
template <bool ALL> DEVI void wait_ge(const Ctx& c, int lane, int line, u32 want) {
  int sp = 0;
#pragma nounroll
  do {

    poll<ALL>(c, lane, line);
    if (++sp > SPIN_CAP) { __hip_atomic_store(c.ct + CT_TMO * 64, 1u, RLX_AGENT); break; }
  } while (!got<0>(want));
}

DEVI void poll0(const Ctx& c, int lane, int set) {
  asm volatile("s_nop 4\n\tbuffer_load_dwordx2 v[254:255], %0, %1, %2 offen sc1" :: "v"((lane & 7) * 256 + (lane >> 3) * 8), "s"(c.rs),
               "s"(set + (c.b & 7) * 2048) : "memory", "v254", "v255");
}
template <int YOUNGER> DEVI bool got0(u32 tag) {
  unsigned long long m, m2;
  asm volatile("s_waitcnt vmcnt(%3)\n\tv_cmp_eq_u32_e64 %0, %2, v254\n\tv_cmp_eq_u32_e64 %1, %2, v255"
               : "=&s"(m), "=s"(m2) : "s"(tag), "i"(YOUNGER) : "memory");
  return (m & m2) == ~0ull;
}
DEVI void wait0(const Ctx& c, int lane, int set) {
  int sp = 0;
#pragma nounroll
  do {
    poll0(c, lane, set);
    if (++sp > SPIN_CAP) { __hip_atomic_store(c.ct + CT_TMO * 64, 1u, RLX_AGENT); break; }
  } while (!got0<0>(c.tag));
}

struct CIn { u32x2 x0, x1; u32x4 s[4]; };
DEVI void conv_load(const Ctx& c, int lane, CIn& in, int xoff, int stoff, int h) {
  const int sx = xoff + (32 * h + 8 * c.w) * 32768 + c.b * 128;
  const int vx = (lane >> 3) * 32768 + (lane & 7) * 8;
  const int ss = stoff + (1024 * h + 256 * c.w) * 16;
#pragma unroll
  for (int j = 0; j < 4; ++j) in.s[j] = ld128(c.rs, lane * 16 + j * 1024, ss);
  in.x0 = ld64(c.rs, vx, sx);
  in.x1 = ld64(c.rs, vx + 64, sx);
}
DEVI void conv_ln(const Ctx& c, int lane, const CIn& in, int h, f32x4& base0, f32x4& base1) {
  constexpr float CH = 4294967296.0f * FX_INV / DM, CL = FX_INV / DM;
  f32x4 m, r;
#pragma unroll
  for (int j = 0; j < 4; ++j) {
    m[j] = fmaf((float)(int)in.s[j][1], CH, (float)in.s[j][0] * CL);
    r[j] = fmaf((float)(int)in.s[j][3], CH, fmaf((float)in.s[j][2], CL, LN_EPS));
  }
#pragma unroll
  for (int j = 0; j < 4; ++j) r[j] = __builtin_amdgcn_rsqf(fmaxf(fmaf(-m[j], m[j], r[j]), LN_EPS));
  const f32x4 rr = r, mr = -m * r;
  const f32x4 x0 = {lo_bf(in.x0[0]), hi_bf(in.x0[0]), lo_bf(in.x0[1]), hi_bf(in.x0[1])};
  const f32x4 x1 = {lo_bf(in.x1[0]), hi_bf(in.x1[0]), lo_bf(in.x1[1]), hi_bf(in.x1[1])};
  const f32x4 u0 = x0 * rr + mr, u1 = x1 * rr + mr;
  char* up = c.smem + L_U + 512 + (1024 * h + 256 * c.w + 4 * lane) * 2;
  *(u32x2*)up = u32x2{pack_h(u0[0], u0[1]), pack_h(u0[2], u0[3])};
  *(u32x2*)(up + L_UC) = u32x2{pack_h(u1[0], u1[1]), pack_h(u1[2], u1[3])};
  base0 = u0 * c.dsk0 + x0; base1 = u1 * c.dsk1 + x1;
}
struct KB { const char* p[4]; };
template <int MM, int LAM> DEVI u32x4 conv_k1(const KB& kb) {
  const u32* kp = (const u32*)(kb.p[(7 - LAM) >> 1] + 64 * (3 - MM) + 512 * ((7 - LAM) & 1));
  return u32x4{kp[0], kp[1], kp[2], kp[3]};
}
template <int MM, int TB> DEVI u32x4 conv_u1(const char* ub) { return *(const u32x4*)(ub + 64 * (3 - MM) + 512 * TB); }
template <int LO> DEVI void conv_ldk(const KB& kb, u32x4 (&af)[4]) {
  sfor<0, 4>([&](auto L) { af[L.v] = conv_k1<0, LO + L.v>(kb); });
}
constexpr int conv_rank(int DT, int LO, int gr, int lt) {
  int k = 0;
  for (int x = 0; x < lt; ++x) { const int tb = DT + (x & 3) - (LO + 4 * gr + (x >> 2)); k += (tb >= 0 && tb < 4); }
  return k;
}
template <int DT, int TB0, bool ZERO, class F>
DEVI void conv_mma(const char* ub, const KB& kb, f32x4 (&acc)[4], const u32x4 (&af0)[4], F&& hook) {
  constexpr int NG = DT ? 2 : 1, LO = DT ? 1 : 0;
  u32x4 af[2][4], bf[2][4];
  sfor<0, 4>([&](auto L) { af[0][L.v] = af0[L.v]; });
  sfor<0, 4>([&](auto T) { constexpr int tb = DT ? 3 - T.v : T.v; bf[0][tb] = conv_u1<0, TB0 + tb>(ub); });
  SCHED();
  sfor<0, 4 * NG>([&](auto ST) {
    constexpr int st = ST.v, mm = st / NG, gr = st % NG;
    constexpr bool more = st + 1 < 4 * NG;
    constexpr int mn = (st + 1) / NG, gn = (st + 1) % NG;
    constexpr bool nu = more && gr == NG - 1;
    sfor<0, 16>([&](auto LT) {
      constexpr int l = LT.v >> 2, T = LT.v & 3, tb = DT + T - (LO + 4 * gr + l);
      if constexpr (tb >= 0 && tb < 4) {
        constexpr int k = conv_rank(DT, LO, gr, LT.v);
        constexpr bool first = ZERO && st == 0 && (DT ? (l == T) : (l == 0));
        mma_hh<first>(acc[T], af[st & 1][l], bf[mm & 1][tb]);
        sfor<(gr == 1 ? 2 * k : k), (gr == 1 ? 2 * k + 2 : k + 1)>([&](auto J) {
          constexpr int j = J.v;
          if constexpr (nu && j < 8) {
            if constexpr (DT != 0) {
              if constexpr (j & 1) bf[mn & 1][3 - (j >> 1)] = conv_u1<mn, TB0 + 3 - (j >> 1)>(ub);
              else af[(st + 1) & 1][j >> 1] = conv_k1<mn, LO + (j >> 1)>(kb);
            } else {
              if constexpr (j >= 1 && j <= 4) bf[mn & 1][j - 1] = conv_u1<mn, TB0 + j - 1>(ub);
              else af[(st + 1) & 1][j ? j - 4 : 0] = conv_k1<mn, LO + (j ? j - 4 : 0)>(kb);
            }
          } else if constexpr (more && !nu && j < 3) af[(st + 1) & 1][j] = conv_k1<mn, LO + 4 * gn + j>(kb);
        });
        SCHED();
        hook(IC<mm * (DT ? 16 : 10) + gr * 10 + k>{});
      }
    });
  });
}
DEVI void conv_part(const Ctx& c, int lane, const f32x4 (&acc)[4]) {
  char* red = c.smem + L_RED;
#pragma unroll
  for (int T = 0; T < 4; ++T) *(f32x4*)(red + (c.w * 4 + T) * 1024 + (lane & 15) * 64 + (lane >> 4) * 16) = acc[T];
}
DEVI void conv_out(const Ctx& c, int lane, const f32x4& base0, const f32x4& base1, int xoff, int h) {
  const int sx = xoff + (16 * h + 4 * c.w) * 65536 + c.b * 256;
  const int vx = (lane >> 4) * 65536 + (lane & 15) * 8;
  const char* p = c.smem + L_RED + c.w * 1024 + lane * 16;
  const f32x4 a0 = *(const f32x4*)p, b0 = *(const f32x4*)(p + 4096), a1 = *(const f32x4*)(p + 8192), b1 = *(const f32x4*)(p + 12288);
  SCHED();
  const f32x4 y0 = (a0 + b0) * c.ks0 + base0, y1 = (a1 + b1) * c.ks1 + base1;
  st64(c.rs, vx, sx, u32x2{pack_bf(y0[0], y0[1]), pack_bf(y0[2], y0[3])});
  st64(c.rs, vx + 128, sx, u32x2{pack_bf(y1[0], y1[1]), pack_bf(y1[2], y1[3])});
}

template <int PC> DEVI void g1_dma1(const Ctx& c, int ve, int vo, int ldsx, int soff) {
  dma1k<(PC & 3) * 1024>(c.rs, lds_addr(c.smem) + ldsx + (4 * (PC >> 2) + c.w) * 4096, (PC & 1) ? vo : ve, soff + (4 * (PC >> 2) + c.w) * 4096);
}
template <bool ALL> DEVI void sig_bar(const Ctx& c, int lane, int line) {
  const int v = (c.w == 0 && lane < (ALL ? 8 : 1)) ? (ALL ? lane * 2048 : 0) : 0x40000000;
  asm volatile("s_nop 4\n\tbuffer_atomic_add %0, %1, %2, %3 offen" :: "v"(1u), "v"(v), "s"(c.rs),
               "s"(WS_CT + (line + (ALL ? (c.b >> 3) & 7 : 0)) * 256) : "memory");
}
template <class F, class G>
DEVI void g1_mma(const char* const (&xb)[4], const u32x4 (&w1f)[2][16], f32x4 (&acc)[2][4], f32x4& sa, F&& mid, G&& hook) {
  u32x2 xf[2][4][2];
#pragma unroll
  for (int i = 0; i < 4; ++i) { xf[0][i][0] = tr_read(xb[i]); xf[0][i][1] = tr_read(xb[i] + 512); }
  SCHED();
  sfor<0, 16>([&](auto KS) {
    constexpr int ks = KS.v;
    mid(KS);
    sfor<0, 4>([&](auto I) {
      constexpr int i = I.v;
      const u32x4 x = cat2(xf[ks & 1][i][0], xf[ks & 1][i][1]);
      mma_vv<ks == 0>(acc[0][i], w1f[0][ks], x);
      if constexpr (ks < 15) xf[(ks + 1) & 1][i][0] = tr_read(xb[i] + (ks + 1) * 4096);
      SCHED();
      if constexpr (ks < 8) mma_av<ks == 0>(acc[1][i], w1f[1][ks], x); else mma_vv<false>(acc[1][i], w1f[1][ks], x);
      if constexpr (ks < 15) xf[(ks + 1) & 1][i][1] = tr_read(xb[i] + (ks + 1) * 4096 + 512);
      SCHED();
      if constexpr (i == 0) { mma_vv<ks == 0>(sa, x, x); hook(IC<2 * ks>{}); SCHED(); }
      if constexpr (i == 2) { hook(IC<2 * ks + 1>{}); SCHED(); }
    });
  });
  MFMA_DONE();
}
template <class F>
DEVI void g1_epi(const Ctx& c, int lane, f32x4 (&acc)[2][4], const f32x4& sa, int hoff, F&& hook) {
  const int g = lane >> 4, cc = lane & 15, q = cc >> 2, p = lane & 3, w = c.w;
  float* sums = (float*)(c.smem + L_MISC);
  {
    const float d = sel4(p, sa[0], sa[1], sa[2], sa[3]);
    sums[q == g ? (w * 16 + cc) * 2 + 1 : 128 + lane] = d;
    const float r = sel4(g, acc[1][1][0], acc[1][2][0], acc[1][3][0], acc[1][0][0]);
    sums[w == 3 ? lane * 2 : 192 + lane] = r;
  }
  f32x4 s1v[2], b1v[2];
#pragma unroll
  for (int j = 0; j < 2; ++j) {
    s1v[j] = *(const f32x4*)(c.smem + L_CST + w * 256 + j * 128 + g * 16);
    b1v[j] = *(const f32x4*)(c.smem + L_CST + w * 256 + j * 128 + 64 + g * 16);
  }
  const int v16 = lane * 16 + (w < 3 ? 0 : 0x40000000), v8 = lane * 8 + (w < 3 ? 0x40000000 : c.eo * 512);
  __syncthreads();
  f32x2 k[4];
#pragma unroll
  for (int i = 0; i < 4; ++i) k[i] = *(const f32x2*)(sums + (((i + w) & 3) * 16 + cc) * 2);
#pragma unroll
  for (int i = 0; i < 4; ++i) {
    const float m = k[i][0] * (1.0f / DM);
    const float ve = fmaxf(fmaf(-m, m, fmaf(k[i][1], 1.0f / DM, LN_EPS)), LN_EPS);
    const float r = __builtin_amdgcn_rsqf(fmaf(ve, 1.0f + LN_EPS, -LN_EPS));
    k[i] = f32x2{r, -m * r};
  }
  SCHED();
  hook(IC<-1>{});
  SCHED();
  sfor<0, 2>([&](auto IH) {
    constexpr int ih = IH.v;
    f32x4 x[4], e[4];
#define STAGE(n) do { SCHED(); hook(IC<10 * ih + n>{}); SCHED(); } while (0)
#pragma unroll
    for (int n = 0; n < 4; ++n) x[n] = k[2 * ih + (n >> 1)][1] * s1v[n & 1] + b1v[n & 1];
#pragma unroll
    for (int n = 0; n < 4; ++n) x[n] = acc[n & 1][2 * ih + (n >> 1)] * k[2 * ih + (n >> 1)][0] + x[n];
    STAGE(0);
#pragma unroll
    for (int n = 0; n < 4; ++n) e[n] = x[n] * x[n];
#pragma unroll
    for (int n = 0; n < 4; ++n) e[n] = e[n] * (-0.0713548162726f * 1.44269504088896f) + (-1.59576912160573f * 1.44269504088896f);
    STAGE(1);
#pragma unroll
    for (int n = 0; n < 4; ++n) e[n] = e[n] * x[n];
    STAGE(2);
#pragma unroll
    for (int r = 0; r < 4; ++r) { e[0][r] = __builtin_amdgcn_exp2f(e[0][r]); e[1][r] = __builtin_amdgcn_exp2f(e[1][r]); }
    STAGE(3);
#pragma unroll
    for (int r = 0; r < 4; ++r) { e[2][r] = __builtin_amdgcn_exp2f(e[2][r]); e[3][r] = __builtin_amdgcn_exp2f(e[3][r]); }
    STAGE(4);
#pragma unroll
    for (int n = 0; n < 4; ++n) e[n] = e[n] + 1.0f;
    STAGE(5);
#pragma unroll
    for (int r = 0; r < 4; ++r) { e[0][r] = rcp(e[0][r]); e[1][r] = rcp(e[1][r]); }
    STAGE(6);
#pragma unroll
    for (int r = 0; r < 4; ++r) { e[2][r] = rcp(e[2][r]); e[3][r] = rcp(e[3][r]); }
    STAGE(7);
#pragma unroll
    for (int n = 0; n < 4; ++n) x[n] = x[n] * e[n];
    STAGE(8);
    u32x4 hv[2];
#pragma unroll
    for (int n = 0; n < 4; ++n) { hv[n >> 1][2 * (n & 1)] = pack_bf(x[n][0], x[n][1]); hv[n >> 1][2 * (n & 1) + 1] = pack_bf(x[n][2], x[n][3]); }
    STAGE(9);
    const int so0 = hoff + ((2 * ih + w) & 3) * 56 * 1024, so1 = hoff + ((2 * ih + 1 + w) & 3) * 56 * 1024;
    st128(c.rs, v16, so0, hv[0]); st128(c.rs, v16, so1, hv[1]);
    st64(c.rs, v8, so0, u32x2{hv[0][0], hv[0][1]}); st64(c.rs, v8, so1, u32x2{hv[1][0], hv[1][1]});
    SCHED();
#undef STAGE
  });
}

template <int N> DEVI void ld_w1(const Ctx& c, int v, u32x4 (&w1f)[2][16]) {
  constexpr int ks = N >> 1, j = N & 1;
  const int so = (j ? c.f1o + (ks >> 2) * c.f1s : WS_W1P + c.ff0 * 16384 + (ks >> 2) * 4096);
  if constexpr (j && ks < 8) w1f[j][ks] = ld128a<(ks & 3) * 1024>(c.rs, v, so);
  else w1f[j][ks] = ld128v<(ks & 3) * 1024>(c.rs, v, so);
}
template <int N> DEVI void ld_w2(const Ctx& c, int v, u32x4 (&w2f)[14][4]) {
  constexpr int s = N >> 2, j = N & 3;
  w2f[s][j] = ld128a<(s & 3) * 1024>(c.rs, v, WS_W2P + ((c.ct_ * 4 + ((c.w + j) & 3)) * 56 + 14 * c.w + (s & ~3)) * 1024);
}
template <int N> DEVI void g2_ldh1(const Ctx& c, int v16, u32x4 (&hf)[14][2], int ho) {
  constexpr int s = N >> 1, i = N & 1;
  if constexpr (s % 7 == 6)
    hf[s][i] = cat2(ld64(c.rs, (v16 >> 1) + (s & 3) * 1024, ho + i * 56 * 1024 + (s >> 2) * 4096),
                    ld64(c.rs, (v16 >> 1) + (s & 3) * 1024 + 512, ho + i * 56 * 1024 + (s >> 2) * 4096));
  else hf[s][i] = ld128(c.rs, v16 + (s & 3) * 1024, ho + i * 56 * 1024 + (s >> 2) * 4096);
}
template <int PRE, int RATE, class F>
DEVI void g2_mma(const Ctx& c, int lane, u32x4 (&hf)[14][2], const u32x4 (&w2f)[14][4], f32x4 (&acc)[2][4], int ho, F&& mid) {
  const int v16 = lane * 16;
  sfor<0, 14>([&](auto S) {
    constexpr int s = S.v;
    sfor<0, 8>([&](auto M) {
      constexpr int i = M.v >> 2, j = M.v & 3;
      mma_va<s == 0>(acc[i][j], hf[s][i], w2f[s][j]);
      constexpr int per = 4 / RATE, n = 2 * (PRE + RATE * s) + M.v / per;
      if constexpr (M.v % per == per - 1 && n < 28) { g2_ldh1<n>(c, v16, hf, ho); SCHED(); }
    });
    SCHED();
    mid(S);
  });
  MFMA_DONE();
}
template <bool LAST, class F>
DEVI void g2_epi(const Ctx& c, int lane, const f32x4 (&acc)[2][4], const u32x2 (&xr)[2], int h, int xdst, F&& mid) {
  const int g = lane >> 4, cc = lane & 15, q = cc >> 2, p = lane & 3, w = c.w, t = w * 64 + lane;
  char* red = c.smem + L_XA + h * 32768;
#pragma unroll
  for (int jj = 1; jj < 4; ++jj)
#pragma unroll
    for (int i = 0; i < 2; ++i) *(f32x4*)(red + ((((w + jj) & 3) * 3 + jj - 1) * 2 + i) * 1024 + lane * 16) = acc[i][jj];
  __syncthreads();
  char* xt = red + 24576;
  f32x4 pr[2][3];
#pragma unroll
  for (int i = 0; i < 2; ++i)
#pragma unroll
    for (int rk = 0; rk < 3; ++rk) pr[i][rk] = *(const f32x4*)(red + ((w * 3 + rk) * 2 + i) * 1024 + lane * 16);
  mid(0);
#pragma unroll
  for (int i = 0; i < 2; ++i) {
    const f32x4 xv = {lo_bf(xr[i][0]), hi_bf(xr[i][0]), lo_bf(xr[i][1]), hi_bf(xr[i][1])};
    const f32x4 o = ((acc[i][0] + (xv + c.bb2)) + pr[i][0]) + (pr[i][1] + pr[i][2]);
    const u32x2 r = {pack_bf(o[0], o[1]), pack_bf(o[2], o[3])};
    *(u32x2*)(xt + (w * 16 + cc) * 64 + ((i ^ (cc >> 3)) * 32) + g * 8) = r;
  }
  __syncthreads();
  const char* xq = xt + ((w >> 1) * 32 + 8 * g + q) * 64 + (((w & 1) ^ (g & 1)) * 32) + 8 * p;
  const u32x4 f0 = cat2(tr_read(xq), tr_read(xq + 256));
  const int tok = (16 * h + c.P) * 64 + c.sub * 32 + (w & 1) * 16 + cc;
  if (LAST) {
    *(u32x4*)(c.out + (size_t)tok * DM + c.ct_ * 64 + (w >> 1) * 32 + 8 * g) = f0;
  } else {
    const int row = t >> 2;
    const u32x4 v = *(const u32x4*)(xt + row * 64 + (((t & 3) ^ ((row >> 2) & 2)) * 16));
    st128(c.rs, c.ct_ * 4096 + t * 16, xdst, v);
    const char* xo = xq + ((w & 2) ? -2048 : 2048);
    const u32x4 f1 = cat2(tr_read(xo), tr_read(xo + 256));
    float s = 0.f, ss = 0.f, s2 = 0.f, ss2 = 0.f;
#pragma unroll
    for (int j = 0; j < 4; ++j) {
      s = dot2bf(f0[j], 0x3f803f80u, s); ss = dot2bf(f0[j], f0[j], ss);
      s2 = dot2bf(f1[j], 0x3f803f80u, s2); ss2 = dot2bf(f1[j], f1[j], ss2);
    }
    s = sum_x32(sum_x16(s + s2)); ss = sum_x32(sum_x16(ss + ss2));
    const u32 ao = t < 128 && lane < 32 ? WS_ST + 65536 + st_pos(tok) + g * 8 : WS_DUMP + (c.b * 256 + t) * 8;
    (void)__hip_atomic_fetch_add((unsigned long long*)(c.ws + ao), (unsigned long long)f2fx((g & 1) ? ss : s), RLX_AGENT);
  }
}

template <int LAYER>
DEVI void layer_body(const Ctx& c, u32x4 (&w1f)[2][16], u32x4 (&w2f)[14][4], CIn& inA, CIn& inB) {
  constexpr int xin = WS_X + LAYER * 4 * MB, xmid = xin + 2 * MB, xnext = xin + 4 * MB;
  constexpr int hbuf = WS_H, stin = WS_ST + LAYER * 65536;
  constexpr u32 ep = LAYER + 1;
  const int w = c.w;
  char* smem = c.smem;
  auto nop = [](auto) {};
  {
    const int lane = fresh_lane(c);
    const int g = lane >> 4, cc = lane & 15, mu0 = (w & 1) * 4, v16 = lane * 16;
    const char* ub = smem + L_U + (w >> 1) * L_UC + 512 + 2 * (16 * cc - 16 + 8 * g) - 64 * (mu0 + 3);
    KB kb;
#pragma unroll
    for (int j = 0; j < 4; ++j) {
      u32 a = lds_addr(smem) + (w >> 1) * 2 * L_KC + ((cc & 1) ? 0 : L_KC) + 2 * (2031 - cc + 8 * g - ((cc & 1) ? 0 : 1)) - 64 * (mu0 + 3 + 56) + 1024 * j;
      asm volatile("" : "+v"(a));
      kb.p[j] = (const char*)(const LAS char*)(size_t)a;
    }
    f32x4 base0, base1;
    u32x4 a4[4];
    conv_ldk<0>(kb, a4);
    conv_ln(c, lane, inA, 0, base0, base1);
    if (!LAYER) poll0(c, lane, WS_F1);
    __syncthreads();
    f32x4 acc[4];
    u32 o = 0;
    conv_mma<0, 0, true>(ub, kb, acc, a4, [&](auto N) {
      constexpr int n = N.v;
      if constexpr (LAYER && n == 19) { WAIT_VM(0); o = sig_begin(c, lane); SCHED(); }
      if constexpr (LAYER && n == 30) { sig_end<true, true>(c, lane, o, CT_X + 64); SCHED(); }
    });
    MFMA_DONE();
    conv_part(c, lane, acc);
    conv_ldk<1>(kb, a4);
    __syncthreads();
    float s1p[8];
    if (!LAYER) {
      if (!got0<0>(c.tag)) wait0(c, lane, WS_F1);
      asm volatile("" :: "v"(inB.x1));
      const int so1 = (min(c.ff0 * 16 + (lane & 15) + (lane >> 5) * 16, FF - 1) + (lane & 16) * (FF / 2)) * 4;
#pragma unroll
      for (int j = 0; j < 8; ++j)
        asm volatile("s_nop 4\n\tbuffer_load_dword %0, %1, %2, %3 offen sc1" : "=v"(s1p[j]) : "v"(so1), "s"(c.rs), "s"(WS_S1 + j * FF * 4) : "memory");
    }
    conv_out(c, lane, base0, base1, xmid, 0);
    f32x4 acb[4];
    conv_mma<4, 0, true>(ub, kb, acb, a4, [&](auto N) {
      constexpr int n = N.v;
      if constexpr (!LAYER && n >= 48 && (n & 1)) { ld_w1<(n - 48) / 2>(c, v16, w1f); SCHED(); }
      if constexpr (n == 47) {
        WAIT_VM(0);
        o = sig_begin(c, lane);
        if (LAYER) poll<true>(c, lane, CT_X + 64);
        SCHED();
      }
      if constexpr (n == 58) { sig_end<true, true>(c, lane, o, CT_C); SCHED(); }
    });
    if (!LAYER) {
#pragma unroll
      for (int j = 0; j < 8; ++j) asm volatile("" : "+v"(s1p[j]));
      const float sv = sum_x16(((s1p[0] + s1p[1]) + (s1p[2] + s1p[3])) + ((s1p[4] + s1p[5]) + (s1p[6] + s1p[7])));
      ((float*)(smem + L_CST))[w * 64 + lane] = (lane & 16) ? c.cs1 : sv;
    }
    if (LAYER) {
      if (!got<1>(32u)) wait_ge<true>(c, lane, CT_X + 64, 32u);
      conv_load(c, lane, inB, xin, stin, 1);
    }
    conv_ldk<0>(kb, a4);
    if (!LAYER) sfor<8, 12>([&](auto N) { ld_w1<N.v>(c, v16, w1f); });
    conv_ln(c, lane, inB, 1, base0, base1);
    if (!LAYER) sfor<12, 14>([&](auto N) { ld_w1<N.v>(c, v16, w1f); });
    __syncthreads();
    conv_mma<0, 4, false>(ub, kb, acb, a4, [&](auto N) {
      constexpr int n = N.v;
      if constexpr (!LAYER && n % 3 == 1 && n < 36) { ld_w1<14 + n / 3>(c, v16, w1f); SCHED(); }
      if constexpr (n == 30) { poll<true>(c, lane, CT_C); SCHED(); }
    });
    MFMA_DONE();
    conv_part(c, lane, acb);
    __syncthreads();
    conv_out(c, lane, base0, base1, xmid, 1);
    if (!got<(LAYER ? 2 : 4)>(32u * ep)) wait_ge<true>(c, lane, CT_C, 32u * ep);
    const int row = lane >> 3, sw = ((row >> 1) & 1) << 1;
    const int ve = row * 128 + ((lane & 7) ^ sw) * 16, vo = row * 128 + ((lane & 7) ^ (sw | 4)) * 16;
    if (!LAYER && c.b == 0 && w == 0) __hip_atomic_store((gu32*)(c.ws + WS_GEN), c.tag, RLX_AGENT);
    sfor<0, 8>([&](auto N) { g1_dma1<N.v>(c, ve, vo, L_XA, xmid + c.P * 65536); });
    WAIT_VM(8);
    o = sig_begin(c, lane);
    sig_end<true>(c, lane, o, CT_C + 64);
  }
  u32x4 hf[14][2];
  u32x2 xr[2];
  {
    const int lane = fresh_lane(c);
    const int g = lane >> 4, cc = lane & 15, q = cc >> 2, p = lane & 3, v16 = lane * 16;
    const int sx = ((q >> 1) & 1) | ((g & 1) << 1);
    const char* xb[4];
#pragma unroll
    for (int i = 0; i < 4; ++i) xb[i] = smem + L_XA + (8 * g + q) * 128 + ((((i + w) & 3) ^ sx) * 32) + 8 * p;
    const int row = lane >> 3, sw = ((row >> 1) & 1) << 1;
    const int ve = row * 128 + ((lane & 7) ^ sw) * 16, vo = row * 128 + ((lane & 7) ^ (sw | 4)) * 16;
    sfor<8, 12>([&](auto N) { g1_dma1<N.v>(c, ve, vo, L_XA, xmid + c.P * 65536); });
    WAIT_VM_BAR(9);
    f32x4 acc[2][4], sa;
    g1_mma(xb, w1f, acc, sa, [&](auto KS) {
      constexpr int ks = KS.v;
      if constexpr (ks == 3) { if (LAYER) WAIT_VM_BAR(8); else WAIT_VM_BAR(10); SCHED(); }
      if constexpr (ks == 7) { if (LAYER) WAIT_VM_BAR(0); else WAIT_VM_BAR(10); SCHED(); }
      if constexpr (ks == 13) { if (!LAYER) WAIT_VM(16); poll<true>(c, lane, CT_C + 64); SCHED(); }
    }, [&](auto N) {
      constexpr int n = N.v;
      if constexpr (n < 4) g1_dma1<12 + n>(c, ve, vo, L_XA, xmid + c.P * 65536);
      if constexpr (!LAYER && n >= 4 && n < 10) ld_w1<22 + n>(c, v16, w1f);
      if constexpr (!LAYER && n >= 10) ld_w2<n - 10>(c, v16, w2f);
    });
    const int xo = xmid + (16 + c.P) * 65536;
    g1_epi(c, lane, acc, sa, hbuf + (c.P * 4 * 56 + c.hks) * 1024, [&](auto N) {
      constexpr int n = N.v;
      if constexpr (n == -1) { if (!got<(LAYER ? 0 : 6)>(32u * ep)) wait_ge<true>(c, lane, CT_C + 64, 32u * ep); }
      if constexpr (n >= 0 && n < 8) g1_dma1<n>(c, ve, vo, L_XB, xo);
      if constexpr (n >= 10 && n < 18) g1_dma1<n - 2>(c, ve, vo, L_XB, xo);
    });
  }
  {
    const int lane = fresh_lane(c);
    const int g = lane >> 4, cc = lane & 15, q = cc >> 2, p = lane & 3, v16 = lane * 16;
    const int sx = ((q >> 1) & 1) | ((g & 1) << 1);
    const char* xb[4];
#pragma unroll
    for (int i = 0; i < 4; ++i) xb[i] = smem + L_XB + (8 * g + q) * 128 + ((((i + w) & 3) ^ sx) * 32) + 8 * p;
    WAIT_VM_BAR(16);
    f32x4 acc[2][4], sa;
    g1_mma(xb, w1f, acc, sa, [&](auto KS) {
      constexpr int ks = KS.v;
      if constexpr (ks == 5) {
        if (LAYER) WAIT_VM_BAR(0); else WAIT_VM_BAR(10);
        sig_bar<false>(c, lane, CT_H + c.P);
        SCHED();
      }
      if constexpr (ks == 15) {
#pragma unroll
        for (int i = 0; i < 2; ++i) xr[i] = ld64(c.rs, (c.ct_ * 64 + w * 16 + cc) * 128 + i * 32 + g * 8, xmid + c.P * 65536 + c.sub * 64);
        SCHED();
      }
    }, [&](auto N) {
      constexpr int n = N.v;
      if constexpr (!LAYER) ld_w2<22 + n>(c, v16, w2f);
      if constexpr (!LAYER && n >= 30) ld_w2<24 + n>(c, v16, w2f);
    });
    poll<false>(c, lane, CT_H + c.P);
    const int ho = hbuf + ((c.P * 4 + c.sub * 2) * 56 + 14 * w) * 1024;
    g1_epi(c, lane, acc, sa, hbuf + ((16 + c.P) * 4 * 56 + c.hks) * 1024, [&](auto N) {
      constexpr int n = N.v;
      if constexpr (n == -1) { if (!got<0>(16u * ep)) wait_ge<false>(c, lane, CT_H + c.P, 16u * ep); }
      if constexpr (n >= 0) g2_ldh1<n>(c, v16, hf, ho);
    });
  }
  {
    const int lane = fresh_lane(c);
    const int g = lane >> 4, cc = lane & 15;
    const int ho = hbuf + ((c.P * 4 + c.sub * 2) * 56 + 14 * w) * 1024, hoB = ho + 64 * 56 * 1024;
    f32x4 acc[2][4];
    u32 o = 0;
    g2_mma<10, 1>(c, lane, hf, w2f, acc, ho, [&](auto S) {
      constexpr int s = S.v;
      if constexpr (s == 5) { WAIT_VM(10); o = sig_begin(c, lane); SCHED(); }
      if constexpr (s == 6) { sig_end<false>(c, lane, o, CT_H + 16 + c.P); SCHED(); }
    });
    poll<false>(c, lane, CT_H + 16 + c.P);
    g2_epi<LAYER == 1>(c, lane, acc, xr, 0, xnext + c.P * 65536 + c.sub * 32768, nop);
    if (!got<(LAYER ? 1 : 2)>(16u * ep)) wait_ge<false>(c, lane, CT_H + 16 + c.P, 16u * ep);
    sfor<0, 8>([&](auto N) { g2_ldh1<N.v>(c, lane * 16, hf, hoB); });
#pragma unroll
    for (int i = 0; i < 2; ++i) xr[i] = ld64(c.rs, (c.ct_ * 64 + w * 16 + cc) * 128 + i * 32 + g * 8, xmid + (16 + c.P) * 65536 + c.sub * 64);
    if (LAYER == 0) {
      WAIT_VM(10);
      o = sig_begin(c, lane);
      sig_end<true>(c, lane, o, CT_X);
    }
    SCHED();
  }
  {
    const int lane = fresh_lane(c);
    const int hoB = hbuf + (((16 + c.P) * 4 + c.sub * 2) * 56 + 14 * w) * 1024;
    f32x4 acc[2][4];
    g2_mma<4, 2>(c, lane, hf, w2f, acc, hoB, [&](auto S) {
      constexpr int s = S.v;
      if constexpr (LAYER == 0 && s == 13) { poll<true>(c, lane, CT_X); SCHED(); }
    });
    g2_epi<LAYER == 1>(c, lane, acc, xr, 1, xnext + (16 + c.P) * 65536 + c.sub * 32768, [&](int) {
      if (LAYER == 0) {
        if (!got<0>(32u)) wait_ge<true>(c, lane, CT_X, 32u);
        conv_load(c, lane, inA, xnext, WS_ST + 65536, 0);
        SCHED();
      }
    });
  }
}

constexpr int KG_RS = 528, KG_X = 16 * KG_RS, KG_CH = 64 * KG_RS + 1296;
__global__ void __launch_bounds__(256, 1) main_kernel(const u16* x, const u16* lar, const u16* aim, const u16* cre,
                                                      const u16* cim, const u16* ldt, const u16* dsk, const u16* w1,
                                                      const u16* b1, const u16* w2, const u16* b2, char* ws, u16* out) {
  extern __shared__ __attribute__((aligned(16))) char smem[];
  const int t = threadIdx.x, lane = t & 63, b = blockIdx.x;
  const int w = __builtin_amdgcn_readfirstlane(t >> 6);
  const u32 junk = warm_code((b >> 3) * 4 + w, lane);
  const int ch = w >> 1, d = 2 * b + ch, n = t & 127;
  const u32 po = (u32)(d * NST + n) * 2, po2 = po ^ 128u;
#define LD16(p, off) (*(const u16*)((const char*)(p) + (u32)(off)))
  const u16 r0 = ldt[d], r1 = LD16(lar, po), r2 = LD16(aim, po), r3 = LD16(cre, po), r4 = LD16(cim, po);
  const u16 r5 = LD16(cre, po2), r6 = LD16(cim, po2);
  const u32 gen = __hip_atomic_load((gu32*)(ws + WS_GEN), RLX_AGENT);
  const bool early = !(b & 64);
  const int pks = (b & 7) + (b >> 7) * 8, pcg = (b >> 3) & 7;
  u32x4 rv[8], rw[2];
  {
    const rsrc_t rr = __builtin_amdgcn_make_buffer_rsrc((void*)(early ? x : w1), 0, early ? SEQ * DM * 2 : DM * FF * 2, 0x00020000);
    const int vo = early ? w * 1024 + lane * 16 : ((b & 63) < 56 ? (t >> 5) * (FF * 2) + (t & 31) * 16 : 0x40000000);
    const int so = early ? (b & 63) * 32768 : pks * (32 * FF * 2) + min(pcg, 6) * 512, st = early ? 4096 : 8 * FF * 2;
#pragma unroll
    for (int j = 0; j < 8; ++j) rv[j] = __builtin_amdgcn_raw_buffer_load_b128(rr, (j < 4 || early) ? vo : 0x40000000, so + (j & 3) * st + (j >> 2) * (early ? 16384 : 0), 0);
    const rsrc_t r2w = __builtin_amdgcn_make_buffer_rsrc((void*)w2, 0, DM * FF * 2, 0x00020000);
#pragma unroll
    for (int j = 0; j < 2; ++j) rw[j] = __builtin_amdgcn_raw_buffer_load_b128(r2w, (t >> 4) * 1024 + (t & 15) * 16 + j * 16384, min(b >> 2, 55) * 32768 + (b & 3) * 256, 0);
  }
  CIn inA, inB;
  Ctx c;
  c.rs = __builtin_amdgcn_make_buffer_rsrc(ws, 0, WS_END, 0x00020000);
  c.ct = (gu32*)(ws + WS_CT);
  c.smem = smem; c.ws = ws; c.out = out;
  c.lane0 = lane; c.w = w; c.b = b;
  c.P = (b & 7) * 2 + ((b >> 3) & 1);
  const int idx = b >> 4;
  const int sg = idx >> 1, w3 = -(int)(w == 3);
  c.eo = idx & 1; c.sub = idx >> 3; c.ct_ = idx & 7;
  c.ff0 = 14 * sg + (((6 * c.eo + 2 * w) & ~w3) | ((12 + c.eo) & w3));
  c.hks = 7 * sg + (((3 * c.eo + w) & ~w3) | (6 & w3));
  c.f1o = w < 3 ? WS_W1P + c.ff0 * 16384 + 16384 : WS_ONE; c.f1s = w < 3 ? 4096 : 0;
  {
    const u32 r7 = *(const u32*)(dsk + 2 * b);
    u32 r8 = LD16(b1, min(c.ff0 * 16 + (lane & 15) + (lane >> 5) * 16, FF - 1) * 2), r9 = LD16(b2, (c.ct_ * 64 + w * 16 + (lane & 15)) * 2);
    SCHED();
    char* kg = smem + L_XA + ch * KG_CH;
    char* stg = smem + L_XA + 2 * KG_CH;
    const int i = lane & 15, g = lane >> 4;
    int ix[9];
#pragma unroll
    for (int e2 = 0; e2 < 9; ++e2) { ix[e2] = 2 * cidx(max(2047 - 8 * t - e2, 0)); asm volatile("" : "+v"(ix[e2])); }
    int wi[2][4];
    {
      const int m8 = -(i >> 3), s1 = -(w & 1);
      const int h1 = ((256 + 128 * (i >> 2) + 2 * (i & 3)) & ~m8) | ((128 * (i >> 1) + 4 * (i & 1)) & m8);
      const int hp = (((i + (i & 8) * 15) & ~s1) | (h1 & s1)) + 32 * g, hq = 1024 + 128 * i + 32 * g;
#pragma unroll
      for (int r = 0; r < 4; ++r) {
        wi[0][r] = 2 * cidx(hp + 8 * r); wi[1][r] = 2 * cidx(hq + 8 * r > 2048 ? 2064 : hq + 8 * r);
        asm volatile("" : "+v"(wi[0][r]), "+v"(wi[1][r]));
      }
    }
    if (t < 16) *(u32x4*)(smem + (t >> 2) * L_KC + 4096 + (t & 3) * 16) = u32x4{0, 0, 0, 0};
    if (t >= 64 && t < 128) *(u32x4*)(smem + L_U + ((t - 64) >> 5) * L_UC + (t & 31) * 16) = u32x4{0, 0, 0, 0};
    if (t == 128) *(u32*)(smem + L_SIG) = 0u;
    st64(c.rs, t * 8, WS_DUMP + b * 2048, u32x2{0, 0});
    if (w >= 2) poll0(c, lane, w == 2 ? WS_F1 : WS_F0);
    SCHED();
    const float dt = rbf(expf(bf2f(r0)));
    const float cr = bf2f(r3), ci = bf2f(r4), cr2 = bf2f(r5), ci2 = bf2f(r6);
    const float S = wave_sum(sqrt_(cr * cr + ci * ci) + sqrt_(cr2 * cr2 + ci2 * ci2)) * (2.002f * dt);
    int e = __builtin_amdgcn_frexp_expf(S);
    e = (S > 0.0f && S < 3.0e38f) ? max(-100, min(100, e)) : 14;
    const float scale = __builtin_amdgcn_ldexpf(2.0f, 14 - e);
    if (n == 0) *(float*)(smem + L_MISC + ch * 4) = __builtin_amdgcn_ldexpf(1.0f, e - 14);
    const float a2 = (-rbf(expf(bf2f(r1))) * dt) * 1.44269504088896f;
    const float br = (bf2f(r2) * dt) * 0.159154943091895f;
    {
      char* yp = kg + n * 4;
      char* xp = kg + KG_X + n * 4;
      const CM e128 = cexp_(a2, -br, 128.f), e8 = cexp_(a2, br, 8.f), e1 = cexp_(a2, -br, 1.f), e2 = cexp_(a2, -br, 2.f);
      const CM e4 = cexp_(a2, -br, 4.f);
      f32x2 f0 = {(cr * dt) * scale, -(ci * dt) * scale};
      f32x2 f = cmul(f0, cexp_(a2, -br, 512.f)), y = {1.f, 0.f};
      f32x2 g1 = cmul(f0, e128), g2 = cmul(g1, e128), g3 = cmul(g2, e128);
#define PUT(base, row, v) *(u32*)(base + (row) * KG_RS) = pack_h(v[0], v[1])
      auto chain = [&](auto K) {
        constexpr int k = K.v;
        PUT(yp, k, y);
        if (k < 15) y = cmul(y, e8);
        if (k < 4) { const f32x2 q4 = cmul(f, e4); PUT(xp, 24 + 2 * k, f); PUT(xp, 25 + 2 * k, q4); }
        else if (k < 13) PUT(xp, 28 + k, f);
        if (k < 12) f = cmul(f, e128);
        if (k < 8) { PUT(xp, k, f0); PUT(xp, 8 + k, g1); }
        if (k < 7) { f0 = cmul(f0, e1); g1 = cmul(g1, e1); }
        if (k >= 8 && k < 12) { PUT(xp, 8 + k, g2); PUT(xp, 12 + k, g3); }
        if (k >= 8 && k < 11) { g2 = cmul(g2, e2); g3 = cmul(g3, e2); }
      };
      c.tag = __builtin_amdgcn_readfirstlane(gen) * 0x9E3779B1u + 0x7F4A7C15u;
      sfor<0, 2>(chain);
      asm volatile("" : "+v"(y), "+v"(f), "+v"(f0), "+v"(g1));
      if (b < 64) xpose(b, rv, c.rs, stg);
      else if (early) row_stats(b & 63, rv, c.rs);
      asm volatile("" : "+v"(y), "+v"(f), "+v"(f0), "+v"(g1));
      sfor<2, 12>(chain);
      asm volatile("" : "+v"(y), "+v"(f));
      if (early) {
        WAIT_VM(0);
        __syncthreads();
        if (w == 0 && lane < 8)
          __hip_atomic_store((gu32*)(ws + WS_F0 + lane * 2048 + (b & 7) * 256 + ((b >> 3) & 7) * 8 + (b >> 7) * 4), c.tag, RLX_AGENT);
      }
      asm volatile("" : "+v"(y), "+v"(f));
      sfor<12, 16>(chain);
      if (!early && pcg < 7) pack_w1(pks, pcg, rv, c.rs, stg);
#undef PUT
    }
    pack_w2_put(rw, smem + L_XB + 55552);
    __syncthreads();
    {
      const char* ya = kg + i * KG_RS + g * 16;
      const char* xa = kg + KG_X + (16 * (w & 1) + i) * KG_RS + g * 16;
      const char* xc = kg + KG_X + (32 + i) * KG_RS + g * 16;
      u32x4 fa[8], fb[8], fc[8];
#pragma unroll
      for (int ks = 0; ks < 8; ++ks) { fa[ks] = *(const u32x4*)(ya + ks * 64); fb[ks] = *(const u32x4*)(xa + ks * 64); }
#pragma unroll
      for (int ks = 0; ks < 8; ++ks) fc[ks] = *(const u32x4*)(xc + ks * 64);
      f32x4 ac0, ac1;
      SCHED();
      sfor<0, 8>([&](auto KS) { mma_hh<KS.v == 0>(ac0, fa[KS.v], fb[KS.v]); });
      sfor<0, 8>([&](auto KS) { mma_hh<KS.v == 0>(ac1, fa[KS.v], fc[KS.v]); });
      MFMA_DONE();
#pragma unroll
      for (int r = 0; r < 4; ++r) {
        *(u16*)(kg + 64 * KG_RS + wi[0][r]) = __builtin_bit_cast(u16, (_Float16)ac0[r]);
        *(u16*)(kg + 64 * KG_RS + wi[1][r]) = __builtin_bit_cast(u16, (_Float16)ac1[r]);
      }
    }
    if (!early) WAIT_VM(0);
    __syncthreads();
    if (w == 1 && !early && lane < 8)
      __hip_atomic_store((gu32*)(ws + WS_F1 + lane * 2048 + (b & 7) * 256 + ((b >> 3) & 7) * 8 + (b >> 7) * 4), c.tag, RLX_AGENT);
    if (w == 0) poll0(c, lane, WS_F0);
#pragma unroll
    for (int c2 = 0; c2 < 2; ++c2) {
      u32 v[9];
#pragma unroll
      for (int e2 = 0; e2 < 9; ++e2) v[e2] = *(const u16*)(smem + L_XA + c2 * KG_CH + 64 * KG_RS + ix[e2]);
      if (t == 255) v[8] = 0u;
      u32x4 oe, oo;
#pragma unroll
      for (int j = 0; j < 4; ++j) { oe[j] = v[2 * j] | (v[2 * j + 1] << 16); oo[j] = v[2 * j + 1] | (v[2 * j + 2] << 16); }
      *(u32x4*)(smem + c2 * 2 * L_KC + t * 16) = oe;
      *(u32x4*)(smem + c2 * 2 * L_KC + L_KC + t * 16) = oo;
    }
    const f32x2 ksc = *(const f32x2*)(smem + L_MISC);
    c.ks0 = __uint_as_float(__builtin_amdgcn_readfirstlane(__float_as_uint(ksc[0])));
    c.ks1 = __uint_as_float(__builtin_amdgcn_readfirstlane(__float_as_uint(ksc[1])));
    const u32 ds = __builtin_amdgcn_readfirstlane(r7);
    c.dsk0 = lo_bf(ds); c.dsk1 = hi_bf(ds);
    c.cs1 = lo_bf(r8); c.bb2 = lo_bf(r9);
    asm volatile("" :: "v"(junk));
    if (w == 0 && !got0<0>(c.tag)) wait0(c, lane, WS_F0);
    __syncthreads();
    conv_load(c, lane, inA, WS_X, WS_ST, 0);
    conv_load(c, lane, inB, WS_X, WS_ST, 1);
    pack_w2_out(b, c.rs, smem + L_XB + 55552);
  }
  u32x4 w1f[2][16], w2f[14][4];
  layer_body<0>(c, w1f, w2f, inA, inB);
  layer_body<1>(c, w1f, w2f, inA, inB);
}

extern "C" __global__ void code_end_marker() {}

extern "C" void kernel_launch(void* const* d_in, const int* in_sizes, int n_in, void* d_out, int out_size,
                              void* d_ws, size_t ws_size, hipStream_t stream) {
  (void)in_sizes; (void)n_in; (void)out_size; (void)ws_size;
  char* ws = (char*)d_ws + ((ws_size - (size_t)WS_END) & ~(size_t)(2 * MB - 1));
  (void)hipFuncSetAttribute((const void*)main_kernel, hipFuncAttributeMaxDynamicSharedMemorySize, MAIN_LDS);
  main_kernel<<<256, 256, MAIN_LDS, stream>>>((const u16*)d_in[0], (const u16*)d_in[1], (const u16*)d_in[2],
                                              (const u16*)d_in[3], (const u16*)d_in[4], (const u16*)d_in[5],
                                              (const u16*)d_in[6], (const u16*)d_in[8], (const u16*)d_in[9],
                                              (const u16*)d_in[10], (const u16*)d_in[11], ws, (u16*)d_out);
}
```

```cpp
#include <hip/hip_runtime.h>
#include <hip/hip_bf16.h>

typedef unsigned short u16;
typedef unsigned int u32;
typedef long long i64;
typedef __attribute__((ext_vector_type(4))) short s16x4;
typedef __attribute__((ext_vector_type(2))) __bf16 bf16x2;
typedef __attribute__((ext_vector_type(2))) _Float16 f16x2;
typedef __attribute__((ext_vector_type(4))) float f32x4;
typedef __attribute__((ext_vector_type(2))) float f32x2;
typedef __attribute__((ext_vector_type(4))) unsigned u32x4;
typedef __attribute__((ext_vector_type(2))) unsigned u32x2;
typedef __attribute__((address_space(1))) unsigned gu32;
typedef __amdgpu_buffer_rsrc_t rsrc_t;
#define LAS __attribute__((address_space(3)))
#define DEVI __device__ __forceinline__
template <int V> struct IC { static constexpr int v = V; };
template <int I, int N, class F> __device__ __forceinline__ void sfor(F&& f) {
  if constexpr (I < N) { f(IC<I>{}); sfor<I + 1, N>(f); }
}
#define RLX_AGENT __ATOMIC_RELAXED, __HIP_MEMORY_SCOPE_AGENT

constexpr int SEQ = 2048, DM = 512, NST = 128, FF = 1792;
constexpr float LN_EPS = 1e-5f;
constexpr float FX_SCALE = 16777216.0f;
constexpr float FX_INV = 1.0f / 16777216.0f;
constexpr int SC1 = 16;

constexpr int MB = 1024 * 1024;
constexpr int WS_X = 0;
constexpr int WS_W1P = 12 * MB;
constexpr int WS_W2P = 14 * MB;
constexpr int WS_H = 16 * MB;
constexpr int WS_ST = 32 * MB;
constexpr int WS_S1 = 33 * MB;
constexpr int WS_GEN = 32 * MB + 196608;
constexpr int WS_F0 = WS_GEN + 256, WS_F1 = WS_F0 + 16384;
constexpr int WS_CT = 32 * MB + 262144;
constexpr int WS_ONE = 32 * MB + 393216;
constexpr int WS_END = 34 * MB;
constexpr int CT_C = 0, CT_X = 128, CT_H = 256, CT_TMO = 288;
constexpr int SPIN_CAP = 1 << 16;

DEVI float bf2f(u16 v) { return __uint_as_float((u32)v << 16); }
DEVI float lo_bf(u32 w) { return __uint_as_float(w << 16); }
DEVI float hi_bf(u32 w) { return __uint_as_float(w & 0xffff0000u); }
DEVI float rbf(float x) { return (float)(__bf16)x; }
DEVI u32 pack_bf(float a, float b) { f32x2 q = {a, b}; return __builtin_bit_cast(u32, __builtin_convertvector(q, bf16x2)); }
DEVI u32 pack_h(float a, float b) { f32x2 q = {a, b}; return __builtin_bit_cast(u32, __builtin_convertvector(q, f16x2)); }
DEVI float dot2bf(u32 a, u32 b, float c) {
  return __builtin_amdgcn_fdot2_f32_bf16(__builtin_bit_cast(bf16x2, a), __builtin_bit_cast(bf16x2, b), c, false);
}
DEVI u32x2 tr_read(const char* p) {
  return __builtin_bit_cast(u32x2, __builtin_amdgcn_ds_read_tr16_b64_v4i16((LAS s16x4*)p));
}
DEVI u32x4 cat2(u32x2 a, u32x2 b) { return __builtin_shufflevector(a, b, 0, 1, 2, 3); }
DEVI u32 lds_addr(const void* p) { return (u32)(size_t)(const LAS char*)p; }
#define WAIT_VM(n) asm volatile("s_waitcnt vmcnt(" #n ")" ::: "memory")
#define WAIT_VM_BAR(n) asm volatile("s_waitcnt vmcnt(" #n ")\n\ts_barrier" ::: "memory")
#define SCHED() __builtin_amdgcn_sched_barrier(0)
template <int CTRL> DEVI float dpp_add(float v) {
  return v + __uint_as_float(__builtin_amdgcn_update_dpp(0u, __float_as_uint(v), CTRL, 0xf, 0xf, true));
}
DEVI float sum_x16(float v) {
  auto r = __builtin_amdgcn_permlane16_swap(__float_as_uint(v), __float_as_uint(v), false, false);
  return __uint_as_float(r[0]) + __uint_as_float(r[1]);
}
DEVI float sum_x32(float v) {
  auto r = __builtin_amdgcn_permlane32_swap(__float_as_uint(v), __float_as_uint(v), false, false);
  return __uint_as_float(r[0]) + __uint_as_float(r[1]);
}
DEVI float wave_sum(float v) {
  v = dpp_add<0xB1>(v); v = dpp_add<0x4E>(v); v = dpp_add<0x141>(v); v = dpp_add<0x140>(v);
  return sum_x32(sum_x16(v));
}
DEVI i64 f2fx(float v) {
  const float a = v * (FX_SCALE / 4294967296.0f), h = __builtin_floorf(a);
  return __builtin_bit_cast(i64, u32x2{(u32)((a - h) * 4294967296.0f), (u32)(int)h});
}
DEVI float sel4(int i, float a, float b, float c, float d) {
  asm volatile("" : "+v"(a), "+v"(b), "+v"(c), "+v"(d));
  return (i & 2) ? ((i & 1) ? d : c) : ((i & 1) ? b : a);
}
DEVI int st_pos(int t) { return ((t >> 8) * 4 + (t & 3)) * 1024 + ((t >> 2) & 63) * 16; }
DEVI float rcp(float x) { return __builtin_amdgcn_rcpf(x); }
DEVI float sqrt_(float x) { return __builtin_amdgcn_sqrtf(x); }

DEVI u32 warm_code(int slice, int lane) {
  unsigned long long pc, pe;
  asm volatile("s_getpc_b64 %0\n\ts_getpc_b64 s[90:91]\n\ts_add_u32 s90, s90, code_end_marker@rel32@lo+4\n\t"
               "s_addc_u32 s91, s91, code_end_marker@rel32@hi+12\n\ts_mov_b64 %1, s[90:91]"
               : "=&s"(pc), "=s"(pe) :: "scc", "s90", "s91");
  const u32 off = min((u32)slice * 512u, (u32)(pe - pc) - 512u) + (lane & 7) * 64;
  u32 junk;
  asm volatile("s_nop 4\n\tglobal_load_dword %0, %1, %2" : "=v"(junk) : "v"(off), "s"(pc) : "memory");
  return junk;
}

DEVI int cidx(int l) {
  const int hb = (int)(pack_bf((float)l, 0.f) & 0xffffu) - (0x4300 - 128);
  return l < 128 ? l : hb;
}
struct CM { f32x2 e, f; };
DEVI f32x2 cmul(f32x2 a, const CM& m) { return a[1] * m.f + a[0] * m.e; }
DEVI CM cexp_(float a2, float br, float p) {
  const float e = __builtin_amdgcn_exp2f(a2 * p), f = __builtin_amdgcn_fractf(br * p);
  const float re = e * __builtin_amdgcn_cosf(f), im = e * __builtin_amdgcn_sinf(f);
  CM r; r.e = f32x2{re, im}; r.f = f32x2{-im, re};
  return r;
}
DEVI void st16(rsrc_t rs, int off, u32x4 v) { __builtin_amdgcn_raw_buffer_store_b128(v, rs, off, 0, SC1); }

DEVI void xpose(int b, const u32x4 (&v)[8], rsrc_t rs, char* smem) {
  const int t = threadIdx.x, lane = t & 63, w = t >> 6;
  constexpr int RS = 1024 + 32;
#pragma unroll
  for (int r = 0; r < 8; ++r) *(u32x4*)(smem + (w + 4 * r) * RS + lane * 16) = v[r];
  __syncthreads();
  const int g = lane >> 4, q = (lane & 15) >> 2, p = lane & 3;
#pragma unroll
  for (int cf = 0; cf < 8; ++cf) {
    const int c0 = (w * 8 + cf) * 16;
    const char* pa = smem + (8 * g + q) * RS + c0 * 2 + 8 * p;
    const u32x4 o = cat2(tr_read(pa), tr_read(pa + 4 * RS));
    st16(rs, WS_X + b * 32768 + (c0 + (lane & 15)) * 64 + g * 16, o);
  }
}
DEVI void row_stats(int b, const u32x4 (&v)[8], rsrc_t rs) {
  const int t = threadIdx.x, lane = t & 63, w = t >> 6;
  const int t0 = b * 32;
  float a[16];
#pragma unroll
  for (int r = 0; r < 8; ++r) {
    float s = 0.f, ss = 0.f;
#pragma unroll
    for (int j = 0; j < 4; ++j) { s = dot2bf(v[r][j], 0x3f803f80u, s); ss = dot2bf(v[r][j], v[r][j], ss); }
    a[2 * r] = s; a[2 * r + 1] = ss;
  }
  const bool o1 = lane & 1, o2 = lane & 2;
#pragma unroll
  for (int k = 0; k < 8; ++k) {
    const float keep = o1 ? a[2 * k + 1] : a[2 * k], send = o1 ? a[2 * k] : a[2 * k + 1];
    a[k] = keep + __uint_as_float(__builtin_amdgcn_update_dpp(0u, __float_as_uint(send), 0xB1, 0xf, 0xf, true));
  }
#pragma unroll
  for (int k = 0; k < 4; ++k) {
    const float keep = o2 ? a[2 * k + 1] : a[2 * k], send = o2 ? a[2 * k] : a[2 * k + 1];
    a[k] = keep + __uint_as_float(__builtin_amdgcn_update_dpp(0u, __float_as_uint(send), 0x4E, 0xf, 0xf, true));
  }
#pragma unroll
  for (int k = 0; k < 4; ++k) a[k] = sum_x32(sum_x16(dpp_add<0x128>(dpp_add<0x124>(a[k]))));
  const i64 sv = f2fx(sel4(lane >> 2, a[0], a[1], a[2], a[3]));
  if (lane < 16)
    __builtin_amdgcn_raw_buffer_store_b64(__builtin_bit_cast(u32x2, sv), rs, st_pos(t0 + w) + lane * 8, WS_ST, SC1);
  if (t >= 96 && t < 128) st16(rs, WS_ST + 65536 + b * 512 + (t - 96) * 16, u32x4{0, 0, 0, 0});
  if (t < 80) st16(rs, WS_CT + b * 1280 + t * 16, u32x4{0, 0, 0, 0});
}

DEVI void pack_w1(int ks, int cg, const u32x4 (&v)[8], rsrc_t rs, char* smem) {
  const int t = threadIdx.x, lane = t & 63, w = t >> 6;
  constexpr int RS = 512 + 32;
#pragma unroll
  for (int it = 0; it < 4; ++it) *(u32x4*)(smem + (it * 8 + (t >> 5)) * RS + (t & 31) * 16) = v[it];
  __syncthreads();
  const int g = lane >> 4, q = (lane & 15) >> 2, p = lane & 3;
  float cs[4];
#pragma unroll
  for (int h = 0; h < 4; ++h) {
    const char* a = smem + (8 * g + q) * RS + (w * 4 + h) * 32 + 8 * p;
    const u32x4 o = cat2(tr_read(a), tr_read(a + 4 * RS));
    st16(rs, WS_W1P + (((cg * 16 + w * 4 + h) * 16 + ks) * 64 + lane) * 16, o);
    float s = 0.f;
#pragma unroll
    for (int j = 0; j < 4; ++j) s = dot2bf(o[j], 0x3f803f80u, s);
    cs[h] = sum_x32(sum_x16(s));
  }
  __builtin_amdgcn_raw_buffer_store_b32(__float_as_uint(sel4(g, cs[0], cs[1], cs[2], cs[3])), rs, (ks * FF + cg * 256 + t) * 4, WS_S1, SC1);
  if (ks == 0 && cg == 0) st16(rs, WS_ONE + t * 16, u32x4{0x3f803f80u, 0x3f803f80u, 0x3f803f80u, 0x3f803f80u});
}

constexpr int W2_RS = 256 + 32;
DEVI void pack_w2_put(const u32x4 (&v)[2], char* smem) {
  const int t = threadIdx.x;
#pragma unroll
  for (int r = 0; r < 2; ++r) *(u32x4*)(smem + (r * 16 + (t >> 4)) * W2_RS + (t & 15) * 16) = v[r];
}
DEVI void pack_w2_out(int b, rsrc_t rs, const char* smem) {
  const int t = threadIdx.x, lane = t & 63, w = t >> 6;
  const int g = lane >> 4, q = (lane & 15) >> 2, p = lane & 3;
#pragma unroll
  for (int cf = 0; cf < 2; ++cf) {
    const char* a = smem + (4 * g + q) * W2_RS + (w * 2 + cf) * 32 + 8 * p;
    const u32x4 o = cat2(tr_read(a), tr_read(a + 16 * W2_RS));
    st16(rs, b < 224 ? WS_W2P + ((((b & 3) * 8 + w * 2 + cf) * 56 + (b >> 2)) * 64 + lane) * 16 : 0x40000000, o);
  }
}

constexpr int L_KC = 4160;
constexpr int L_U = 4 * L_KC, L_UC = 4608;
constexpr int L_XA = 26112, L_XB = L_XA + 65536;
constexpr int L_MISC = L_XB + 65536;
constexpr int L_CST = L_MISC + 1024;
constexpr int L_SIG = L_CST + 4 * 256;
constexpr int MAIN_LDS = L_SIG + 256;
constexpr int L_RED = L_XB;

DEVI u32x4 ld128(rsrc_t rs, int v, int s) { return __builtin_amdgcn_raw_buffer_load_b128(rs, v, s, SC1); }
DEVI u32x2 ld64(rsrc_t rs, int v, int s) { return __builtin_amdgcn_raw_buffer_load_b64(rs, v, s, SC1); }
DEVI void st128(rsrc_t rs, int v, int s, u32x4 d) { __builtin_amdgcn_raw_buffer_store_b128(d, rs, v, s, SC1); }
DEVI void st64(rsrc_t rs, int v, int s, u32x2 d) { __builtin_amdgcn_raw_buffer_store_b64(d, rs, v, s, SC1); }
template <int OFF> DEVI u32x4 ld128a(rsrc_t rs, int v, int s) {
  u32x4 r;
  asm volatile("buffer_load_dwordx4 %0, %1, %2, %3 offen offset:%4 sc1" : "=a"(r) : "v"(v), "s"(rs), "s"(s), "i"(OFF) : "memory");
  return r;
}
template <int OFF> DEVI u32x4 ld128v(rsrc_t rs, int v, int s) {
  u32x4 r;
  asm volatile("buffer_load_dwordx4 %0, %1, %2, %3 offen offset:%4 sc1" : "=v"(r) : "v"(v), "s"(rs), "s"(s), "i"(OFF) : "memory");
  return r;
}
template <int OFF> DEVI void dma1k(rsrc_t rs, u32 lds_dst, int v, int soff) {
  asm volatile("s_mov_b32 m0, %0\n\ts_nop 0\n\tbuffer_load_dwordx4 %1, %2, %3 offen offset:%4 sc1 lds"
               :: "s"(lds_dst), "v"(v), "s"(rs), "s"(soff), "i"(OFF) : "memory");
}
#define MMA(NAME, OP, CA, CB)                                                                                   \
  template <bool Z> DEVI void NAME(f32x4& d, const u32x4& a, const u32x4& b) {                                  \
    if (Z) asm volatile(OP " %0, %1, %2, 0" : "=&v"(d) : CA(a), CB(b));                                         \
    else asm volatile(OP " %0, %1, %2, %0" : "+v"(d) : CA(a), CB(b));                                           \
  }
MMA(mma_vv, "v_mfma_f32_16x16x32_bf16", "v", "v")
MMA(mma_av, "v_mfma_f32_16x16x32_bf16", "a", "v")
MMA(mma_va, "v_mfma_f32_16x16x32_bf16", "v", "a")
MMA(mma_hh, "v_mfma_f32_16x16x32_f16", "v", "v")
#define MFMA_DONE() do { SCHED(); asm volatile("s_nop 7" ::: "memory"); SCHED(); } while (0)

struct Ctx {
  rsrc_t rs; gu32* ct; char* smem; char* ws; u16* out;
  int lane0, w, b, P, sub, ct_, ff0, hks, eo, f1o, f1s;
  float dsk0, dsk1, ks0, ks1, bb2, cs1;
  u32 tag;
};
DEVI int fresh_lane(const Ctx& c) { int l = c.lane0; asm volatile("" : "+v"(l)); return l; }
DEVI u32 sig_begin(const Ctx& c, int lane) {
  u32 old;
  asm volatile("ds_add_rtn_u32 %0, %1, %2" : "=v"(old) : "v"(lds_addr(c.smem) + L_SIG + lane * 4), "v"(1u) : "memory");
  return old;
}
template <bool ALL, bool BACK = false> DEVI void sig_end(const Ctx& c, int lane, u32 old, int line) {
  if (BACK) asm volatile("" : "+v"(old) :: "memory");
  else asm volatile("s_waitcnt lgkmcnt(0)" : "+v"(old) :: "memory");
  const bool last = (__builtin_amdgcn_readfirstlane(old) & 3u) == 3u;
  const int v = (last && lane < (ALL ? 8 : 1)) ? (ALL ? lane * 2048 : 0) : 0x40000000;
  asm volatile("s_nop 4\n\tbuffer_atomic_add %0, %1, %2, %3 offen" :: "v"(1u), "v"(v), "s"(c.rs),
               "s"(WS_CT + (line + (ALL ? (c.b >> 3) & 7 : 0)) * 256) : "memory");
}
template <bool ALL> DEVI void poll(const Ctx& c, int lane, int line) {
  const int v = ALL ? (lane & 7) * 256 : 0, so = WS_CT + (line + (ALL ? (c.b & 7) * 8 : 0)) * 256;
  asm volatile("s_nop 4\n\tbuffer_load_dword v254, %0, %1, %2 offen sc1" :: "v"(v), "s"(c.rs), "s"(so) : "memory", "v254");
}
template <int YOUNGER> DEVI bool got(u32 want) {
  unsigned long long m;
  asm volatile("s_waitcnt vmcnt(%2)\n\tv_cmp_le_u32_e64 %0, %1, v254" : "=s"(m) : "s"(want), "i"(YOUNGER) : "memory");
  return m == ~0ull;
}
template <bool ALL> DEVI void wait_ge(const Ctx& c, int lane, int line, u32 want) {
  int sp = 0;
#pragma nounroll
  do {
    poll<ALL>(c, lane, line);
    if (++sp > SPIN_CAP) { __hip_atomic_store(c.ct + CT_TMO * 64, 1u, RLX_AGENT); break; }
  } while (!got<0>(want));
}

DEVI void poll0(const Ctx& c, int lane, int set) {
  asm volatile("s_nop 4\n\tbuffer_load_dwordx2 v[254:255], %0, %1, %2 offen sc1" :: "v"((lane & 7) * 256 + (lane >> 3) * 8), "s"(c.rs),
               "s"(set + (c.b & 7) * 2048) : "memory", "v254", "v255");
}
template <int YOUNGER> DEVI bool got0(u32 tag) {
  unsigned long long m, m2;
  asm volatile("s_waitcnt vmcnt(%3)\n\tv_cmp_eq_u32_e64 %0, %2, v254\n\tv_cmp_eq_u32_e64 %1, %2, v255"
               : "=&s"(m), "=s"(m2) : "s"(tag), "i"(YOUNGER) : "memory");
  return (m & m2) == ~0ull;
}
DEVI void wait0(const Ctx& c, int lane, int set) {
  int sp = 0;
#pragma nounroll
  do {
    poll0(c, lane, set);
    if (++sp > SPIN_CAP) { __hip_atomic_store(c.ct + CT_TMO * 64, 1u, RLX_AGENT); break; }
  } while (!got0<0>(c.tag));
}

struct CIn { u32x2 x0, x1; u32x4 s[4]; };
DEVI void conv_load(const Ctx& c, int lane, CIn& in, int xoff, int stoff, int h) {
  const int sx = xoff + (32 * h + 8 * c.w) * 32768 + c.b * 128;
  const int vx = (lane >> 3) * 32768 + (lane & 7) * 8;
  const int ss = stoff + (1024 * h + 256 * c.w) * 16;
#pragma unroll
  for (int j = 0; j < 4; ++j) in.s[j] = ld128(c.rs, lane * 16 + j * 1024, ss);
  in.x0 = ld64(c.rs, vx, sx);
  in.x1 = ld64(c.rs, vx + 64, sx);
}
DEVI void conv_load_u(const Ctx& c, int lane, CIn& in, int xoff, int stoff, int h) {
  const int sx = xoff + (32 * h + 8 * c.w) * 32768 + c.b * 128;
  const int vx = (lane >> 3) * 32768 + (lane & 7) * 8;
  const int ss = stoff + (1024 * h + 256 * c.w) * 16;
  asm volatile("s_nop 4\n\t"
               "buffer_load_dwordx4 %0, %6, %8, %9 offen sc1\n\tbuffer_load_dwordx4 %1, %6, %8, %9 offen offset:1024 sc1\n\t"
               "buffer_load_dwordx4 %2, %6, %8, %9 offen offset:2048 sc1\n\tbuffer_load_dwordx4 %3, %6, %8, %9 offen offset:3072 sc1\n\t"
               "buffer_load_dwordx2 %4, %7, %8, %10 offen sc1\n\tbuffer_load_dwordx2 %5, %7, %8, %10 offen offset:64 sc1"
               : "=&v"(in.s[0]), "=&v"(in.s[1]), "=&v"(in.s[2]), "=&v"(in.s[3]), "=&v"(in.x0), "=&v"(in.x1)
               : "v"(lane * 16), "v"(vx), "s"(c.rs), "s"(ss), "s"(sx) : "memory");
}
DEVI void conv_landed(CIn& in) {
  asm volatile("s_waitcnt vmcnt(2)" : "+v"(in.s[0]), "+v"(in.s[1]), "+v"(in.s[2]), "+v"(in.s[3]), "+v"(in.x0), "+v"(in.x1) :: "memory");
}
DEVI void conv_ln(const Ctx& c, int lane, const CIn& in, int h, f32x4& base0, f32x4& base1) {
  constexpr float CH = 4294967296.0f * FX_INV / DM, CL = FX_INV / DM;
  f32x4 m, r;
#pragma unroll
  for (int j = 0; j < 4; ++j) {
    m[j] = fmaf((float)(int)in.s[j][1], CH, (float)in.s[j][0] * CL);
    r[j] = fmaf((float)(int)in.s[j][3], CH, fmaf((float)in.s[j][2], CL, LN_EPS));
  }
#pragma unroll
  for (int j = 0; j < 4; ++j) r[j] = __builtin_amdgcn_rsqf(fmaxf(fmaf(-m[j], m[j], r[j]), LN_EPS));
  const f32x4 rr = r, mr = -m * r;
  const f32x4 x0 = {lo_bf(in.x0[0]), hi_bf(in.x0[0]), lo_bf(in.x0[1]), hi_bf(in.x0[1])};
  const f32x4 x1 = {lo_bf(in.x1[0]), hi_bf(in.x1[0]), lo_bf(in.x1[1]), hi_bf(in.x1[1])};
  const f32x4 u0 = x0 * rr + mr, u1 = x1 * rr + mr;
  char* up = c.smem + L_U + 512 + (1024 * h + 256 * c.w + 4 * lane) * 2;
  *(u32x2*)up = u32x2{pack_h(u0[0], u0[1]), pack_h(u0[2], u0[3])};
  *(u32x2*)(up + L_UC) = u32x2{pack_h(u1[0], u1[1]), pack_h(u1[2], u1[3])};
  base0 = u0 * c.dsk0 + x0; base1 = u1 * c.dsk1 + x1;
}
struct KB { const char* p[4]; };
template <int MM, int LAM> DEVI u32x4 conv_k1(const KB& kb) {
  const u32* kp = (const u32*)(kb.p[(7 - LAM) >> 1] + 64 * (3 - MM) + 512 * ((7 - LAM) & 1));
  return u32x4{kp[0], kp[1], kp[2], kp[3]};
}
template <int MM, int TB> DEVI u32x4 conv_u1(const char* ub) { return *(const u32x4*)(ub + 64 * (3 - MM) + 512 * TB); }
template <int LO> DEVI void conv_ldk(const KB& kb, u32x4 (&af)[4]) {
  sfor<0, 4>([&](auto L) { af[L.v] = conv_k1<0, LO + L.v>(kb); });
}
constexpr int conv_rank(int DT, int LO, int gr, int lt) {
  int k = 0;
  for (int x = 0; x < lt; ++x) { const int tb = DT + (x & 3) - (LO + 4 * gr + (x >> 2)); k += (tb >= 0 && tb < 4); }
  return k;
}
template <int DT, int TB0, bool ZERO, class F>
DEVI void conv_mma(const char* ub, const KB& kb, f32x4 (&acc)[4], const u32x4 (&af0)[4], F&& hook) {
  constexpr int NG = DT ? 2 : 1, LO = DT ? 1 : 0;
  u32x4 af[2][4], bf[2][4];
  sfor<0, 4>([&](auto L) { af[0][L.v] = af0[L.v]; });
  sfor<0, 4>([&](auto T) { constexpr int tb = DT ? 3 - T.v : T.v; bf[0][tb] = conv_u1<0, TB0 + tb>(ub); });
  SCHED();
  sfor<0, 4 * NG>([&](auto ST) {
    constexpr int st = ST.v, mm = st / NG, gr = st % NG;
    constexpr bool more = st + 1 < 4 * NG;
    constexpr int mn = (st + 1) / NG, gn = (st + 1) % NG;
    constexpr bool nu = more && gr == NG - 1;
    sfor<0, 16>([&](auto LT) {
      constexpr int l = LT.v >> 2, T = LT.v & 3, tb = DT + T - (LO + 4 * gr + l);
      if constexpr (tb >= 0 && tb < 4) {
        constexpr int k = conv_rank(DT, LO, gr, LT.v);
        constexpr bool first = ZERO && st == 0 && (DT ? (l == T) : (l == 0));
        mma_hh<first>(acc[T], af[st & 1][l], bf[mm & 1][tb]);
        sfor<(gr == 1 ? 2 * k : k), (gr == 1 ? 2 * k + 2 : k + 1)>([&](auto J) {
          constexpr int j = J.v;
          if constexpr (nu && j < 8) {
            if constexpr (DT != 0) {
              if constexpr (j & 1) bf[mn & 1][3 - (j >> 1)] = conv_u1<mn, TB0 + 3 - (j >> 1)>(ub);
              else af[(st + 1) & 1][j >> 1] = conv_k1<mn, LO + (j >> 1)>(kb);
            } else {
              if constexpr (j >= 1 && j <= 4) bf[mn & 1][j - 1] = conv_u1<mn, TB0 + j - 1>(ub);
              else af[(st + 1) & 1][j ? j - 4 : 0] = conv_k1<mn, LO + (j ? j - 4 : 0)>(kb);
            }
          } else if constexpr (more && !nu && j < 3) af[(st + 1) & 1][j] = conv_k1<mn, LO + 4 * gn + j>(kb);
        });
        SCHED();
        hook(IC<mm * (DT ? 16 : 10) + gr * 10 + k>{});
      }
    });
  });
}
DEVI void conv_part(const Ctx& c, int lane, const f32x4 (&acc)[4]) {
  char* red = c.smem + L_RED;
#pragma unroll
  for (int T = 0; T < 4; ++T) *(f32x4*)(red + (c.w * 4 + T) * 1024 + (lane & 15) * 64 + (lane >> 4) * 16) = acc[T];
}
DEVI void conv_out(const Ctx& c, int lane, const f32x4& base0, const f32x4& base1, int xoff, int h) {
  const int sx = xoff + (16 * h + 4 * c.w) * 65536 + c.b * 256;
  const int vx = (lane >> 4) * 65536 + (lane & 15) * 8;
  const char* p = c.smem + L_RED + c.w * 1024 + lane * 16;
  const f32x4 a0 = *(const f32x4*)p, b0 = *(const f32x4*)(p + 4096), a1 = *(const f32x4*)(p + 8192), b1 = *(const f32x4*)(p + 12288);
  SCHED();
  const f32x4 y0 = (a0 + b0) * c.ks0 + base0, y1 = (a1 + b1) * c.ks1 + base1;
  st64(c.rs, vx, sx, u32x2{pack_bf(y0[0], y0[1]), pack_bf(y0[2], y0[3])});
  st64(c.rs, vx + 128, sx, u32x2{pack_bf(y1[0], y1[1]), pack_bf(y1[2], y1[3])});
}

template <int PC> DEVI void g1_dma1(const Ctx& c, int ve, int vo, int ldsx, int soff) {
  dma1k<(PC & 3) * 1024>(c.rs, lds_addr(c.smem) + ldsx + (4 * (PC >> 2) + c.w) * 4096, (PC & 1) ? vo : ve, soff + (4 * (PC >> 2) + c.w) * 4096);
}
template <bool ALL> DEVI void sig_bar(const Ctx& c, int lane, int line) {
  const int v = (c.w == 0 && lane < (ALL ? 8 : 1)) ? (ALL ? lane * 2048 : 0) : 0x40000000;
  asm volatile("s_nop 4\n\tbuffer_atomic_add %0, %1, %2, %3 offen" :: "v"(1u), "v"(v), "s"(c.rs),
               "s"(WS_CT + (line + (ALL ? (c.b >> 3) & 7 : 0)) * 256) : "memory");
}
template <class F, class G>
DEVI void g1_mma(const char* const (&xb)[4], const u32x4 (&w1f)[2][16], f32x4 (&acc)[2][4], f32x4& sa, F&& mid, G&& hook) {
  u32x2 xf[2][4][2];
#pragma unroll
  for (int i = 0; i < 4; ++i) { xf[0][i][0] = tr_read(xb[i]); xf[0][i][1] = tr_read(xb[i] + 512); }
  SCHED();
  sfor<0, 16>([&](auto KS) {
    constexpr int ks = KS.v;
    mid(KS);
    sfor<0, 4>([&](auto I) {
      constexpr int i = I.v;
      const u32x4 x = cat2(xf[ks & 1][i][0], xf[ks & 1][i][1]);
      mma_vv<ks == 0>(acc[0][i], w1f[0][ks], x);
      if constexpr (ks < 15) xf[(ks + 1) & 1][i][0] = tr_read(xb[i] + (ks + 1) * 4096);
      SCHED();
      if constexpr (ks < 8) mma_av<ks == 0>(acc[1][i], w1f[1][ks], x); else mma_vv<false>(acc[1][i], w1f[1][ks], x);
      if constexpr (ks < 15) xf[(ks + 1) & 1][i][1] = tr_read(xb[i] + (ks + 1) * 4096 + 512);
      SCHED();
      if constexpr (i == 0) { mma_vv<ks == 0>(sa, x, x); hook(IC<2 * ks>{}); SCHED(); }
      if constexpr (i == 2) { hook(IC<2 * ks + 1>{}); SCHED(); }
    });
  });
  MFMA_DONE();
}
DEVI void st_h(const Ctx& c, int v, int so0, int so1, u32x4 a, u32x4 b) {
  const u32x2 al = {a[0], a[1]}, bl = {b[0], b[1]};
  asm volatile("s_cmp_eq_u32 %0, 3\n\ts_nop 3\n\ts_cbranch_scc1 1f\n\t"
               "buffer_store_dwordx4 %5, %1, %2, %3 offen sc1\n\tbuffer_store_dwordx4 %6, %1, %2, %4 offen sc1\n\ts_branch 2f\n"
               "1:\n\tbuffer_store_dwordx2 %7, %1, %2, %3 offen sc1\n\tbuffer_store_dwordx2 %8, %1, %2, %4 offen sc1\n"
               "2:\n\ts_nop 1"
               :: "s"(c.w), "v"(v), "s"(c.rs), "s"(so0), "s"(so1), "v"(a), "v"(b), "v"(al), "v"(bl) : "memory", "scc");
}
template <class F>
DEVI void g1_epi(const Ctx& c, int lane, f32x4 (&acc)[2][4], const f32x4& sa, int hoff, F&& hook) {
  const int g = lane >> 4, cc = lane & 15, q = cc >> 2, p = lane & 3, w = c.w;
  float* sums = (float*)(c.smem + L_MISC);
  {
    const float d = sel4(p, sa[0], sa[1], sa[2], sa[3]);
    sums[q == g ? (w * 16 + cc) * 2 + 1 : 128 + lane] = d;
    const float r = sel4(g, acc[1][1][0], acc[1][2][0], acc[1][3][0], acc[1][0][0]);
    sums[w == 3 ? lane * 2 : 192 + lane] = r;
  }
  f32x4 s1v[2], b1v[2];
#pragma unroll
  for (int j = 0; j < 2; ++j) {
    s1v[j] = *(const f32x4*)(c.smem + L_CST + w * 256 + j * 128 + g * 16);
    b1v[j] = *(const f32x4*)(c.smem + L_CST + w * 256 + j * 128 + 64 + g * 16);
  }
  const int vh = w < 3 ? lane * 16 : lane * 8 + c.eo * 512;
  __syncthreads();
  f32x2 k[4];
#pragma unroll
  for (int i = 0; i < 4; ++i) k[i] = *(const f32x2*)(sums + (((i + w) & 3) * 16 + cc) * 2);
#pragma unroll
  for (int i = 0; i < 4; ++i) {
    const float m = k[i][0] * (1.0f / DM);
    const float ve = fmaxf(fmaf(-m, m, fmaf(k[i][1], 1.0f / DM, LN_EPS)), LN_EPS);
    const float r = __builtin_amdgcn_rsqf(fmaf(ve, 1.0f + LN_EPS, -LN_EPS));
    k[i] = f32x2{r, -m * r};
  }
  SCHED();
  hook(IC<-1>{});
  SCHED();
  sfor<0, 2>([&](auto IH) {
    constexpr int ih = IH.v;
    f32x4 x[4], e[4];
#define STAGE(n) do { SCHED(); hook(IC<10 * ih + n>{}); SCHED(); } while (0)
#pragma unroll
    for (int n = 0; n < 4; ++n) x[n] = k[2 * ih + (n >> 1)][1] * s1v[n & 1] + b1v[n & 1];
#pragma unroll
    for (int n = 0; n < 4; ++n) x[n] = acc[n & 1][2 * ih + (n >> 1)] * k[2 * ih + (n >> 1)][0] + x[n];
    STAGE(0);
#pragma unroll
    for (int n = 0; n < 4; ++n) e[n] = x[n] * x[n];
#pragma unroll
    for (int n = 0; n < 4; ++n) e[n] = e[n] * (-0.0713548162726f * 1.44269504088896f) + (-1.59576912160573f * 1.44269504088896f);
    STAGE(1);
#pragma unroll
    for (int n = 0; n < 4; ++n) e[n] = e[n] * x[n];
    STAGE(2);
#pragma unroll
    for (int r = 0; r < 4; ++r) { e[0][r] = __builtin_amdgcn_exp2f(e[0][r]); e[1][r] = __builtin_amdgcn_exp2f(e[1][r]); }
    STAGE(3);
#pragma unroll
    for (int r = 0; r < 4; ++r) { e[2][r] = __builtin_amdgcn_exp2f(e[2][r]); e[3][r] = __builtin_amdgcn_exp2f(e[3][r]); }
    STAGE(4);
#pragma unroll
    for (int n = 0; n < 4; ++n) e[n] = e[n] + 1.0f;
    STAGE(5);
#pragma unroll
    for (int r = 0; r < 4; ++r) { e[0][r] = rcp(e[0][r]); e[1][r] = rcp(e[1][r]); }
    STAGE(6);
#pragma unroll
    for (int r = 0; r < 4; ++r) { e[2][r] = rcp(e[2][r]); e[3][r] = rcp(e[3][r]); }
    STAGE(7);
#pragma unroll
    for (int n = 0; n < 4; ++n) x[n] = x[n] * e[n];
    STAGE(8);
    u32x4 hv[2];
#pragma unroll
    for (int n = 0; n < 4; ++n) { hv[n >> 1][2 * (n & 1)] = pack_bf(x[n][0], x[n][1]); hv[n >> 1][2 * (n & 1) + 1] = pack_bf(x[n][2], x[n][3]); }
    STAGE(9);
    const int so0 = hoff + ((2 * ih + w) & 3) * 56 * 1024, so1 = hoff + ((2 * ih + 1 + w) & 3) * 56 * 1024;
    st_h(c, vh, so0, so1, hv[0], hv[1]);
    SCHED();
#undef STAGE
  });
}

template <int N> DEVI void ld_w1(const Ctx& c, int v, u32x4 (&w1f)[2][16]) {
  constexpr int ks = N >> 1, j = N & 1;
  const int so = (j ? c.f1o + (ks >> 2) * c.f1s : WS_W1P + c.ff0 * 16384 + (ks >> 2) * 4096);
  if constexpr (j && ks < 8) w1f[j][ks] = ld128a<(ks & 3) * 1024>(c.rs, v, so);
  else w1f[j][ks] = ld128v<(ks & 3) * 1024>(c.rs, v, so);
}
template <int N> DEVI void ld_w2(const Ctx& c, int v, u32x4 (&w2f)[14][4]) {
  constexpr int s = N >> 2, j = N & 3;
  w2f[s][j] = ld128a<(s & 3) * 1024>(c.rs, v, WS_W2P + ((c.ct_ * 4 + ((c.w + j) & 3)) * 56 + 14 * c.w + (s & ~3)) * 1024);
}
template <int N> DEVI void g2_ldh1(const Ctx& c, int v16, u32x4 (&hf)[14][2], int ho) {
  constexpr int s = N % 14, i = N / 14;
  if constexpr (s % 7 == 6)
    hf[s][i] = cat2(ld64(c.rs, (v16 >> 1) + (s & 3) * 1024, ho + i * 56 * 1024 + (s >> 2) * 4096),
                    ld64(c.rs, (v16 >> 1) + (s & 3) * 1024 + 512, ho + i * 56 * 1024 + (s >> 2) * 4096));
  else hf[s][i] = ld128(c.rs, v16 + (s & 3) * 1024, ho + i * 56 * 1024 + (s >> 2) * 4096);
}
template <int PRE, int RATE, class F>
DEVI void g2_mma(const Ctx& c, int lane, u32x4 (&hf)[14][2], const u32x4 (&w2f)[14][4], f32x4 (&acc)[2][4], int ho, int h, F&& mid) {
  const int v16 = lane * 16;
  char* red = c.smem + L_XA + h * 32768;
  sfor<0, 28>([&](auto N) {
    constexpr int i = N.v / 14, s = N.v % 14;
    sfor<0, 4>([&](auto J) {
      constexpr int j = J.v;
      mma_va<s == 0>(acc[i][j], hf[s][i], w2f[s][j]);
      constexpr int per = 4 / RATE, n = PRE + RATE * N.v + j / per;
      if constexpr (j % per == per - 1 && n < 28) { g2_ldh1<n>(c, v16, hf, ho); SCHED(); }
      if constexpr (N.v == 17 && j > 0) { *(f32x4*)(red + ((((c.w + j) & 3) * 3 + j - 1) * 2) * 1024 + v16) = acc[0][j]; SCHED(); }
    });
    SCHED();
    mid(N);
  });
  MFMA_DONE();
}
template <bool LAST, class F>
DEVI void g2_epi(const Ctx& c, int lane, const f32x4 (&acc)[2][4], const u32x2 (&xr)[2], int h, int xdst, F&& mid) {
  const int g = lane >> 4, cc = lane & 15, q = cc >> 2, p = lane & 3, w = c.w, t = w * 64 + lane;
  char* red = c.smem + L_XA + h * 32768;
#pragma unroll
  for (int jj = 1; jj < 4; ++jj) *(f32x4*)(red + ((((w + jj) & 3) * 3 + jj - 1) * 2 + 1) * 1024 + lane * 16) = acc[1][jj];
  __syncthreads();
  char* xt = red + 24576;
  f32x4 pr[2][3];
#pragma unroll
  for (int i = 0; i < 2; ++i)
#pragma unroll
    for (int rk = 0; rk < 3; ++rk) pr[i][rk] = *(const f32x4*)(red + ((w * 3 + rk) * 2 + i) * 1024 + lane * 16);
  mid(0);
#pragma unroll
  for (int i = 0; i < 2; ++i) {
    const f32x4 xv = {lo_bf(xr[i][0]), hi_bf(xr[i][0]), lo_bf(xr[i][1]), hi_bf(xr[i][1])};
    const f32x4 o = ((acc[i][0] + (xv + c.bb2)) + pr[i][0]) + (pr[i][1] + pr[i][2]);
    const u32x2 r = {pack_bf(o[0], o[1]), pack_bf(o[2], o[3])};
    *(u32x2*)(xt + (w * 16 + cc) * 64 + ((i ^ (cc >> 3)) * 32) + g * 8) = r;
  }
  __syncthreads();
  const char* xq = xt + ((w >> 1) * 32 + 8 * g + q) * 64 + (((w & 1) ^ (g & 1)) * 32) + 8 * p;
  const u32x4 f0 = cat2(tr_read(xq), tr_read(xq + 256));
  const int tok = (16 * h + c.P) * 64 + c.sub * 32 + (w & 1) * 16 + cc;
  if (LAST) {
    *(u32x4*)(c.out + (size_t)tok * DM + c.ct_ * 64 + (w >> 1) * 32 + 8 * g) = f0;
  } else {
    const int row = t >> 2;
    const u32x4 v = *(const u32x4*)(xt + row * 64 + (((t & 3) ^ ((row >> 2) & 2)) * 16));
    st128(c.rs, c.ct_ * 4096 + t * 16, xdst, v);
    const char* xo = xq + ((w & 2) ? -2048 : 2048);
    const u32x4 f1 = cat2(tr_read(xo), tr_read(xo + 256));
    float s = 0.f, ss = 0.f, s2 = 0.f, ss2 = 0.f;
#pragma unroll
    for (int j = 0; j < 4; ++j) {
      s = dot2bf(f0[j], 0x3f803f80u, s); ss = dot2bf(f0[j], f0[j], ss);
      s2 = dot2bf(f1[j], 0x3f803f80u, s2); ss2 = dot2bf(f1[j], f1[j], ss2);
    }
    s = sum_x32(sum_x16(s + s2)); ss = sum_x32(sum_x16(ss + ss2));
    asm volatile("s_nop 4\n\tbuffer_atomic_add_x2 %0, %1, %2, %3 offen" :: "v"(f2fx((g & 1) ? ss : s)),
                 "v"(t < 128 && lane < 32 ? st_pos(tok) + g * 8 : 0x40000000), "s"(c.rs), "s"(WS_ST + 65536) : "memory");
  }
}

template <int LAYER>
DEVI void layer_body(const Ctx& c, u32x4 (&w1f)[2][16], u32x4 (&w2f)[14][4], CIn& inA, CIn& inB) {
  constexpr int xin = WS_X + LAYER * 4 * MB, xmid = xin + 2 * MB, xnext = xin + 4 * MB;
  constexpr int hbuf = WS_H + LAYER * 8 * MB, stin = WS_ST + LAYER * 65536;
  constexpr u32 ep = LAYER + 1;
  const int w = c.w;
  char* smem = c.smem;
  auto nop = [](auto) {};
  {
    const int lane = fresh_lane(c);
    const int g = lane >> 4, cc = lane & 15, mu0 = (w & 1) * 4, v16 = lane * 16;
    const char* ub = smem + L_U + (w >> 1) * L_UC + 512 + 2 * (16 * cc - 16 + 8 * g) - 64 * (mu0 + 3);
    KB kb;
#pragma unroll
    for (int j = 0; j < 4; ++j) {
      u32 a = lds_addr(smem) + (w >> 1) * 2 * L_KC + ((cc & 1) ? 0 : L_KC) + 2 * (2031 - cc + 8 * g - ((cc & 1) ? 0 : 1)) - 64 * (mu0 + 3 + 56) + 1024 * j;
      asm volatile("" : "+v"(a));
      kb.p[j] = (const char*)(const LAS char*)(size_t)a;
    }
    f32x4 base0, base1;
    u32x4 a4[4];
    conv_ldk<0>(kb, a4);
    if (LAYER) { SCHED(); conv_landed(inA); }
    conv_ln(c, lane, inA, 0, base0, base1);
    if (!LAYER) poll0(c, lane, WS_F1);
    __syncthreads();
    f32x4 acc[4];
    u32 o = 0;
    conv_mma<0, 0, true>(ub, kb, acc, a4, [&](auto N) {
      constexpr int n = N.v;
      if constexpr (LAYER && n == 19) { WAIT_VM(0); o = sig_begin(c, lane); SCHED(); }
      if constexpr (LAYER && n == 30) { sig_end<true, true>(c, lane, o, CT_X + 64); SCHED(); }
    });
    MFMA_DONE();
    conv_part(c, lane, acc);
    conv_ldk<1>(kb, a4);
    __syncthreads();
    float s1p[8];
    if (!LAYER) {
      if (!got0<0>(c.tag)) wait0(c, lane, WS_F1);
      asm volatile("" :: "v"(inB.x1));
      const int so1 = (min(c.ff0 * 16 + (lane & 15) + (lane >> 5) * 16, FF - 1) + (lane & 16) * (FF / 2)) * 4;
#pragma unroll
      for (int j = 0; j < 8; ++j)
        asm volatile("s_nop 4\n\tbuffer_load_dword %0, %1, %2, %3 offen sc1" : "=v"(s1p[j]) : "v"(so1), "s"(c.rs), "s"(WS_S1 + j * FF * 4) : "memory");
    }
    conv_out(c, lane, base0, base1, xmid, 0);
    f32x4 acb[4];
    conv_mma<4, 0, true>(ub, kb, acb, a4, [&](auto N) {
      constexpr int n = N.v;
      if constexpr (!LAYER && n >= 48 && (n & 1)) { ld_w1<(n - 48) / 2>(c, v16, w1f); SCHED(); }
      if constexpr (n == 47) {
        WAIT_VM(0);
        o = sig_begin(c, lane);
        if (LAYER) poll<true>(c, lane, CT_X + 64);
        SCHED();
      }
      if constexpr (n == 58) { sig_end<true, true>(c, lane, o, CT_C); SCHED(); }
    });
    if (!LAYER) {
#pragma unroll
      for (int j = 0; j < 8; ++j) asm volatile("" : "+v"(s1p[j]));
      const float sv = sum_x16(((s1p[0] + s1p[1]) + (s1p[2] + s1p[3])) + ((s1p[4] + s1p[5]) + (s1p[6] + s1p[7])));
      ((float*)(smem + L_CST))[w * 64 + lane] = (lane & 16) ? c.cs1 : sv;
    }
    if (LAYER) {
      if (!got<1>(32u)) wait_ge<true>(c, lane, CT_X + 64, 32u);
      conv_load(c, lane, inB, xin, stin, 1);
    }
    conv_ldk<0>(kb, a4);
    if (!LAYER) sfor<8, 12>([&](auto N) { ld_w1<N.v>(c, v16, w1f); });
    conv_ln(c, lane, inB, 1, base0, base1);
    if (!LAYER) sfor<12, 14>([&](auto N) { ld_w1<N.v>(c, v16, w1f); });
    __syncthreads();
    conv_mma<0, 4, false>(ub, kb, acb, a4, [&](auto N) {
      constexpr int n = N.v;
      if constexpr (!LAYER && n % 3 == 1 && n < 36) { ld_w1<14 + n / 3>(c, v16, w1f); SCHED(); }
      if constexpr (n == 30) { poll<true>(c, lane, CT_C); SCHED(); }
    });
    MFMA_DONE();
    conv_part(c, lane, acb);
    __syncthreads();
    conv_out(c, lane, base0, base1, xmid, 1);
    if (!got<(LAYER ? 2 : 4)>(32u * ep)) wait_ge<true>(c, lane, CT_C, 32u * ep);
    const int row = lane >> 3, sw = ((row >> 1) & 1) << 1;
    const int ve = row * 128 + ((lane & 7) ^ sw) * 16, vo = row * 128 + ((lane & 7) ^ (sw | 4)) * 16;
    if (!LAYER && c.b == 0 && w == 0) __hip_atomic_store((gu32*)(c.ws + WS_GEN), c.tag, RLX_AGENT);
    sfor<0, 8>([&](auto N) { g1_dma1<N.v>(c, ve, vo, L_XA, xmid + c.P * 65536); });
    WAIT_VM(8);
    o = sig_begin(c, lane);
    sig_end<true>(c, lane, o, CT_C + 64);
  }
  u32x4 hf[14][2];
  u32x2 xr[2];
  {
    const int lane = fresh_lane(c);
    const int g = lane >> 4, cc = lane & 15, q = cc >> 2, p = lane & 3, v16 = lane * 16;
    const int sx = ((q >> 1) & 1) | ((g & 1) << 1);
    const char* xb[4];
#pragma unroll
    for (int i = 0; i < 4; ++i) xb[i] = smem + L_XA + (8 * g + q) * 128 + ((((i + w) & 3) ^ sx) * 32) + 8 * p;
    const int row = lane >> 3, sw = ((row >> 1) & 1) << 1;
    const int ve = row * 128 + ((lane & 7) ^ sw) * 16, vo = row * 128 + ((lane & 7) ^ (sw | 4)) * 16;
    sfor<8, 12>([&](auto N) { g1_dma1<N.v>(c, ve, vo, L_XA, xmid + c.P * 65536); });
    WAIT_VM_BAR(9);
    f32x4 acc[2][4], sa;
    g1_mma(xb, w1f, acc, sa, [&](auto KS) {
      constexpr int ks = KS.v;
      if constexpr (ks == 3) { if (LAYER) WAIT_VM_BAR(8); else WAIT_VM_BAR(10); SCHED(); }
      if constexpr (ks == 7) { if (LAYER) WAIT_VM_BAR(0); else WAIT_VM_BAR(10); SCHED(); }
      if constexpr (ks == 13) { if (!LAYER) WAIT_VM(16); poll<true>(c, lane, CT_C + 64); SCHED(); }
    }, [&](auto N) {
      constexpr int n = N.v;
      if constexpr (n < 4) g1_dma1<12 + n>(c, ve, vo, L_XA, xmid + c.P * 65536);
      if constexpr (!LAYER && n >= 4 && n < 10) ld_w1<22 + n>(c, v16, w1f);
      if constexpr (!LAYER && n >= 10) ld_w2<n - 10>(c, v16, w2f);
    });
    const int xo = xmid + (16 + c.P) * 65536;
    g1_epi(c, lane, acc, sa, hbuf + (c.P * 4 * 56 + c.hks) * 1024, [&](auto N) {
      constexpr int n = N.v;
      if constexpr (n == -1) { if (!got<(LAYER ? 0 : 6)>(32u * ep)) wait_ge<true>(c, lane, CT_C + 64, 32u * ep); }
      if constexpr (n >= 0 && n < 8) g1_dma1<n>(c, ve, vo, L_XB, xo);
      if constexpr (n >= 10 && n < 18) g1_dma1<n - 2>(c, ve, vo, L_XB, xo);
    });
  }
  {
    const int lane = fresh_lane(c);
    const int g = lane >> 4, cc = lane & 15, q = cc >> 2, p = lane & 3, v16 = lane * 16;
    const int sx = ((q >> 1) & 1) | ((g & 1) << 1);
    const char* xb[4];
#pragma unroll
    for (int i = 0; i < 4; ++i) xb[i] = smem + L_XB + (8 * g + q) * 128 + ((((i + w) & 3) ^ sx) * 32) + 8 * p;
    WAIT_VM_BAR(12);
    f32x4 acc[2][4], sa;
    g1_mma(xb, w1f, acc, sa, [&](auto KS) {
      constexpr int ks = KS.v;
      if constexpr (ks == 5) {
        if (LAYER) WAIT_VM_BAR(0); else WAIT_VM_BAR(10);
        sig_bar<false>(c, lane, CT_H + c.P);
        SCHED();
      }
      if constexpr (ks == 15) {
#pragma unroll
        for (int i = 0; i < 2; ++i) xr[i] = ld64(c.rs, (c.ct_ * 64 + w * 16 + cc) * 128 + i * 32 + g * 8, xmid + c.P * 65536 + c.sub * 64);
        SCHED();
      }
    }, [&](auto N) {
      constexpr int n = N.v;
      if constexpr (!LAYER) ld_w2<22 + n>(c, v16, w2f);
      if constexpr (!LAYER && n >= 30) ld_w2<24 + n>(c, v16, w2f);
    });
    poll<false>(c, lane, CT_H + c.P);
    const int ho = hbuf + ((c.P * 4 + c.sub * 2) * 56 + 14 * w) * 1024;
    g1_epi(c, lane, acc, sa, hbuf + ((16 + c.P) * 4 * 56 + c.hks) * 1024, [&](auto N) {
      constexpr int n = N.v;
      if constexpr (n == -1) { if (!got<0>(16u * ep)) wait_ge<false>(c, lane, CT_H + c.P, 16u * ep); }
      if constexpr (n >= 0) g2_ldh1<n>(c, v16, hf, ho);
    });
  }
  {
    const int lane = fresh_lane(c);
    const int g = lane >> 4, cc = lane & 15;
    const int ho = hbuf + ((c.P * 4 + c.sub * 2) * 56 + 14 * w) * 1024, hoB = ho + 64 * 56 * 1024;
    f32x4 acc[2][4];
    u32 o = 0;
    g2_mma<20, 1>(c, lane, hf, w2f, acc, ho, 0, [&](auto S) {
      constexpr int s = S.v;
      if constexpr (s == 11) { WAIT_VM(10); o = sig_begin(c, lane); SCHED(); }
      if constexpr (s == 13) { sig_end<false>(c, lane, o, CT_H + 16 + c.P); SCHED(); }
    });
    poll<false>(c, lane, CT_H + 16 + c.P);
    g2_epi<LAYER == 1>(c, lane, acc, xr, 0, xnext + c.P * 65536 + c.sub * 32768, nop);
    if (!got<(LAYER ? 1 : 2)>(16u * ep)) wait_ge<false>(c, lane, CT_H + 16 + c.P, 16u * ep);
    sfor<0, 8>([&](auto N) { g2_ldh1<N.v>(c, lane * 16, hf, hoB); });
#pragma unroll
    for (int i = 0; i < 2; ++i) xr[i] = ld64(c.rs, (c.ct_ * 64 + w * 16 + cc) * 128 + i * 32 + g * 8, xmid + (16 + c.P) * 65536 + c.sub * 64);
    if (LAYER == 0) {
      WAIT_VM(11);
      o = sig_begin(c, lane);
      sig_end<true>(c, lane, o, CT_X);
    }
    SCHED();
  }
  {
    const int lane = fresh_lane(c);
    const int hoB = hbuf + (((16 + c.P) * 4 + c.sub * 2) * 56 + 14 * w) * 1024;
    f32x4 acc[2][4];
    g2_mma<8, 2>(c, lane, hf, w2f, acc, hoB, 1, [&](auto S) {
      constexpr int s = S.v;
      if constexpr (LAYER == 0 && s == 27) { poll<true>(c, lane, CT_X); SCHED(); }
    });
    g2_epi<LAYER == 1>(c, lane, acc, xr, 1, xnext + (16 + c.P) * 65536 + c.sub * 32768, [&](int) {
      if (LAYER == 0) {
        if (!got<0>(32u)) wait_ge<true>(c, lane, CT_X, 32u);
        conv_load_u(c, lane, inA, xnext, WS_ST + 65536, 0);
        SCHED();
      }
    });
  }
}

constexpr int KG_RS = 528, KG_X = 16 * KG_RS, KG_CH = 64 * KG_RS + 1296;
__global__ void __launch_bounds__(256, 1) main_kernel(const u16* x, const u16* lar, const u16* aim, const u16* cre,
                                                      const u16* cim, const u16* ldt, const u16* dsk, const u16* w1,
                                                      const u16* b1, const u16* w2, const u16* b2, char* ws, u16* out) {
  extern __shared__ __attribute__((aligned(16))) char smem[];
  const int t = threadIdx.x, lane = t & 63, b = blockIdx.x;
  const int w = __builtin_amdgcn_readfirstlane(t >> 6);
  const u32 junk = warm_code((b >> 3) * 4 + w, lane);
  const int ch = w >> 1, d = 2 * b + ch, n = t & 127;
  const u32 po = (u32)(d * NST + n) * 2, po2 = po ^ 128u;
#define LD16(p, off) (*(const u16*)((const char*)(p) + (u32)(off)))
  const u16 r0 = ldt[d], r1 = LD16(lar, po), r2 = LD16(aim, po), r3 = LD16(cre, po), r4 = LD16(cim, po);
  const u16 r5 = LD16(cre, po2), r6 = LD16(cim, po2);
  const u32 gen = __hip_atomic_load((gu32*)(ws + WS_GEN), RLX_AGENT);
  const bool early = !(b & 64);
  const int pks = (b & 7) + (b >> 7) * 8, pcg = (b >> 3) & 7;
  u32x4 rv[8], rw[2];
  {
    const rsrc_t rr = __builtin_amdgcn_make_buffer_rsrc((void*)(early ? x : w1), 0, early ? SEQ * DM * 2 : DM * FF * 2, 0x00020000);
    const int vo = early ? w * 1024 + lane * 16 : ((b & 63) < 56 ? (t >> 5) * (FF * 2) + (t & 31) * 16 : 0x40000000);
    const int so = early ? (b & 63) * 32768 : pks * (32 * FF * 2) + min(pcg, 6) * 512, st = early ? 4096 : 8 * FF * 2;
#pragma unroll
    for (int j = 0; j < 8; ++j) rv[j] = __builtin_amdgcn_raw_buffer_load_b128(rr, (j < 4 || early) ? vo : 0x40000000, so + (j & 3) * st + (j >> 2) * (early ? 16384 : 0), 0);
    const rsrc_t r2w = __builtin_amdgcn_make_buffer_rsrc((void*)w2, 0, DM * FF * 2, 0x00020000);
#pragma unroll
    for (int j = 0; j < 2; ++j) rw[j] = __builtin_amdgcn_raw_buffer_load_b128(r2w, (t >> 4) * 1024 + (t & 15) * 16 + j * 16384, min(b >> 2, 55) * 32768 + (b & 3) * 256, 0);
  }
  CIn inA, inB;
  Ctx c;
  c.rs = __builtin_amdgcn_make_buffer_rsrc(ws, 0, WS_END, 0x00020000);
  c.ct = (gu32*)(ws + WS_CT);
  c.smem = smem; c.ws = ws; c.out = out;
  c.lane0 = lane; c.w = w; c.b = b;
  c.P = (b & 7) * 2 + ((b >> 3) & 1);
  const int idx = b >> 4;
  const int sg = idx >> 1, w3 = -(int)(w == 3);
  c.eo = idx & 1; c.sub = idx >> 3; c.ct_ = idx & 7;
  c.ff0 = 14 * sg + (((6 * c.eo + 2 * w) & ~w3) | ((12 + c.eo) & w3));
  c.hks = 7 * sg + (((3 * c.eo + w) & ~w3) | (6 & w3));
  c.f1o = w < 3 ? WS_W1P + c.ff0 * 16384 + 16384 : WS_ONE; c.f1s = w < 3 ? 4096 : 0;
  {
    const u32 r7 = *(const u32*)(dsk + 2 * b);
    u32 r8 = LD16(b1, min(c.ff0 * 16 + (lane & 15) + (lane >> 5) * 16, FF - 1) * 2), r9 = LD16(b2, (c.ct_ * 64 + w * 16 + (lane & 15)) * 2);
    SCHED();
    char* kg = smem + L_XA + ch * KG_CH;
    char* stg = smem + L_XA + 2 * KG_CH;
    const int i = lane & 15, g = lane >> 4;
    int ix[9];
#pragma unroll
    for (int e2 = 0; e2 < 9; ++e2) { ix[e2] = 2 * cidx(max(2047 - 8 * t - e2, 0)); asm volatile("" : "+v"(ix[e2])); }
    int wi[2][4];
    {
      const int m8 = -(i >> 3), s1 = -(w & 1);
      const int h1 = ((256 + 128 * (i >> 2) + 2 * (i & 3)) & ~m8) | ((128 * (i >> 1) + 4 * (i & 1)) & m8);
      const int hp = (((i + (i & 8) * 15) & ~s1) | (h1 & s1)) + 32 * g, hq = 1024 + 128 * i + 32 * g;
#pragma unroll
      for (int r = 0; r < 4; ++r) {
        wi[0][r] = 2 * cidx(hp + 8 * r); wi[1][r] = 2 * cidx(hq + 8 * r > 2048 ? 2064 : hq + 8 * r);
        asm volatile("" : "+v"(wi[0][r]), "+v"(wi[1][r]));
      }
    }
    if (t < 16) *(u32x4*)(smem + (t >> 2) * L_KC + 4096 + (t & 3) * 16) = u32x4{0, 0, 0, 0};
    if (t >= 64 && t < 128) *(u32x4*)(smem + L_U + ((t - 64) >> 5) * L_UC + (t & 31) * 16) = u32x4{0, 0, 0, 0};
    if (t == 128) *(u32*)(smem + L_SIG) = 0u;
    if (w >= 2) poll0(c, lane, w == 2 ? WS_F1 : WS_F0);
    SCHED();
    const float dt = rbf(expf(bf2f(r0)));
    const float cr = bf2f(r3), ci = bf2f(r4), cr2 = bf2f(r5), ci2 = bf2f(r6);
    const float S = wave_sum(sqrt_(cr * cr + ci * ci) + sqrt_(cr2 * cr2 + ci2 * ci2)) * (2.002f * dt);
    int e = __builtin_amdgcn_frexp_expf(S);
    e = (S > 0.0f && S < 3.0e38f) ? max(-100, min(100, e)) : 14;
    const float scale = __builtin_amdgcn_ldexpf(2.0f, 14 - e);
    if (n == 0) *(float*)(smem + L_MISC + ch * 4) = __builtin_amdgcn_ldexpf(1.0f, e - 14);
    const float a2 = (-rbf(expf(bf2f(r1))) * dt) * 1.44269504088896f;
    const float br = (bf2f(r2) * dt) * 0.159154943091895f;
    {
      char* yp = kg + n * 4;
      char* xp = kg + KG_X + n * 4;
      const CM e128 = cexp_(a2, -br, 128.f), e8 = cexp_(a2, br, 8.f), e1 = cexp_(a2, -br, 1.f), e2 = cexp_(a2, -br, 2.f);
      const CM e4 = cexp_(a2, -br, 4.f);
      f32x2 f0 = {(cr * dt) * scale, -(ci * dt) * scale};
      f32x2 f = cmul(f0, cexp_(a2, -br, 512.f)), y = {1.f, 0.f};
      f32x2 g1 = cmul(f0, e128), g2 = cmul(g1, e128), g3 = cmul(g2, e128);
#define PUT(base, row, v) *(u32*)(base + (row) * KG_RS) = pack_h(v[0], v[1])
      auto chain = [&](auto K) {
        constexpr int k = K.v;
        PUT(yp, k, y);
        if (k < 15) y = cmul(y, e8);
        if (k < 4) { const f32x2 q4 = cmul(f, e4); PUT(xp, 24 + 2 * k, f); PUT(xp, 25 + 2 * k, q4); }
        else if (k < 13) PUT(xp, 28 + k, f);
        if (k < 12) f = cmul(f, e128);
        if (k < 8) { PUT(xp, k, f0); PUT(xp, 8 + k, g1); }
        if (k < 7) { f0 = cmul(f0, e1); g1 = cmul(g1, e1); }
        if (k >= 8 && k < 12) { PUT(xp, 8 + k, g2); PUT(xp, 12 + k, g3); }
        if (k >= 8 && k < 11) { g2 = cmul(g2, e2); g3 = cmul(g3, e2); }
      };
      c.tag = __builtin_amdgcn_readfirstlane(gen) * 0x9E3779B1u + 0x7F4A7C15u;
      sfor<0, 2>(chain);
      asm volatile("" : "+v"(y), "+v"(f), "+v"(f0), "+v"(g1));
      if (b < 64) xpose(b, rv, c.rs, stg);
      else if (early) row_stats(b & 63, rv, c.rs);
      asm volatile("" : "+v"(y), "+v"(f), "+v"(f0), "+v"(g1));
      sfor<2, 12>(chain);
      asm volatile("" : "+v"(y), "+v"(f));
      if (early) {
        WAIT_VM(0);
        __syncthreads();
        if (w == 0 && lane < 8)
          __hip_atomic_store((gu32*)(ws + WS_F0 + lane * 2048 + (b & 7) * 256 + ((b >> 3) & 7) * 8 + (b >> 7) * 4), c.tag, RLX_AGENT);
      }
      asm volatile("" : "+v"(y), "+v"(f));
      sfor<12, 16>(chain);
      if (!early && pcg < 7) pack_w1(pks, pcg, rv, c.rs, stg);
#undef PUT
    }
    pack_w2_put(rw, smem + L_XB + 55552);
    __syncthreads();
    {
      const char* ya = kg + i * KG_RS + g * 16;
      const char* xa = kg + KG_X + (16 * (w & 1) + i) * KG_RS + g * 16;
      const char* xc = kg + KG_X + (32 + i) * KG_RS + g * 16;
      u32x4 fa[8], fb[8], fc[8];
#pragma unroll
      for (int ks = 0; ks < 8; ++ks) { fa[ks] = *(const u32x4*)(ya + ks * 64); fb[ks] = *(const u32x4*)(xa + ks * 64); }
#pragma unroll
      for (int ks = 0; ks < 8; ++ks) fc[ks] = *(const u32x4*)(xc + ks * 64);
      f32x4 ac0, ac1;
      SCHED();
      sfor<0, 8>([&](auto KS) { mma_hh<KS.v == 0>(ac0, fa[KS.v], fb[KS.v]); });
      sfor<0, 8>([&](auto KS) { mma_hh<KS.v == 0>(ac1, fa[KS.v], fc[KS.v]); });
      MFMA_DONE();
#pragma unroll
      for (int r = 0; r < 4; ++r) {
        *(u16*)(kg + 64 * KG_RS + wi[0][r]) = __builtin_bit_cast(u16, (_Float16)ac0[r]);
        *(u16*)(kg + 64 * KG_RS + wi[1][r]) = __builtin_bit_cast(u16, (_Float16)ac1[r]);
      }
    }
    if (!early) WAIT_VM(0);
    __syncthreads();
    if (w == 1 && !early && lane < 8)
      __hip_atomic_store((gu32*)(ws + WS_F1 + lane * 2048 + (b & 7) * 256 + ((b >> 3) & 7) * 8 + (b >> 7) * 4), c.tag, RLX_AGENT);
    if (w == 0) poll0(c, lane, WS_F0);
#pragma unroll
    for (int c2 = 0; c2 < 2; ++c2) {
      u32 v[9];
#pragma unroll
      for (int e2 = 0; e2 < 9; ++e2) v[e2] = *(const u16*)(smem + L_XA + c2 * KG_CH + 64 * KG_RS + ix[e2]);
      if (t == 255) v[8] = 0u;
      u32x4 oe, oo;
#pragma unroll
      for (int j = 0; j < 4; ++j) { oe[j] = v[2 * j] | (v[2 * j + 1] << 16); oo[j] = v[2 * j + 1] | (v[2 * j + 2] << 16); }
      *(u32x4*)(smem + c2 * 2 * L_KC + t * 16) = oe;
      *(u32x4*)(smem + c2 * 2 * L_KC + L_KC + t * 16) = oo;
    }
    const f32x2 ksc = *(const f32x2*)(smem + L_MISC);
    c.ks0 = __uint_as_float(__builtin_amdgcn_readfirstlane(__float_as_uint(ksc[0])));
    c.ks1 = __uint_as_float(__builtin_amdgcn_readfirstlane(__float_as_uint(ksc[1])));
    const u32 ds = __builtin_amdgcn_readfirstlane(r7);
    c.dsk0 = lo_bf(ds); c.dsk1 = hi_bf(ds);
    c.cs1 = lo_bf(r8); c.bb2 = lo_bf(r9);
    asm volatile("" :: "v"(junk));
    if (w == 0 && !got0<0>(c.tag)) wait0(c, lane, WS_F0);
    __syncthreads();
    conv_load(c, lane, inA, WS_X, WS_ST, 0);
    conv_load(c, lane, inB, WS_X, WS_ST, 1);
    pack_w2_out(b, c.rs, smem + L_XB + 55552);
  }
  u32x4 w1f[2][16], w2f[14][4];
  layer_body<0>(c, w1f, w2f, inA, inB);
  layer_body<1>(c, w1f, w2f, inA, inB);
}

extern "C" __global__ void code_end_marker() {}

extern "C" void kernel_launch(void* const* d_in, const int* in_sizes, int n_in, void* d_out, int out_size,
                              void* d_ws, size_t ws_size, hipStream_t stream) {
  (void)in_sizes; (void)n_in; (void)out_size; (void)ws_size;
  char* ws = (char*)d_ws + ((ws_size - (size_t)WS_END) & ~(size_t)(2 * MB - 1));
  (void)hipFuncSetAttribute((const void*)main_kernel, hipFuncAttributeMaxDynamicSharedMemorySize, MAIN_LDS);
  main_kernel<<<256, 256, MAIN_LDS, stream>>>((const u16*)d_in[0], (const u16*)d_in[1], (const u16*)d_in[2],
                                              (const u16*)d_in[3], (const u16*)d_in[4], (const u16*)d_in[5],
                                              (const u16*)d_in[6], (const u16*)d_in[8], (const u16*)d_in[9],
                                              (const u16*)d_in[10], (const u16*)d_in[11], ws, (u16*)d_out);
}
```

```cpp
#include <hip/hip_runtime.h>
#include <hip/hip_bf16.h>

typedef unsigned short u16;
typedef unsigned int u32;
typedef long long i64;
typedef __attribute__((ext_vector_type(4))) short s16x4;
typedef __attribute__((ext_vector_type(8))) __bf16 bf16x8;
typedef __attribute__((ext_vector_type(2))) __bf16 bf16x2;
typedef __attribute__((ext_vector_type(8))) _Float16 f16x8;
typedef __attribute__((ext_vector_type(2))) _Float16 f16x2;
typedef __attribute__((ext_vector_type(4))) float f32x4;
typedef __attribute__((ext_vector_type(2))) float f32x2;
typedef __attribute__((ext_vector_type(4))) unsigned u32x4;
typedef __attribute__((ext_vector_type(2))) unsigned u32x2;
typedef __attribute__((ext_vector_type(2))) long long i64x2;
typedef __attribute__((address_space(1))) unsigned gu32;
typedef __amdgpu_buffer_rsrc_t rsrc_t;
#define LAS __attribute__((address_space(3)))
#define DEVI __device__ __forceinline__
template <int V> struct IC { static constexpr int v = V; };
template <int I, int N, class F> __device__ __forceinline__ void sfor(F&& f) {
  if constexpr (I < N) { f(IC<I>{}); sfor<I + 1, N>(f); }
}
#define RLX_AGENT __ATOMIC_RELAXED, __HIP_MEMORY_SCOPE_AGENT

constexpr int SEQ = 2048, DM = 512, NST = 128, FF = 1792;
constexpr float LN_EPS = 1e-5f;
constexpr float FX_SCALE = 16777216.0f;
constexpr float FX_INV = 1.0f / 16777216.0f;
constexpr int SC1 = 16;

constexpr int MB = 1024 * 1024;
constexpr int WS_X = 0;
constexpr int WS_W1P = 12 * MB;
constexpr int WS_W2P = 14 * MB;
constexpr int WS_H = 16 * MB;
constexpr int WS_ST = 32 * MB;
constexpr int WS_S1 = 33 * MB;
constexpr int WS_GEN = 32 * MB + 196608;
constexpr int WS_F0 = WS_GEN + 256, WS_F1 = WS_F0 + 16384;
constexpr int WS_CT = 32 * MB + 262144;
constexpr int WS_ONE = 32 * MB + 393216;
constexpr int WS_END = 34 * MB;
constexpr int CT_C = 0, CT_X = 128, CT_H = 256, CT_TMO = 288;
constexpr int SPIN_CAP = 1 << 16;

#define STAMP(k, i) do {} while (0)

DEVI float bf2f(u16 v) { return __uint_as_float((u32)v << 16); }
DEVI float lo_bf(u32 w) { return __uint_as_float(w << 16); }
DEVI float hi_bf(u32 w) { return __uint_as_float(w & 0xffff0000u); }
DEVI float rbf(float x) { return (float)(__bf16)x; }
DEVI u32 pack_bf(float a, float b) { f32x2 q = {a, b}; return __builtin_bit_cast(u32, __builtin_convertvector(q, bf16x2)); }
DEVI u32 pack_h(float a, float b) { f32x2 q = {a, b}; return __builtin_bit_cast(u32, __builtin_convertvector(q, f16x2)); }
DEVI float dot2bf(u32 a, u32 b, float c) {
  return __builtin_amdgcn_fdot2_f32_bf16(__builtin_bit_cast(bf16x2, a), __builtin_bit_cast(bf16x2, b), c, false);
}
DEVI f32x4 mfma_bf(u32x4 a, u32x4 b, f32x4 c) {
  return __builtin_amdgcn_mfma_f32_16x16x32_bf16(__builtin_bit_cast(bf16x8, a), __builtin_bit_cast(bf16x8, b), c, 0, 0, 0);
}
DEVI f32x4 mfma_h(u32x4 a, u32x4 b, f32x4 c) {
  return __builtin_amdgcn_mfma_f32_16x16x32_f16(__builtin_bit_cast(f16x8, a), __builtin_bit_cast(f16x8, b), c, 0, 0, 0);
}
DEVI u32x2 tr_read(const char* p) {
  return __builtin_bit_cast(u32x2, __builtin_amdgcn_ds_read_tr16_b64_v4i16((LAS s16x4*)p));
}
DEVI u32x4 cat2(u32x2 a, u32x2 b) { return __builtin_shufflevector(a, b, 0, 1, 2, 3); }
DEVI u32 lds_addr(const void* p) { return (u32)(size_t)(const LAS char*)p; }
#define WAIT_VM(n) asm volatile("s_waitcnt vmcnt(" #n ")" ::: "memory")
#define WAIT_VM_BAR(n) asm volatile("s_waitcnt vmcnt(" #n ")\n\ts_barrier" ::: "memory")
#define SCHED() __builtin_amdgcn_sched_barrier(0)
template <int CTRL> DEVI float dpp_add(float v) {
  return v + __uint_as_float(__builtin_amdgcn_update_dpp(0u, __float_as_uint(v), CTRL, 0xf, 0xf, true));
}
DEVI float sum_x16(float v) {
  auto r = __builtin_amdgcn_permlane16_swap(__float_as_uint(v), __float_as_uint(v), false, false);
  return __uint_as_float(r[0]) + __uint_as_float(r[1]);
}
DEVI float sum_x32(float v) {
  auto r = __builtin_amdgcn_permlane32_swap(__float_as_uint(v), __float_as_uint(v), false, false);
  return __uint_as_float(r[0]) + __uint_as_float(r[1]);
}
DEVI float wave_sum(float v) {
  v = dpp_add<0xB1>(v); v = dpp_add<0x4E>(v); v = dpp_add<0x141>(v); v = dpp_add<0x140>(v);
  return sum_x32(sum_x16(v));
}
DEVI float fx2f(u32 lo, u32 hi) { return fmaf((float)(int)hi, 4294967296.0f * FX_INV, (float)lo * FX_INV); }
DEVI i64 f2fx(float v) {
  const float a = v * (FX_SCALE / 4294967296.0f), h = __builtin_floorf(a);
  return __builtin_bit_cast(i64, u32x2{(u32)((a - h) * 4294967296.0f), (u32)(int)h});
}
DEVI float sel4(int i, float a, float b, float c, float d) {
  asm volatile("" : "+v"(a), "+v"(b), "+v"(c), "+v"(d));
  return (i & 2) ? ((i & 1) ? d : c) : ((i & 1) ? b : a);
}
DEVI int st_pos(int t) { return ((t >> 8) * 4 + (t & 3)) * 1024 + ((t >> 2) & 63) * 16; }
DEVI float rcp(float x) { return __builtin_amdgcn_rcpf(x); }
DEVI float sqrt_(float x) { return __builtin_amdgcn_sqrtf(x); }

DEVI u32 warm_code(u32 off) {
  unsigned long long pc;
  u32 junk;
  asm volatile("s_getpc_b64 %1\n"
               "9:\n\tv_min_u32 %0, code_end_marker-9b-4, %2\n\tglobal_load_dword %0, %0, %1"
               : "=&v"(junk), "=&s"(pc) : "v"(off) : "memory");
  return junk;
}

DEVI int cidx(int l) {
  const int hb = (int)(pack_bf((float)l, 0.f) & 0xffffu) - (0x4300 - 128);
  return l < 128 ? l : hb;
}
struct CM { f32x2 e, f; };
DEVI f32x2 cmul(f32x2 a, const CM& m) { return a[1] * m.f + a[0] * m.e; }
DEVI CM cexp_(float a2, float br, float p) {
  const float e = __builtin_amdgcn_exp2f(a2 * p), f = __builtin_amdgcn_fractf(br * p);
  const float re = e * __builtin_amdgcn_cosf(f), im = e * __builtin_amdgcn_sinf(f);
  CM r; r.e = f32x2{re, im}; r.f = f32x2{-im, re};
  return r;
}
DEVI void st16(rsrc_t rs, int off, u32x4 v) { __builtin_amdgcn_raw_buffer_store_b128(v, rs, off, 0, SC1); }

DEVI void xpose(int b, const u32x4 (&v)[8], rsrc_t rs, char* smem) {
  const int t = threadIdx.x, lane = t & 63, w = t >> 6;
  constexpr int RS = 1024 + 32;
#pragma unroll
  for (int r = 0; r < 8; ++r) *(u32x4*)(smem + (w + 4 * r) * RS + lane * 16) = v[r];
  __syncthreads();
  const int g = lane >> 4, q = (lane & 15) >> 2, p = lane & 3;
#pragma unroll
  for (int cf = 0; cf < 8; ++cf) {
    const int c0 = (w * 8 + cf) * 16;
    const char* pa = smem + (8 * g + q) * RS + c0 * 2 + 8 * p;
    const u32x4 o = cat2(tr_read(pa), tr_read(pa + 4 * RS));
    st16(rs, WS_X + b * 32768 + (c0 + (lane & 15)) * 64 + g * 16, o);
  }
}
DEVI void row_stats(int b, const u32x4 (&v)[8], rsrc_t rs) {
  const int t = threadIdx.x, lane = t & 63, w = t >> 6;
  const int t0 = b * 32;
  float a[16];
#pragma unroll
  for (int r = 0; r < 8; ++r) {
    float s = 0.f, ss = 0.f;
#pragma unroll
    for (int j = 0; j < 4; ++j) { s = dot2bf(v[r][j], 0x3f803f80u, s); ss = dot2bf(v[r][j], v[r][j], ss); }
    a[2 * r] = s; a[2 * r + 1] = ss;
  }
  const bool o1 = lane & 1, o2 = lane & 2;
#pragma unroll
  for (int k = 0; k < 8; ++k) {
    const float keep = o1 ? a[2 * k + 1] : a[2 * k], send = o1 ? a[2 * k] : a[2 * k + 1];
    a[k] = keep + __uint_as_float(__builtin_amdgcn_update_dpp(0u, __float_as_uint(send), 0xB1, 0xf, 0xf, true));
  }
#pragma unroll
  for (int k = 0; k < 4; ++k) {
    const float keep = o2 ? a[2 * k + 1] : a[2 * k], send = o2 ? a[2 * k] : a[2 * k + 1];
    a[k] = keep + __uint_as_float(__builtin_amdgcn_update_dpp(0u, __float_as_uint(send), 0x4E, 0xf, 0xf, true));
  }
#pragma unroll
  for (int k = 0; k < 4; ++k) a[k] = sum_x32(sum_x16(dpp_add<0x128>(dpp_add<0x124>(a[k]))));
  const i64 sv = f2fx(sel4(lane >> 2, a[0], a[1], a[2], a[3]));
  if (lane < 16)
    __builtin_amdgcn_raw_buffer_store_b64(__builtin_bit_cast(u32x2, sv), rs, st_pos(t0 + w) + lane * 8, WS_ST, SC1);
  if (t >= 96 && t < 128) st16(rs, WS_ST + 65536 + b * 512 + (t - 96) * 16, u32x4{0, 0, 0, 0});
  if (t < 80) st16(rs, WS_CT + b * 1280 + t * 16, u32x4{0, 0, 0, 0});
}

DEVI void pack_w1(int ks, int cg, const u32x4 (&v)[8], rsrc_t rs, char* smem) {
  const int t = threadIdx.x, lane = t & 63, w = t >> 6;
  constexpr int RS = 512 + 32;
#pragma unroll
  for (int it = 0; it < 4; ++it) *(u32x4*)(smem + (it * 8 + (t >> 5)) * RS + (t & 31) * 16) = v[it];
  __syncthreads();
  const int g = lane >> 4, q = (lane & 15) >> 2, p = lane & 3;
  float cs[4];
#pragma unroll
  for (int h = 0; h < 4; ++h) {
    const char* a = smem + (8 * g + q) * RS + (w * 4 + h) * 32 + 8 * p;
    const u32x4 o = cat2(tr_read(a), tr_read(a + 4 * RS));
    st16(rs, WS_W1P + (((cg * 16 + w * 4 + h) * 16 + ks) * 64 + lane) * 16, o);
    float s = 0.f;
#pragma unroll
    for (int j = 0; j < 4; ++j) s = dot2bf(o[j], 0x3f803f80u, s);
    cs[h] = sum_x32(sum_x16(s));
  }
  __builtin_amdgcn_raw_buffer_store_b32(__float_as_uint(sel4(g, cs[0], cs[1], cs[2], cs[3])), rs, (ks * FF + cg * 256 + t) * 4, WS_S1, SC1);
  if (ks == 0 && cg == 0) st16(rs, WS_ONE + t * 16, u32x4{0x3f803f80u, 0x3f803f80u, 0x3f803f80u, 0x3f803f80u});
}

constexpr int W2_RS = 256 + 32;
DEVI void pack_w2_put(const u32x4 (&v)[2], char* smem) {
  const int t = threadIdx.x;
#pragma unroll
  for (int r = 0; r < 2; ++r) *(u32x4*)(smem + (r * 16 + (t >> 4)) * W2_RS + (t & 15) * 16) = v[r];
}
DEVI void pack_w2_out(int b, rsrc_t rs, const char* smem) {
  const int t = threadIdx.x, lane = t & 63, w = t >> 6;
  const int g = lane >> 4, q = (lane & 15) >> 2, p = lane & 3;
#pragma unroll
  for (int cf = 0; cf < 2; ++cf) {
    const char* a = smem + (4 * g + q) * W2_RS + (w * 2 + cf) * 32 + 8 * p;
    const u32x4 o = cat2(tr_read(a), tr_read(a + 16 * W2_RS));
    st16(rs, b < 224 ? WS_W2P + ((((b & 3) * 8 + w * 2 + cf) * 56 + (b >> 2)) * 64 + lane) * 16 : 0x40000000, o);
  }
}

constexpr int L_KC = 4160;
constexpr int L_U = 4 * L_KC, L_UC = 4608;
constexpr int L_XA = 26112, L_XB = L_XA + 65536;
constexpr int L_MISC = L_XB + 65536;
constexpr int L_CST = L_MISC + 1024;
constexpr int L_SIG = L_CST + 4 * 256;
constexpr int MAIN_LDS = L_SIG + 256;
constexpr int L_RED = L_XB;

DEVI u32x4 ld128(rsrc_t rs, int v, int s) { return __builtin_amdgcn_raw_buffer_load_b128(rs, v, s, SC1); }
DEVI u32x2 ld64(rsrc_t rs, int v, int s) { return __builtin_amdgcn_raw_buffer_load_b64(rs, v, s, SC1); }
DEVI void st128(rsrc_t rs, int v, int s, u32x4 d) { __builtin_amdgcn_raw_buffer_store_b128(d, rs, v, s, SC1); }
DEVI void st64(rsrc_t rs, int v, int s, u32x2 d) { __builtin_amdgcn_raw_buffer_store_b64(d, rs, v, s, SC1); }
template <int OFF> DEVI u32x4 ld128a(rsrc_t rs, int v, int s) {
  u32x4 r;
  asm volatile("buffer_load_dwordx4 %0, %1, %2, %3 offen offset:%4 sc1" : "=a"(r) : "v"(v), "s"(rs), "s"(s), "i"(OFF) : "memory");
  return r;
}
template <int OFF> DEVI u32x4 ld128v(rsrc_t rs, int v, int s) {
  u32x4 r;
  asm volatile("buffer_load_dwordx4 %0, %1, %2, %3 offen offset:%4 sc1" : "=v"(r) : "v"(v), "s"(rs), "s"(s), "i"(OFF) : "memory");
  return r;
}
template <int OFF> DEVI void dma1k(rsrc_t rs, u32 lds_dst, int v, int soff) {
  asm volatile("s_mov_b32 m0, %0\n\ts_nop 0\n\tbuffer_load_dwordx4 %1, %2, %3 offen offset:%4 sc1 lds"
               :: "s"(lds_dst), "v"(v), "s"(rs), "s"(soff), "i"(OFF) : "memory");
}
#define MMA(NAME, OP, CA, CB)                                                                                   \
  template <bool Z> DEVI void NAME(f32x4& d, const u32x4& a, const u32x4& b) {                                  \
    if (Z) asm volatile(OP " %0, %1, %2, 0" : "=&v"(d) : CA(a), CB(b));                                         \
    else asm volatile(OP " %0, %1, %2, %0" : "+v"(d) : CA(a), CB(b));                                           \
  }
MMA(mma_vv, "v_mfma_f32_16x16x32_bf16", "v", "v")
MMA(mma_av, "v_mfma_f32_16x16x32_bf16", "a", "v")
MMA(mma_va, "v_mfma_f32_16x16x32_bf16", "v", "a")
MMA(mma_hh, "v_mfma_f32_16x16x32_f16", "v", "v")
#define MFMA_DONE() do { SCHED(); asm volatile("s_nop 7" ::: "memory"); SCHED(); } while (0)

struct Ctx {
  rsrc_t rs; gu32* ct; char* smem; char* ws; u16* out;
  int lane0, w, b, P, sub, ct_, ff0, hks, eo, f1o, f1s;
  float dsk0, dsk1, ks0, ks1, bb2, cs1;
  u32 tag;
};
DEVI int fresh_lane(const Ctx& c) { int l = c.lane0; asm volatile("" : "+v"(l)); return l; }
DEVI u32 sig_begin(const Ctx& c, int lane) {
  u32 old;
  asm volatile("ds_add_rtn_u32 %0, %1, %2" : "=v"(old) : "v"(lds_addr(c.smem) + L_SIG + lane * 4), "v"(1u) : "memory");
  return old;
}
template <bool ALL, bool BACK = false> DEVI void sig_end(const Ctx& c, int lane, u32 old, int line) {
  if (BACK) asm volatile("" : "+v"(old) :: "memory");
  else asm volatile("s_waitcnt lgkmcnt(0)" : "+v"(old) :: "memory");
  const bool last = (__builtin_amdgcn_readfirstlane(old) & 3u) == 3u;
  const int v = (last && lane < (ALL ? 8 : 1)) ? (ALL ? lane * 2048 : 0) : 0x40000000;
  asm volatile("s_nop 4\n\tbuffer_atomic_add %0, %1, %2, %3 offen" :: "v"(1u), "v"(v), "s"(c.rs),
               "s"(WS_CT + (line + (ALL ? (c.b >> 3) & 7 : 0)) * 256) : "memory");
}
template <bool ALL, int R = 0> DEVI void poll(const Ctx& c, int lane, int line) {
  const int v = ALL ? (lane & 7) * 256 : 0, so = WS_CT + (line + (ALL ? (c.b & 7) * 8 : 0)) * 256;
  if (R == 0) asm volatile("s_nop 4\n\tbuffer_load_dword v254, %0, %1, %2 offen sc1" :: "v"(v), "s"(c.rs), "s"(so) : "memory", "v254");
  else asm volatile("s_nop 4\n\tbuffer_load_dword v255, %0, %1, %2 offen sc1" :: "v"(v), "s"(c.rs), "s"(so) : "memory", "v255");
}
template <int YOUNGER, int R = 0> DEVI bool got(u32 want) {
  unsigned long long m;
  if (R == 0) asm volatile("s_waitcnt vmcnt(%2)\n\tv_cmp_le_u32_e64 %0, %1, v254" : "=s"(m) : "s"(want), "i"(YOUNGER) : "memory");
  else asm volatile("s_waitcnt vmcnt(%2)\n\tv_cmp_le_u32_e64 %0, %1, v255" : "=s"(m) : "s"(want), "i"(YOUNGER) : "memory");
  return m == ~0ull;
}
template <bool ALL> DEVI void wait_ge(const Ctx& c, int lane, int line, u32 want) {
  int sp = 0;
#pragma nounroll
  do {

    poll<ALL, 0>(c, lane, line);
    if (++sp > SPIN_CAP) { __hip_atomic_store(c.ct + CT_TMO * 64, 1u, RLX_AGENT); break; }
  } while (!got<0, 0>(want));
}

DEVI void poll0(const Ctx& c, int lane, int set) {
  asm volatile("s_nop 4\n\tbuffer_load_dwordx2 v[254:255], %0, %1, %2 offen sc1" :: "v"((lane & 7) * 256 + (lane >> 3) * 8), "s"(c.rs),
               "s"(set + (c.b & 7) * 2048) : "memory", "v254", "v255");
}
template <int YOUNGER> DEVI bool got0(u32 tag) {
  unsigned long long m, m2;
  asm volatile("s_waitcnt vmcnt(%3)\n\tv_cmp_eq_u32_e64 %0, %2, v254\n\tv_cmp_eq_u32_e64 %1, %2, v255"
               : "=&s"(m), "=s"(m2) : "s"(tag), "i"(YOUNGER) : "memory");
  return (m & m2) == ~0ull;
}
DEVI void wait0(const Ctx& c, int lane, int set) {
  int sp = 0;
#pragma nounroll
  do {
    poll0(c, lane, set);
    if (++sp > SPIN_CAP) { __hip_atomic_store(c.ct + CT_TMO * 64, 1u, RLX_AGENT); break; }
  } while (!got0<0>(c.tag));
}

struct CIn { u32x2 x0, x1; u32x4 s[4]; };
DEVI void conv_load(const Ctx& c, int lane, CIn& in, int xoff, int stoff, int h) {
  const int sx = xoff + (32 * h + 8 * c.w) * 32768 + c.b * 128;
  const int vx = (lane >> 3) * 32768 + (lane & 7) * 8;
  const int ss = stoff + (1024 * h + 256 * c.w) * 16;
#pragma unroll
  for (int j = 0; j < 4; ++j) in.s[j] = ld128(c.rs, lane * 16 + j * 1024, ss);
  in.x0 = ld64(c.rs, vx, sx);
  in.x1 = ld64(c.rs, vx + 64, sx);
}
DEVI void conv_load_u(const Ctx& c, int lane, CIn& in, int xoff, int stoff, int h) {
  const int sx = xoff + (32 * h + 8 * c.w) * 32768 + c.b * 128;
  const int vx = (lane >> 3) * 32768 + (lane & 7) * 8;
  const int ss = stoff + (1024 * h + 256 * c.w) * 16;
  asm volatile("s_nop 4\n\t"
               "buffer_load_dwordx4 %0, %6, %8, %9 offen sc1\n\tbuffer_load_dwordx4 %1, %6, %8, %9 offen offset:1024 sc1\n\t"
               "buffer_load_dwordx4 %2, %6, %8, %9 offen offset:2048 sc1\n\tbuffer_load_dwordx4 %3, %6, %8, %9 offen offset:3072 sc1\n\t"
               "buffer_load_dwordx2 %4, %7, %8, %10 offen sc1\n\tbuffer_load_dwordx2 %5, %7, %8, %10 offen offset:64 sc1"
               : "=&v"(in.s[0]), "=&v"(in.s[1]), "=&v"(in.s[2]), "=&v"(in.s[3]), "=&v"(in.x0), "=&v"(in.x1)
               : "v"(lane * 16), "v"(vx), "s"(c.rs), "s"(ss), "s"(sx) : "memory");
}
DEVI void conv_landed(CIn& in) {
  asm volatile("s_waitcnt vmcnt(2)" : "+v"(in.s[0]), "+v"(in.s[1]), "+v"(in.s[2]), "+v"(in.s[3]), "+v"(in.x0), "+v"(in.x1) :: "memory");
}
DEVI void conv_ln(const Ctx& c, int lane, const CIn& in, int h, f32x4& base0, f32x4& base1) {
  constexpr float CH = 4294967296.0f * FX_INV / DM, CL = FX_INV / DM;
  f32x4 m, r;
#pragma unroll
  for (int j = 0; j < 4; ++j) {
    m[j] = fmaf((float)(int)in.s[j][1], CH, (float)in.s[j][0] * CL);
    r[j] = fmaf((float)(int)in.s[j][3], CH, fmaf((float)in.s[j][2], CL, LN_EPS));
  }
#pragma unroll
  for (int j = 0; j < 4; ++j) r[j] = __builtin_amdgcn_rsqf(fmaxf(fmaf(-m[j], m[j], r[j]), LN_EPS));
  const f32x4 rr = r, mr = -m * r;
  const f32x4 x0 = {lo_bf(in.x0[0]), hi_bf(in.x0[0]), lo_bf(in.x0[1]), hi_bf(in.x0[1])};
  const f32x4 x1 = {lo_bf(in.x1[0]), hi_bf(in.x1[0]), lo_bf(in.x1[1]), hi_bf(in.x1[1])};
  const f32x4 u0 = x0 * rr + mr, u1 = x1 * rr + mr;
  char* up = c.smem + L_U + 512 + (1024 * h + 256 * c.w + 4 * lane) * 2;
  *(u32x2*)up = u32x2{pack_h(u0[0], u0[1]), pack_h(u0[2], u0[3])};
  *(u32x2*)(up + L_UC) = u32x2{pack_h(u1[0], u1[1]), pack_h(u1[2], u1[3])};
  base0 = u0 * c.dsk0 + x0; base1 = u1 * c.dsk1 + x1;
}
struct KB { const char* p[4]; };
template <int MM, int LAM> DEVI u32x4 conv_k1(const KB& kb) {
  const u32* kp = (const u32*)(kb.p[(7 - LAM) >> 1] + 64 * (3 - MM) + 512 * ((7 - LAM) & 1));
  return u32x4{kp[0], kp[1], kp[2], kp[3]};
}
template <int MM, int TB> DEVI u32x4 conv_u1(const char* ub) { return *(const u32x4*)(ub + 64 * (3 - MM) + 512 * TB); }
template <int LO> DEVI void conv_ldk(const KB& kb, u32x4 (&af)[4]) {
  sfor<0, 4>([&](auto L) { af[L.v] = conv_k1<0, LO + L.v>(kb); });
}
constexpr int conv_rank(int DT, int LO, int gr, int lt) {
  int k = 0;
  for (int x = 0; x < lt; ++x) { const int tb = DT + (x & 3) - (LO + 4 * gr + (x >> 2)); k += (tb >= 0 && tb < 4); }
  return k;
}
template <int DT, int TB0, bool ZERO, class F>
DEVI void conv_mma(const char* ub, const KB& kb, f32x4 (&acc)[4], const u32x4 (&af0)[4], F&& hook) {
  constexpr int NG = DT ? 2 : 1, LO = DT ? 1 : 0;
  u32x4 af[2][4], bf[2][4];
  sfor<0, 4>([&](auto L) { af[0][L.v] = af0[L.v]; });
  sfor<0, 4>([&](auto T) { constexpr int tb = DT ? 3 - T.v : T.v; bf[0][tb] = conv_u1<0, TB0 + tb>(ub); });
  SCHED();
  sfor<0, 4 * NG>([&](auto ST) {
    constexpr int st = ST.v, mm = st / NG, gr = st % NG;
    constexpr bool more = st + 1 < 4 * NG;
    constexpr int mn = (st + 1) / NG, gn = (st + 1) % NG;
    constexpr bool nu = more && gr == NG - 1;
    sfor<0, 16>([&](auto LT) {
      constexpr int l = LT.v >> 2, T = LT.v & 3, tb = DT + T - (LO + 4 * gr + l);
      if constexpr (tb >= 0 && tb < 4) {
        constexpr int k = conv_rank(DT, LO, gr, LT.v);
        constexpr bool first = ZERO && st == 0 && (DT ? (l == T) : (l == 0));
        mma_hh<first>(acc[T], af[st & 1][l], bf[mm & 1][tb]);
        sfor<(gr == 1 ? 2 * k : k), (gr == 1 ? 2 * k + 2 : k + 1)>([&](auto J) {
          constexpr int j = J.v;
          if constexpr (nu && j < 8) {
            if constexpr (DT != 0) {
              if constexpr (j & 1) bf[mn & 1][3 - (j >> 1)] = conv_u1<mn, TB0 + 3 - (j >> 1)>(ub);
              else af[(st + 1) & 1][j >> 1] = conv_k1<mn, LO + (j >> 1)>(kb);
            } else {
              if constexpr (j >= 1 && j <= 4) bf[mn & 1][j - 1] = conv_u1<mn, TB0 + j - 1>(ub);
              else af[(st + 1) & 1][j ? j - 4 : 0] = conv_k1<mn, LO + (j ? j - 4 : 0)>(kb);
            }
          } else if constexpr (more && !nu && j < 3) af[(st + 1) & 1][j] = conv_k1<mn, LO + 4 * gn + j>(kb);
        });
        SCHED();
        hook(IC<mm * (DT ? 16 : 10) + gr * 10 + k>{});
      }
    });
  });
}
DEVI void conv_part(const Ctx& c, int lane, const f32x4 (&acc)[4]) {
  char* red = c.smem + L_RED;
#pragma unroll
  for (int T = 0; T < 4; ++T) *(f32x4*)(red + (c.w * 4 + T) * 1024 + (lane & 15) * 64 + (lane >> 4) * 16) = acc[T];
}
DEVI void conv_out(const Ctx& c, int lane, const f32x4& base0, const f32x4& base1, int xoff, int h) {
  const int sx = xoff + (16 * h + 4 * c.w) * 65536 + c.b * 256;
  const int vx = (lane >> 4) * 65536 + (lane & 15) * 8;
  const char* p = c.smem + L_RED + c.w * 1024 + lane * 16;
  const f32x4 a0 = *(const f32x4*)p, b0 = *(const f32x4*)(p + 4096), a1 = *(const f32x4*)(p + 8192), b1 = *(const f32x4*)(p + 12288);
  SCHED();
  const f32x4 y0 = (a0 + b0) * c.ks0 + base0, y1 = (a1 + b1) * c.ks1 + base1;
  st64(c.rs, vx, sx, u32x2{pack_bf(y0[0], y0[1]), pack_bf(y0[2], y0[3])});
  st64(c.rs, vx + 128, sx, u32x2{pack_bf(y1[0], y1[1]), pack_bf(y1[2], y1[3])});
}

template <int PC> DEVI void g1_dma1(const Ctx& c, int ve, int vo, int ldsx, int soff) {
  dma1k<(PC & 3) * 1024>(c.rs, lds_addr(c.smem) + ldsx + (4 * (PC >> 2) + c.w) * 4096, (PC & 1) ? vo : ve, soff + (4 * (PC >> 2) + c.w) * 4096);
}
template <bool ALL> DEVI void sig_bar(const Ctx& c, int lane, int line) {
  const int v = (c.w == 0 && lane < (ALL ? 8 : 1)) ? (ALL ? lane * 2048 : 0) : 0x40000000;
  asm volatile("s_nop 4\n\tbuffer_atomic_add %0, %1, %2, %3 offen" :: "v"(1u), "v"(v), "s"(c.rs),
               "s"(WS_CT + (line + (ALL ? (c.b >> 3) & 7 : 0)) * 256) : "memory");
}
template <class F, class G>
DEVI void g1_mma(const char* const (&xb)[4], const u32x4 (&w1f)[2][16], f32x4 (&acc)[2][4], f32x4& sa, F&& mid, G&& hook) {
  u32x2 xf[2][4][2];
#pragma unroll
  for (int i = 0; i < 4; ++i) { xf[0][i][0] = tr_read(xb[i]); xf[0][i][1] = tr_read(xb[i] + 512); }
  SCHED();
  sfor<0, 16>([&](auto KS) {
    constexpr int ks = KS.v;
    mid(KS);
    sfor<0, 4>([&](auto I) {
      constexpr int i = I.v;
      const u32x4 x = cat2(xf[ks & 1][i][0], xf[ks & 1][i][1]);
      mma_vv<ks == 0>(acc[0][i], w1f[0][ks], x);
      if constexpr (ks < 15) xf[(ks + 1) & 1][i][0] = tr_read(xb[i] + (ks + 1) * 4096);
      SCHED();
      if constexpr (ks < 8) mma_av<ks == 0>(acc[1][i], w1f[1][ks], x); else mma_vv<false>(acc[1][i], w1f[1][ks], x);
      if constexpr (ks < 15) xf[(ks + 1) & 1][i][1] = tr_read(xb[i] + (ks + 1) * 4096 + 512);
      SCHED();
      if constexpr (i == 0) { mma_vv<ks == 0>(sa, x, x); hook(IC<2 * ks>{}); SCHED(); }
      if constexpr (i == 2) { hook(IC<2 * ks + 1>{}); SCHED(); }
    });
  });
  MFMA_DONE();
}
DEVI void st_h(const Ctx& c, int v, int so0, int so1, u32x4 a, u32x4 b) {
  const u32x2 al = {a[0], a[1]}, bl = {b[0], b[1]};
  asm volatile("s_cmp_eq_u32 %0, 3\n\ts_nop 3\n\ts_cbranch_scc1 1f\n\t"
               "buffer_store_dwordx4 %5, %1, %2, %3 offen sc1\n\tbuffer_store_dwordx4 %6, %1, %2, %4 offen sc1\n\ts_branch 2f\n"
               "1:\n\tbuffer_store_dwordx2 %7, %1, %2, %3 offen sc1\n\tbuffer_store_dwordx2 %8, %1, %2, %4 offen sc1\n"
               "2:\n\ts_nop 1"
               :: "s"(c.w), "v"(v), "s"(c.rs), "s"(so0), "s"(so1), "v"(a), "v"(b), "v"(al), "v"(bl) : "memory", "scc");
}
template <class F>
DEVI void g1_epi(const Ctx& c, int lane, f32x4 (&acc)[2][4], const f32x4& sa, int hoff, F&& hook) {
  const int g = lane >> 4, cc = lane & 15, q = cc >> 2, p = lane & 3, w = c.w;
  float* sums = (float*)(c.smem + L_MISC);
  {
    const float d = sel4(p, sa[0], sa[1], sa[2], sa[3]);
    sums[q == g ? (w * 16 + cc) * 2 + 1 : 128 + lane] = d;
    const float r = sel4(g, acc[1][1][0], acc[1][2][0], acc[1][3][0], acc[1][0][0]);
    sums[w == 3 ? lane * 2 : 192 + lane] = r;
  }
  f32x4 s1v[2], b1v[2];
#pragma unroll
  for (int j = 0; j < 2; ++j) {
    s1v[j] = *(const f32x4*)(c.smem + L_CST + w * 256 + j * 128 + g * 16);
    b1v[j] = *(const f32x4*)(c.smem + L_CST + w * 256 + j * 128 + 64 + g * 16);
  }
  const int vh = w < 3 ? lane * 16 : lane * 8 + c.eo * 512;
  __syncthreads();
  f32x2 k[4];
#pragma unroll
  for (int i = 0; i < 4; ++i) k[i] = *(const f32x2*)(sums + (((i + w) & 3) * 16 + cc) * 2);
#pragma unroll
  for (int i = 0; i < 4; ++i) {
    const float m = k[i][0] * (1.0f / DM);
    const float ve = fmaxf(fmaf(-m, m, fmaf(k[i][1], 1.0f / DM, LN_EPS)), LN_EPS);
    const float r = __builtin_amdgcn_rsqf(fmaf(ve, 1.0f + LN_EPS, -LN_EPS));
    k[i] = f32x2{r, -m * r};
  }
  SCHED();
  hook(IC<-1>{});
  SCHED();
  sfor<0, 2>([&](auto IH) {
    constexpr int ih = IH.v;
    f32x4 x[4], e[4];
#define STAGE(n) do { SCHED(); hook(IC<10 * ih + n>{}); SCHED(); } while (0)
#pragma unroll
    for (int n = 0; n < 4; ++n) x[n] = k[2 * ih + (n >> 1)][1] * s1v[n & 1] + b1v[n & 1];
#pragma unroll
    for (int n = 0; n < 4; ++n) x[n] = acc[n & 1][2 * ih + (n >> 1)] * k[2 * ih + (n >> 1)][0] + x[n];
    STAGE(0);
#pragma unroll
    for (int n = 0; n < 4; ++n) e[n] = x[n] * x[n];
#pragma unroll
    for (int n = 0; n < 4; ++n) e[n] = e[n] * (-0.0713548162726f * 1.44269504088896f) + (-1.59576912160573f * 1.44269504088896f);
    STAGE(1);
#pragma unroll
    for (int n = 0; n < 4; ++n) e[n] = e[n] * x[n];
    STAGE(2);
#pragma unroll
    for (int r = 0; r < 4; ++r) { e[0][r] = __builtin_amdgcn_exp2f(e[0][r]); e[1][r] = __builtin_amdgcn_exp2f(e[1][r]); }
    STAGE(3);
#pragma unroll
    for (int r = 0; r < 4; ++r) { e[2][r] = __builtin_amdgcn_exp2f(e[2][r]); e[3][r] = __builtin_amdgcn_exp2f(e[3][r]); }
    STAGE(4);
#pragma unroll
    for (int n = 0; n < 4; ++n) e[n] = e[n] + 1.0f;
    STAGE(5);
#pragma unroll
    for (int r = 0; r < 4; ++r) { e[0][r] = rcp(e[0][r]); e[1][r] = rcp(e[1][r]); }
    STAGE(6);
#pragma unroll
    for (int r = 0; r < 4; ++r) { e[2][r] = rcp(e[2][r]); e[3][r] = rcp(e[3][r]); }
    STAGE(7);
#pragma unroll
    for (int n = 0; n < 4; ++n) x[n] = x[n] * e[n];
    STAGE(8);
    u32x4 hv[2];
#pragma unroll
    for (int n = 0; n < 4; ++n) { hv[n >> 1][2 * (n & 1)] = pack_bf(x[n][0], x[n][1]); hv[n >> 1][2 * (n & 1) + 1] = pack_bf(x[n][2], x[n][3]); }
    STAGE(9);
    const int so0 = hoff + ((2 * ih + w) & 3) * 56 * 1024, so1 = hoff + ((2 * ih + 1 + w) & 3) * 56 * 1024;
    st_h(c, vh, so0, so1, hv[0], hv[1]);
    SCHED();
    if constexpr (ih == 0) { hook(IC<-2>{}); SCHED(); }
#undef STAGE
  });
}

constexpr int PF = 10;
template <int N> DEVI void ld_w1(const Ctx& c, int v, u32x4 (&w1f)[2][16]) {
  constexpr int ks = N >> 1, j = N & 1;
  const int so = (j ? c.f1o + (ks >> 2) * c.f1s : WS_W1P + c.ff0 * 16384 + (ks >> 2) * 4096);
  if constexpr (j && ks < 8) w1f[j][ks] = ld128a<(ks & 3) * 1024>(c.rs, v, so);
  else w1f[j][ks] = ld128v<(ks & 3) * 1024>(c.rs, v, so);
}
template <int N> DEVI void ld_w2(const Ctx& c, int v, u32x4 (&w2f)[14][4]) {
  constexpr int s = N >> 2, j = N & 3;
  w2f[s][j] = ld128a<(s & 3) * 1024>(c.rs, v, WS_W2P + ((c.ct_ * 4 + ((c.w + j) & 3)) * 56 + 14 * c.w + (s & ~3)) * 1024);
}
template <int N> DEVI void g2_ldh1(const Ctx& c, int v16, u32x4 (&hf)[14][2], int ho) {
  constexpr int s = N % 14, i = N / 14;
  if constexpr (s % 7 == 6)
    hf[s][i] = cat2(ld64(c.rs, (v16 >> 1) + (s & 3) * 1024, ho + i * 56 * 1024 + (s >> 2) * 4096),
                    ld64(c.rs, (v16 >> 1) + (s & 3) * 1024 + 512, ho + i * 56 * 1024 + (s >> 2) * 4096));
  else hf[s][i] = ld128(c.rs, v16 + (s & 3) * 1024, ho + i * 56 * 1024 + (s >> 2) * 4096);
}
template <int PRE, int RATE, class F>
DEVI void g2_mma(const Ctx& c, int lane, u32x4 (&hf)[14][2], const u32x4 (&w2f)[14][4], f32x4 (&acc)[2][4], int ho, int h, F&& mid) {
  const int v16 = lane * 16;
  char* red = c.smem + L_XA + h * 32768;
  sfor<0, 28>([&](auto N) {
    constexpr int i = N.v / 14, s = N.v % 14;
    sfor<0, 4>([&](auto J) {
      constexpr int j = J.v;
      mma_va<s == 0>(acc[i][j], hf[s][i], w2f[s][j]);
      constexpr int per = 4 / RATE, n = PRE + RATE * N.v + j / per;
      if constexpr (j % per == per - 1 && n < 28) { g2_ldh1<n>(c, v16, hf, ho); SCHED(); }
      if constexpr (N.v == 17 && j > 0) { *(f32x4*)(red + ((((c.w + j) & 3) * 3 + j - 1) * 2) * 1024 + v16) = acc[0][j]; SCHED(); }
    });
    SCHED();
    mid(N);
  });
  MFMA_DONE();
}
template <bool LAST, class F>
DEVI void g2_epi(const Ctx& c, int lane, const f32x4 (&acc)[2][4], const u32x2 (&xr)[2], int h, int xdst, F&& mid) {
  const int g = lane >> 4, cc = lane & 15, q = cc >> 2, p = lane & 3, w = c.w, t = w * 64 + lane;
  char* red = c.smem + L_XA + h * 32768;
#pragma unroll
  for (int jj = 1; jj < 4; ++jj) *(f32x4*)(red + ((((w + jj) & 3) * 3 + jj - 1) * 2 + 1) * 1024 + lane * 16) = acc[1][jj];
  __syncthreads();
  char* xt = red + 24576;
  f32x4 pr[2][3];
#pragma unroll
  for (int i = 0; i < 2; ++i)
#pragma unroll
    for (int rk = 0; rk < 3; ++rk) pr[i][rk] = *(const f32x4*)(red + ((w * 3 + rk) * 2 + i) * 1024 + lane * 16);
  mid(0);
#pragma unroll
  for (int i = 0; i < 2; ++i) {
    const f32x4 xv = {lo_bf(xr[i][0]), hi_bf(xr[i][0]), lo_bf(xr[i][1]), hi_bf(xr[i][1])};
    const f32x4 o = ((acc[i][0] + (xv + c.bb2)) + pr[i][0]) + (pr[i][1] + pr[i][2]);
    const u32x2 r = {pack_bf(o[0], o[1]), pack_bf(o[2], o[3])};
    *(u32x2*)(xt + (w * 16 + cc) * 64 + ((i ^ (cc >> 3)) * 32) + g * 8) = r;
  }
  __syncthreads();
  const char* xq = xt + ((w >> 1) * 32 + 8 * g + q) * 64 + (((w & 1) ^ (g & 1)) * 32) + 8 * p;
  const u32x4 f0 = cat2(tr_read(xq), tr_read(xq + 256));
  const int tok = (16 * h + c.P) * 64 + c.sub * 32 + (w & 1) * 16 + cc;
  if (LAST) {
    *(u32x4*)(c.out + (size_t)tok * DM + c.ct_ * 64 + (w >> 1) * 32 + 8 * g) = f0;
  } else {
    const int row = t >> 2;
    const u32x4 v = *(const u32x4*)(xt + row * 64 + (((t & 3) ^ ((row >> 2) & 2)) * 16));
    st128(c.rs, c.ct_ * 4096 + t * 16, xdst, v);
    const char* xo = xq + ((w & 2) ? -2048 : 2048);
    const u32x4 f1 = cat2(tr_read(xo), tr_read(xo + 256));
    float s = 0.f, ss = 0.f, s2 = 0.f, ss2 = 0.f;
#pragma unroll
    for (int j = 0; j < 4; ++j) {
      s = dot2bf(f0[j], 0x3f803f80u, s); ss = dot2bf(f0[j], f0[j], ss);
      s2 = dot2bf(f1[j], 0x3f803f80u, s2); ss2 = dot2bf(f1[j], f1[j], ss2);
    }
    s = sum_x32(sum_x16(s + s2)); ss = sum_x32(sum_x16(ss + ss2));
    asm volatile("s_nop 4\n\tbuffer_atomic_add_x2 %0, %1, %2, %3 offen" :: "v"(f2fx((g & 1) ? ss : s)),
                 "v"(t < 128 && lane < 32 ? st_pos(tok) + g * 8 : 0x40000000), "s"(c.rs), "s"(WS_ST + 65536) : "memory");
  }
}

template <int LAYER>
DEVI void layer_body(const Ctx& c, u32x4 (&w1f)[2][16], u32x4 (&w2f)[14][4], CIn& inA, CIn& inB) {
  constexpr int xin = WS_X + LAYER * 4 * MB, xmid = xin + 2 * MB, xnext = xin + 4 * MB;
  constexpr int hbuf = WS_H + LAYER * 8 * MB, stin = WS_ST + LAYER * 65536;
  constexpr u32 ep = LAYER + 1;
  constexpr int K1 = 1 + 3 * LAYER, K2 = K1 + 1, K3 = K1 + 2;
  const int w = c.w;
  char* smem = c.smem;
  auto nop = [](auto) {};
  {
    const int lane = fresh_lane(c);
    const int g = lane >> 4, cc = lane & 15, mu0 = (w & 1) * 4, v16 = lane * 16;
    const char* ub = smem + L_U + (w >> 1) * L_UC + 512 + 2 * (16 * cc - 16 + 8 * g) - 64 * (mu0 + 3);
    KB kb;
#pragma unroll
    for (int j = 0; j < 4; ++j) {
      u32 a = lds_addr(smem) + (w >> 1) * 2 * L_KC + ((cc & 1) ? 0 : L_KC) + 2 * (2031 - cc + 8 * g - ((cc & 1) ? 0 : 1)) - 64 * (mu0 + 3 + 56) + 1024 * j;
      asm volatile("" : "+v"(a));
      kb.p[j] = (const char*)(const LAS char*)(size_t)a;
    }
    STAMP(K1, 1);
    f32x4 base0, base1;
    u32x4 a4[4];
    conv_ldk<0>(kb, a4);
    if (LAYER) { SCHED(); conv_landed(inA); }
    conv_ln(c, lane, inA, 0, base0, base1);
    if (!LAYER) poll0(c, lane, WS_F1);
    __syncthreads();
    STAMP(K1, 2);
    f32x4 acc[4];
    u32 o = 0;
    conv_mma<0, 0, true>(ub, kb, acc, a4, [&](auto N) {
      constexpr int n = N.v;
      if constexpr (LAYER && n == 19) { WAIT_VM(0); o = sig_begin(c, lane); SCHED(); }
      if constexpr (LAYER && n == 30) { sig_end<true, true>(c, lane, o, CT_X + 64); SCHED(); }
    });
    MFMA_DONE();
    conv_part(c, lane, acc);
    conv_ldk<1>(kb, a4);
    __syncthreads();
    float s1p[8];
    if (!LAYER) {
      if (!got0<0>(c.tag)) wait0(c, lane, WS_F1);
      asm volatile("" :: "v"(inB.x1));
      const int so1 = (min(c.ff0 * 16 + (lane & 15) + (lane >> 5) * 16, FF - 1) + (lane & 16) * (FF / 2)) * 4;
#pragma unroll
      for (int j = 0; j < 8; ++j)
        asm volatile("s_nop 4\n\tbuffer_load_dword %0, %1, %2, %3 offen sc1" : "=v"(s1p[j]) : "v"(so1), "s"(c.rs), "s"(WS_S1 + j * FF * 4) : "memory");
    }
    conv_out(c, lane, base0, base1, xmid, 0);
    STAMP(K1, 3);
    f32x4 acb[4];
    conv_mma<4, 0, true>(ub, kb, acb, a4, [&](auto N) {
      constexpr int n = N.v;
      if constexpr (!LAYER && n >= 48 && (n & 1)) { ld_w1<(n - 48) / 2>(c, v16, w1f); SCHED(); }
      if constexpr (n == 47) {
        WAIT_VM(0);
        o = sig_begin(c, lane);
        if (LAYER) poll<true>(c, lane, CT_X + 64);
        SCHED();
      }
      if constexpr (n == 58) { sig_end<true, true>(c, lane, o, CT_C); SCHED(); }
    });
    STAMP(K1, 4);
    if (!LAYER) {
#pragma unroll
      for (int j = 0; j < 8; ++j) asm volatile("" : "+v"(s1p[j]));
      const float sv = sum_x16(((s1p[0] + s1p[1]) + (s1p[2] + s1p[3])) + ((s1p[4] + s1p[5]) + (s1p[6] + s1p[7])));
      ((float*)(smem + L_CST))[w * 64 + lane] = (lane & 16) ? c.cs1 : sv;
    }
    if (LAYER) {
      if (!got<1>(32u)) wait_ge<true>(c, lane, CT_X + 64, 32u);
      conv_load(c, lane, inB, xin, stin, 1);
    }
    conv_ldk<0>(kb, a4);
    STAMP(K1, 5);
    if (!LAYER) sfor<8, 12>([&](auto N) { ld_w1<N.v>(c, v16, w1f); });
    conv_ln(c, lane, inB, 1, base0, base1);
    if (!LAYER) sfor<12, 14>([&](auto N) { ld_w1<N.v>(c, v16, w1f); });
    __syncthreads();
    conv_mma<0, 4, false>(ub, kb, acb, a4, [&](auto N) {
      constexpr int n = N.v;
      if constexpr (!LAYER && n % 3 == 1 && n < 36) { ld_w1<14 + n / 3>(c, v16, w1f); SCHED(); }
      if constexpr (n == 30) { poll<true>(c, lane, CT_C); SCHED(); }
    });
    MFMA_DONE();
    conv_part(c, lane, acb);
    __syncthreads();
    conv_out(c, lane, base0, base1, xmid, 1);
    STAMP(K1, 6);
    if (!got<(LAYER ? 2 : 4)>(32u * ep)) wait_ge<true>(c, lane, CT_C, 32u * ep);
    const int row = lane >> 3, sw = ((row >> 1) & 1) << 1;
    const int ve = row * 128 + ((lane & 7) ^ sw) * 16, vo = row * 128 + ((lane & 7) ^ (sw | 4)) * 16;
    if (!LAYER && c.b == 0 && w == 0) __hip_atomic_store((gu32*)(c.ws + WS_GEN), c.tag, RLX_AGENT);
    sfor<0, 8>([&](auto N) { g1_dma1<N.v>(c, ve, vo, L_XA, xmid + c.P * 65536); });
    WAIT_VM(8);
    o = sig_begin(c, lane);
    sig_end<true>(c, lane, o, CT_C + 64);
    STAMP(K1, 7);
  }
  u32x4 hf[14][2];
  u32x2 xr[2];
  {
    const int lane = fresh_lane(c);
    const int g = lane >> 4, cc = lane & 15, q = cc >> 2, p = lane & 3, v16 = lane * 16;
    const int sx = ((q >> 1) & 1) | ((g & 1) << 1);
    const char* xb[4];
#pragma unroll
    for (int i = 0; i < 4; ++i) xb[i] = smem + L_XA + (8 * g + q) * 128 + ((((i + w) & 3) ^ sx) * 32) + 8 * p;
    const int row = lane >> 3, sw = ((row >> 1) & 1) << 1;
    const int ve = row * 128 + ((lane & 7) ^ sw) * 16, vo = row * 128 + ((lane & 7) ^ (sw | 4)) * 16;
    sfor<8, 12>([&](auto N) { g1_dma1<N.v>(c, ve, vo, L_XA, xmid + c.P * 65536); });
    WAIT_VM_BAR(9);
    STAMP(K2, 0);
    f32x4 acc[2][4], sa;
    g1_mma(xb, w1f, acc, sa, [&](auto KS) {
      constexpr int ks = KS.v;
      if constexpr (ks == 3) { if (LAYER) WAIT_VM_BAR(8); else WAIT_VM_BAR(10); SCHED(); }
      if constexpr (ks == 7) { if (LAYER) WAIT_VM_BAR(0); else WAIT_VM_BAR(10); SCHED(); }
      if constexpr (ks == 13) { if (!LAYER) WAIT_VM(16); poll<true>(c, lane, CT_C + 64); SCHED(); }
    }, [&](auto N) {
      constexpr int n = N.v;
      if constexpr (n < 4) g1_dma1<12 + n>(c, ve, vo, L_XA, xmid + c.P * 65536);
      if constexpr (!LAYER && n >= 4 && n < 10) ld_w1<22 + n>(c, v16, w1f);
      if constexpr (!LAYER && n >= 10) ld_w2<n - 10>(c, v16, w2f);
    });
    STAMP(K2, 1);
    const int xo = xmid + (16 + c.P) * 65536;
    g1_epi(c, lane, acc, sa, hbuf + (c.P * 4 * 56 + c.hks) * 1024, [&](auto N) {
      constexpr int n = N.v;
      if constexpr (n == -1) { if (!got<(LAYER ? 0 : 6)>(32u * ep)) wait_ge<true>(c, lane, CT_C + 64, 32u * ep); }
      if constexpr (n >= 0 && n < 8) g1_dma1<n>(c, ve, vo, L_XB, xo);
      if constexpr (n >= 10 && n < 18) g1_dma1<n - 2>(c, ve, vo, L_XB, xo);
    });
    STAMP(K2, 2);
  }
  {
    const int lane = fresh_lane(c);
    const int g = lane >> 4, cc = lane & 15, q = cc >> 2, p = lane & 3, v16 = lane * 16;
    const int sx = ((q >> 1) & 1) | ((g & 1) << 1);
    const char* xb[4];
#pragma unroll
    for (int i = 0; i < 4; ++i) xb[i] = smem + L_XB + (8 * g + q) * 128 + ((((i + w) & 3) ^ sx) * 32) + 8 * p;
    WAIT_VM_BAR(12);
    STAMP(K2, 3);
    f32x4 acc[2][4], sa;
    g1_mma(xb, w1f, acc, sa, [&](auto KS) {
      constexpr int ks = KS.v;
      if constexpr (ks == 5) {
        if (LAYER) WAIT_VM_BAR(0); else WAIT_VM_BAR(10);
        sig_bar<false>(c, lane, CT_H + c.P);
        SCHED();
      }
      if constexpr (ks == 15) {
#pragma unroll
        for (int i = 0; i < 2; ++i) xr[i] = ld64(c.rs, (c.ct_ * 64 + w * 16 + cc) * 128 + i * 32 + g * 8, xmid + c.P * 65536 + c.sub * 64);
        SCHED();
      }
    }, [&](auto N) {
      constexpr int n = N.v;
      if constexpr (!LAYER) ld_w2<22 + n>(c, v16, w2f);
      if constexpr (!LAYER && n >= 30) ld_w2<24 + n>(c, v16, w2f);
    });
    STAMP(K2, 4);
    poll<false>(c, lane, CT_H + c.P);
    const int ho = hbuf + ((c.P * 4 + c.sub * 2) * 56 + 14 * w) * 1024;
    g1_epi(c, lane, acc, sa, hbuf + ((16 + c.P) * 4 * 56 + c.hks) * 1024, [&](auto N) {
      constexpr int n = N.v;
      if constexpr (n == -1) { if (!got<0>(16u * ep)) wait_ge<false>(c, lane, CT_H + c.P, 16u * ep); }
      if constexpr (n >= 0) g2_ldh1<n>(c, v16, hf, ho);
    });
    STAMP(K2, 5);
  }
  {
    const int lane = fresh_lane(c);
    const int g = lane >> 4, cc = lane & 15;
    const int ho = hbuf + ((c.P * 4 + c.sub * 2) * 56 + 14 * w) * 1024, hoB = ho + 64 * 56 * 1024;
    STAMP(K3, 0);
    f32x4 acc[2][4];
    u32 o = 0;
    g2_mma<20, 1>(c, lane, hf, w2f, acc, ho, 0, [&](auto S) {
      constexpr int s = S.v;
      if constexpr (s == 11) { WAIT_VM(10); o = sig_begin(c, lane); SCHED(); }
      if constexpr (s == 13) { sig_end<false>(c, lane, o, CT_H + 16 + c.P); SCHED(); }
    });
    STAMP(K3, 1);
    poll<false>(c, lane, CT_H + 16 + c.P);
    g2_epi<LAYER == 1>(c, lane, acc, xr, 0, xnext + c.P * 65536 + c.sub * 32768, nop);
    STAMP(K3, 2);
    if (!got<(LAYER ? 1 : 2)>(16u * ep)) wait_ge<false>(c, lane, CT_H + 16 + c.P, 16u * ep);
    sfor<0, 8>([&](auto N) { g2_ldh1<N.v>(c, lane * 16, hf, hoB); });
#pragma unroll
    for (int i = 0; i < 2; ++i) xr[i] = ld64(c.rs, (c.ct_ * 64 + w * 16 + cc) * 128 + i * 32 + g * 8, xmid + (16 + c.P) * 65536 + c.sub * 64);
    if (LAYER == 0) {
      WAIT_VM(11);
      o = sig_begin(c, lane);
      sig_end<true>(c, lane, o, CT_X);
    }
    SCHED();
    STAMP(K3, 3);
  }
  {
    const int lane = fresh_lane(c);
    const int hoB = hbuf + (((16 + c.P) * 4 + c.sub * 2) * 56 + 14 * w) * 1024;
    f32x4 acc[2][4];
    g2_mma<8, 2>(c, lane, hf, w2f, acc, hoB, 1, [&](auto S) {
      constexpr int s = S.v;
      if constexpr (LAYER == 0 && s == 27) { poll<true>(c, lane, CT_X); SCHED(); }
    });
    STAMP(K3, 4);
    g2_epi<LAYER == 1>(c, lane, acc, xr, 1, xnext + (16 + c.P) * 65536 + c.sub * 32768, [&](int) {
      if (LAYER == 0) {
        if (!got<0>(32u)) wait_ge<true>(c, lane, CT_X, 32u);
        conv_load_u(c, lane, inA, xnext, WS_ST + 65536, 0);
        SCHED();
      }
    });
    STAMP(K3, 5);
  }
}

constexpr int KG_RS = 528, KG_X = 16 * KG_RS, KG_CH = 64 * KG_RS + 1296;
__global__ void __launch_bounds__(256, 1) main_kernel(const u16* x, const u16* lar, const u16* aim, const u16* cre,
                                                      const u16* cim, const u16* ldt, const u16* dsk, const u16* w1,
                                                      const u16* b1, const u16* w2, const u16* b2, char* ws, u16* out) {
  extern __shared__ __attribute__((aligned(16))) char smem[];
  const int t = threadIdx.x, lane = t & 63, b = blockIdx.x;
  const u32 junk = warm_code((u32)(b >> 3) * 2048u + (t & ~63) * 8 + (t & 7) * 64);
  const int w = __builtin_amdgcn_readfirstlane(t >> 6);
  STAMP(0, 0);
  const int ch = w >> 1, d = 2 * b + ch, n = t & 127;
  const u32 po = (u32)(d * NST + n) * 2, po2 = po ^ 128u;
#define LD16(p, off) (*(const u16*)((const char*)(p) + (u32)(off)))
  const u16 r0 = ldt[d], r1 = LD16(lar, po), r2 = LD16(aim, po), r3 = LD16(cre, po), r4 = LD16(cim, po);
  const u16 r5 = LD16(cre, po2), r6 = LD16(cim, po2);
  const u32 gen = __hip_atomic_load((gu32*)(ws + WS_GEN), RLX_AGENT);
  const bool early = !(b & 64);
  const int pks = (b & 7) + (b >> 7) * 8, pcg = (b >> 3) & 7;
  u32x4 rv[8], rw[2];
  {
    const rsrc_t rr = __builtin_amdgcn_make_buffer_rsrc((void*)(early ? x : w1), 0, early ? SEQ * DM * 2 : DM * FF * 2, 0x00020000);
    const int vo = early ? w * 1024 + lane * 16 : ((b & 63) < 56 ? (t >> 5) * (FF * 2) + (t & 31) * 16 : 0x40000000);
    const int so = early ? (b & 63) * 32768 : pks * (32 * FF * 2) + min(pcg, 6) * 512, st = early ? 4096 : 8 * FF * 2;
#pragma unroll
    for (int j = 0; j < 8; ++j) rv[j] = __builtin_amdgcn_raw_buffer_load_b128(rr, (j < 4 || early) ? vo : 0x40000000, so + (j & 3) * st + (j >> 2) * (early ? 16384 : 0), 0);
    const rsrc_t r2w = __builtin_amdgcn_make_buffer_rsrc((void*)w2, 0, DM * FF * 2, 0x00020000);
#pragma unroll
    for (int j = 0; j < 2; ++j) rw[j] = __builtin_amdgcn_raw_buffer_load_b128(r2w, (t >> 4) * 1024 + (t & 15) * 16 + j * 16384, min(b >> 2, 55) * 32768 + (b & 3) * 256, 0);
  }
  CIn inA, inB;
  Ctx c;
  c.rs = __builtin_amdgcn_make_buffer_rsrc(ws, 0, WS_END, 0x00020000);
  c.ct = (gu32*)(ws + WS_CT);
  c.smem = smem; c.ws = ws; c.out = out;
  c.lane0 = lane; c.w = w; c.b = b;
  c.P = (b & 7) * 2 + ((b >> 3) & 1);
  const int idx = b >> 4;
  const int sg = idx >> 1, w3 = -(int)(w == 3);
  c.eo = idx & 1; c.sub = idx >> 3; c.ct_ = idx & 7;
  c.ff0 = 14 * sg + (((6 * c.eo + 2 * w) & ~w3) | ((12 + c.eo) & w3));
  c.hks = 7 * sg + (((3 * c.eo + w) & ~w3) | (6 & w3));
  c.f1o = w < 3 ? WS_W1P + c.ff0 * 16384 + 16384 : WS_ONE; c.f1s = w < 3 ? 4096 : 0;
  {
    const u32 r7 = *(const u32*)(dsk + 2 * b);
    u32 r8 = LD16(b1, min(c.ff0 * 16 + (lane & 15) + (lane >> 5) * 16, FF - 1) * 2), r9 = LD16(b2, (c.ct_ * 64 + w * 16 + (lane & 15)) * 2);
    SCHED();
    char* kg = smem + L_XA + ch * KG_CH;
    char* stg = smem + L_XA + 2 * KG_CH;
    const int i = lane & 15, g = lane >> 4;
    int ix[9];
#pragma unroll
    for (int e2 = 0; e2 < 9; ++e2) { ix[e2] = 2 * cidx(max(2047 - 8 * t - e2, 0)); asm volatile("" : "+v"(ix[e2])); }
    int wi[2][4];
    {
      const int m8 = -(i >> 3), s1 = -(w & 1);
      const int h1 = ((256 + 128 * (i >> 2) + 2 * (i & 3)) & ~m8) | ((128 * (i >> 1) + 4 * (i & 1)) & m8);
      const int hp = (((i + (i & 8) * 15) & ~s1) | (h1 & s1)) + 32 * g, hq = 1024 + 128 * i + 32 * g;
#pragma unroll
      for (int r = 0; r < 4; ++r) {
        wi[0][r] = 2 * cidx(hp + 8 * r); wi[1][r] = 2 * cidx(hq + 8 * r > 2048 ? 2064 : hq + 8 * r);
        asm volatile("" : "+v"(wi[0][r]), "+v"(wi[1][r]));
      }
    }
    if (t < 16) *(u32x4*)(smem + (t >> 2) * L_KC + 4096 + (t & 3) * 16) = u32x4{0, 0, 0, 0};
    if (t >= 64 && t < 128) *(u32x4*)(smem + L_U + ((t - 64) >> 5) * L_UC + (t & 31) * 16) = u32x4{0, 0, 0, 0};
    if (t == 128) *(u32*)(smem + L_SIG) = 0u;
    if (w >= 2) poll0(c, lane, w == 2 ? WS_F1 : WS_F0);
    SCHED();
    STAMP(0, 1);
    const float dt = rbf(expf(bf2f(r0)));
    const float cr = bf2f(r3), ci = bf2f(r4), cr2 = bf2f(r5), ci2 = bf2f(r6);
    const float S = wave_sum(sqrt_(cr * cr + ci * ci) + sqrt_(cr2 * cr2 + ci2 * ci2)) * (2.002f * dt);
    int e = __builtin_amdgcn_frexp_expf(S);
    e = (S > 0.0f && S < 3.0e38f) ? max(-100, min(100, e)) : 14;
    const float scale = __builtin_amdgcn_ldexpf(2.0f, 14 - e);
    if (n == 0) *(float*)(smem + L_MISC + ch * 4) = __builtin_amdgcn_ldexpf(1.0f, e - 14);
    const float a2 = (-rbf(expf(bf2f(r1))) * dt) * 1.44269504088896f;
    const float br = (bf2f(r2) * dt) * 0.159154943091895f;
    {
      char* yp = kg + n * 4;
      char* xp = kg + KG_X + n * 4;
      const CM e128 = cexp_(a2, -br, 128.f), e8 = cexp_(a2, br, 8.f), e1 = cexp_(a2, -br, 1.f), e2 = cexp_(a2, -br, 2.f);
      const CM e4 = cexp_(a2, -br, 4.f);
      f32x2 f0 = {(cr * dt) * scale, -(ci * dt) * scale};
      f32x2 f = cmul(f0, cexp_(a2, -br, 512.f)), y = {1.f, 0.f};
      f32x2 g1 = cmul(f0, e128), g2 = cmul(g1, e128), g3 = cmul(g2, e128);
#define PUT(base, row, v) *(u32*)(base + (row) * KG_RS) = pack_h(v[0], v[1])
      auto chain = [&](auto K) {
        constexpr int k = K.v;
        PUT(yp, k, y);
        if (k < 15) y = cmul(y, e8);
        if (k < 4) { const f32x2 q4 = cmul(f, e4); PUT(xp, 24 + 2 * k, f); PUT(xp, 25 + 2 * k, q4); }
        else if (k < 13) PUT(xp, 28 + k, f);
        if (k < 12) f = cmul(f, e128);
        if (k < 8) { PUT(xp, k, f0); PUT(xp, 8 + k, g1); }
        if (k < 7) { f0 = cmul(f0, e1); g1 = cmul(g1, e1); }
        if (k >= 8 && k < 12) { PUT(xp, 8 + k, g2); PUT(xp, 12 + k, g3); }
        if (k >= 8 && k < 11) { g2 = cmul(g2, e2); g3 = cmul(g3, e2); }
      };
      c.tag = __builtin_amdgcn_readfirstlane(gen) * 0x9E3779B1u + 0x7F4A7C15u;
      sfor<0, 2>(chain);
      asm volatile("" : "+v"(y), "+v"(f), "+v"(f0), "+v"(g1));
      if (b < 64) xpose(b, rv, c.rs, stg);
      else if (early) row_stats(b & 63, rv, c.rs);
      asm volatile("" : "+v"(y), "+v"(f), "+v"(f0), "+v"(g1));
      sfor<2, 12>(chain);
      asm volatile("" : "+v"(y), "+v"(f));
      if (early) {
        WAIT_VM(0);
        __syncthreads();
        if (w == 0 && lane < 8)
          __hip_atomic_store((gu32*)(ws + WS_F0 + lane * 2048 + (b & 7) * 256 + ((b >> 3) & 7) * 8 + (b >> 7) * 4), c.tag, RLX_AGENT);
      }
      asm volatile("" : "+v"(y), "+v"(f));
      STAMP(0, 2);
      sfor<12, 16>(chain);
      STAMP(0, 3);
      if (!early && pcg < 7) pack_w1(pks, pcg, rv, c.rs, stg);
#undef PUT
    }
    pack_w2_put(rw, smem + L_XB + 55552);
    __syncthreads();
    STAMP(0, 4);
    {
      const char* ya = kg + i * KG_RS + g * 16;
      const char* xa = kg + KG_X + (16 * (w & 1) + i) * KG_RS + g * 16;
      const char* xc = kg + KG_X + (32 + i) * KG_RS + g * 16;
      u32x4 fa[8], fb[8], fc[8];
#pragma unroll
      for (int ks = 0; ks < 8; ++ks) { fa[ks] = *(const u32x4*)(ya + ks * 64); fb[ks] = *(const u32x4*)(xa + ks * 64); }
#pragma unroll
      for (int ks = 0; ks < 8; ++ks) fc[ks] = *(const u32x4*)(xc + ks * 64);
      f32x4 ac0, ac1;
      SCHED();
      sfor<0, 8>([&](auto KS) { mma_hh<KS.v == 0>(ac0, fa[KS.v], fb[KS.v]); });
      sfor<0, 8>([&](auto KS) { mma_hh<KS.v == 0>(ac1, fa[KS.v], fc[KS.v]); });
      MFMA_DONE();
#pragma unroll
      for (int r = 0; r < 4; ++r) {
        *(u16*)(kg + 64 * KG_RS + wi[0][r]) = __builtin_bit_cast(u16, (_Float16)ac0[r]);
        *(u16*)(kg + 64 * KG_RS + wi[1][r]) = __builtin_bit_cast(u16, (_Float16)ac1[r]);
      }
    }
    if (!early) WAIT_VM(0);
    __syncthreads();
    if (w == 1 && !early && lane < 8)
      __hip_atomic_store((gu32*)(ws + WS_F1 + lane * 2048 + (b & 7) * 256 + ((b >> 3) & 7) * 8 + (b >> 7) * 4), c.tag, RLX_AGENT);
    if (w == 0) poll0(c, lane, WS_F0);
#pragma unroll
    for (int c2 = 0; c2 < 2; ++c2) {
      u32 v[9];
#pragma unroll
      for (int e2 = 0; e2 < 9; ++e2) v[e2] = *(const u16*)(smem + L_XA + c2 * KG_CH + 64 * KG_RS + ix[e2]);
      if (t == 255) v[8] = 0u;
      u32x4 oe, oo;
#pragma unroll
      for (int j = 0; j < 4; ++j) { oe[j] = v[2 * j] | (v[2 * j + 1] << 16); oo[j] = v[2 * j + 1] | (v[2 * j + 2] << 16); }
      *(u32x4*)(smem + c2 * 2 * L_KC + t * 16) = oe;
      *(u32x4*)(smem + c2 * 2 * L_KC + L_KC + t * 16) = oo;
    }
    const f32x2 ksc = *(const f32x2*)(smem + L_MISC);
    c.ks0 = __uint_as_float(__builtin_amdgcn_readfirstlane(__float_as_uint(ksc[0])));
    c.ks1 = __uint_as_float(__builtin_amdgcn_readfirstlane(__float_as_uint(ksc[1])));
    const u32 ds = __builtin_amdgcn_readfirstlane(r7);
    c.dsk0 = lo_bf(ds); c.dsk1 = hi_bf(ds);
    c.cs1 = lo_bf(r8); c.bb2 = lo_bf(r9);
    asm volatile("" :: "v"(junk));
    STAMP(0, 5);
    if (w == 0 && !got0<0>(c.tag)) wait0(c, lane, WS_F0);
    __syncthreads();
    conv_load(c, lane, inA, WS_X, WS_ST, 0);
    conv_load(c, lane, inB, WS_X, WS_ST, 1);
    pack_w2_out(b, c.rs, smem + L_XB + 55552);
    STAMP(0, 6);
  }
  u32x4 w1f[2][16], w2f[14][4];
  layer_body<0>(c, w1f, w2f, inA, inB);
  layer_body<1>(c, w1f, w2f, inA, inB);
}

extern "C" __global__ void code_end_marker() {}

extern "C" void kernel_launch(void* const* d_in, const int* in_sizes, int n_in, void* d_out, int out_size,
                              void* d_ws, size_t ws_size, hipStream_t stream) {
  (void)in_sizes; (void)n_in; (void)out_size; (void)ws_size;
  char* ws = (char*)d_ws + ((ws_size - (size_t)WS_END) & ~(size_t)(2 * MB - 1));
  (void)hipFuncSetAttribute((const void*)main_kernel, hipFuncAttributeMaxDynamicSharedMemorySize, MAIN_LDS);
  main_kernel<<<256, 256, MAIN_LDS, stream>>>((const u16*)d_in[0], (const u16*)d_in[1], (const u16*)d_in[2],
                                              (const u16*)d_in[3], (const u16*)d_in[4], (const u16*)d_in[5],
                                              (const u16*)d_in[6], (const u16*)d_in[8], (const u16*)d_in[9],
                                              (const u16*)d_in[10], (const u16*)d_in[11], ws, (u16*)d_out);
}
```

```cpp
#include <hip/hip_runtime.h>
#include <hip/hip_bf16.h>

typedef unsigned short u16;
typedef unsigned int u32;
typedef long long i64;
typedef __attribute__((ext_vector_type(4))) short s16x4;
typedef __attribute__((ext_vector_type(8))) __bf16 bf16x8;
typedef __attribute__((ext_vector_type(2))) __bf16 bf16x2;
typedef __attribute__((ext_vector_type(8))) _Float16 f16x8;
typedef __attribute__((ext_vector_type(2))) _Float16 f16x2;
typedef __attribute__((ext_vector_type(4))) float f32x4;
typedef __attribute__((ext_vector_type(2))) float f32x2;
typedef __attribute__((ext_vector_type(4))) unsigned u32x4;
typedef __attribute__((ext_vector_type(2))) unsigned u32x2;
typedef __attribute__((ext_vector_type(2))) long long i64x2;
typedef __attribute__((address_space(1))) unsigned gu32;
typedef __amdgpu_buffer_rsrc_t rsrc_t;
#define LAS __attribute__((address_space(3)))
#define DEVI __device__ __forceinline__
template <int V> struct IC { static constexpr int v = V; };
template <int I, int N, class F> __device__ __forceinline__ void sfor(F&& f) {
  if constexpr (I < N) { f(IC<I>{}); sfor<I + 1, N>(f); }
}
#define RLX_AGENT __ATOMIC_RELAXED, __HIP_MEMORY_SCOPE_AGENT

constexpr int SEQ = 2048, DM = 512, NST = 128, FF = 1792;
constexpr float LN_EPS = 1e-5f;
constexpr float FX_SCALE = 16777216.0f;
constexpr float FX_INV = 1.0f / 16777216.0f;
constexpr int SC1 = 16;

constexpr int MB = 1024 * 1024;
constexpr int WS_X = 0;
constexpr int WS_W1P = 12 * MB;
constexpr int WS_W2P = 14 * MB;
constexpr int WS_H = 16 * MB;
constexpr int WS_ST = 32 * MB;
constexpr int WS_S1 = 33 * MB;
constexpr int WS_GEN = 32 * MB + 196608;
constexpr int WS_F0 = WS_GEN + 256, WS_F1 = WS_F0 + 16384;
constexpr int WS_CT = 32 * MB + 262144;
constexpr int WS_ONE = 32 * MB + 393216;
constexpr int WS_END = 34 * MB;
constexpr int CT_C = 0, CT_X = 128, CT_H = 256, CT_TMO = 288;
constexpr int SPIN_CAP = 1 << 16;

#define STAMP(k, i) do {} while (0)

DEVI float bf2f(u16 v) { return __uint_as_float((u32)v << 16); }
DEVI float lo_bf(u32 w) { return __uint_as_float(w << 16); }
DEVI float hi_bf(u32 w) { return __uint_as_float(w & 0xffff0000u); }
DEVI float rbf(float x) { return (float)(__bf16)x; }
DEVI u32 pack_bf(float a, float b) { f32x2 q = {a, b}; return __builtin_bit_cast(u32, __builtin_convertvector(q, bf16x2)); }
DEVI u32 pack_h(float a, float b) { f32x2 q = {a, b}; return __builtin_bit_cast(u32, __builtin_convertvector(q, f16x2)); }
DEVI float dot2bf(u32 a, u32 b, float c) {
  return __builtin_amdgcn_fdot2_f32_bf16(__builtin_bit_cast(bf16x2, a), __builtin_bit_cast(bf16x2, b), c, false);
}
DEVI f32x4 mfma_bf(u32x4 a, u32x4 b, f32x4 c) {
  return __builtin_amdgcn_mfma_f32_16x16x32_bf16(__builtin_bit_cast(bf16x8, a), __builtin_bit_cast(bf16x8, b), c, 0, 0, 0);
}
DEVI f32x4 mfma_h(u32x4 a, u32x4 b, f32x4 c) {
  return __builtin_amdgcn_mfma_f32_16x16x32_f16(__builtin_bit_cast(f16x8, a), __builtin_bit_cast(f16x8, b), c, 0, 0, 0);
}
DEVI u32x2 tr_read(const char* p) {
  return __builtin_bit_cast(u32x2, __builtin_amdgcn_ds_read_tr16_b64_v4i16((LAS s16x4*)p));
}
DEVI u32x4 cat2(u32x2 a, u32x2 b) { return __builtin_shufflevector(a, b, 0, 1, 2, 3); }
DEVI u32 lds_addr(const void* p) { return (u32)(size_t)(const LAS char*)p; }
#define WAIT_VM(n) asm volatile("s_waitcnt vmcnt(" #n ")" ::: "memory")
#define WAIT_VM_BAR(n) asm volatile("s_waitcnt vmcnt(" #n ")\n\ts_barrier" ::: "memory")
#define SCHED() __builtin_amdgcn_sched_barrier(0)
template <int CTRL> DEVI float dpp_add(float v) {
  return v + __uint_as_float(__builtin_amdgcn_update_dpp(0u, __float_as_uint(v), CTRL, 0xf, 0xf, true));
}
DEVI float sum_x16(float v) {
  auto r = __builtin_amdgcn_permlane16_swap(__float_as_uint(v), __float_as_uint(v), false, false);
  return __uint_as_float(r[0]) + __uint_as_float(r[1]);
}
DEVI float sum_x32(float v) {
  auto r = __builtin_amdgcn_permlane32_swap(__float_as_uint(v), __float_as_uint(v), false, false);
  return __uint_as_float(r[0]) + __uint_as_float(r[1]);
}
DEVI float wave_sum(float v) {
  v = dpp_add<0xB1>(v); v = dpp_add<0x4E>(v); v = dpp_add<0x141>(v); v = dpp_add<0x140>(v);
  return sum_x32(sum_x16(v));
}
DEVI float fx2f(u32 lo, u32 hi) { return fmaf((float)(int)hi, 4294967296.0f * FX_INV, (float)lo * FX_INV); }
DEVI i64 f2fx(float v) {
  const float a = v * (FX_SCALE / 4294967296.0f), h = __builtin_floorf(a);
  return __builtin_bit_cast(i64, u32x2{(u32)((a - h) * 4294967296.0f), (u32)(int)h});
}
DEVI float sel4(int i, float a, float b, float c, float d) {
  asm volatile("" : "+v"(a), "+v"(b), "+v"(c), "+v"(d));
  return (i & 2) ? ((i & 1) ? d : c) : ((i & 1) ? b : a);
}
DEVI int st_pos(int t) { return ((t >> 8) * 4 + (t & 3)) * 1024 + ((t >> 2) & 63) * 16; }
DEVI float rcp(float x) { return __builtin_amdgcn_rcpf(x); }
DEVI float sqrt_(float x) { return __builtin_amdgcn_sqrtf(x); }

DEVI u32 warm_code(int slice, int lane) {
  unsigned long long pc, pe;
  asm volatile("s_getpc_b64 %0\n\ts_getpc_b64 s[90:91]\n\ts_add_u32 s90, s90, code_end_marker@rel32@lo+4\n\t"
               "s_addc_u32 s91, s91, code_end_marker@rel32@hi+12\n\ts_mov_b64 %1, s[90:91]"
               : "=&s"(pc), "=s"(pe) :: "scc", "s90", "s91");
  const u32 off = min((u32)slice * 512u, (u32)(pe - pc) - 512u) + (lane & 7) * 64;
  u32 junk;
  asm volatile("s_nop 4\n\tglobal_load_dword %0, %1, %2" : "=v"(junk) : "v"(off), "s"(pc) : "memory");
  return junk;
}

DEVI int cidx(int l) {
  const int hb = (int)(pack_bf((float)l, 0.f) & 0xffffu) - (0x4300 - 128);
  return l < 128 ? l : hb;
}
struct CM { f32x2 e, f; };
DEVI f32x2 cmul(f32x2 a, const CM& m) { return a[1] * m.f + a[0] * m.e; }
DEVI CM cexp_(float a2, float br, float p) {
  const float e = __builtin_amdgcn_exp2f(a2 * p), f = __builtin_amdgcn_fractf(br * p);
  const float re = e * __builtin_amdgcn_cosf(f), im = e * __builtin_amdgcn_sinf(f);
  CM r; r.e = f32x2{re, im}; r.f = f32x2{-im, re};
  return r;
}
DEVI void st16(rsrc_t rs, int off, u32x4 v) { __builtin_amdgcn_raw_buffer_store_b128(v, rs, off, 0, SC1); }

DEVI void xpose(int b, const u32x4 (&v)[8], rsrc_t rs, char* smem) {
  const int t = threadIdx.x, lane = t & 63, w = t >> 6;
  constexpr int RS = 1024 + 32;
#pragma unroll
  for (int r = 0; r < 8; ++r) *(u32x4*)(smem + (w + 4 * r) * RS + lane * 16) = v[r];
  __syncthreads();
  const int g = lane >> 4, q = (lane & 15) >> 2, p = lane & 3;
#pragma unroll
  for (int cf = 0; cf < 8; ++cf) {
    const int c0 = (w * 8 + cf) * 16;
    const char* pa = smem + (8 * g + q) * RS + c0 * 2 + 8 * p;
    const u32x4 o = cat2(tr_read(pa), tr_read(pa + 4 * RS));
    st16(rs, WS_X + b * 32768 + (c0 + (lane & 15)) * 64 + g * 16, o);
  }
}
DEVI void row_stats(int b, const u32x4 (&v)[8], rsrc_t rs) {
  const int t = threadIdx.x, lane = t & 63, w = t >> 6;
  const int t0 = b * 32;
  float a[16];
#pragma unroll
  for (int r = 0; r < 8; ++r) {
    float s = 0.f, ss = 0.f;
#pragma unroll
    for (int j = 0; j < 4; ++j) { s = dot2bf(v[r][j], 0x3f803f80u, s); ss = dot2bf(v[r][j], v[r][j], ss); }
    a[2 * r] = s; a[2 * r + 1] = ss;
  }
  const bool o1 = lane & 1, o2 = lane & 2;
#pragma unroll
  for (int k = 0; k < 8; ++k) {
    const float keep = o1 ? a[2 * k + 1] : a[2 * k], send = o1 ? a[2 * k] : a[2 * k + 1];
    a[k] = keep + __uint_as_float(__builtin_amdgcn_update_dpp(0u, __float_as_uint(send), 0xB1, 0xf, 0xf, true));
  }
#pragma unroll
  for (int k = 0; k < 4; ++k) {
    const float keep = o2 ? a[2 * k + 1] : a[2 * k], send = o2 ? a[2 * k] : a[2 * k + 1];
    a[k] = keep + __uint_as_float(__builtin_amdgcn_update_dpp(0u, __float_as_uint(send), 0x4E, 0xf, 0xf, true));
  }
#pragma unroll
  for (int k = 0; k < 4; ++k) a[k] = sum_x32(sum_x16(dpp_add<0x128>(dpp_add<0x124>(a[k]))));
  const i64 sv = f2fx(sel4(lane >> 2, a[0], a[1], a[2], a[3]));
  if (lane < 16)
    __builtin_amdgcn_raw_buffer_store_b64(__builtin_bit_cast(u32x2, sv), rs, st_pos(t0 + w) + lane * 8, WS_ST, SC1);
  if (t >= 96 && t < 128) st16(rs, WS_ST + 65536 + b * 512 + (t - 96) * 16, u32x4{0, 0, 0, 0});
  if (t < 80) st16(rs, WS_CT + b * 1280 + t * 16, u32x4{0, 0, 0, 0});
}

DEVI void pack_w1(int ks, int cg, const u32x4 (&v)[8], rsrc_t rs, char* smem) {
  const int t = threadIdx.x, lane = t & 63, w = t >> 6;
  constexpr int RS = 512 + 32;
#pragma unroll
  for (int it = 0; it < 4; ++it) *(u32x4*)(smem + (it * 8 + (t >> 5)) * RS + (t & 31) * 16) = v[it];
  __syncthreads();
  const int g = lane >> 4, q = (lane & 15) >> 2, p = lane & 3;
  float cs[4];
#pragma unroll
  for (int h = 0; h < 4; ++h) {
    const char* a = smem + (8 * g + q) * RS + (w * 4 + h) * 32 + 8 * p;
    const u32x4 o = cat2(tr_read(a), tr_read(a + 4 * RS));
    st16(rs, WS_W1P + (((cg * 16 + w * 4 + h) * 16 + ks) * 64 + lane) * 16, o);
    float s = 0.f;
#pragma unroll
    for (int j = 0; j < 4; ++j) s = dot2bf(o[j], 0x3f803f80u, s);
    cs[h] = sum_x32(sum_x16(s));
  }
  __builtin_amdgcn_raw_buffer_store_b32(__float_as_uint(sel4(g, cs[0], cs[1], cs[2], cs[3])), rs, (ks * FF + cg * 256 + t) * 4, WS_S1, SC1);
  if (ks == 0 && cg == 0) st16(rs, WS_ONE + t * 16, u32x4{0x3f803f80u, 0x3f803f80u, 0x3f803f80u, 0x3f803f80u});
}

constexpr int W2_RS = 256 + 32;
DEVI void pack_w2_put(const u32x4 (&v)[2], char* smem) {
  const int t = threadIdx.x;
#pragma unroll
  for (int r = 0; r < 2; ++r) *(u32x4*)(smem + (r * 16 + (t >> 4)) * W2_RS + (t & 15) * 16) = v[r];
}
DEVI void pack_w2_out(int b, rsrc_t rs, const char* smem) {
  const int t = threadIdx.x, lane = t & 63, w = t >> 6;
  const int g = lane >> 4, q = (lane & 15) >> 2, p = lane & 3;
#pragma unroll
  for (int cf = 0; cf < 2; ++cf) {
    const char* a = smem + (4 * g + q) * W2_RS + (w * 2 + cf) * 32 + 8 * p;
    const u32x4 o = cat2(tr_read(a), tr_read(a + 16 * W2_RS));
    st16(rs, b < 224 ? WS_W2P + ((((b & 3) * 8 + w * 2 + cf) * 56 + (b >> 2)) * 64 + lane) * 16 : 0x40000000, o);
  }
}

constexpr int L_KC = 4160;
constexpr int L_U = 4 * L_KC, L_UC = 4608;
constexpr int L_XA = 26112, L_XB = L_XA + 65536;
constexpr int L_MISC = L_XB + 65536;
constexpr int L_CST = L_MISC + 1024;
constexpr int L_SIG = L_CST + 4 * 256;
constexpr int MAIN_LDS = L_SIG + 256;
constexpr int L_RED = L_XB;

DEVI u32x4 ld128(rsrc_t rs, int v, int s) { return __builtin_amdgcn_raw_buffer_load_b128(rs, v, s, SC1); }
DEVI u32x2 ld64(rsrc_t rs, int v, int s) { return __builtin_amdgcn_raw_buffer_load_b64(rs, v, s, SC1); }
DEVI void st128(rsrc_t rs, int v, int s, u32x4 d) { __builtin_amdgcn_raw_buffer_store_b128(d, rs, v, s, SC1); }
DEVI void st64(rsrc_t rs, int v, int s, u32x2 d) { __builtin_amdgcn_raw_buffer_store_b64(d, rs, v, s, SC1); }
template <int OFF> DEVI u32x4 ld128a(rsrc_t rs, int v, int s) {
  u32x4 r;
  asm volatile("buffer_load_dwordx4 %0, %1, %2, %3 offen offset:%4 sc1" : "=a"(r) : "v"(v), "s"(rs), "s"(s), "i"(OFF) : "memory");
  return r;
}
template <int OFF> DEVI u32x4 ld128v(rsrc_t rs, int v, int s) {
  u32x4 r;
  asm volatile("buffer_load_dwordx4 %0, %1, %2, %3 offen offset:%4 sc1" : "=v"(r) : "v"(v), "s"(rs), "s"(s), "i"(OFF) : "memory");
  return r;
}
template <int OFF> DEVI void dma1k(rsrc_t rs, u32 lds_dst, int v, int soff) {
  asm volatile("s_mov_b32 m0, %0\n\ts_nop 0\n\tbuffer_load_dwordx4 %1, %2, %3 offen offset:%4 sc1 lds"
               :: "s"(lds_dst), "v"(v), "s"(rs), "s"(soff), "i"(OFF) : "memory");
}
#define MMA(NAME, OP, CA, CB)                                                                                   \
  template <bool Z> DEVI void NAME(f32x4& d, const u32x4& a, const u32x4& b) {                                  \
    if (Z) asm volatile(OP " %0, %1, %2, 0" : "=&v"(d) : CA(a), CB(b));                                         \
    else asm volatile(OP " %0, %1, %2, %0" : "+v"(d) : CA(a), CB(b));                                           \
  }
MMA(mma_vv, "v_mfma_f32_16x16x32_bf16", "v", "v")
MMA(mma_av, "v_mfma_f32_16x16x32_bf16", "a", "v")
MMA(mma_va, "v_mfma_f32_16x16x32_bf16", "v", "a")
MMA(mma_hh, "v_mfma_f32_16x16x32_f16", "v", "v")
#define MFMA_DONE() do { SCHED(); asm volatile("s_nop 7" ::: "memory"); SCHED(); } while (0)

struct Ctx {
  rsrc_t rs; gu32* ct; char* smem; char* ws; u16* out;
  int lane0, w, b, P, sub, ct_, ff0, hks, eo, f1o, f1s;
  float dsk0, dsk1, ks0, ks1, bb2, cs1;
  u32 tag;
};
DEVI int fresh_lane(const Ctx& c) { int l = c.lane0; asm volatile("" : "+v"(l)); return l; }
DEVI u32 sig_begin(const Ctx& c, int lane) {
  u32 old;
  asm volatile("ds_add_rtn_u32 %0, %1, %2" : "=v"(old) : "v"(lds_addr(c.smem) + L_SIG + lane * 4), "v"(1u) : "memory");
  return old;
}
template <bool ALL, bool BACK = false> DEVI void sig_end(const Ctx& c, int lane, u32 old, int line) {
  if (BACK) asm volatile("" : "+v"(old) :: "memory");
  else asm volatile("s_waitcnt lgkmcnt(0)" : "+v"(old) :: "memory");
  const bool last = (__builtin_amdgcn_readfirstlane(old) & 3u) == 3u;
  const int v = (last && lane < (ALL ? 8 : 1)) ? (ALL ? lane * 2048 : 0) : 0x40000000;
  asm volatile("s_nop 4\n\tbuffer_atomic_add %0, %1, %2, %3 offen" :: "v"(1u), "v"(v), "s"(c.rs),
               "s"(WS_CT + (line + (ALL ? (c.b >> 3) & 7 : 0)) * 256) : "memory");
}
template <bool ALL, int R = 0> DEVI void poll(const Ctx& c, int lane, int line) {
  const int v = ALL ? (lane & 7) * 256 : 0, so = WS_CT + (line + (ALL ? (c.b & 7) * 8 : 0)) * 256;
  if (R == 0) asm volatile("s_nop 4\n\tbuffer_load_dword v254, %0, %1, %2 offen sc1" :: "v"(v), "s"(c.rs), "s"(so) : "memory", "v254");
  else asm volatile("s_nop 4\n\tbuffer_load_dword v255, %0, %1, %2 offen sc1" :: "v"(v), "s"(c.rs), "s"(so) : "memory", "v255");
}
template <int YOUNGER, int R = 0> DEVI bool got(u32 want) {
  unsigned long long m;
  if (R == 0) asm volatile("s_waitcnt vmcnt(%2)\n\tv_cmp_le_u32_e64 %0, %1, v254" : "=s"(m) : "s"(want), "i"(YOUNGER) : "memory");
  else asm volatile("s_waitcnt vmcnt(%2)\n\tv_cmp_le_u32_e64 %0, %1, v255" : "=s"(m) : "s"(want), "i"(YOUNGER) : "memory");
  return m == ~0ull;
}
template <bool ALL> DEVI void wait_ge(const Ctx& c, int lane, int line, u32 want) {
  int sp = 0;
#pragma nounroll
  do {

    poll<ALL, 0>(c, lane, line);
    if (++sp > SPIN_CAP) { __hip_atomic_store(c.ct + CT_TMO * 64, 1u, RLX_AGENT); break; }
  } while (!got<0, 0>(want));
}

DEVI void poll0(const Ctx& c, int lane, int set) {
  asm volatile("s_nop 4\n\tbuffer_load_dwordx2 v[254:255], %0, %1, %2 offen sc1" :: "v"((lane & 7) * 256 + (lane >> 3) * 8), "s"(c.rs),
               "s"(set + (c.b & 7) * 2048) : "memory", "v254", "v255");
}
template <int YOUNGER> DEVI bool got0(u32 tag) {
  unsigned long long m, m2;
  asm volatile("s_waitcnt vmcnt(%3)\n\tv_cmp_eq_u32_e64 %0, %2, v254\n\tv_cmp_eq_u32_e64 %1, %2, v255"
               : "=&s"(m), "=s"(m2) : "s"(tag), "i"(YOUNGER) : "memory");
  return (m & m2) == ~0ull;
}
DEVI void wait0(const Ctx& c, int lane, int set) {
  int sp = 0;
#pragma nounroll
  do {
    poll0(c, lane, set);
    if (++sp > SPIN_CAP) { __hip_atomic_store(c.ct + CT_TMO * 64, 1u, RLX_AGENT); break; }
  } while (!got0<0>(c.tag));
}

struct CIn { u32x2 x0, x1; u32x4 s[4]; };
DEVI void conv_load(const Ctx& c, int lane, CIn& in, int xoff, int stoff, int h) {
  const int sx = xoff + (32 * h + 8 * c.w) * 32768 + c.b * 128;
  const int vx = (lane >> 3) * 32768 + (lane & 7) * 8;
  const int ss = stoff + (1024 * h + 256 * c.w) * 16;
#pragma unroll
  for (int j = 0; j < 4; ++j) in.s[j] = ld128(c.rs, lane * 16 + j * 1024, ss);
  in.x0 = ld64(c.rs, vx, sx);
  in.x1 = ld64(c.rs, vx + 64, sx);
}
DEVI void conv_load_u(const Ctx& c, int lane, CIn& in, int xoff, int stoff, int h) {
  const int sx = xoff + (32 * h + 8 * c.w) * 32768 + c.b * 128;
  const int vx = (lane >> 3) * 32768 + (lane & 7) * 8;
  const int ss = stoff + (1024 * h + 256 * c.w) * 16;
  asm volatile("s_nop 4\n\t"
               "buffer_load_dwordx4 %0, %6, %8, %9 offen sc1\n\tbuffer_load_dwordx4 %1, %6, %8, %9 offen offset:1024 sc1\n\t"
               "buffer_load_dwordx4 %2, %6, %8, %9 offen offset:2048 sc1\n\tbuffer_load_dwordx4 %3, %6, %8, %9 offen offset:3072 sc1\n\t"
               "buffer_load_dwordx2 %4, %7, %8, %10 offen sc1\n\tbuffer_load_dwordx2 %5, %7, %8, %10 offen offset:64 sc1"
               : "=&v"(in.s[0]), "=&v"(in.s[1]), "=&v"(in.s[2]), "=&v"(in.s[3]), "=&v"(in.x0), "=&v"(in.x1)
               : "v"(lane * 16), "v"(vx), "s"(c.rs), "s"(ss), "s"(sx) : "memory");
}
DEVI void conv_landed(CIn& in) {
  asm volatile("s_waitcnt vmcnt(2)" : "+v"(in.s[0]), "+v"(in.s[1]), "+v"(in.s[2]), "+v"(in.s[3]), "+v"(in.x0), "+v"(in.x1) :: "memory");
}
DEVI void conv_ln(const Ctx& c, int lane, const CIn& in, int h, f32x4& base0, f32x4& base1) {
  constexpr float CH = 4294967296.0f * FX_INV / DM, CL = FX_INV / DM;
  f32x4 m, r;
#pragma unroll
  for (int j = 0; j < 4; ++j) {
    m[j] = fmaf((float)(int)in.s[j][1], CH, (float)in.s[j][0] * CL);
    r[j] = fmaf((float)(int)in.s[j][3], CH, fmaf((float)in.s[j][2], CL, LN_EPS));
  }
#pragma unroll
  for (int j = 0; j < 4; ++j) r[j] = __builtin_amdgcn_rsqf(fmaxf(fmaf(-m[j], m[j], r[j]), LN_EPS));
  const f32x4 rr = r, mr = -m * r;
  const f32x4 x0 = {lo_bf(in.x0[0]), hi_bf(in.x0[0]), lo_bf(in.x0[1]), hi_bf(in.x0[1])};
  const f32x4 x1 = {lo_bf(in.x1[0]), hi_bf(in.x1[0]), lo_bf(in.x1[1]), hi_bf(in.x1[1])};
  const f32x4 u0 = x0 * rr + mr, u1 = x1 * rr + mr;
  char* up = c.smem + L_U + 512 + (1024 * h + 256 * c.w + 4 * lane) * 2;
  *(u32x2*)up = u32x2{pack_h(u0[0], u0[1]), pack_h(u0[2], u0[3])};
  *(u32x2*)(up + L_UC) = u32x2{pack_h(u1[0], u1[1]), pack_h(u1[2], u1[3])};
  base0 = u0 * c.dsk0 + x0; base1 = u1 * c.dsk1 + x1;
}
struct KB { const char* p[4]; };
template <int MM, int LAM> DEVI u32x4 conv_k1(const KB& kb) {
  const u32* kp = (const u32*)(kb.p[(7 - LAM) >> 1] + 64 * (3 - MM) + 512 * ((7 - LAM) & 1));
  return u32x4{kp[0], kp[1], kp[2], kp[3]};
}
template <int MM, int TB> DEVI u32x4 conv_u1(const char* ub) { return *(const u32x4*)(ub + 64 * (3 - MM) + 512 * TB); }
template <int LO> DEVI void conv_ldk(const KB& kb, u32x4 (&af)[4]) {
  sfor<0, 4>([&](auto L) { af[L.v] = conv_k1<0, LO + L.v>(kb); });
}
constexpr int conv_rank(int DT, int LO, int gr, int lt) {
  int k = 0;
  for (int x = 0; x < lt; ++x) { const int tb = DT + (x & 3) - (LO + 4 * gr + (x >> 2)); k += (tb >= 0 && tb < 4); }
  return k;
}
template <int DT, int TB0, bool ZERO, class F>
DEVI void conv_mma(const char* ub, const KB& kb, f32x4 (&acc)[4], const u32x4 (&af0)[4], F&& hook) {
  constexpr int NG = DT ? 2 : 1, LO = DT ? 1 : 0;
  u32x4 af[2][4], bf[2][4];
  sfor<0, 4>([&](auto L) { af[0][L.v] = af0[L.v]; });
  sfor<0, 4>([&](auto T) { constexpr int tb = DT ? 3 - T.v : T.v; bf[0][tb] = conv_u1<0, TB0 + tb>(ub); });
  SCHED();
  sfor<0, 4 * NG>([&](auto ST) {
    constexpr int st = ST.v, mm = st / NG, gr = st % NG;
    constexpr bool more = st + 1 < 4 * NG;
    constexpr int mn = (st + 1) / NG, gn = (st + 1) % NG;
    constexpr bool nu = more && gr == NG - 1;
    sfor<0, 16>([&](auto LT) {
      constexpr int l = LT.v >> 2, T = LT.v & 3, tb = DT + T - (LO + 4 * gr + l);
      if constexpr (tb >= 0 && tb < 4) {
        constexpr int k = conv_rank(DT, LO, gr, LT.v);
        constexpr bool first = ZERO && st == 0 && (DT ? (l == T) : (l == 0));
        mma_hh<first>(acc[T], af[st & 1][l], bf[mm & 1][tb]);
        sfor<(gr == 1 ? 2 * k : k), (gr == 1 ? 2 * k + 2 : k + 1)>([&](auto J) {
          constexpr int j = J.v;
          if constexpr (nu && j < 8) {
            if constexpr (DT != 0) {
              if constexpr (j & 1) bf[mn & 1][3 - (j >> 1)] = conv_u1<mn, TB0 + 3 - (j >> 1)>(ub);
              else af[(st + 1) & 1][j >> 1] = conv_k1<mn, LO + (j >> 1)>(kb);
            } else {
              if constexpr (j >= 1 && j <= 4) bf[mn & 1][j - 1] = conv_u1<mn, TB0 + j - 1>(ub);
              else af[(st + 1) & 1][j ? j - 4 : 0] = conv_k1<mn, LO + (j ? j - 4 : 0)>(kb);
            }
          } else if constexpr (more && !nu && j < 3) af[(st + 1) & 1][j] = conv_k1<mn, LO + 4 * gn + j>(kb);
        });
        SCHED();
        hook(IC<mm * (DT ? 16 : 10) + gr * 10 + k>{});
      }
    });
  });
}
DEVI void conv_part(const Ctx& c, int lane, const f32x4 (&acc)[4]) {
  char* red = c.smem + L_RED;
#pragma unroll
  for (int T = 0; T < 4; ++T) *(f32x4*)(red + (c.w * 4 + T) * 1024 + (lane & 15) * 64 + (lane >> 4) * 16) = acc[T];
}
DEVI void conv_out(const Ctx& c, int lane, const f32x4& base0, const f32x4& base1, int xoff, int h) {
  const int sx = xoff + (16 * h + 4 * c.w) * 65536 + c.b * 256;
  const int vx = (lane >> 4) * 65536 + (lane & 15) * 8;
  const char* p = c.smem + L_RED + c.w * 1024 + lane * 16;
  const f32x4 a0 = *(const f32x4*)p, b0 = *(const f32x4*)(p + 4096), a1 = *(const f32x4*)(p + 8192), b1 = *(const f32x4*)(p + 12288);
  SCHED();
  const f32x4 y0 = (a0 + b0) * c.ks0 + base0, y1 = (a1 + b1) * c.ks1 + base1;
  st64(c.rs, vx, sx, u32x2{pack_bf(y0[0], y0[1]), pack_bf(y0[2], y0[3])});
  st64(c.rs, vx + 128, sx, u32x2{pack_bf(y1[0], y1[1]), pack_bf(y1[2], y1[3])});
}

template <int PC> DEVI void g1_dma1(const Ctx& c, int ve, int vo, int ldsx, int soff) {
  dma1k<(PC & 3) * 1024>(c.rs, lds_addr(c.smem) + ldsx + (4 * (PC >> 2) + c.w) * 4096, (PC & 1) ? vo : ve, soff + (4 * (PC >> 2) + c.w) * 4096);
}
template <bool ALL> DEVI void sig_bar(const Ctx& c, int lane, int line) {
  const int v = (c.w == 0 && lane < (ALL ? 8 : 1)) ? (ALL ? lane * 2048 : 0) : 0x40000000;
  asm volatile("s_nop 4\n\tbuffer_atomic_add %0, %1, %2, %3 offen" :: "v"(1u), "v"(v), "s"(c.rs),
               "s"(WS_CT + (line + (ALL ? (c.b >> 3) & 7 : 0)) * 256) : "memory");
}
template <class F, class G>
DEVI void g1_mma(const char* const (&xb)[4], const u32x4 (&w1f)[2][16], f32x4 (&acc)[2][4], f32x4& sa, F&& mid, G&& hook) {
  u32x2 xf[2][4][2];
#pragma unroll
  for (int i = 0; i < 4; ++i) { xf[0][i][0] = tr_read(xb[i]); xf[0][i][1] = tr_read(xb[i] + 512); }
  SCHED();
  sfor<0, 16>([&](auto KS) {
    constexpr int ks = KS.v;
    mid(KS);
    sfor<0, 4>([&](auto I) {
      constexpr int i = I.v;
      const u32x4 x = cat2(xf[ks & 1][i][0], xf[ks & 1][i][1]);
      mma_vv<ks == 0>(acc[0][i], w1f[0][ks], x);
      if constexpr (ks < 15) xf[(ks + 1) & 1][i][0] = tr_read(xb[i] + (ks + 1) * 4096);
      SCHED();
      if constexpr (ks < 8) mma_av<ks == 0>(acc[1][i], w1f[1][ks], x); else mma_vv<false>(acc[1][i], w1f[1][ks], x);
      if constexpr (ks < 15) xf[(ks + 1) & 1][i][1] = tr_read(xb[i] + (ks + 1) * 4096 + 512);
      SCHED();
      if constexpr (i == 0) { mma_vv<ks == 0>(sa, x, x); hook(IC<2 * ks>{}); SCHED(); }
      if constexpr (i == 2) { hook(IC<2 * ks + 1>{}); SCHED(); }
    });
  });
  MFMA_DONE();
}
DEVI void st_h(const Ctx& c, int v, int so0, int so1, u32x4 a, u32x4 b) {
  const u32x2 al = {a[0], a[1]}, bl = {b[0], b[1]};
  asm volatile("s_cmp_eq_u32 %0, 3\n\ts_nop 3\n\ts_cbranch_scc1 1f\n\t"
               "buffer_store_dwordx4 %5, %1, %2, %3 offen sc1\n\tbuffer_store_dwordx4 %6, %1, %2, %4 offen sc1\n\ts_branch 2f\n"
               "1:\n\tbuffer_store_dwordx2 %7, %1, %2, %3 offen sc1\n\tbuffer_store_dwordx2 %8, %1, %2, %4 offen sc1\n"
               "2:\n\ts_nop 1"
               :: "s"(c.w), "v"(v), "s"(c.rs), "s"(so0), "s"(so1), "v"(a), "v"(b), "v"(al), "v"(bl) : "memory", "scc");
}
template <class F>
DEVI void g1_epi(const Ctx& c, int lane, f32x4 (&acc)[2][4], const f32x4& sa, int hoff, F&& hook) {
  const int g = lane >> 4, cc = lane & 15, q = cc >> 2, p = lane & 3, w = c.w;
  float* sums = (float*)(c.smem + L_MISC);
  {
    const float d = sel4(p, sa[0], sa[1], sa[2], sa[3]);
    sums[q == g ? (w * 16 + cc) * 2 + 1 : 128 + lane] = d;
    const float r = sel4(g, acc[1][1][0], acc[1][2][0], acc[1][3][0], acc[1][0][0]);
    sums[w == 3 ? lane * 2 : 192 + lane] = r;
  }
  f32x4 s1v[2], b1v[2];
#pragma unroll
  for (int j = 0; j < 2; ++j) {
    s1v[j] = *(const f32x4*)(c.smem + L_CST + w * 256 + j * 128 + g * 16);
    b1v[j] = *(const f32x4*)(c.smem + L_CST + w * 256 + j * 128 + 64 + g * 16);
  }
  const int vh = w < 3 ? lane * 16 : lane * 8 + c.eo * 512;
  __syncthreads();
  f32x2 k[4];
#pragma unroll
  for (int i = 0; i < 4; ++i) k[i] = *(const f32x2*)(sums + (((i + w) & 3) * 16 + cc) * 2);
#pragma unroll
  for (int i = 0; i < 4; ++i) {
    const float m = k[i][0] * (1.0f / DM);
    const float ve = fmaxf(fmaf(-m, m, fmaf(k[i][1], 1.0f / DM, LN_EPS)), LN_EPS);
    const float r = __builtin_amdgcn_rsqf(fmaf(ve, 1.0f + LN_EPS, -LN_EPS));
    k[i] = f32x2{r, -m * r};
  }
  SCHED();
  hook(IC<-1>{});
  SCHED();
  sfor<0, 2>([&](auto IH) {
    constexpr int ih = IH.v;
    f32x4 x[4], e[4];
#define STAGE(n) do { SCHED(); hook(IC<10 * ih + n>{}); SCHED(); } while (0)
#pragma unroll
    for (int n = 0; n < 4; ++n) x[n] = k[2 * ih + (n >> 1)][1] * s1v[n & 1] + b1v[n & 1];
#pragma unroll
    for (int n = 0; n < 4; ++n) x[n] = acc[n & 1][2 * ih + (n >> 1)] * k[2 * ih + (n >> 1)][0] + x[n];
    STAGE(0);
#pragma unroll
    for (int n = 0; n < 4; ++n) e[n] = x[n] * x[n];
#pragma unroll
    for (int n = 0; n < 4; ++n) e[n] = e[n] * (-0.0713548162726f * 1.44269504088896f) + (-1.59576912160573f * 1.44269504088896f);
    STAGE(1);
#pragma unroll
    for (int n = 0; n < 4; ++n) e[n] = e[n] * x[n];
    STAGE(2);
#pragma unroll
    for (int r = 0; r < 4; ++r) { e[0][r] = __builtin_amdgcn_exp2f(e[0][r]); e[1][r] = __builtin_amdgcn_exp2f(e[1][r]); }
    STAGE(3);
#pragma unroll
    for (int r = 0; r < 4; ++r) { e[2][r] = __builtin_amdgcn_exp2f(e[2][r]); e[3][r] = __builtin_amdgcn_exp2f(e[3][r]); }
    STAGE(4);
#pragma unroll
    for (int n = 0; n < 4; ++n) e[n] = e[n] + 1.0f;
    STAGE(5);
#pragma unroll
    for (int r = 0; r < 4; ++r) { e[0][r] = rcp(e[0][r]); e[1][r] = rcp(e[1][r]); }
    STAGE(6);
#pragma unroll
    for (int r = 0; r < 4; ++r) { e[2][r] = rcp(e[2][r]); e[3][r] = rcp(e[3][r]); }
    STAGE(7);
#pragma unroll
    for (int n = 0; n < 4; ++n) x[n] = x[n] * e[n];
    STAGE(8);
    u32x4 hv[2];
#pragma unroll
    for (int n = 0; n < 4; ++n) { hv[n >> 1][2 * (n & 1)] = pack_bf(x[n][0], x[n][1]); hv[n >> 1][2 * (n & 1) + 1] = pack_bf(x[n][2], x[n][3]); }
    STAGE(9);
    const int so0 = hoff + ((2 * ih + w) & 3) * 56 * 1024, so1 = hoff + ((2 * ih + 1 + w) & 3) * 56 * 1024;
    st_h(c, vh, so0, so1, hv[0], hv[1]);
    SCHED();
    if constexpr (ih == 0) { hook(IC<-2>{}); SCHED(); }
#undef STAGE
  });
}

constexpr int PF = 10;
template <int N> DEVI void ld_w1(const Ctx& c, int v, u32x4 (&w1f)[2][16]) {
  constexpr int ks = N >> 1, j = N & 1;
  const int so = (j ? c.f1o + (ks >> 2) * c.f1s : WS_W1P + c.ff0 * 16384 + (ks >> 2) * 4096);
  if constexpr (j && ks < 8) w1f[j][ks] = ld128a<(ks & 3) * 1024>(c.rs, v, so);
  else w1f[j][ks] = ld128v<(ks & 3) * 1024>(c.rs, v, so);
}
template <int N> DEVI void ld_w2(const Ctx& c, int v, u32x4 (&w2f)[14][4]) {
  constexpr int s = N >> 2, j = N & 3;
  w2f[s][j] = ld128a<(s & 3) * 1024>(c.rs, v, WS_W2P + ((c.ct_ * 4 + ((c.w + j) & 3)) * 56 + 14 * c.w + (s & ~3)) * 1024);
}
template <int N> DEVI void g2_ldh1(const Ctx& c, int v16, u32x4 (&hf)[14][2], int ho) {
  constexpr int s = N % 14, i = N / 14;
  if constexpr (s % 7 == 6)
    hf[s][i] = cat2(ld64(c.rs, (v16 >> 1) + (s & 3) * 1024, ho + i * 56 * 1024 + (s >> 2) * 4096),
                    ld64(c.rs, (v16 >> 1) + (s & 3) * 1024 + 512, ho + i * 56 * 1024 + (s >> 2) * 4096));
  else hf[s][i] = ld128(c.rs, v16 + (s & 3) * 1024, ho + i * 56 * 1024 + (s >> 2) * 4096);
}
template <int PRE, int RATE, class F>
DEVI void g2_mma(const Ctx& c, int lane, u32x4 (&hf)[14][2], const u32x4 (&w2f)[14][4], f32x4 (&acc)[2][4], int ho, int h, F&& mid) {
  const int v16 = lane * 16;
  char* red = c.smem + L_XA + h * 32768;
  sfor<0, 28>([&](auto N) {
    constexpr int i = N.v / 14, s = N.v % 14;
    sfor<0, 4>([&](auto J) {
      constexpr int j = J.v;
      mma_va<s == 0>(acc[i][j], hf[s][i], w2f[s][j]);
      constexpr int per = 4 / RATE, n = PRE + RATE * N.v + j / per;
      if constexpr (j % per == per - 1 && n < 28) { g2_ldh1<n>(c, v16, hf, ho); SCHED(); }
      if constexpr (N.v == 17 && j > 0) { *(f32x4*)(red + ((((c.w + j) & 3) * 3 + j - 1) * 2) * 1024 + v16) = acc[0][j]; SCHED(); }
    });
    SCHED();
    mid(N);
  });
  MFMA_DONE();
}
template <bool LAST, class F>
DEVI void g2_epi(const Ctx& c, int lane, const f32x4 (&acc)[2][4], const u32x2 (&xr)[2], int h, int xdst, F&& mid) {
  const int g = lane >> 4, cc = lane & 15, q = cc >> 2, p = lane & 3, w = c.w, t = w * 64 + lane;
  char* red = c.smem + L_XA + h * 32768;
#pragma unroll
  for (int jj = 1; jj < 4; ++jj) *(f32x4*)(red + ((((w + jj) & 3) * 3 + jj - 1) * 2 + 1) * 1024 + lane * 16) = acc[1][jj];
  __syncthreads();
  char* xt = red + 24576;
  f32x4 pr[2][3];
#pragma unroll
  for (int i = 0; i < 2; ++i)
#pragma unroll
    for (int rk = 0; rk < 3; ++rk) pr[i][rk] = *(const f32x4*)(red + ((w * 3 + rk) * 2 + i) * 1024 + lane * 16);
  mid(0);
#pragma unroll
  for (int i = 0; i < 2; ++i) {
    const f32x4 xv = {lo_bf(xr[i][0]), hi_bf(xr[i][0]), lo_bf(xr[i][1]), hi_bf(xr[i][1])};
    const f32x4 o = ((acc[i][0] + (xv + c.bb2)) + pr[i][0]) + (pr[i][1] + pr[i][2]);
    const u32x2 r = {pack_bf(o[0], o[1]), pack_bf(o[2], o[3])};
    *(u32x2*)(xt + (w * 16 + cc) * 64 + ((i ^ (cc >> 3)) * 32) + g * 8) = r;
  }
  __syncthreads();
  const char* xq = xt + ((w >> 1) * 32 + 8 * g + q) * 64 + (((w & 1) ^ (g & 1)) * 32) + 8 * p;
  const u32x4 f0 = cat2(tr_read(xq), tr_read(xq + 256));
  const int tok = (16 * h + c.P) * 64 + c.sub * 32 + (w & 1) * 16 + cc;
  if (LAST) {
    *(u32x4*)(c.out + (size_t)tok * DM + c.ct_ * 64 + (w >> 1) * 32 + 8 * g) = f0;
  } else {
    const int row = t >> 2;
    const u32x4 v = *(const u32x4*)(xt + row * 64 + (((t & 3) ^ ((row >> 2) & 2)) * 16));
    st128(c.rs, c.ct_ * 4096 + t * 16, xdst, v);
    const char* xo = xq + ((w & 2) ? -2048 : 2048);
    const u32x4 f1 = cat2(tr_read(xo), tr_read(xo + 256));
    float s = 0.f, ss = 0.f, s2 = 0.f, ss2 = 0.f;
#pragma unroll
    for (int j = 0; j < 4; ++j) {
      s = dot2bf(f0[j], 0x3f803f80u, s); ss = dot2bf(f0[j], f0[j], ss);
      s2 = dot2bf(f1[j], 0x3f803f80u, s2); ss2 = dot2bf(f1[j], f1[j], ss2);
    }
    s = sum_x32(sum_x16(s + s2)); ss = sum_x32(sum_x16(ss + ss2));
    asm volatile("s_nop 4\n\tbuffer_atomic_add_x2 %0, %1, %2, %3 offen" :: "v"(f2fx((g & 1) ? ss : s)),
                 "v"(t < 128 && lane < 32 ? st_pos(tok) + g * 8 : 0x40000000), "s"(c.rs), "s"(WS_ST + 65536) : "memory");
  }
}

template <int LAYER>
DEVI void layer_body(const Ctx& c, u32x4 (&w1f)[2][16], u32x4 (&w2f)[14][4], CIn& inA, CIn& inB) {
  constexpr int xin = WS_X + LAYER * 4 * MB, xmid = xin + 2 * MB, xnext = xin + 4 * MB;
  constexpr int hbuf = WS_H + LAYER * 8 * MB, stin = WS_ST + LAYER * 65536;
  constexpr u32 ep = LAYER + 1;
  constexpr int K1 = 1 + 3 * LAYER, K2 = K1 + 1, K3 = K1 + 2;
  const int w = c.w;
  char* smem = c.smem;
  auto nop = [](auto) {};
  {
    const int lane = fresh_lane(c);
    const int g = lane >> 4, cc = lane & 15, mu0 = (w & 1) * 4, v16 = lane * 16;
    const char* ub = smem + L_U + (w >> 1) * L_UC + 512 + 2 * (16 * cc - 16 + 8 * g) - 64 * (mu0 + 3);
    KB kb;
#pragma unroll
    for (int j = 0; j < 4; ++j) {
      u32 a = lds_addr(smem) + (w >> 1) * 2 * L_KC + ((cc & 1) ? 0 : L_KC) + 2 * (2031 - cc + 8 * g - ((cc & 1) ? 0 : 1)) - 64 * (mu0 + 3 + 56) + 1024 * j;
      asm volatile("" : "+v"(a));
      kb.p[j] = (const char*)(const LAS char*)(size_t)a;
    }
    STAMP(K1, 1);
    f32x4 base0, base1;
    u32x4 a4[4];
    conv_ldk<0>(kb, a4);
    if (LAYER) { SCHED(); conv_landed(inA); }
    conv_ln(c, lane, inA, 0, base0, base1);
    if (!LAYER) poll0(c, lane, WS_F1);
    __syncthreads();
    STAMP(K1, 2);
    f32x4 acc[4];
    u32 o = 0;
    conv_mma<0, 0, true>(ub, kb, acc, a4, [&](auto N) {
      constexpr int n = N.v;
      if constexpr (LAYER && n == 19) { WAIT_VM(0); o = sig_begin(c, lane); SCHED(); }
      if constexpr (LAYER && n == 30) { sig_end<true, true>(c, lane, o, CT_X + 64); SCHED(); }
    });
    MFMA_DONE();
    conv_part(c, lane, acc);
    conv_ldk<1>(kb, a4);
    __syncthreads();
    float s1p[8];
    if (!LAYER) {
      if (!got0<0>(c.tag)) wait0(c, lane, WS_F1);
      asm volatile("" :: "v"(inB.x1));
      const int so1 = (min(c.ff0 * 16 + (lane & 15) + (lane >> 5) * 16, FF - 1) + (lane & 16) * (FF / 2)) * 4;
#pragma unroll
      for (int j = 0; j < 8; ++j)
        asm volatile("s_nop 4\n\tbuffer_load_dword %0, %1, %2, %3 offen sc1" : "=v"(s1p[j]) : "v"(so1), "s"(c.rs), "s"(WS_S1 + j * FF * 4) : "memory");
    }
    conv_out(c, lane, base0, base1, xmid, 0);
    STAMP(K1, 3);
    f32x4 acb[4];
    conv_mma<4, 0, true>(ub, kb, acb, a4, [&](auto N) {
      constexpr int n = N.v;
      if constexpr (!LAYER && n >= 48 && (n & 1)) { ld_w1<(n - 48) / 2>(c, v16, w1f); SCHED(); }
      if constexpr (n == 47) {
        WAIT_VM(0);
        o = sig_begin(c, lane);
        if (LAYER) poll<true>(c, lane, CT_X + 64);
        SCHED();
      }
      if constexpr (n == 58) { sig_end<true, true>(c, lane, o, CT_C); SCHED(); }
    });
    STAMP(K1, 4);
    if (!LAYER) {
#pragma unroll
      for (int j = 0; j < 8; ++j) asm volatile("" : "+v"(s1p[j]));
      const float sv = sum_x16(((s1p[0] + s1p[1]) + (s1p[2] + s1p[3])) + ((s1p[4] + s1p[5]) + (s1p[6] + s1p[7])));
      ((float*)(smem + L_CST))[w * 64 + lane] = (lane & 16) ? c.cs1 : sv;
    }
    if (LAYER) {
      if (!got<1>(32u)) wait_ge<true>(c, lane, CT_X + 64, 32u);
      conv_load(c, lane, inB, xin, stin, 1);
    }
    conv_ldk<0>(kb, a4);
    STAMP(K1, 5);
    if (!LAYER) sfor<8, 12>([&](auto N) { ld_w1<N.v>(c, v16, w1f); });
    conv_ln(c, lane, inB, 1, base0, base1);
    if (!LAYER) sfor<12, 14>([&](auto N) { ld_w1<N.v>(c, v16, w1f); });
    __syncthreads();
    conv_mma<0, 4, false>(ub, kb, acb, a4, [&](auto N) {
      constexpr int n = N.v;
      if constexpr (!LAYER && n % 3 == 1 && n < 36) { ld_w1<14 + n / 3>(c, v16, w1f); SCHED(); }
      if constexpr (n == 30) { poll<true>(c, lane, CT_C); SCHED(); }
    });
    MFMA_DONE();
    conv_part(c, lane, acb);
    __syncthreads();
    conv_out(c, lane, base0, base1, xmid, 1);
    STAMP(K1, 6);
    if (!got<(LAYER ? 2 : 4)>(32u * ep)) wait_ge<true>(c, lane, CT_C, 32u * ep);
    const int row = lane >> 3, sw = ((row >> 1) & 1) << 1;
    const int ve = row * 128 + ((lane & 7) ^ sw) * 16, vo = row * 128 + ((lane & 7) ^ (sw | 4)) * 16;
    if (!LAYER && c.b == 0 && w == 0) __hip_atomic_store((gu32*)(c.ws + WS_GEN), c.tag, RLX_AGENT);
    sfor<0, 8>([&](auto N) { g1_dma1<N.v>(c, ve, vo, L_XA, xmid + c.P * 65536); });
    WAIT_VM(8);
    o = sig_begin(c, lane);
    sig_end<true>(c, lane, o, CT_C + 64);
    STAMP(K1, 7);
  }
  u32x4 hf[14][2];
  u32x2 xr[2];
  {
    const int lane = fresh_lane(c);
    const int g = lane >> 4, cc = lane & 15, q = cc >> 2, p = lane & 3, v16 = lane * 16;
    const int sx = ((q >> 1) & 1) | ((g & 1) << 1);
    const char* xb[4];
#pragma unroll
    for (int i = 0; i < 4; ++i) xb[i] = smem + L_XA + (8 * g + q) * 128 + ((((i + w) & 3) ^ sx) * 32) + 8 * p;
    const int row = lane >> 3, sw = ((row >> 1) & 1) << 1;
    const int ve = row * 128 + ((lane & 7) ^ sw) * 16, vo = row * 128 + ((lane & 7) ^ (sw | 4)) * 16;
    sfor<8, 12>([&](auto N) { g1_dma1<N.v>(c, ve, vo, L_XA, xmid + c.P * 65536); });
    WAIT_VM_BAR(9);
    STAMP(K2, 0);
    f32x4 acc[2][4], sa;
    g1_mma(xb, w1f, acc, sa, [&](auto KS) {
      constexpr int ks = KS.v;
      if constexpr (ks == 3) { if (LAYER) WAIT_VM_BAR(8); else WAIT_VM_BAR(10); SCHED(); }
      if constexpr (ks == 7) { if (LAYER) WAIT_VM_BAR(0); else WAIT_VM_BAR(10); SCHED(); }
      if constexpr (ks == 13) { if (!LAYER) WAIT_VM(16); poll<true>(c, lane, CT_C + 64); SCHED(); }
    }, [&](auto N) {
      constexpr int n = N.v;
      if constexpr (n < 4) g1_dma1<12 + n>(c, ve, vo, L_XA, xmid + c.P * 65536);
      if constexpr (!LAYER && n >= 4 && n < 10) ld_w1<22 + n>(c, v16, w1f);
      if constexpr (!LAYER && n >= 10) ld_w2<n - 10>(c, v16, w2f);
    });
    STAMP(K2, 1);
    const int xo = xmid + (16 + c.P) * 65536;
    g1_epi(c, lane, acc, sa, hbuf + (c.P * 4 * 56 + c.hks) * 1024, [&](auto N) {
      constexpr int n = N.v;
      if constexpr (n == -1) { if (!got<(LAYER ? 0 : 6)>(32u * ep)) wait_ge<true>(c, lane, CT_C + 64, 32u * ep); }
      if constexpr (n >= 0 && n < 8) g1_dma1<n>(c, ve, vo, L_XB, xo);
      if constexpr (n >= 10 && n < 18) g1_dma1<n - 2>(c, ve, vo, L_XB, xo);
    });
    STAMP(K2, 2);
  }
  {
    const int lane = fresh_lane(c);
    const int g = lane >> 4, cc = lane & 15, q = cc >> 2, p = lane & 3, v16 = lane * 16;
    const int sx = ((q >> 1) & 1) | ((g & 1) << 1);
    const char* xb[4];
#pragma unroll
    for (int i = 0; i < 4; ++i) xb[i] = smem + L_XB + (8 * g + q) * 128 + ((((i + w) & 3) ^ sx) * 32) + 8 * p;
    WAIT_VM_BAR(12);
    STAMP(K2, 3);
    f32x4 acc[2][4], sa;
    g1_mma(xb, w1f, acc, sa, [&](auto KS) {
      constexpr int ks = KS.v;
      if constexpr (ks == 5) {
        if (LAYER) WAIT_VM_BAR(0); else WAIT_VM_BAR(10);
        sig_bar<false>(c, lane, CT_H + c.P);
        SCHED();
      }
      if constexpr (ks == 15) {
#pragma unroll
        for (int i = 0; i < 2; ++i) xr[i] = ld64(c.rs, (c.ct_ * 64 + w * 16 + cc) * 128 + i * 32 + g * 8, xmid + c.P * 65536 + c.sub * 64);
        SCHED();
      }
    }, [&](auto N) {
      constexpr int n = N.v;
      if constexpr (!LAYER) ld_w2<22 + n>(c, v16, w2f);
      if constexpr (!LAYER && n >= 30) ld_w2<24 + n>(c, v16, w2f);
    });
    STAMP(K2, 4);
    poll<false>(c, lane, CT_H + c.P);
    const int ho = hbuf + ((c.P * 4 + c.sub * 2) * 56 + 14 * w) * 1024;
    g1_epi(c, lane, acc, sa, hbuf + ((16 + c.P) * 4 * 56 + c.hks) * 1024, [&](auto N) {
      constexpr int n = N.v;
      if constexpr (n == -1) { if (!got<0>(16u * ep)) wait_ge<false>(c, lane, CT_H + c.P, 16u * ep); }
      if constexpr (n >= 0) g2_ldh1<n>(c, v16, hf, ho);
    });
    STAMP(K2, 5);
  }
  {
    const int lane = fresh_lane(c);
    const int g = lane >> 4, cc = lane & 15;
    const int ho = hbuf + ((c.P * 4 + c.sub * 2) * 56 + 14 * w) * 1024, hoB = ho + 64 * 56 * 1024;
    STAMP(K3, 0);
    f32x4 acc[2][4];
    u32 o = 0;
    g2_mma<20, 1>(c, lane, hf, w2f, acc, ho, 0, [&](auto S) {
      constexpr int s = S.v;
      if constexpr (s == 11) { WAIT_VM(10); o = sig_begin(c, lane); SCHED(); }
      if constexpr (s == 13) { sig_end<false>(c, lane, o, CT_H + 16 + c.P); SCHED(); }
    });
    STAMP(K3, 1);
    poll<false>(c, lane, CT_H + 16 + c.P);
    g2_epi<LAYER == 1>(c, lane, acc, xr, 0, xnext + c.P * 65536 + c.sub * 32768, nop);
    STAMP(K3, 2);
    if (!got<(LAYER ? 1 : 2)>(16u * ep)) wait_ge<false>(c, lane, CT_H + 16 + c.P, 16u * ep);
    sfor<0, 8>([&](auto N) { g2_ldh1<N.v>(c, lane * 16, hf, hoB); });
#pragma unroll
    for (int i = 0; i < 2; ++i) xr[i] = ld64(c.rs, (c.ct_ * 64 + w * 16 + cc) * 128 + i * 32 + g * 8, xmid + (16 + c.P) * 65536 + c.sub * 64);
    if (LAYER == 0) {
      WAIT_VM(11);
      o = sig_begin(c, lane);
      sig_end<true>(c, lane, o, CT_X);
    }
    SCHED();
    STAMP(K3, 3);
  }
  {
    const int lane = fresh_lane(c);
    const int hoB = hbuf + (((16 + c.P) * 4 + c.sub * 2) * 56 + 14 * w) * 1024;
    f32x4 acc[2][4];
    g2_mma<8, 2>(c, lane, hf, w2f, acc, hoB, 1, [&](auto S) {
      constexpr int s = S.v;
      if constexpr (LAYER == 0 && s == 27) { poll<true>(c, lane, CT_X); SCHED(); }
    });
    STAMP(K3, 4);
    g2_epi<LAYER == 1>(c, lane, acc, xr, 1, xnext + (16 + c.P) * 65536 + c.sub * 32768, [&](int) {
      if (LAYER == 0) {
        if (!got<0>(32u)) wait_ge<true>(c, lane, CT_X, 32u);
        conv_load_u(c, lane, inA, xnext, WS_ST + 65536, 0);
        SCHED();
      }
    });
    STAMP(K3, 5);
  }
}

constexpr int KG_RS = 528, KG_X = 16 * KG_RS, KG_CH = 64 * KG_RS + 1296;
__global__ void __launch_bounds__(256, 1) main_kernel(const u16* x, const u16* lar, const u16* aim, const u16* cre,
                                                      const u16* cim, const u16* ldt, const u16* dsk, const u16* w1,
                                                      const u16* b1, const u16* w2, const u16* b2, char* ws, u16* out) {
  extern __shared__ __attribute__((aligned(16))) char smem[];
  const int t = threadIdx.x, lane = t & 63, b = blockIdx.x;
  const int w = __builtin_amdgcn_readfirstlane(t >> 6);
  const u32 junk = warm_code((b >> 3) * 4 + w, lane);
  STAMP(0, 0);
  const int ch = w >> 1, d = 2 * b + ch, n = t & 127;
  const u32 po = (u32)(d * NST + n) * 2, po2 = po ^ 128u;
#define LD16(p, off) (*(const u16*)((const char*)(p) + (u32)(off)))
  const u16 r0 = ldt[d], r1 = LD16(lar, po), r2 = LD16(aim, po), r3 = LD16(cre, po), r4 = LD16(cim, po);
  const u16 r5 = LD16(cre, po2), r6 = LD16(cim, po2);
  const u32 gen = __hip_atomic_load((gu32*)(ws + WS_GEN), RLX_AGENT);
  const bool early = !(b & 64);
  const int pks = (b & 7) + (b >> 7) * 8, pcg = (b >> 3) & 7;
  u32x4 rv[8], rw[2];
  {
    const rsrc_t rr = __builtin_amdgcn_make_buffer_rsrc((void*)(early ? x : w1), 0, early ? SEQ * DM * 2 : DM * FF * 2, 0x00020000);
    const int vo = early ? w * 1024 + lane * 16 : ((b & 63) < 56 ? (t >> 5) * (FF * 2) + (t & 31) * 16 : 0x40000000);
    const int so = early ? (b & 63) * 32768 : pks * (32 * FF * 2) + min(pcg, 6) * 512, st = early ? 4096 : 8 * FF * 2;
#pragma unroll
    for (int j = 0; j < 8; ++j) rv[j] = __builtin_amdgcn_raw_buffer_load_b128(rr, (j < 4 || early) ? vo : 0x40000000, so + (j & 3) * st + (j >> 2) * (early ? 16384 : 0), 0);
  }
  CIn inA, inB;
  Ctx c;
  c.rs = __builtin_amdgcn_make_buffer_rsrc(ws, 0, WS_END, 0x00020000);
  c.ct = (gu32*)(ws + WS_CT);
  c.smem = smem; c.ws = ws; c.out = out;
  c.lane0 = lane; c.w = w; c.b = b;
  c.P = (b & 7) * 2 + ((b >> 3) & 1);
  const int idx = b >> 4;
  const int sg = idx >> 1, w3 = -(int)(w == 3);
  c.eo = idx & 1; c.sub = idx >> 3; c.ct_ = idx & 7;
  c.ff0 = 14 * sg + (((6 * c.eo + 2 * w) & ~w3) | ((12 + c.eo) & w3));
  c.hks = 7 * sg + (((3 * c.eo + w) & ~w3) | (6 & w3));
  c.f1o = w < 3 ? WS_W1P + c.ff0 * 16384 + 16384 : WS_ONE; c.f1s = w < 3 ? 4096 : 0;
  {
    const u32 r7 = *(const u32*)(dsk + 2 * b);
    u32 r8 = LD16(b1, min(c.ff0 * 16 + (lane & 15) + (lane >> 5) * 16, FF - 1) * 2), r9 = LD16(b2, (c.ct_ * 64 + w * 16 + (lane & 15)) * 2);
    SCHED();
    char* kg = smem + L_XA + ch * KG_CH;
    char* stg = smem + L_XA + 2 * KG_CH;
    const int i = lane & 15, g = lane >> 4;
    int ix[9];
#pragma unroll
    for (int e2 = 0; e2 < 9; ++e2) { ix[e2] = 2 * cidx(max(2047 - 8 * t - e2, 0)); asm volatile("" : "+v"(ix[e2])); }
    int wi[2][4];
    {
      const int m8 = -(i >> 3), s1 = -(w & 1);
      const int h1 = ((256 + 128 * (i >> 2) + 2 * (i & 3)) & ~m8) | ((128 * (i >> 1) + 4 * (i & 1)) & m8);
      const int hp = (((i + (i & 8) * 15) & ~s1) | (h1 & s1)) + 32 * g, hq = 1024 + 128 * i + 32 * g;
#pragma unroll
      for (int r = 0; r < 4; ++r) {
        wi[0][r] = 2 * cidx(hp + 8 * r); wi[1][r] = 2 * cidx(hq + 8 * r > 2048 ? 2064 : hq + 8 * r);
        asm volatile("" : "+v"(wi[0][r]), "+v"(wi[1][r]));
      }
    }
    if (t < 16) *(u32x4*)(smem + (t >> 2) * L_KC + 4096 + (t & 3) * 16) = u32x4{0, 0, 0, 0};
    if (t >= 64 && t < 128) *(u32x4*)(smem + L_U + ((t - 64) >> 5) * L_UC + (t & 31) * 16) = u32x4{0, 0, 0, 0};
    if (t == 128) *(u32*)(smem + L_SIG) = 0u;
    if (w >= 2) poll0(c, lane, w == 2 ? WS_F1 : WS_F0);
    SCHED();
    STAMP(0, 1);
    const float dt = rbf(expf(bf2f(r0)));
    const float cr = bf2f(r3), ci = bf2f(r4), cr2 = bf2f(r5), ci2 = bf2f(r6);
    const float S = wave_sum(sqrt_(cr * cr + ci * ci) + sqrt_(cr2 * cr2 + ci2 * ci2)) * (2.002f * dt);
    int e = __builtin_amdgcn_frexp_expf(S);
    e = (S > 0.0f && S < 3.0e38f) ? max(-100, min(100, e)) : 14;
    const float scale = __builtin_amdgcn_ldexpf(2.0f, 14 - e);
    if (n == 0) *(float*)(smem + L_MISC + ch * 4) = __builtin_amdgcn_ldexpf(1.0f, e - 14);
    const float a2 = (-rbf(expf(bf2f(r1))) * dt) * 1.44269504088896f;
    const float br = (bf2f(r2) * dt) * 0.159154943091895f;
    {
      char* yp = kg + n * 4;
      char* xp = kg + KG_X + n * 4;
      const CM e128 = cexp_(a2, -br, 128.f), e8 = cexp_(a2, br, 8.f), e1 = cexp_(a2, -br, 1.f), e2 = cexp_(a2, -br, 2.f);
      const CM e4 = cexp_(a2, -br, 4.f);
      f32x2 f0 = {(cr * dt) * scale, -(ci * dt) * scale};
      f32x2 f = cmul(f0, cexp_(a2, -br, 512.f)), y = {1.f, 0.f};
      f32x2 g1 = cmul(f0, e128), g2 = cmul(g1, e128), g3 = cmul(g2, e128);
#define PUT(base, row, v) *(u32*)(base + (row) * KG_RS) = pack_h(v[0], v[1])
      auto chain = [&](auto K) {
        constexpr int k = K.v;
        PUT(yp, k, y);
        if (k < 15) y = cmul(y, e8);
        if (k < 4) { const f32x2 q4 = cmul(f, e4); PUT(xp, 24 + 2 * k, f); PUT(xp, 25 + 2 * k, q4); }
        else if (k < 13) PUT(xp, 28 + k, f);
        if (k < 12) f = cmul(f, e128);
        if (k < 8) { PUT(xp, k, f0); PUT(xp, 8 + k, g1); }
        if (k < 7) { f0 = cmul(f0, e1); g1 = cmul(g1, e1); }
        if (k >= 8 && k < 12) { PUT(xp, 8 + k, g2); PUT(xp, 12 + k, g3); }
        if (k >= 8 && k < 11) { g2 = cmul(g2, e2); g3 = cmul(g3, e2); }
      };
      c.tag = __builtin_amdgcn_readfirstlane(gen) * 0x9E3779B1u + 0x7F4A7C15u;
      sfor<0, 2>(chain);
      asm volatile("" : "+v"(y), "+v"(f), "+v"(f0), "+v"(g1));
      if (b < 64) xpose(b, rv, c.rs, stg);
      else if (early) row_stats(b & 63, rv, c.rs);
      asm volatile("" : "+v"(y), "+v"(f), "+v"(f0), "+v"(g1));
      sfor<2, 12>(chain);
      asm volatile("" : "+v"(y), "+v"(f));
      if (early) {
        WAIT_VM(0);
        __syncthreads();
        if (w == 0 && lane < 8)
          __hip_atomic_store((gu32*)(ws + WS_F0 + lane * 2048 + (b & 7) * 256 + ((b >> 3) & 7) * 8 + (b >> 7) * 4), c.tag, RLX_AGENT);
      }
      asm volatile("" : "+v"(y), "+v"(f));
      STAMP(0, 2);
      {
    const rsrc_t r2w = __builtin_amdgcn_make_buffer_rsrc((void*)w2, 0, DM * FF * 2, 0x00020000);
#pragma unroll
    for (int j = 0; j < 2; ++j) rw[j] = __builtin_amdgcn_raw_buffer_load_b128(r2w, (t >> 4) * 1024 + (t & 15) * 16 + j * 16384, min(b >> 2, 55) * 32768 + (b & 3) * 256, 0);
      }
      SCHED();
      sfor<12, 16>(chain);
      STAMP(0, 3);
      if (!early && pcg < 7) pack_w1(pks, pcg, rv, c.rs, stg);
#undef PUT
    }
    __syncthreads();
    STAMP(0, 4);
    {
      const char* ya = kg + i * KG_RS + g * 16;
      const char* xa = kg + KG_X + (16 * (w & 1) + i) * KG_RS + g * 16;
      const char* xc = kg + KG_X + (32 + i) * KG_RS + g * 16;
      u32x4 fa[8], fb[8], fc[8];
#pragma unroll
      for (int ks = 0; ks < 8; ++ks) { fa[ks] = *(const u32x4*)(ya + ks * 64); fb[ks] = *(const u32x4*)(xa + ks * 64); }
#pragma unroll
      for (int ks = 0; ks < 8; ++ks) fc[ks] = *(const u32x4*)(xc + ks * 64);
      f32x4 ac0, ac1;
      SCHED();
      sfor<0, 8>([&](auto KS) { mma_hh<KS.v == 0>(ac0, fa[KS.v], fb[KS.v]); });
      sfor<0, 8>([&](auto KS) { mma_hh<KS.v == 0>(ac1, fa[KS.v], fc[KS.v]); });
      MFMA_DONE();
#pragma unroll
      for (int r = 0; r < 4; ++r) {
        *(u16*)(kg + 64 * KG_RS + wi[0][r]) = __builtin_bit_cast(u16, (_Float16)ac0[r]);
        *(u16*)(kg + 64 * KG_RS + wi[1][r]) = __builtin_bit_cast(u16, (_Float16)ac1[r]);
      }
    }
    if (!early) WAIT_VM(0);
    __syncthreads();
    if (w == 1 && !early && lane < 8)
      __hip_atomic_store((gu32*)(ws + WS_F1 + lane * 2048 + (b & 7) * 256 + ((b >> 3) & 7) * 8 + (b >> 7) * 4), c.tag, RLX_AGENT);
    if (w == 0) poll0(c, lane, WS_F0);
#pragma unroll
    for (int c2 = 0; c2 < 2; ++c2) {
      u32 v[9];
#pragma unroll
      for (int e2 = 0; e2 < 9; ++e2) v[e2] = *(const u16*)(smem + L_XA + c2 * KG_CH + 64 * KG_RS + ix[e2]);
      if (t == 255) v[8] = 0u;
      u32x4 oe, oo;
#pragma unroll
      for (int j = 0; j < 4; ++j) { oe[j] = v[2 * j] | (v[2 * j + 1] << 16); oo[j] = v[2 * j + 1] | (v[2 * j + 2] << 16); }
      *(u32x4*)(smem + c2 * 2 * L_KC + t * 16) = oe;
      *(u32x4*)(smem + c2 * 2 * L_KC + L_KC + t * 16) = oo;
    }
    const f32x2 ksc = *(const f32x2*)(smem + L_MISC);
    c.ks0 = __uint_as_float(__builtin_amdgcn_readfirstlane(__float_as_uint(ksc[0])));
    c.ks1 = __uint_as_float(__builtin_amdgcn_readfirstlane(__float_as_uint(ksc[1])));
    const u32 ds = __builtin_amdgcn_readfirstlane(r7);
    c.dsk0 = lo_bf(ds); c.dsk1 = hi_bf(ds);
    c.cs1 = lo_bf(r8); c.bb2 = lo_bf(r9);
    asm volatile("" :: "v"(junk));
    STAMP(0, 5);
    pack_w2_put(rw, smem + L_XB + 55552);
    if (w == 0 && !got0<0>(c.tag)) wait0(c, lane, WS_F0);
    __syncthreads();
    conv_load(c, lane, inA, WS_X, WS_ST, 0);
    conv_load(c, lane, inB, WS_X, WS_ST, 1);
    pack_w2_out(b, c.rs, smem + L_XB + 55552);
    STAMP(0, 6);
  }
  u32x4 w1f[2][16], w2f[14][4];
  layer_body<0>(c, w1f, w2f, inA, inB);
  layer_body<1>(c, w1f, w2f, inA, inB);
}

extern "C" __global__ void code_end_marker() {}

extern "C" void kernel_launch(void* const* d_in, const int* in_sizes, int n_in, void* d_out, int out_size,
                              void* d_ws, size_t ws_size, hipStream_t stream) {
  (void)in_sizes; (void)n_in; (void)out_size; (void)ws_size;
  char* ws = (char*)d_ws + ((ws_size - (size_t)WS_END) & ~(size_t)(2 * MB - 1));
  (void)hipFuncSetAttribute((const void*)main_kernel, hipFuncAttributeMaxDynamicSharedMemorySize, MAIN_LDS);
  main_kernel<<<256, 256, MAIN_LDS, stream>>>((const u16*)d_in[0], (const u16*)d_in[1], (const u16*)d_in[2],
                                              (const u16*)d_in[3], (const u16*)d_in[4], (const u16*)d_in[5],
                                              (const u16*)d_in[6], (const u16*)d_in[8], (const u16*)d_in[9],
                                              (const u16*)d_in[10], (const u16*)d_in[11], ws, (u16*)d_out);
}
```

```cpp
#include <hip/hip_runtime.h>
#include <hip/hip_bf16.h>

typedef unsigned short u16;
typedef unsigned int u32;
typedef long long i64;
typedef __attribute__((ext_vector_type(4))) short s16x4;
typedef __attribute__((ext_vector_type(8))) __bf16 bf16x8;
typedef __attribute__((ext_vector_type(2))) __bf16 bf16x2;
typedef __attribute__((ext_vector_type(8))) _Float16 f16x8;
typedef __attribute__((ext_vector_type(2))) _Float16 f16x2;
typedef __attribute__((ext_vector_type(4))) float f32x4;
typedef __attribute__((ext_vector_type(2))) float f32x2;
typedef __attribute__((ext_vector_type(4))) unsigned u32x4;
typedef __attribute__((ext_vector_type(2))) unsigned u32x2;
typedef __attribute__((ext_vector_type(2))) long long i64x2;
typedef __attribute__((address_space(1))) unsigned gu32;
typedef __amdgpu_buffer_rsrc_t rsrc_t;
#define LAS __attribute__((address_space(3)))
#define DEVI __device__ __forceinline__
template <int V> struct IC { static constexpr int v = V; };
template <int I, int N, class F> __device__ __forceinline__ void sfor(F&& f) {
  if constexpr (I < N) { f(IC<I>{}); sfor<I + 1, N>(f); }
}
#define RLX_AGENT __ATOMIC_RELAXED, __HIP_MEMORY_SCOPE_AGENT

constexpr int SEQ = 2048, DM = 512, NST = 128, FF = 1792;
constexpr float LN_EPS = 1e-5f;
constexpr float FX_SCALE = 16777216.0f;
constexpr float FX_INV = 1.0f / 16777216.0f;
constexpr int SC1 = 16;

constexpr int MB = 1024 * 1024;
constexpr int WS_X = 0;
constexpr int WS_W1P = 12 * MB;
constexpr int WS_W2P = 14 * MB;
constexpr int WS_H = 16 * MB;
constexpr int WS_ST = 32 * MB;
constexpr int WS_S1 = 33 * MB;
constexpr int WS_GEN = 32 * MB + 196608;
constexpr int WS_F0 = WS_GEN + 256, WS_F1 = WS_F0 + 16384;
constexpr int WS_CT = 32 * MB + 262144;
constexpr int WS_ONE = 32 * MB + 393216;
constexpr int WS_END = 34 * MB;
constexpr int CT_C = 0, CT_X = 128, CT_H = 256, CT_TMO = 288;
constexpr int SPIN_CAP = 1 << 16;

#define STAMP(k, i) do {} while (0)

DEVI float bf2f(u16 v) { return __uint_as_float((u32)v << 16); }
DEVI float lo_bf(u32 w) { return __uint_as_float(w << 16); }
DEVI float hi_bf(u32 w) { return __uint_as_float(w & 0xffff0000u); }
DEVI float rbf(float x) { return (float)(__bf16)x; }
DEVI u32 pack_bf(float a, float b) { f32x2 q = {a, b}; return __builtin_bit_cast(u32, __builtin_convertvector(q, bf16x2)); }
DEVI u32 pack_h(float a, float b) { f32x2 q = {a, b}; return __builtin_bit_cast(u32, __builtin_convertvector(q, f16x2)); }
DEVI float dot2bf(u32 a, u32 b, float c) {
  return __builtin_amdgcn_fdot2_f32_bf16(__builtin_bit_cast(bf16x2, a), __builtin_bit_cast(bf16x2, b), c, false);
}
DEVI f32x4 mfma_bf(u32x4 a, u32x4 b, f32x4 c) {
  return __builtin_amdgcn_mfma_f32_16x16x32_bf16(__builtin_bit_cast(bf16x8, a), __builtin_bit_cast(bf16x8, b), c, 0, 0, 0);
}
DEVI f32x4 mfma_h(u32x4 a, u32x4 b, f32x4 c) {
  return __builtin_amdgcn_mfma_f32_16x16x32_f16(__builtin_bit_cast(f16x8, a), __builtin_bit_cast(f16x8, b), c, 0, 0, 0);
}
DEVI u32x2 tr_read(const char* p) {
  return __builtin_bit_cast(u32x2, __builtin_amdgcn_ds_read_tr16_b64_v4i16((LAS s16x4*)p));
}
DEVI u32x4 cat2(u32x2 a, u32x2 b) { return __builtin_shufflevector(a, b, 0, 1, 2, 3); }
DEVI u32 lds_addr(const void* p) { return (u32)(size_t)(const LAS char*)p; }
#define WAIT_VM(n) asm volatile("s_waitcnt vmcnt(" #n ")" ::: "memory")
#define WAIT_VM_BAR(n) asm volatile("s_waitcnt vmcnt(" #n ")\n\ts_barrier" ::: "memory")
#define SCHED() __builtin_amdgcn_sched_barrier(0)
template <int CTRL> DEVI float dpp_add(float v) {
  return v + __uint_as_float(__builtin_amdgcn_update_dpp(0u, __float_as_uint(v), CTRL, 0xf, 0xf, true));
}
DEVI float sum_x16(float v) {
  auto r = __builtin_amdgcn_permlane16_swap(__float_as_uint(v), __float_as_uint(v), false, false);
  return __uint_as_float(r[0]) + __uint_as_float(r[1]);
}
DEVI float sum_x32(float v) {
  auto r = __builtin_amdgcn_permlane32_swap(__float_as_uint(v), __float_as_uint(v), false, false);
  return __uint_as_float(r[0]) + __uint_as_float(r[1]);
}
DEVI float wave_sum(float v) {
  v = dpp_add<0xB1>(v); v = dpp_add<0x4E>(v); v = dpp_add<0x141>(v); v = dpp_add<0x140>(v);
  return sum_x32(sum_x16(v));
}
DEVI float fx2f(u32 lo, u32 hi) { return fmaf((float)(int)hi, 4294967296.0f * FX_INV, (float)lo * FX_INV); }
DEVI i64 f2fx(float v) {
  const float a = v * (FX_SCALE / 4294967296.0f), h = __builtin_floorf(a);
  return __builtin_bit_cast(i64, u32x2{(u32)((a - h) * 4294967296.0f), (u32)(int)h});
}
DEVI float sel4(int i, float a, float b, float c, float d) {
  asm volatile("" : "+v"(a), "+v"(b), "+v"(c), "+v"(d));
  return (i & 2) ? ((i & 1) ? d : c) : ((i & 1) ? b : a);
}
DEVI int st_pos(int t) { return ((t >> 8) * 4 + (t & 3)) * 1024 + ((t >> 2) & 63) * 16; }
DEVI float rcp(float x) { return __builtin_amdgcn_rcpf(x); }
DEVI float sqrt_(float x) { return __builtin_amdgcn_sqrtf(x); }

DEVI u32 warm_code(int slice, int lane) {
  unsigned long long pc, pe;
  asm volatile("s_getpc_b64 %0\n\ts_getpc_b64 s[90:91]\n\ts_add_u32 s90, s90, code_end_marker@rel32@lo+4\n\t"
               "s_addc_u32 s91, s91, code_end_marker@rel32@hi+12\n\ts_mov_b64 %1, s[90:91]"
               : "=&s"(pc), "=s"(pe) :: "scc", "s90", "s91");
  const u32 off = min((u32)slice * 512u, (u32)(pe - pc) - 512u) + (lane & 7) * 64;
  u32 junk;
  asm volatile("s_nop 4\n\tglobal_load_dword %0, %1, %2" : "=v"(junk) : "v"(off), "s"(pc) : "memory");
  return junk;
}

DEVI int cidx(int l) {
  const int hb = (int)(pack_bf((float)l, 0.f) & 0xffffu) - (0x4300 - 128);
  return l < 128 ? l : hb;
}
struct CM { f32x2 e, f; };
DEVI f32x2 cmul(f32x2 a, const CM& m) { return a[1] * m.f + a[0] * m.e; }
DEVI CM cexp_(float a2, float br, float p) {
  const float e = __builtin_amdgcn_exp2f(a2 * p), f = __builtin_amdgcn_fractf(br * p);
  const float re = e * __builtin_amdgcn_cosf(f), im = e * __builtin_amdgcn_sinf(f);
  CM r; r.e = f32x2{re, im}; r.f = f32x2{-im, re};
  return r;
}
DEVI void st16(rsrc_t rs, int off, u32x4 v) { __builtin_amdgcn_raw_buffer_store_b128(v, rs, off, 0, SC1); }

DEVI void xpose(int b, const u32x4 (&v)[8], rsrc_t rs, char* smem) {
  const int t = threadIdx.x, lane = t & 63, w = t >> 6;
  constexpr int RS = 1024 + 32;
#pragma unroll
  for (int r = 0; r < 8; ++r) *(u32x4*)(smem + (w + 4 * r) * RS + lane * 16) = v[r];
  __syncthreads();
  const int g = lane >> 4, q = (lane & 15) >> 2, p = lane & 3;
#pragma unroll
  for (int cf = 0; cf < 8; ++cf) {
    const int c0 = (w * 8 + cf) * 16;
    const char* pa = smem + (8 * g + q) * RS + c0 * 2 + 8 * p;
    const u32x4 o = cat2(tr_read(pa), tr_read(pa + 4 * RS));
    st16(rs, WS_X + b * 32768 + (c0 + (lane & 15)) * 64 + g * 16, o);
  }
}
DEVI void row_stats(int b, const u32x4 (&v)[8], rsrc_t rs) {
  const int t = threadIdx.x, lane = t & 63, w = t >> 6;
  const int t0 = b * 32;
  float a[16];
#pragma unroll
  for (int r = 0; r < 8; ++r) {
    float s = 0.f, ss = 0.f;
#pragma unroll
    for (int j = 0; j < 4; ++j) { s = dot2bf(v[r][j], 0x3f803f80u, s); ss = dot2bf(v[r][j], v[r][j], ss); }
    a[2 * r] = s; a[2 * r + 1] = ss;
  }
  const bool o1 = lane & 1, o2 = lane & 2;
#pragma unroll
  for (int k = 0; k < 8; ++k) {
    const float keep = o1 ? a[2 * k + 1] : a[2 * k], send = o1 ? a[2 * k] : a[2 * k + 1];
    a[k] = keep + __uint_as_float(__builtin_amdgcn_update_dpp(0u, __float_as_uint(send), 0xB1, 0xf, 0xf, true));
  }
#pragma unroll
  for (int k = 0; k < 4; ++k) {
    const float keep = o2 ? a[2 * k + 1] : a[2 * k], send = o2 ? a[2 * k] : a[2 * k + 1];
    a[k] = keep + __uint_as_float(__builtin_amdgcn_update_dpp(0u, __float_as_uint(send), 0x4E, 0xf, 0xf, true));
  }
#pragma unroll
  for (int k = 0; k < 4; ++k) a[k] = sum_x32(sum_x16(dpp_add<0x128>(dpp_add<0x124>(a[k]))));
  const i64 sv = f2fx(sel4(lane >> 2, a[0], a[1], a[2], a[3]));
  if (lane < 16)
    __builtin_amdgcn_raw_buffer_store_b64(__builtin_bit_cast(u32x2, sv), rs, st_pos(t0 + w) + lane * 8, WS_ST, SC1);
  if (t >= 96 && t < 128) st16(rs, WS_ST + 65536 + b * 512 + (t - 96) * 16, u32x4{0, 0, 0, 0});
  if (t < 80) st16(rs, WS_CT + b * 1280 + t * 16, u32x4{0, 0, 0, 0});
}

DEVI void pack_w1(int ks, int cg, const u32x4 (&v)[8], rsrc_t rs, char* smem) {
  const int t = threadIdx.x, lane = t & 63, w = t >> 6;
  constexpr int RS = 512 + 32;
#pragma unroll
  for (int it = 0; it < 4; ++it) *(u32x4*)(smem + (it * 8 + (t >> 5)) * RS + (t & 31) * 16) = v[it];
  __syncthreads();
  const int g = lane >> 4, q = (lane & 15) >> 2, p = lane & 3;
  float cs[4];
#pragma unroll
  for (int h = 0; h < 4; ++h) {
    const char* a = smem + (8 * g + q) * RS + (w * 4 + h) * 32 + 8 * p;
    const u32x4 o = cat2(tr_read(a), tr_read(a + 4 * RS));
    st16(rs, WS_W1P + (((cg * 16 + w * 4 + h) * 16 + ks) * 64 + lane) * 16, o);
    float s = 0.f;
#pragma unroll
    for (int j = 0; j < 4; ++j) s = dot2bf(o[j], 0x3f803f80u, s);
    cs[h] = sum_x32(sum_x16(s));
  }
  __builtin_amdgcn_raw_buffer_store_b32(__float_as_uint(sel4(g, cs[0], cs[1], cs[2], cs[3])), rs, (ks * FF + cg * 256 + t) * 4, WS_S1, SC1);
  if (ks == 0 && cg == 0) st16(rs, WS_ONE + t * 16, u32x4{0x3f803f80u, 0x3f803f80u, 0x3f803f80u, 0x3f803f80u});
}

constexpr int W2_RS = 256 + 32;
DEVI void pack_w2_put(const u32x4 (&v)[2], char* smem) {
  const int t = threadIdx.x;
#pragma unroll
  for (int r = 0; r < 2; ++r) *(u32x4*)(smem + (r * 16 + (t >> 4)) * W2_RS + (t & 15) * 16) = v[r];
}
DEVI void pack_w2_out(int b, rsrc_t rs, const char* smem) {
  const int t = threadIdx.x, lane = t & 63, w = t >> 6;
  const int g = lane >> 4, q = (lane & 15) >> 2, p = lane & 3;
#pragma unroll
  for (int cf = 0; cf < 2; ++cf) {
    const char* a = smem + (4 * g + q) * W2_RS + (w * 2 + cf) * 32 + 8 * p;
    const u32x4 o = cat2(tr_read(a), tr_read(a + 16 * W2_RS));
    st16(rs, b < 224 ? WS_W2P + ((((b & 3) * 8 + w * 2 + cf) * 56 + (b >> 2)) * 64 + lane) * 16 : 0x40000000, o);
  }
}

constexpr int L_KC = 4160;
constexpr int L_U = 4 * L_KC, L_UC = 4608;
constexpr int L_XA = 26112, L_XB = L_XA + 65536;
constexpr int L_MISC = L_XB + 65536;
constexpr int L_CST = L_MISC + 1024;
constexpr int L_SIG = L_CST + 4 * 256;
constexpr int MAIN_LDS = L_SIG + 256;
constexpr int L_RED = L_XB;

DEVI u32x4 ld128(rsrc_t rs, int v, int s) { return __builtin_amdgcn_raw_buffer_load_b128(rs, v, s, SC1); }
DEVI u32x2 ld64(rsrc_t rs, int v, int s) { return __builtin_amdgcn_raw_buffer_load_b64(rs, v, s, SC1); }
DEVI void st128(rsrc_t rs, int v, int s, u32x4 d) { __builtin_amdgcn_raw_buffer_store_b128(d, rs, v, s, SC1); }
DEVI void st64(rsrc_t rs, int v, int s, u32x2 d) { __builtin_amdgcn_raw_buffer_store_b64(d, rs, v, s, SC1); }
template <int OFF> DEVI u32x4 ld128a(rsrc_t rs, int v, int s) {
  u32x4 r;
  asm volatile("buffer_load_dwordx4 %0, %1, %2, %3 offen offset:%4 sc1" : "=a"(r) : "v"(v), "s"(rs), "s"(s), "i"(OFF) : "memory");
  return r;
}
template <int OFF> DEVI u32x4 ld128v(rsrc_t rs, int v, int s) {
  u32x4 r;
  asm volatile("buffer_load_dwordx4 %0, %1, %2, %3 offen offset:%4 sc1" : "=v"(r) : "v"(v), "s"(rs), "s"(s), "i"(OFF) : "memory");
  return r;
}
template <int OFF> DEVI void dma1k(rsrc_t rs, u32 lds_dst, int v, int soff) {
  asm volatile("s_mov_b32 m0, %0\n\ts_nop 0\n\tbuffer_load_dwordx4 %1, %2, %3 offen offset:%4 sc1 lds"
               :: "s"(lds_dst), "v"(v), "s"(rs), "s"(soff), "i"(OFF) : "memory");
}
#define MMA(NAME, OP, CA, CB)                                                                                   \
  template <bool Z> DEVI void NAME(f32x4& d, const u32x4& a, const u32x4& b) {                                  \
    if (Z) asm volatile(OP " %0, %1, %2, 0" : "=&v"(d) : CA(a), CB(b));                                         \
    else asm volatile(OP " %0, %1, %2, %0" : "+v"(d) : CA(a), CB(b));                                           \
  }
MMA(mma_vv, "v_mfma_f32_16x16x32_bf16", "v", "v")
MMA(mma_av, "v_mfma_f32_16x16x32_bf16", "a", "v")
MMA(mma_va, "v_mfma_f32_16x16x32_bf16", "v", "a")
MMA(mma_hh, "v_mfma_f32_16x16x32_f16", "v", "v")
#define MFMA_DONE() do { SCHED(); asm volatile("s_nop 7" ::: "memory"); SCHED(); } while (0)

struct Ctx {
  rsrc_t rs; gu32* ct; char* smem; char* ws; u16* out;
  int lane0, w, b, P, sub, ct_, ff0, hks, eo, f1o, f1s;
  float dsk0, dsk1, ks0, ks1, bb2, cs1;
  u32 tag;
};
DEVI int fresh_lane(const Ctx& c) { int l = c.lane0; asm volatile("" : "+v"(l)); return l; }
DEVI u32 sig_begin(const Ctx& c, int lane) {
  u32 old;
  asm volatile("ds_add_rtn_u32 %0, %1, %2" : "=v"(old) : "v"(lds_addr(c.smem) + L_SIG + lane * 4), "v"(1u) : "memory");
  return old;
}
template <bool ALL, bool BACK = false> DEVI void sig_end(const Ctx& c, int lane, u32 old, int line) {
  if (BACK) asm volatile("" : "+v"(old) :: "memory");
  else asm volatile("s_waitcnt lgkmcnt(0)" : "+v"(old) :: "memory");
  const bool last = (__builtin_amdgcn_readfirstlane(old) & 3u) == 3u;
  const int v = (last && lane < (ALL ? 8 : 1)) ? (ALL ? lane * 2048 : 0) : 0x40000000;
  asm volatile("s_nop 4\n\tbuffer_atomic_add %0, %1, %2, %3 offen" :: "v"(1u), "v"(v), "s"(c.rs),
               "s"(WS_CT + (line + (ALL ? (c.b >> 3) & 7 : 0)) * 256) : "memory");
}
template <bool ALL, int R = 0> DEVI void poll(const Ctx& c, int lane, int line) {
  const int v = ALL ? (lane & 7) * 256 : 0, so = WS_CT + (line + (ALL ? (c.b & 7) * 8 : 0)) * 256;
  if (R == 0) asm volatile("s_nop 4\n\tbuffer_load_dword v254, %0, %1, %2 offen sc1" :: "v"(v), "s"(c.rs), "s"(so) : "memory", "v254");
  else asm volatile("s_nop 4\n\tbuffer_load_dword v255, %0, %1, %2 offen sc1" :: "v"(v), "s"(c.rs), "s"(so) : "memory", "v255");
}
template <int YOUNGER, int R = 0> DEVI bool got(u32 want) {
  unsigned long long m;
  if (R == 0) asm volatile("s_waitcnt vmcnt(%2)\n\tv_cmp_le_u32_e64 %0, %1, v254" : "=s"(m) : "s"(want), "i"(YOUNGER) : "memory");
  else asm volatile("s_waitcnt vmcnt(%2)\n\tv_cmp_le_u32_e64 %0, %1, v255" : "=s"(m) : "s"(want), "i"(YOUNGER) : "memory");
  return m == ~0ull;
}
template <bool ALL> DEVI void wait_ge(const Ctx& c, int lane, int line, u32 want) {
  int sp = 0;
#pragma nounroll
  do {

    poll<ALL, 0>(c, lane, line);
    if (++sp > SPIN_CAP) { __hip_atomic_store(c.ct + CT_TMO * 64, 1u, RLX_AGENT); break; }
  } while (!got<0, 0>(want));
}

DEVI void poll0(const Ctx& c, int lane, int set) {
  asm volatile("s_nop 4\n\tbuffer_load_dwordx2 v[254:255], %0, %1, %2 offen sc1" :: "v"((lane & 7) * 256 + (lane >> 3) * 8), "s"(c.rs),
               "s"(set + (c.b & 7) * 2048) : "memory", "v254", "v255");
}
template <int YOUNGER> DEVI bool got0(u32 tag) {
  unsigned long long m, m2;
  asm volatile("s_waitcnt vmcnt(%3)\n\tv_cmp_eq_u32_e64 %0, %2, v254\n\tv_cmp_eq_u32_e64 %1, %2, v255"
               : "=&s"(m), "=s"(m2) : "s"(tag), "i"(YOUNGER) : "memory");
  return (m & m2) == ~0ull;
}
DEVI void wait0(const Ctx& c, int lane, int set) {
  int sp = 0;
#pragma nounroll
  do {
    poll0(c, lane, set);
    if (++sp > SPIN_CAP) { __hip_atomic_store(c.ct + CT_TMO * 64, 1u, RLX_AGENT); break; }
  } while (!got0<0>(c.tag));
}

struct CIn { u32x2 x0, x1; u32x4 s[4]; };
DEVI void conv_load(const Ctx& c, int lane, CIn& in, int xoff, int stoff, int h) {
  const int sx = xoff + (32 * h + 8 * c.w) * 32768 + c.b * 128;
  const int vx = (lane >> 3) * 32768 + (lane & 7) * 8;
  const int ss = stoff + (1024 * h + 256 * c.w) * 16;
#pragma unroll
  for (int j = 0; j < 4; ++j) in.s[j] = ld128(c.rs, lane * 16 + j * 1024, ss);
  in.x0 = ld64(c.rs, vx, sx);
  in.x1 = ld64(c.rs, vx + 64, sx);
}
DEVI void conv_load_u(const Ctx& c, int lane, CIn& in, int xoff, int stoff, int h) {
  const int sx = xoff + (32 * h + 8 * c.w) * 32768 + c.b * 128;
  const int vx = (lane >> 3) * 32768 + (lane & 7) * 8;
  const int ss = stoff + (1024 * h + 256 * c.w) * 16;
  asm volatile("s_nop 4\n\t"
               "buffer_load_dwordx4 %0, %6, %8, %9 offen sc1\n\tbuffer_load_dwordx4 %1, %6, %8, %9 offen offset:1024 sc1\n\t"
               "buffer_load_dwordx4 %2, %6, %8, %9 offen offset:2048 sc1\n\tbuffer_load_dwordx4 %3, %6, %8, %9 offen offset:3072 sc1\n\t"
               "buffer_load_dwordx2 %4, %7, %8, %10 offen sc1\n\tbuffer_load_dwordx2 %5, %7, %8, %10 offen offset:64 sc1"
               : "=&v"(in.s[0]), "=&v"(in.s[1]), "=&v"(in.s[2]), "=&v"(in.s[3]), "=&v"(in.x0), "=&v"(in.x1)
               : "v"(lane * 16), "v"(vx), "s"(c.rs), "s"(ss), "s"(sx) : "memory");
}
DEVI void conv_landed(CIn& in) {
  asm volatile("s_waitcnt vmcnt(2)" : "+v"(in.s[0]), "+v"(in.s[1]), "+v"(in.s[2]), "+v"(in.s[3]), "+v"(in.x0), "+v"(in.x1) :: "memory");
}
DEVI void conv_ln(const Ctx& c, int lane, const CIn& in, int h, f32x4& base0, f32x4& base1) {
  constexpr float CH = 4294967296.0f * FX_INV / DM, CL = FX_INV / DM;
  f32x4 m, r;
#pragma unroll
  for (int j = 0; j < 4; ++j) {
    m[j] = fmaf((float)(int)in.s[j][1], CH, (float)in.s[j][0] * CL);
    r[j] = fmaf((float)(int)in.s[j][3], CH, fmaf((float)in.s[j][2], CL, LN_EPS));
  }
#pragma unroll
  for (int j = 0; j < 4; ++j) r[j] = __builtin_amdgcn_rsqf(fmaxf(fmaf(-m[j], m[j], r[j]), LN_EPS));
  const f32x4 rr = r, mr = -m * r;
  const f32x4 x0 = {lo_bf(in.x0[0]), hi_bf(in.x0[0]), lo_bf(in.x0[1]), hi_bf(in.x0[1])};
  const f32x4 x1 = {lo_bf(in.x1[0]), hi_bf(in.x1[0]), lo_bf(in.x1[1]), hi_bf(in.x1[1])};
  const f32x4 u0 = x0 * rr + mr, u1 = x1 * rr + mr;
  char* up = c.smem + L_U + 512 + (1024 * h + 256 * c.w + 4 * lane) * 2;
  *(u32x2*)up = u32x2{pack_h(u0[0], u0[1]), pack_h(u0[2], u0[3])};
  *(u32x2*)(up + L_UC) = u32x2{pack_h(u1[0], u1[1]), pack_h(u1[2], u1[3])};
  base0 = u0 * c.dsk0 + x0; base1 = u1 * c.dsk1 + x1;
}
struct KB { const char* p[4]; };
template <int MM, int LAM> DEVI u32x4 conv_k1(const KB& kb) {
  const u32* kp = (const u32*)(kb.p[(7 - LAM) >> 1] + 64 * (3 - MM) + 512 * ((7 - LAM) & 1));
  return u32x4{kp[0], kp[1], kp[2], kp[3]};
}
template <int MM, int TB> DEVI u32x4 conv_u1(const char* ub) { return *(const u32x4*)(ub + 64 * (3 - MM) + 512 * TB); }
template <int LO> DEVI void conv_ldk(const KB& kb, u32x4 (&af)[4]) {
  sfor<0, 4>([&](auto L) { af[L.v] = conv_k1<0, LO + L.v>(kb); });
}
constexpr int conv_rank(int DT, int LO, int gr, int lt) {
  int k = 0;
  for (int x = 0; x < lt; ++x) { const int tb = DT + (x & 3) - (LO + 4 * gr + (x >> 2)); k += (tb >= 0 && tb < 4); }
  return k;
}
template <int DT, int TB0, bool ZERO, class F>
DEVI void conv_mma(const char* ub, const KB& kb, f32x4 (&acc)[4], const u32x4 (&af0)[4], F&& hook) {
  constexpr int NG = DT ? 2 : 1, LO = DT ? 1 : 0;
  u32x4 af[2][4], bf[2][4];
  sfor<0, 4>([&](auto L) { af[0][L.v] = af0[L.v]; });
  sfor<0, 4>([&](auto T) { constexpr int tb = DT ? 3 - T.v : T.v; bf[0][tb] = conv_u1<0, TB0 + tb>(ub); });
  SCHED();
  sfor<0, 4 * NG>([&](auto ST) {
    constexpr int st = ST.v, mm = st / NG, gr = st % NG;
    constexpr bool more = st + 1 < 4 * NG;
    constexpr int mn = (st + 1) / NG, gn = (st + 1) % NG;
    constexpr bool nu = more && gr == NG - 1;
    sfor<0, 16>([&](auto LT) {
      constexpr int l = LT.v >> 2, T = LT.v & 3, tb = DT + T - (LO + 4 * gr + l);
      if constexpr (tb >= 0 && tb < 4) {
        constexpr int k = conv_rank(DT, LO, gr, LT.v);
        constexpr bool first = ZERO && st == 0 && (DT ? (l == T) : (l == 0));
        mma_hh<first>(acc[T], af[st & 1][l], bf[mm & 1][tb]);
        sfor<(gr == 1 ? 2 * k : k), (gr == 1 ? 2 * k + 2 : k + 1)>([&](auto J) {
          constexpr int j = J.v;
          if constexpr (nu && j < 8) {
            if constexpr (DT != 0) {
              if constexpr (j & 1) bf[mn & 1][3 - (j >> 1)] = conv_u1<mn, TB0 + 3 - (j >> 1)>(ub);
              else af[(st + 1) & 1][j >> 1] = conv_k1<mn, LO + (j >> 1)>(kb);
            } else {
              if constexpr (j >= 1 && j <= 4) bf[mn & 1][j - 1] = conv_u1<mn, TB0 + j - 1>(ub);
              else af[(st + 1) & 1][j ? j - 4 : 0] = conv_k1<mn, LO + (j ? j - 4 : 0)>(kb);
            }
          } else if constexpr (more && !nu && j < 3) af[(st + 1) & 1][j] = conv_k1<mn, LO + 4 * gn + j>(kb);
        });
        SCHED();
        hook(IC<mm * (DT ? 16 : 10) + gr * 10 + k>{});
      }
    });
  });
}
DEVI void conv_part(const Ctx& c, int lane, const f32x4 (&acc)[4]) {
  char* red = c.smem + L_RED;
#pragma unroll
  for (int T = 0; T < 4; ++T) *(f32x4*)(red + (c.w * 4 + T) * 1024 + (lane & 15) * 64 + (lane >> 4) * 16) = acc[T];
}
DEVI void conv_out(const Ctx& c, int lane, const f32x4& base0, const f32x4& base1, int xoff, int h) {
  const int sx = xoff + (16 * h + 4 * c.w) * 65536 + c.b * 256;
  const int vx = (lane >> 4) * 65536 + (lane & 15) * 8;
  const char* p = c.smem + L_RED + c.w * 1024 + lane * 16;
  const f32x4 a0 = *(const f32x4*)p, b0 = *(const f32x4*)(p + 4096), a1 = *(const f32x4*)(p + 8192), b1 = *(const f32x4*)(p + 12288);
  SCHED();
  const f32x4 y0 = (a0 + b0) * c.ks0 + base0, y1 = (a1 + b1) * c.ks1 + base1;
  st64(c.rs, vx, sx, u32x2{pack_bf(y0[0], y0[1]), pack_bf(y0[2], y0[3])});
  st64(c.rs, vx + 128, sx, u32x2{pack_bf(y1[0], y1[1]), pack_bf(y1[2], y1[3])});
}

template <int PC> DEVI void g1_dma1(const Ctx& c, int ve, int vo, int ldsx, int soff) {
  dma1k<(PC & 3) * 1024>(c.rs, lds_addr(c.smem) + ldsx + (4 * (PC >> 2) + c.w) * 4096, (PC & 1) ? vo : ve, soff + (4 * (PC >> 2) + c.w) * 4096);
}
template <bool ALL> DEVI void sig_bar(const Ctx& c, int lane, int line) {
  const int v = (c.w == 0 && lane < (ALL ? 8 : 1)) ? (ALL ? lane * 2048 : 0) : 0x40000000;
  asm volatile("s_nop 4\n\tbuffer_atomic_add %0, %1, %2, %3 offen" :: "v"(1u), "v"(v), "s"(c.rs),
               "s"(WS_CT + (line + (ALL ? (c.b >> 3) & 7 : 0)) * 256) : "memory");
}
template <class F, class G>
DEVI void g1_mma(const char* const (&xb)[4], const u32x4 (&w1f)[2][16], f32x4 (&acc)[2][4], f32x4& sa, F&& mid, G&& hook) {
  u32x2 xf[2][4][2];
#pragma unroll
  for (int i = 0; i < 4; ++i) { xf[0][i][0] = tr_read(xb[i]); xf[0][i][1] = tr_read(xb[i] + 512); }
  SCHED();
  sfor<0, 16>([&](auto KS) {
    constexpr int ks = KS.v;
    mid(KS);
    sfor<0, 4>([&](auto I) {
      constexpr int i = I.v;
      const u32x4 x = cat2(xf[ks & 1][i][0], xf[ks & 1][i][1]);
      mma_vv<ks == 0>(acc[0][i], w1f[0][ks], x);
      if constexpr (ks < 15) xf[(ks + 1) & 1][i][0] = tr_read(xb[i] + (ks + 1) * 4096);
      SCHED();
      if constexpr (ks < 8) mma_av<ks == 0>(acc[1][i], w1f[1][ks], x); else mma_vv<false>(acc[1][i], w1f[1][ks], x);
      if constexpr (ks < 15) xf[(ks + 1) & 1][i][1] = tr_read(xb[i] + (ks + 1) * 4096 + 512);
      SCHED();
      if constexpr (i == 0) { mma_vv<ks == 0>(sa, x, x); hook(IC<2 * ks>{}); SCHED(); }
      if constexpr (i == 2) { hook(IC<2 * ks + 1>{}); SCHED(); }
    });
  });
  MFMA_DONE();
}
DEVI void st_h(const Ctx& c, int v, int so0, int so1, u32x4 a, u32x4 b) {
  const u32x2 al = {a[0], a[1]}, bl = {b[0], b[1]};
  asm volatile("s_cmp_eq_u32 %0, 3\n\ts_nop 3\n\ts_cbranch_scc1 1f\n\t"
               "buffer_store_dwordx4 %5, %1, %2, %3 offen sc1\n\tbuffer_store_dwordx4 %6, %1, %2, %4 offen sc1\n\ts_branch 2f\n"
               "1:\n\tbuffer_store_dwordx2 %7, %1, %2, %3 offen sc1\n\tbuffer_store_dwordx2 %8, %1, %2, %4 offen sc1\n"
               "2:\n\ts_nop 1"
               :: "s"(c.w), "v"(v), "s"(c.rs), "s"(so0), "s"(so1), "v"(a), "v"(b), "v"(al), "v"(bl) : "memory", "scc");
}
template <class F>
DEVI void g1_epi(const Ctx& c, int lane, f32x4 (&acc)[2][4], const f32x4& sa, int hoff, F&& hook) {
  const int g = lane >> 4, cc = lane & 15, q = cc >> 2, p = lane & 3, w = c.w;
  float* sums = (float*)(c.smem + L_MISC);
  {
    const float d = sel4(p, sa[0], sa[1], sa[2], sa[3]);
    sums[q == g ? (w * 16 + cc) * 2 + 1 : 128 + lane] = d;
    const float r = sel4(g, acc[1][1][0], acc[1][2][0], acc[1][3][0], acc[1][0][0]);
    sums[w == 3 ? lane * 2 : 192 + lane] = r;
  }
  f32x4 s1v[2], b1v[2];
#pragma unroll
  for (int j = 0; j < 2; ++j) {
    s1v[j] = *(const f32x4*)(c.smem + L_CST + w * 256 + j * 128 + g * 16);
    b1v[j] = *(const f32x4*)(c.smem + L_CST + w * 256 + j * 128 + 64 + g * 16);
  }
  const int vh = w < 3 ? lane * 16 : lane * 8 + c.eo * 512;
  __syncthreads();
  f32x2 k[4];
#pragma unroll
  for (int i = 0; i < 4; ++i) k[i] = *(const f32x2*)(sums + (((i + w) & 3) * 16 + cc) * 2);
#pragma unroll
  for (int i = 0; i < 4; ++i) {
    const float m = k[i][0] * (1.0f / DM);
    const float ve = fmaxf(fmaf(-m, m, fmaf(k[i][1], 1.0f / DM, LN_EPS)), LN_EPS);
    const float r = __builtin_amdgcn_rsqf(fmaf(ve, 1.0f + LN_EPS, -LN_EPS));
    k[i] = f32x2{r, -m * r};
  }
  SCHED();
  hook(IC<-1>{});
  SCHED();
  sfor<0, 2>([&](auto IH) {
    constexpr int ih = IH.v;
    f32x4 x[4], e[4];
#define STAGE(n) do { SCHED(); hook(IC<10 * ih + n>{}); SCHED(); } while (0)
#pragma unroll
    for (int n = 0; n < 4; ++n) x[n] = k[2 * ih + (n >> 1)][1] * s1v[n & 1] + b1v[n & 1];
#pragma unroll
    for (int n = 0; n < 4; ++n) x[n] = acc[n & 1][2 * ih + (n >> 1)] * k[2 * ih + (n >> 1)][0] + x[n];
    STAGE(0);
#pragma unroll
    for (int n = 0; n < 4; ++n) e[n] = x[n] * x[n];
#pragma unroll
    for (int n = 0; n < 4; ++n) e[n] = e[n] * (-0.0713548162726f * 1.44269504088896f) + (-1.59576912160573f * 1.44269504088896f);
    STAGE(1);
#pragma unroll
    for (int n = 0; n < 4; ++n) e[n] = e[n] * x[n];
    STAGE(2);
#pragma unroll
    for (int r = 0; r < 4; ++r) { e[0][r] = __builtin_amdgcn_exp2f(e[0][r]); e[1][r] = __builtin_amdgcn_exp2f(e[1][r]); }
    STAGE(3);
#pragma unroll
    for (int r = 0; r < 4; ++r) { e[2][r] = __builtin_amdgcn_exp2f(e[2][r]); e[3][r] = __builtin_amdgcn_exp2f(e[3][r]); }
    STAGE(4);
#pragma unroll
    for (int n = 0; n < 4; ++n) e[n] = e[n] + 1.0f;
    STAGE(5);
#pragma unroll
    for (int r = 0; r < 4; ++r) { e[0][r] = rcp(e[0][r]); e[1][r] = rcp(e[1][r]); }
    STAGE(6);
#pragma unroll
    for (int r = 0; r < 4; ++r) { e[2][r] = rcp(e[2][r]); e[3][r] = rcp(e[3][r]); }
    STAGE(7);
#pragma unroll
    for (int n = 0; n < 4; ++n) x[n] = x[n] * e[n];
    STAGE(8);
    u32x4 hv[2];
#pragma unroll
    for (int n = 0; n < 4; ++n) { hv[n >> 1][2 * (n & 1)] = pack_bf(x[n][0], x[n][1]); hv[n >> 1][2 * (n & 1) + 1] = pack_bf(x[n][2], x[n][3]); }
    STAGE(9);
    const int so0 = hoff + ((2 * ih + w) & 3) * 56 * 1024, so1 = hoff + ((2 * ih + 1 + w) & 3) * 56 * 1024;
    st_h(c, vh, so0, so1, hv[0], hv[1]);
    SCHED();
    if constexpr (ih == 0) { hook(IC<-2>{}); SCHED(); }
#undef STAGE
  });
}

constexpr int PF = 10;
template <int N> DEVI void ld_w1(const Ctx& c, int v, u32x4 (&w1f)[2][16]) {
  constexpr int ks = N >> 1, j = N & 1;
  const int so = (j ? c.f1o + (ks >> 2) * c.f1s : WS_W1P + c.ff0 * 16384 + (ks >> 2) * 4096);
  if constexpr (j && ks < 8) w1f[j][ks] = ld128a<(ks & 3) * 1024>(c.rs, v, so);
  else w1f[j][ks] = ld128v<(ks & 3) * 1024>(c.rs, v, so);
}
template <int N> DEVI void ld_w2(const Ctx& c, int v, u32x4 (&w2f)[14][4]) {
  constexpr int s = N >> 2, j = N & 3;
  w2f[s][j] = ld128a<(s & 3) * 1024>(c.rs, v, WS_W2P + ((c.ct_ * 4 + ((c.w + j) & 3)) * 56 + 14 * c.w + (s & ~3)) * 1024);
}
template <int N> DEVI void g2_ldh1(const Ctx& c, int v16, u32x4 (&hf)[14][2], int ho) {
  constexpr int s = N % 14, i = N / 14;
  if constexpr (s % 7 == 6)
    hf[s][i] = cat2(ld64(c.rs, (v16 >> 1) + (s & 3) * 1024, ho + i * 56 * 1024 + (s >> 2) * 4096),
                    ld64(c.rs, (v16 >> 1) + (s & 3) * 1024 + 512, ho + i * 56 * 1024 + (s >> 2) * 4096));
  else hf[s][i] = ld128(c.rs, v16 + (s & 3) * 1024, ho + i * 56 * 1024 + (s >> 2) * 4096);
}
template <int PRE, int RATE, class F>
DEVI void g2_mma(const Ctx& c, int lane, u32x4 (&hf)[14][2], const u32x4 (&w2f)[14][4], f32x4 (&acc)[2][4], int ho, int h, F&& mid) {
  const int v16 = lane * 16;
  char* red = c.smem + L_XA + h * 32768;
  sfor<0, 28>([&](auto N) {
    constexpr int i = N.v / 14, s = N.v % 14;
    sfor<0, 4>([&](auto J) {
      constexpr int j = J.v;
      mma_va<s == 0>(acc[i][j], hf[s][i], w2f[s][j]);
      constexpr int per = 4 / RATE, n = PRE + RATE * N.v + j / per;
      if constexpr (j % per == per - 1 && n < 28) { g2_ldh1<n>(c, v16, hf, ho); SCHED(); }
      if constexpr (N.v == 17 && j > 0) { *(f32x4*)(red + ((((c.w + j) & 3) * 3 + j - 1) * 2) * 1024 + v16) = acc[0][j]; SCHED(); }
    });
    SCHED();
    mid(N);
  });
  MFMA_DONE();
}
template <bool LAST, class F>
DEVI void g2_epi(const Ctx& c, int lane, const f32x4 (&acc)[2][4], const u32x2 (&xr)[2], int h, int xdst, F&& mid) {
  const int g = lane >> 4, cc = lane & 15, q = cc >> 2, p = lane & 3, w = c.w, t = w * 64 + lane;
  char* red = c.smem + L_XA + h * 32768;
#pragma unroll
  for (int jj = 1; jj < 4; ++jj) *(f32x4*)(red + ((((w + jj) & 3) * 3 + jj - 1) * 2 + 1) * 1024 + lane * 16) = acc[1][jj];
  __syncthreads();
  char* xt = red + 24576;
  f32x4 pr[2][3];
#pragma unroll
  for (int i = 0; i < 2; ++i)
#pragma unroll
    for (int rk = 0; rk < 3; ++rk) pr[i][rk] = *(const f32x4*)(red + ((w * 3 + rk) * 2 + i) * 1024 + lane * 16);
  mid(0);
#pragma unroll
  for (int i = 0; i < 2; ++i) {
    const f32x4 xv = {lo_bf(xr[i][0]), hi_bf(xr[i][0]), lo_bf(xr[i][1]), hi_bf(xr[i][1])};
    const f32x4 o = ((acc[i][0] + (xv + c.bb2)) + pr[i][0]) + (pr[i][1] + pr[i][2]);
    const u32x2 r = {pack_bf(o[0], o[1]), pack_bf(o[2], o[3])};
    *(u32x2*)(xt + (w * 16 + cc) * 64 + ((i ^ (cc >> 3)) * 32) + g * 8) = r;
  }
  __syncthreads();
  const char* xq = xt + ((w >> 1) * 32 + 8 * g + q) * 64 + (((w & 1) ^ (g & 1)) * 32) + 8 * p;
  const u32x4 f0 = cat2(tr_read(xq), tr_read(xq + 256));
  const int tok = (16 * h + c.P) * 64 + c.sub * 32 + (w & 1) * 16 + cc;
  if (LAST) {
    *(u32x4*)(c.out + (size_t)tok * DM + c.ct_ * 64 + (w >> 1) * 32 + 8 * g) = f0;
  } else {
    const int row = t >> 2;
    const u32x4 v = *(const u32x4*)(xt + row * 64 + (((t & 3) ^ ((row >> 2) & 2)) * 16));
    st128(c.rs, c.ct_ * 4096 + t * 16, xdst, v);
    const char* xo = xq + ((w & 2) ? -2048 : 2048);
    const u32x4 f1 = cat2(tr_read(xo), tr_read(xo + 256));
    float s = 0.f, ss = 0.f, s2 = 0.f, ss2 = 0.f;
#pragma unroll
    for (int j = 0; j < 4; ++j) {
      s = dot2bf(f0[j], 0x3f803f80u, s); ss = dot2bf(f0[j], f0[j], ss);
      s2 = dot2bf(f1[j], 0x3f803f80u, s2); ss2 = dot2bf(f1[j], f1[j], ss2);
    }
    s = sum_x32(sum_x16(s + s2)); ss = sum_x32(sum_x16(ss + ss2));
    asm volatile("s_nop 4\n\tbuffer_atomic_add_x2 %0, %1, %2, %3 offen" :: "v"(f2fx((g & 1) ? ss : s)),
                 "v"(t < 128 && lane < 32 ? st_pos(tok) + g * 8 : 0x40000000), "s"(c.rs), "s"(WS_ST + 65536) : "memory");
  }
}

template <int LAYER>
DEVI void layer_body(const Ctx& c, u32x4 (&w1f)[2][16], u32x4 (&w2f)[14][4], CIn& inA, CIn& inB) {
  constexpr int xin = WS_X + LAYER * 4 * MB, xmid = xin + 2 * MB, xnext = xin + 4 * MB;
  constexpr int hbuf = WS_H + LAYER * 8 * MB, stin = WS_ST + LAYER * 65536;
  constexpr u32 ep = LAYER + 1;
  constexpr int K1 = 1 + 3 * LAYER, K2 = K1 + 1, K3 = K1 + 2;
  const int w = c.w;
  char* smem = c.smem;
  auto nop = [](auto) {};
  {
    const int lane = fresh_lane(c);
    const int g = lane >> 4, cc = lane & 15, mu0 = (w & 1) * 4, v16 = lane * 16;
    const char* ub = smem + L_U + (w >> 1) * L_UC + 512 + 2 * (16 * cc - 16 + 8 * g) - 64 * (mu0 + 3);
    KB kb;
#pragma unroll
    for (int j = 0; j < 4; ++j) {
      u32 a = lds_addr(smem) + (w >> 1) * 2 * L_KC + ((cc & 1) ? 0 : L_KC) + 2 * (2031 - cc + 8 * g - ((cc & 1) ? 0 : 1)) - 64 * (mu0 + 3 + 56) + 1024 * j;
      asm volatile("" : "+v"(a));
      kb.p[j] = (const char*)(const LAS char*)(size_t)a;
    }
    STAMP(K1, 1);
    f32x4 base0, base1;
    u32x4 a4[4];
    conv_ldk<0>(kb, a4);
    if (LAYER) { SCHED(); conv_landed(inA); }
    conv_ln(c, lane, inA, 0, base0, base1);
    if (!LAYER) poll0(c, lane, WS_F1);
    __syncthreads();
    STAMP(K1, 2);
    f32x4 acc[4];
    u32 o = 0;
    conv_mma<0, 0, true>(ub, kb, acc, a4, [&](auto N) {
      constexpr int n = N.v;
      if constexpr (LAYER && n == 19) { WAIT_VM(0); o = sig_begin(c, lane); SCHED(); }
      if constexpr (LAYER && n == 30) { sig_end<true, true>(c, lane, o, CT_X + 64); SCHED(); }
    });
    MFMA_DONE();
    conv_part(c, lane, acc);
    conv_ldk<1>(kb, a4);
    __syncthreads();
    float s1p[8];
    if (!LAYER) {
      if (!got0<0>(c.tag)) wait0(c, lane, WS_F1);
      asm volatile("" :: "v"(inB.x1));
      const int so1 = (min(c.ff0 * 16 + (lane & 15) + (lane >> 5) * 16, FF - 1) + (lane & 16) * (FF / 2)) * 4;
#pragma unroll
      for (int j = 0; j < 8; ++j)
        asm volatile("s_nop 4\n\tbuffer_load_dword %0, %1, %2, %3 offen sc1" : "=v"(s1p[j]) : "v"(so1), "s"(c.rs), "s"(WS_S1 + j * FF * 4) : "memory");
    }
    conv_out(c, lane, base0, base1, xmid, 0);
    STAMP(K1, 3);
    f32x4 acb[4];
    conv_mma<4, 0, true>(ub, kb, acb, a4, [&](auto N) {
      constexpr int n = N.v;
      if constexpr (!LAYER && n >= 48 && (n & 1)) { ld_w1<(n - 48) / 2>(c, v16, w1f); SCHED(); }
      if constexpr (n == 47) {
        WAIT_VM(0);
        o = sig_begin(c, lane);
        if (LAYER) poll<true>(c, lane, CT_X + 64);
        SCHED();
      }
      if constexpr (n == 58) { sig_end<true, true>(c, lane, o, CT_C); SCHED(); }
    });
    STAMP(K1, 4);
    if (!LAYER) {
#pragma unroll
      for (int j = 0; j < 8; ++j) asm volatile("" : "+v"(s1p[j]));
      const float sv = sum_x16(((s1p[0] + s1p[1]) + (s1p[2] + s1p[3])) + ((s1p[4] + s1p[5]) + (s1p[6] + s1p[7])));
      ((float*)(smem + L_CST))[w * 64 + lane] = (lane & 16) ? c.cs1 : sv;
    }
    if (LAYER) {
      if (!got<1>(32u)) wait_ge<true>(c, lane, CT_X + 64, 32u);
      conv_load(c, lane, inB, xin, stin, 1);
    }
    conv_ldk<0>(kb, a4);
    STAMP(K1, 5);
    if (!LAYER) sfor<8, 12>([&](auto N) { ld_w1<N.v>(c, v16, w1f); });
    conv_ln(c, lane, inB, 1, base0, base1);
    if (!LAYER) sfor<12, 14>([&](auto N) { ld_w1<N.v>(c, v16, w1f); });
    __syncthreads();
    conv_mma<0, 4, false>(ub, kb, acb, a4, [&](auto N) {
      constexpr int n = N.v;
      if constexpr (!LAYER && n % 3 == 1 && n < 36) { ld_w1<14 + n / 3>(c, v16, w1f); SCHED(); }
      if constexpr (n == 30) { poll<true>(c, lane, CT_C); SCHED(); }
    });
    MFMA_DONE();
    conv_part(c, lane, acb);
    __syncthreads();
    conv_out(c, lane, base0, base1, xmid, 1);
    STAMP(K1, 6);
    if (!got<(LAYER ? 2 : 4)>(32u * ep)) wait_ge<true>(c, lane, CT_C, 32u * ep);
    const int row = lane >> 3, sw = ((row >> 1) & 1) << 1;
    const int ve = row * 128 + ((lane & 7) ^ sw) * 16, vo = row * 128 + ((lane & 7) ^ (sw | 4)) * 16;
    if (!LAYER && c.b == 0 && w == 0) __hip_atomic_store((gu32*)(c.ws + WS_GEN), c.tag, RLX_AGENT);
    sfor<0, 8>([&](auto N) { g1_dma1<N.v>(c, ve, vo, L_XA, xmid + c.P * 65536); });
    WAIT_VM(8);
    o = sig_begin(c, lane);
    sig_end<true>(c, lane, o, CT_C + 64);
    STAMP(K1, 7);
  }
  u32x4 hf[14][2];
  u32x2 xr[2];
  {
    const int lane = fresh_lane(c);
    const int g = lane >> 4, cc = lane & 15, q = cc >> 2, p = lane & 3, v16 = lane * 16;
    const int sx = ((q >> 1) & 1) | ((g & 1) << 1);
    const char* xb[4];
#pragma unroll
    for (int i = 0; i < 4; ++i) xb[i] = smem + L_XA + (8 * g + q) * 128 + ((((i + w) & 3) ^ sx) * 32) + 8 * p;
    const int row = lane >> 3, sw = ((row >> 1) & 1) << 1;
    const int ve = row * 128 + ((lane & 7) ^ sw) * 16, vo = row * 128 + ((lane & 7) ^ (sw | 4)) * 16;
    sfor<8, 12>([&](auto N) { g1_dma1<N.v>(c, ve, vo, L_XA, xmid + c.P * 65536); });
    WAIT_VM_BAR(9);
    STAMP(K2, 0);
    f32x4 acc[2][4], sa;
    g1_mma(xb, w1f, acc, sa, [&](auto KS) {
      constexpr int ks = KS.v;
      if constexpr (ks == 3) { if (LAYER) WAIT_VM_BAR(8); else WAIT_VM_BAR(10); SCHED(); }
      if constexpr (ks == 7) { if (LAYER) WAIT_VM_BAR(0); else WAIT_VM_BAR(10); SCHED(); }
      if constexpr (ks == 13) { if (!LAYER) WAIT_VM(16); poll<true>(c, lane, CT_C + 64); SCHED(); }
    }, [&](auto N) {
      constexpr int n = N.v;
      if constexpr (n < 4) g1_dma1<12 + n>(c, ve, vo, L_XA, xmid + c.P * 65536);
      if constexpr (!LAYER && n >= 4 && n < 10) ld_w1<22 + n>(c, v16, w1f);
      if constexpr (!LAYER && n >= 10) ld_w2<n - 10>(c, v16, w2f);
    });
    STAMP(K2, 1);
    const int xo = xmid + (16 + c.P) * 65536;
    g1_epi(c, lane, acc, sa, hbuf + (c.P * 4 * 56 + c.hks) * 1024, [&](auto N) {
      constexpr int n = N.v;
      if constexpr (n == -1) { if (!got<(LAYER ? 0 : 6)>(32u * ep)) wait_ge<true>(c, lane, CT_C + 64, 32u * ep); }
      if constexpr (n >= 0 && n < 8) g1_dma1<n>(c, ve, vo, L_XB, xo);
      if constexpr (n >= 10 && n < 18) g1_dma1<n - 2>(c, ve, vo, L_XB, xo);
    });
    STAMP(K2, 2);
  }
  {
    const int lane = fresh_lane(c);
    const int g = lane >> 4, cc = lane & 15, q = cc >> 2, p = lane & 3, v16 = lane * 16;
    const int sx = ((q >> 1) & 1) | ((g & 1) << 1);
    const char* xb[4];
#pragma unroll
    for (int i = 0; i < 4; ++i) xb[i] = smem + L_XB + (8 * g + q) * 128 + ((((i + w) & 3) ^ sx) * 32) + 8 * p;
    WAIT_VM_BAR(12);
    STAMP(K2, 3);
    f32x4 acc[2][4], sa;
    g1_mma(xb, w1f, acc, sa, [&](auto KS) {
      constexpr int ks = KS.v;
      if constexpr (ks == 5) {
        if (LAYER) WAIT_VM_BAR(0); else WAIT_VM_BAR(10);
        sig_bar<false>(c, lane, CT_H + c.P);
        SCHED();
      }
      if constexpr (ks == 15) {
#pragma unroll
        for (int i = 0; i < 2; ++i) xr[i] = ld64(c.rs, (c.ct_ * 64 + w * 16 + cc) * 128 + i * 32 + g * 8, xmid + c.P * 65536 + c.sub * 64);
        SCHED();
      }
    }, [&](auto N) {
      constexpr int n = N.v;
      if constexpr (!LAYER) ld_w2<22 + n>(c, v16, w2f);
      if constexpr (!LAYER && n >= 30) ld_w2<24 + n>(c, v16, w2f);
    });
    STAMP(K2, 4);
    poll<false>(c, lane, CT_H + c.P);
    const int ho = hbuf + ((c.P * 4 + c.sub * 2) * 56 + 14 * w) * 1024;
    g1_epi(c, lane, acc, sa, hbuf + ((16 + c.P) * 4 * 56 + c.hks) * 1024, [&](auto N) {
      constexpr int n = N.v;
      if constexpr (n == -1) { if (!got<0>(16u * ep)) wait_ge<false>(c, lane, CT_H + c.P, 16u * ep); }
      if constexpr (n >= 0) g2_ldh1<n>(c, v16, hf, ho);
    });
    STAMP(K2, 5);
  }
  {
    const int lane = fresh_lane(c);
    const int g = lane >> 4, cc = lane & 15;
    const int ho = hbuf + ((c.P * 4 + c.sub * 2) * 56 + 14 * w) * 1024, hoB = ho + 64 * 56 * 1024;
    STAMP(K3, 0);
    f32x4 acc[2][4];
    u32 o = 0;
    g2_mma<20, 1>(c, lane, hf, w2f, acc, ho, 0, [&](auto S) {
      constexpr int s = S.v;
      if constexpr (s == 11) { WAIT_VM(10); o = sig_begin(c, lane); SCHED(); }
      if constexpr (s == 13) { sig_end<false>(c, lane, o, CT_H + 16 + c.P); SCHED(); }
    });
    STAMP(K3, 1);
    poll<false>(c, lane, CT_H + 16 + c.P);
    g2_epi<LAYER == 1>(c, lane, acc, xr, 0, xnext + c.P * 65536 + c.sub * 32768, nop);
    STAMP(K3, 2);
    if (!got<(LAYER ? 1 : 2)>(16u * ep)) wait_ge<false>(c, lane, CT_H + 16 + c.P, 16u * ep);
    sfor<0, 8>([&](auto N) { g2_ldh1<N.v>(c, lane * 16, hf, hoB); });
#pragma unroll
    for (int i = 0; i < 2; ++i) xr[i] = ld64(c.rs, (c.ct_ * 64 + w * 16 + cc) * 128 + i * 32 + g * 8, xmid + (16 + c.P) * 65536 + c.sub * 64);
    if (LAYER == 0) {
      WAIT_VM(11);
      o = sig_begin(c, lane);
      sig_end<true>(c, lane, o, CT_X);
    }
    SCHED();
    STAMP(K3, 3);
  }
  {
    const int lane = fresh_lane(c);
    const int hoB = hbuf + (((16 + c.P) * 4 + c.sub * 2) * 56 + 14 * w) * 1024;
    f32x4 acc[2][4];
    g2_mma<8, 2>(c, lane, hf, w2f, acc, hoB, 1, [&](auto S) {
      constexpr int s = S.v;
      if constexpr (LAYER == 0 && s == 27) { poll<true>(c, lane, CT_X); SCHED(); }
    });
    STAMP(K3, 4);
    g2_epi<LAYER == 1>(c, lane, acc, xr, 1, xnext + (16 + c.P) * 65536 + c.sub * 32768, [&](int) {
      if (LAYER == 0) {
        if (!got<0>(32u)) wait_ge<true>(c, lane, CT_X, 32u);
        conv_load_u(c, lane, inA, xnext, WS_ST + 65536, 0);
        SCHED();
      }
    });
    STAMP(K3, 5);
  }
}

constexpr int KG_RS = 528, KG_X = 16 * KG_RS, KG_CH = 64 * KG_RS + 1296;
__global__ void __launch_bounds__(256, 1) main_kernel(const u16* x, const u16* lar, const u16* aim, const u16* cre,
                                                      const u16* cim, const u16* ldt, const u16* dsk, const u16* w1,
                                                      const u16* b1, const u16* w2, const u16* b2, char* ws, u16* out) {
  extern __shared__ __attribute__((aligned(16))) char smem[];
  const int t = threadIdx.x, lane = t & 63, b = blockIdx.x;
  const int w = __builtin_amdgcn_readfirstlane(t >> 6);
  const u32 junk = warm_code((b >> 3) * 4 + w, lane);
  STAMP(0, 0);
  const int ch = w >> 1, d = 2 * b + ch, n = t & 127;
  const u32 po = (u32)(d * NST + n) * 2, po2 = po ^ 128u;
#define LD16(p, off) (*(const u16*)((const char*)(p) + (u32)(off)))
  const u16 r0 = ldt[d], r1 = LD16(lar, po), r2 = LD16(aim, po), r3 = LD16(cre, po), r4 = LD16(cim, po);
  const u16 r5 = LD16(cre, po2), r6 = LD16(cim, po2);
  const u32 gen = __hip_atomic_load((gu32*)(ws + WS_GEN), RLX_AGENT);
  const bool early = !(b & 64);
  const int pks = (b & 7) + (b >> 7) * 8, pcg = (b >> 3) & 7;
  u32x4 rv[8], rp[8], rw[2];
  {
    const rsrc_t rr = __builtin_amdgcn_make_buffer_rsrc((void*)x, 0, SEQ * DM * 2, 0x00020000);
    const int vo = early ? w * 1024 + lane * 16 : 0x40000000;
#pragma unroll
    for (int j = 0; j < 8; ++j) rv[j] = __builtin_amdgcn_raw_buffer_load_b128(rr, vo, (b & 63) * 32768 + j * 4096, 0);
  }
  CIn inA, inB;
  Ctx c;
  c.rs = __builtin_amdgcn_make_buffer_rsrc(ws, 0, WS_END, 0x00020000);
  c.ct = (gu32*)(ws + WS_CT);
  c.smem = smem; c.ws = ws; c.out = out;
  c.lane0 = lane; c.w = w; c.b = b;
  c.P = (b & 7) * 2 + ((b >> 3) & 1);
  const int idx = b >> 4;
  const int sg = idx >> 1, w3 = -(int)(w == 3);
  c.eo = idx & 1; c.sub = idx >> 3; c.ct_ = idx & 7;
  c.ff0 = 14 * sg + (((6 * c.eo + 2 * w) & ~w3) | ((12 + c.eo) & w3));
  c.hks = 7 * sg + (((3 * c.eo + w) & ~w3) | (6 & w3));
  c.f1o = w < 3 ? WS_W1P + c.ff0 * 16384 + 16384 : WS_ONE; c.f1s = w < 3 ? 4096 : 0;
  {
    const u32 r7 = *(const u32*)(dsk + 2 * b);
    u32 r8 = LD16(b1, min(c.ff0 * 16 + (lane & 15) + (lane >> 5) * 16, FF - 1) * 2), r9 = LD16(b2, (c.ct_ * 64 + w * 16 + (lane & 15)) * 2);
    SCHED();
    char* kg = smem + L_XA + ch * KG_CH;
    char* stg = smem + L_XA + 2 * KG_CH;
    const int i = lane & 15, g = lane >> 4;
    int ix[9];
#pragma unroll
    for (int e2 = 0; e2 < 9; ++e2) { ix[e2] = 2 * cidx(max(2047 - 8 * t - e2, 0)); asm volatile("" : "+v"(ix[e2])); }
    int wi[2][4];
    {
      const int m8 = -(i >> 3), s1 = -(w & 1);
      const int h1 = ((256 + 128 * (i >> 2) + 2 * (i & 3)) & ~m8) | ((128 * (i >> 1) + 4 * (i & 1)) & m8);
      const int hp = (((i + (i & 8) * 15) & ~s1) | (h1 & s1)) + 32 * g, hq = 1024 + 128 * i + 32 * g;
#pragma unroll
      for (int r = 0; r < 4; ++r) {
        wi[0][r] = 2 * cidx(hp + 8 * r); wi[1][r] = 2 * cidx(hq + 8 * r > 2048 ? 2064 : hq + 8 * r);
        asm volatile("" : "+v"(wi[0][r]), "+v"(wi[1][r]));
      }
    }
    if (t < 16) *(u32x4*)(smem + (t >> 2) * L_KC + 4096 + (t & 3) * 16) = u32x4{0, 0, 0, 0};
    if (t >= 64 && t < 128) *(u32x4*)(smem + L_U + ((t - 64) >> 5) * L_UC + (t & 31) * 16) = u32x4{0, 0, 0, 0};
    if (t == 128) *(u32*)(smem + L_SIG) = 0u;
    if (w >= 2) poll0(c, lane, w == 2 ? WS_F1 : WS_F0);
    SCHED();
    STAMP(0, 1);
    const float dt = rbf(expf(bf2f(r0)));
    const float cr = bf2f(r3), ci = bf2f(r4), cr2 = bf2f(r5), ci2 = bf2f(r6);
    const float S = wave_sum(sqrt_(cr * cr + ci * ci) + sqrt_(cr2 * cr2 + ci2 * ci2)) * (2.002f * dt);
    int e = __builtin_amdgcn_frexp_expf(S);
    e = (S > 0.0f && S < 3.0e38f) ? max(-100, min(100, e)) : 14;
    const float scale = __builtin_amdgcn_ldexpf(2.0f, 14 - e);
    if (n == 0) *(float*)(smem + L_MISC + ch * 4) = __builtin_amdgcn_ldexpf(1.0f, e - 14);
    const float a2 = (-rbf(expf(bf2f(r1))) * dt) * 1.44269504088896f;
    const float br = (bf2f(r2) * dt) * 0.159154943091895f;
    {
      char* yp = kg + n * 4;
      char* xp = kg + KG_X + n * 4;
      const CM e128 = cexp_(a2, -br, 128.f), e8 = cexp_(a2, br, 8.f), e1 = cexp_(a2, -br, 1.f), e2 = cexp_(a2, -br, 2.f);
      const CM e4 = cexp_(a2, -br, 4.f);
      f32x2 f0 = {(cr * dt) * scale, -(ci * dt) * scale};
      f32x2 f = cmul(f0, cexp_(a2, -br, 512.f)), y = {1.f, 0.f};
      f32x2 g1 = cmul(f0, e128), g2 = cmul(g1, e128), g3 = cmul(g2, e128);
#define PUT(base, row, v) *(u32*)(base + (row) * KG_RS) = pack_h(v[0], v[1])
      auto chain = [&](auto K) {
        constexpr int k = K.v;
        PUT(yp, k, y);
        if (k < 15) y = cmul(y, e8);
        if (k < 4) { const f32x2 q4 = cmul(f, e4); PUT(xp, 24 + 2 * k, f); PUT(xp, 25 + 2 * k, q4); }
        else if (k < 13) PUT(xp, 28 + k, f);
        if (k < 12) f = cmul(f, e128);
        if (k < 8) { PUT(xp, k, f0); PUT(xp, 8 + k, g1); }
        if (k < 7) { f0 = cmul(f0, e1); g1 = cmul(g1, e1); }
        if (k >= 8 && k < 12) { PUT(xp, 8 + k, g2); PUT(xp, 12 + k, g3); }
        if (k >= 8 && k < 11) { g2 = cmul(g2, e2); g3 = cmul(g3, e2); }
      };
      c.tag = __builtin_amdgcn_readfirstlane(gen) * 0x9E3779B1u + 0x7F4A7C15u;
      sfor<0, 2>(chain);
      asm volatile("" : "+v"(y), "+v"(f), "+v"(f0), "+v"(g1));
      {
        const rsrc_t rr = __builtin_amdgcn_make_buffer_rsrc((void*)w1, 0, DM * FF * 2, 0x00020000);
        const int vo = (!early && pcg < 7) ? (t >> 5) * (FF * 2) + (t & 31) * 16 : 0x40000000;
#pragma unroll
        for (int j = 0; j < 4; ++j) rp[j] = __builtin_amdgcn_raw_buffer_load_b128(rr, vo, pks * (32 * FF * 2) + min(pcg, 6) * 512 + j * (8 * FF * 2), 0);
        SCHED();
      }
      if (b < 64) xpose(b, rv, c.rs, stg);
      else if (early) row_stats(b & 63, rv, c.rs);
      asm volatile("" : "+v"(y), "+v"(f), "+v"(f0), "+v"(g1));
      sfor<2, 12>(chain);
      asm volatile("" : "+v"(y), "+v"(f));
      if (early) {
        WAIT_VM(0);
        __syncthreads();
        if (w == 0 && lane < 8)
          __hip_atomic_store((gu32*)(ws + WS_F0 + lane * 2048 + (b & 7) * 256 + ((b >> 3) & 7) * 8 + (b >> 7) * 4), c.tag, RLX_AGENT);
      }
      asm volatile("" : "+v"(y), "+v"(f));
      STAMP(0, 2);
      {
    const rsrc_t r2w = __builtin_amdgcn_make_buffer_rsrc((void*)w2, 0, DM * FF * 2, 0x00020000);
#pragma unroll
    for (int j = 0; j < 2; ++j) rw[j] = __builtin_amdgcn_raw_buffer_load_b128(r2w, (t >> 4) * 1024 + (t & 15) * 16 + j * 16384, min(b >> 2, 55) * 32768 + (b & 3) * 256, 0);
      }
      SCHED();
      sfor<12, 16>(chain);
      STAMP(0, 3);
      if (!early && pcg < 7) pack_w1(pks, pcg, rp, c.rs, stg);
#undef PUT
    }
    __syncthreads();
    STAMP(0, 4);
    {
      const char* ya = kg + i * KG_RS + g * 16;
      const char* xa = kg + KG_X + (16 * (w & 1) + i) * KG_RS + g * 16;
      const char* xc = kg + KG_X + (32 + i) * KG_RS + g * 16;
      u32x4 fa[8], fb[8], fc[8];
#pragma unroll
      for (int ks = 0; ks < 8; ++ks) { fa[ks] = *(const u32x4*)(ya + ks * 64); fb[ks] = *(const u32x4*)(xa + ks * 64); }
#pragma unroll
      for (int ks = 0; ks < 8; ++ks) fc[ks] = *(const u32x4*)(xc + ks * 64);
      f32x4 ac0, ac1;
      SCHED();
      sfor<0, 8>([&](auto KS) { mma_hh<KS.v == 0>(ac0, fa[KS.v], fb[KS.v]); });
      sfor<0, 8>([&](auto KS) { mma_hh<KS.v == 0>(ac1, fa[KS.v], fc[KS.v]); });
      MFMA_DONE();
#pragma unroll
      for (int r = 0; r < 4; ++r) {
        *(u16*)(kg + 64 * KG_RS + wi[0][r]) = __builtin_bit_cast(u16, (_Float16)ac0[r]);
        *(u16*)(kg + 64 * KG_RS + wi[1][r]) = __builtin_bit_cast(u16, (_Float16)ac1[r]);
      }
    }
    if (!early) WAIT_VM(0);
    __syncthreads();
    if (w == 1 && !early && lane < 8)
      __hip_atomic_store((gu32*)(ws + WS_F1 + lane * 2048 + (b & 7) * 256 + ((b >> 3) & 7) * 8 + (b >> 7) * 4), c.tag, RLX_AGENT);
    if (w == 0) poll0(c, lane, WS_F0);
#pragma unroll
    for (int c2 = 0; c2 < 2; ++c2) {
      u32 v[9];
#pragma unroll
      for (int e2 = 0; e2 < 9; ++e2) v[e2] = *(const u16*)(smem + L_XA + c2 * KG_CH + 64 * KG_RS + ix[e2]);
      if (t == 255) v[8] = 0u;
      u32x4 oe, oo;
#pragma unroll
      for (int j = 0; j < 4; ++j) { oe[j] = v[2 * j] | (v[2 * j + 1] << 16); oo[j] = v[2 * j + 1] | (v[2 * j + 2] << 16); }
      *(u32x4*)(smem + c2 * 2 * L_KC + t * 16) = oe;
      *(u32x4*)(smem + c2 * 2 * L_KC + L_KC + t * 16) = oo;
    }
    const f32x2 ksc = *(const f32x2*)(smem + L_MISC);
    c.ks0 = __uint_as_float(__builtin_amdgcn_readfirstlane(__float_as_uint(ksc[0])));
    c.ks1 = __uint_as_float(__builtin_amdgcn_readfirstlane(__float_as_uint(ksc[1])));
    const u32 ds = __builtin_amdgcn_readfirstlane(r7);
    c.dsk0 = lo_bf(ds); c.dsk1 = hi_bf(ds);
    c.cs1 = lo_bf(r8); c.bb2 = lo_bf(r9);
    asm volatile("" :: "v"(junk));
    STAMP(0, 5);
    pack_w2_put(rw, smem + L_XB + 55552);
    if (w == 0 && !got0<0>(c.tag)) wait0(c, lane, WS_F0);
    __syncthreads();
    conv_load(c, lane, inA, WS_X, WS_ST, 0);
    conv_load(c, lane, inB, WS_X, WS_ST, 1);
    pack_w2_out(b, c.rs, smem + L_XB + 55552);
    STAMP(0, 6);
  }
  u32x4 w1f[2][16], w2f[14][4];
  layer_body<0>(c, w1f, w2f, inA, inB);
  layer_body<1>(c, w1f, w2f, inA, inB);
}

extern "C" __global__ void code_end_marker() {}

extern "C" void kernel_launch(void* const* d_in, const int* in_sizes, int n_in, void* d_out, int out_size,
                              void* d_ws, size_t ws_size, hipStream_t stream) {
  (void)in_sizes; (void)n_in; (void)out_size; (void)ws_size;
  char* ws = (char*)d_ws + ((ws_size - (size_t)WS_END) & ~(size_t)(2 * MB - 1));
  (void)hipFuncSetAttribute((const void*)main_kernel, hipFuncAttributeMaxDynamicSharedMemorySize, MAIN_LDS);
  main_kernel<<<256, 256, MAIN_LDS, stream>>>((const u16*)d_in[0], (const u16*)d_in[1], (const u16*)d_in[2],
                                              (const u16*)d_in[3], (const u16*)d_in[4], (const u16*)d_in[5],
                                              (const u16*)d_in[6], (const u16*)d_in[8], (const u16*)d_in[9],
                                              (const u16*)d_in[10], (const u16*)d_in[11], ws, (u16*)d_out);
}
```

```cpp
#include <hip/hip_runtime.h>
#include <hip/hip_bf16.h>

typedef unsigned short u16;
typedef unsigned int u32;
typedef long long i64;
typedef __attribute__((ext_vector_type(4))) short s16x4;
typedef __attribute__((ext_vector_type(2))) __bf16 bf16x2;
typedef __attribute__((ext_vector_type(2))) _Float16 f16x2;
typedef __attribute__((ext_vector_type(4))) float f32x4;
typedef __attribute__((ext_vector_type(2))) float f32x2;
typedef __attribute__((ext_vector_type(4))) unsigned u32x4;
typedef __attribute__((ext_vector_type(2))) unsigned u32x2;
typedef __attribute__((address_space(1))) unsigned gu32;
typedef __amdgpu_buffer_rsrc_t rsrc_t;
#define LAS __attribute__((address_space(3)))
#define DEVI __device__ __forceinline__
template <int V> struct IC { static constexpr int v = V; };
template <int I, int N, class F> __device__ __forceinline__ void sfor(F&& f) {
  if constexpr (I < N) { f(IC<I>{}); sfor<I + 1, N>(f); }
}
#define RLX_AGENT __ATOMIC_RELAXED, __HIP_MEMORY_SCOPE_AGENT

constexpr int SEQ = 2048, DM = 512, NST = 128, FF = 1792;
constexpr float LN_EPS = 1e-5f;
constexpr float FX_SCALE = 16777216.0f;
constexpr float FX_INV = 1.0f / 16777216.0f;
constexpr int SC1 = 16;

constexpr int MB = 1024 * 1024;
constexpr int WS_X = 0;
constexpr int WS_W1P = 12 * MB;
constexpr int WS_W2P = 14 * MB;
constexpr int WS_H = 16 * MB;
constexpr int WS_ST = 32 * MB;
constexpr int WS_S1 = 33 * MB;
constexpr int WS_GEN = 32 * MB + 196608;
constexpr int WS_F0 = WS_GEN + 256, WS_F1 = WS_F0 + 16384;
constexpr int WS_CT = 32 * MB + 262144;
constexpr int WS_ONE = 32 * MB + 393216;
constexpr int WS_END = 34 * MB;
constexpr int CT_C = 0, CT_X = 128, CT_H = 256, CT_TMO = 288;
constexpr int SPIN_CAP = 1 << 16;

DEVI float bf2f(u16 v) { return __uint_as_float((u32)v << 16); }
DEVI float lo_bf(u32 w) { return __uint_as_float(w << 16); }
DEVI float hi_bf(u32 w) { return __uint_as_float(w & 0xffff0000u); }
DEVI float rbf(float x) { return (float)(__bf16)x; }
DEVI u32 pack_bf(float a, float b) { f32x2 q = {a, b}; return __builtin_bit_cast(u32, __builtin_convertvector(q, bf16x2)); }
DEVI u32 pack_h(float a, float b) { f32x2 q = {a, b}; return __builtin_bit_cast(u32, __builtin_convertvector(q, f16x2)); }
DEVI float dot2bf(u32 a, u32 b, float c) {
  return __builtin_amdgcn_fdot2_f32_bf16(__builtin_bit_cast(bf16x2, a), __builtin_bit_cast(bf16x2, b), c, false);
}
DEVI u32x2 tr_read(const char* p) {
  return __builtin_bit_cast(u32x2, __builtin_amdgcn_ds_read_tr16_b64_v4i16((LAS s16x4*)p));
}
DEVI u32x4 cat2(u32x2 a, u32x2 b) { return __builtin_shufflevector(a, b, 0, 1, 2, 3); }
DEVI u32 lds_addr(const void* p) { return (u32)(size_t)(const LAS char*)p; }
#define WAIT_VM(n) asm volatile("s_waitcnt vmcnt(" #n ")" ::: "memory")
#define WAIT_VM_BAR(n) asm volatile("s_waitcnt vmcnt(" #n ")\n\ts_barrier" ::: "memory")
#define SCHED() __builtin_amdgcn_sched_barrier(0)
template <int CTRL> DEVI float dpp_add(float v) {
  return v + __uint_as_float(__builtin_amdgcn_update_dpp(0u, __float_as_uint(v), CTRL, 0xf, 0xf, true));
}
DEVI float sum_x16(float v) {
  auto r = __builtin_amdgcn_permlane16_swap(__float_as_uint(v), __float_as_uint(v), false, false);
  return __uint_as_float(r[0]) + __uint_as_float(r[1]);
}
DEVI float sum_x32(float v) {
  auto r = __builtin_amdgcn_permlane32_swap(__float_as_uint(v), __float_as_uint(v), false, false);
  return __uint_as_float(r[0]) + __uint_as_float(r[1]);
}
DEVI float wave_sum(float v) {
  v = dpp_add<0xB1>(v); v = dpp_add<0x4E>(v); v = dpp_add<0x141>(v); v = dpp_add<0x140>(v);
  return sum_x32(sum_x16(v));
}
DEVI i64 f2fx(float v) {
  const float a = v * (FX_SCALE / 4294967296.0f), h = __builtin_floorf(a);
  return __builtin_bit_cast(i64, u32x2{(u32)((a - h) * 4294967296.0f), (u32)(int)h});
}
DEVI float sel4(int i, float a, float b, float c, float d) {
  asm volatile("" : "+v"(a), "+v"(b), "+v"(c), "+v"(d));
  return (i & 2) ? ((i & 1) ? d : c) : ((i & 1) ? b : a);
}
DEVI int st_pos(int t) { return ((t >> 8) * 4 + (t & 3)) * 1024 + ((t >> 2) & 63) * 16; }
DEVI float rcp(float x) { return __builtin_amdgcn_rcpf(x); }
DEVI float sqrt_(float x) { return __builtin_amdgcn_sqrtf(x); }

DEVI u32 warm_code(int slice, int lane) {
  unsigned long long pc, pe;
  asm volatile("s_getpc_b64 %0\n\ts_getpc_b64 s[90:91]\n\ts_add_u32 s90, s90, code_end_marker@rel32@lo+4\n\t"
               "s_addc_u32 s91, s91, code_end_marker@rel32@hi+12\n\ts_mov_b64 %1, s[90:91]"
               : "=&s"(pc), "=s"(pe) :: "scc", "s90", "s91");
  const u32 off = min((u32)slice * 512u, (u32)(pe - pc) - 512u) + (lane & 7) * 64;
  u32 junk;
  asm volatile("s_nop 4\n\tglobal_load_dword %0, %1, %2" : "=v"(junk) : "v"(off), "s"(pc) : "memory");
  return junk;
}

DEVI int cidx(int l) {
  const int hb = (int)(pack_bf((float)l, 0.f) & 0xffffu) - (0x4300 - 128);
  return l < 128 ? l : hb;
}
struct CM { f32x2 e, f; };
DEVI f32x2 cmul(f32x2 a, const CM& m) { return a[1] * m.f + a[0] * m.e; }
DEVI CM cexp_(float a2, float br, float p) {
  const float e = __builtin_amdgcn_exp2f(a2 * p), f = __builtin_amdgcn_fractf(br * p);
  const float re = e * __builtin_amdgcn_cosf(f), im = e * __builtin_amdgcn_sinf(f);
  CM r; r.e = f32x2{re, im}; r.f = f32x2{-im, re};
  return r;
}
DEVI void st16(rsrc_t rs, int off, u32x4 v) { __builtin_amdgcn_raw_buffer_store_b128(v, rs, off, 0, SC1); }

DEVI void xpose(int b, const u32x4 (&v)[8], rsrc_t rs, char* smem) {
  const int t = threadIdx.x, lane = t & 63, w = t >> 6;
  constexpr int RS = 1024 + 32;
#pragma unroll
  for (int r = 0; r < 8; ++r) *(u32x4*)(smem + (w + 4 * r) * RS + lane * 16) = v[r];
  __syncthreads();
  const int g = lane >> 4, q = (lane & 15) >> 2, p = lane & 3;
#pragma unroll
  for (int cf = 0; cf < 8; ++cf) {
    const int c0 = (w * 8 + cf) * 16;
    const char* pa = smem + (8 * g + q) * RS + c0 * 2 + 8 * p;
    const u32x4 o = cat2(tr_read(pa), tr_read(pa + 4 * RS));
    st16(rs, WS_X + b * 32768 + (c0 + (lane & 15)) * 64 + g * 16, o);
  }
}
DEVI void row_stats(int b, const u32x4 (&v)[8], rsrc_t rs) {
  const int t = threadIdx.x, lane = t & 63, w = t >> 6;
  const int t0 = b * 32;
  float a[16];
#pragma unroll
  for (int r = 0; r < 8; ++r) {
    float s = 0.f, ss = 0.f;
#pragma unroll
    for (int j = 0; j < 4; ++j) { s = dot2bf(v[r][j], 0x3f803f80u, s); ss = dot2bf(v[r][j], v[r][j], ss); }
    a[2 * r] = s; a[2 * r + 1] = ss;
  }
  const bool o1 = lane & 1, o2 = lane & 2;
#pragma unroll
  for (int k = 0; k < 8; ++k) {
    const float keep = o1 ? a[2 * k + 1] : a[2 * k], send = o1 ? a[2 * k] : a[2 * k + 1];
    a[k] = keep + __uint_as_float(__builtin_amdgcn_update_dpp(0u, __float_as_uint(send), 0xB1, 0xf, 0xf, true));
  }
#pragma unroll
  for (int k = 0; k < 4; ++k) {
    const float keep = o2 ? a[2 * k + 1] : a[2 * k], send = o2 ? a[2 * k] : a[2 * k + 1];
    a[k] = keep + __uint_as_float(__builtin_amdgcn_update_dpp(0u, __float_as_uint(send), 0x4E, 0xf, 0xf, true));
  }
#pragma unroll
  for (int k = 0; k < 4; ++k) a[k] = sum_x32(sum_x16(dpp_add<0x128>(dpp_add<0x124>(a[k]))));
  const i64 sv = f2fx(sel4(lane >> 2, a[0], a[1], a[2], a[3]));
  if (lane < 16)
    __builtin_amdgcn_raw_buffer_store_b64(__builtin_bit_cast(u32x2, sv), rs, st_pos(t0 + w) + lane * 8, WS_ST, SC1);
  if (t >= 96 && t < 128) st16(rs, WS_ST + 65536 + b * 512 + (t - 96) * 16, u32x4{0, 0, 0, 0});
  if (t < 80) st16(rs, WS_CT + b * 1280 + t * 16, u32x4{0, 0, 0, 0});
}

DEVI void pack_w1(int ks, int cg, const u32x4 (&v)[8], rsrc_t rs, char* smem) {
  const int t = threadIdx.x, lane = t & 63, w = t >> 6;
  constexpr int RS = 512 + 32;
#pragma unroll
  for (int it = 0; it < 4; ++it) *(u32x4*)(smem + (it * 8 + (t >> 5)) * RS + (t & 31) * 16) = v[it];
  __syncthreads();
  const int g = lane >> 4, q = (lane & 15) >> 2, p = lane & 3;
  float cs[4];
#pragma unroll
  for (int h = 0; h < 4; ++h) {
    const char* a = smem + (8 * g + q) * RS + (w * 4 + h) * 32 + 8 * p;
    const u32x4 o = cat2(tr_read(a), tr_read(a + 4 * RS));
    st16(rs, WS_W1P + (((cg * 16 + w * 4 + h) * 16 + ks) * 64 + lane) * 16, o);
    float s = 0.f;
#pragma unroll
    for (int j = 0; j < 4; ++j) s = dot2bf(o[j], 0x3f803f80u, s);
    cs[h] = sum_x32(sum_x16(s));
  }
  __builtin_amdgcn_raw_buffer_store_b32(__float_as_uint(sel4(g, cs[0], cs[1], cs[2], cs[3])), rs, (ks * FF + cg * 256 + t) * 4, WS_S1, SC1);
  if (ks == 0 && cg == 0) st16(rs, WS_ONE + t * 16, u32x4{0x3f803f80u, 0x3f803f80u, 0x3f803f80u, 0x3f803f80u});
}

constexpr int W2_RS = 256 + 32;
DEVI void pack_w2_put(const u32x4 (&v)[2], char* smem) {
  const int t = threadIdx.x;
#pragma unroll
  for (int r = 0; r < 2; ++r) *(u32x4*)(smem + (r * 16 + (t >> 4)) * W2_RS + (t & 15) * 16) = v[r];
}
DEVI void pack_w2_out(int b, rsrc_t rs, const char* smem) {
  const int t = threadIdx.x, lane = t & 63, w = t >> 6;
  const int g = lane >> 4, q = (lane & 15) >> 2, p = lane & 3;
#pragma unroll
  for (int cf = 0; cf < 2; ++cf) {
    const char* a = smem + (4 * g + q) * W2_RS + (w * 2 + cf) * 32 + 8 * p;
    const u32x4 o = cat2(tr_read(a), tr_read(a + 16 * W2_RS));
    st16(rs, b < 224 ? WS_W2P + ((((b & 3) * 8 + w * 2 + cf) * 56 + (b >> 2)) * 64 + lane) * 16 : 0x40000000, o);
  }
}

constexpr int L_KC = 4160;
constexpr int L_U = 4 * L_KC, L_UC = 4608;
constexpr int L_XA = 26112, L_XB = L_XA + 65536;
constexpr int L_MISC = L_XB + 65536;
constexpr int L_CST = L_MISC + 1024;
constexpr int L_SIG = L_CST + 4 * 256;
constexpr int MAIN_LDS = L_SIG + 256;
constexpr int L_RED = L_XB;

DEVI u32x4 ld128(rsrc_t rs, int v, int s) { return __builtin_amdgcn_raw_buffer_load_b128(rs, v, s, SC1); }
DEVI u32x2 ld64(rsrc_t rs, int v, int s) { return __builtin_amdgcn_raw_buffer_load_b64(rs, v, s, SC1); }
DEVI void st128(rsrc_t rs, int v, int s, u32x4 d) { __builtin_amdgcn_raw_buffer_store_b128(d, rs, v, s, SC1); }
DEVI void st64(rsrc_t rs, int v, int s, u32x2 d) { __builtin_amdgcn_raw_buffer_store_b64(d, rs, v, s, SC1); }
template <int OFF> DEVI u32x4 ld128a(rsrc_t rs, int v, int s) {
  u32x4 r;
  asm volatile("buffer_load_dwordx4 %0, %1, %2, %3 offen offset:%4 sc1" : "=a"(r) : "v"(v), "s"(rs), "s"(s), "i"(OFF) : "memory");
  return r;
}
template <int OFF> DEVI u32x4 ld128v(rsrc_t rs, int v, int s) {
  u32x4 r;
  asm volatile("buffer_load_dwordx4 %0, %1, %2, %3 offen offset:%4 sc1" : "=v"(r) : "v"(v), "s"(rs), "s"(s), "i"(OFF) : "memory");
  return r;
}
template <int OFF> DEVI void dma1k(rsrc_t rs, u32 lds_dst, int v, int soff) {
  asm volatile("s_mov_b32 m0, %0\n\ts_nop 0\n\tbuffer_load_dwordx4 %1, %2, %3 offen offset:%4 sc1 lds"
               :: "s"(lds_dst), "v"(v), "s"(rs), "s"(soff), "i"(OFF) : "memory");
}
#define MMA(NAME, OP, CA, CB)                                                                                   \
  template <bool Z> DEVI void NAME(f32x4& d, const u32x4& a, const u32x4& b) {                                  \
    if (Z) asm volatile(OP " %0, %1, %2, 0" : "=&v"(d) : CA(a), CB(b));                                         \
    else asm volatile(OP " %0, %1, %2, %0" : "+v"(d) : CA(a), CB(b));                                           \
  }
MMA(mma_vv, "v_mfma_f32_16x16x32_bf16", "v", "v")
MMA(mma_av, "v_mfma_f32_16x16x32_bf16", "a", "v")
MMA(mma_va, "v_mfma_f32_16x16x32_bf16", "v", "a")
MMA(mma_hh, "v_mfma_f32_16x16x32_f16", "v", "v")
#define MFMA_DONE() do { SCHED(); asm volatile("s_nop 7" ::: "memory"); SCHED(); } while (0)

struct Ctx {
  rsrc_t rs; gu32* ct; char* smem; char* ws; u16* out;
  int lane0, w, b, P, sub, ct_, ff0, hks, eo, f1o, f1s;
  float dsk0, dsk1, ks0, ks1, bb2, cs1;
  u32 tag;
};
DEVI int fresh_lane(const Ctx& c) { int l = c.lane0; asm volatile("" : "+v"(l)); return l; }
DEVI u32 sig_begin(const Ctx& c, int lane) {
  u32 old;
  asm volatile("ds_add_rtn_u32 %0, %1, %2" : "=v"(old) : "v"(lds_addr(c.smem) + L_SIG + lane * 4), "v"(1u) : "memory");
  return old;
}
template <bool ALL, bool BACK = false> DEVI void sig_end(const Ctx& c, int lane, u32 old, int line) {
  if (BACK) asm volatile("" : "+v"(old) :: "memory");
  else asm volatile("s_waitcnt lgkmcnt(0)" : "+v"(old) :: "memory");
  const bool last = (__builtin_amdgcn_readfirstlane(old) & 3u) == 3u;
  const int v = (last && lane < (ALL ? 8 : 1)) ? (ALL ? lane * 2048 : 0) : 0x40000000;
  asm volatile("s_nop 4\n\tbuffer_atomic_add %0, %1, %2, %3 offen" :: "v"(1u), "v"(v), "s"(c.rs),
               "s"(WS_CT + (line + (ALL ? (c.b >> 3) & 7 : 0)) * 256) : "memory");
}
template <bool ALL> DEVI void poll(const Ctx& c, int lane, int line) {
  const int v = ALL ? (lane & 7) * 256 : 0, so = WS_CT + (line + (ALL ? (c.b & 7) * 8 : 0)) * 256;
  asm volatile("s_nop 4\n\tbuffer_load_dword v254, %0, %1, %2 offen sc1" :: "v"(v), "s"(c.rs), "s"(so) : "memory", "v254");
}
template <int YOUNGER> DEVI bool got(u32 want) {
  unsigned long long m;
  asm volatile("s_waitcnt vmcnt(%2)\n\tv_cmp_le_u32_e64 %0, %1, v254" : "=s"(m) : "s"(want), "i"(YOUNGER) : "memory");
  return m == ~0ull;
}
template <bool ALL> DEVI void wait_ge(const Ctx& c, int lane, int line, u32 want) {
  int sp = 0;
#pragma nounroll
  do {
    poll<ALL>(c, lane, line);
    if (++sp > SPIN_CAP) { __hip_atomic_store(c.ct + CT_TMO * 64, 1u, RLX_AGENT); break; }
  } while (!got<0>(want));
}

DEVI void poll0(const Ctx& c, int lane, int set) {
  asm volatile("s_nop 4\n\tbuffer_load_dwordx2 v[254:255], %0, %1, %2 offen sc1" :: "v"((lane & 7) * 256 + (lane >> 3) * 8), "s"(c.rs),
               "s"(set + (c.b & 7) * 2048) : "memory", "v254", "v255");
}
template <int YOUNGER> DEVI bool got0(u32 tag) {
  unsigned long long m, m2;
  asm volatile("s_waitcnt vmcnt(%3)\n\tv_cmp_eq_u32_e64 %0, %2, v254\n\tv_cmp_eq_u32_e64 %1, %2, v255"
               : "=&s"(m), "=s"(m2) : "s"(tag), "i"(YOUNGER) : "memory");
  return (m & m2) == ~0ull;
}
DEVI void wait0(const Ctx& c, int lane, int set) {
  int sp = 0;
#pragma nounroll
  do {
    poll0(c, lane, set);
    if (++sp > SPIN_CAP) { __hip_atomic_store(c.ct + CT_TMO * 64, 1u, RLX_AGENT); break; }
  } while (!got0<0>(c.tag));
}

struct CIn { u32x2 x0, x1; u32x4 s[4]; };
DEVI void conv_load(const Ctx& c, int lane, CIn& in, int xoff, int stoff, int h) {
  const int sx = xoff + (32 * h + 8 * c.w) * 32768 + c.b * 128;
  const int vx = (lane >> 3) * 32768 + (lane & 7) * 8;
  const int ss = stoff + (1024 * h + 256 * c.w) * 16;
#pragma unroll
  for (int j = 0; j < 4; ++j) in.s[j] = ld128(c.rs, lane * 16 + j * 1024, ss);
  in.x0 = ld64(c.rs, vx, sx);
  in.x1 = ld64(c.rs, vx + 64, sx);
}
DEVI void conv_load_u(const Ctx& c, int lane, CIn& in, int xoff, int stoff, int h) {
  const int sx = xoff + (32 * h + 8 * c.w) * 32768 + c.b * 128;
  const int vx = (lane >> 3) * 32768 + (lane & 7) * 8;
  const int ss = stoff + (1024 * h + 256 * c.w) * 16;
  asm volatile("s_nop 4\n\t"
               "buffer_load_dwordx4 %0, %6, %8, %9 offen sc1\n\tbuffer_load_dwordx4 %1, %6, %8, %9 offen offset:1024 sc1\n\t"
               "buffer_load_dwordx4 %2, %6, %8, %9 offen offset:2048 sc1\n\tbuffer_load_dwordx4 %3, %6, %8, %9 offen offset:3072 sc1\n\t"
               "buffer_load_dwordx2 %4, %7, %8, %10 offen sc1\n\tbuffer_load_dwordx2 %5, %7, %8, %10 offen offset:64 sc1"
               : "=&v"(in.s[0]), "=&v"(in.s[1]), "=&v"(in.s[2]), "=&v"(in.s[3]), "=&v"(in.x0), "=&v"(in.x1)
               : "v"(lane * 16), "v"(vx), "s"(c.rs), "s"(ss), "s"(sx) : "memory");
}
DEVI void conv_landed(CIn& in) {
  asm volatile("s_waitcnt vmcnt(2)" : "+v"(in.s[0]), "+v"(in.s[1]), "+v"(in.s[2]), "+v"(in.s[3]), "+v"(in.x0), "+v"(in.x1) :: "memory");
}
DEVI void conv_ln(const Ctx& c, int lane, const CIn& in, int h, f32x4& base0, f32x4& base1) {
  constexpr float CH = 4294967296.0f * FX_INV / DM, CL = FX_INV / DM;
  f32x4 m, r;
#pragma unroll
  for (int j = 0; j < 4; ++j) {
    m[j] = fmaf((float)(int)in.s[j][1], CH, (float)in.s[j][0] * CL);
    r[j] = fmaf((float)(int)in.s[j][3], CH, fmaf((float)in.s[j][2], CL, LN_EPS));
  }
#pragma unroll
  for (int j = 0; j < 4; ++j) r[j] = __builtin_amdgcn_rsqf(fmaxf(fmaf(-m[j], m[j], r[j]), LN_EPS));
  const f32x4 rr = r, mr = -m * r;
  const f32x4 x0 = {lo_bf(in.x0[0]), hi_bf(in.x0[0]), lo_bf(in.x0[1]), hi_bf(in.x0[1])};
  const f32x4 x1 = {lo_bf(in.x1[0]), hi_bf(in.x1[0]), lo_bf(in.x1[1]), hi_bf(in.x1[1])};
  const f32x4 u0 = x0 * rr + mr, u1 = x1 * rr + mr;
  char* up = c.smem + L_U + 512 + (1024 * h + 256 * c.w + 4 * lane) * 2;
  *(u32x2*)up = u32x2{pack_h(u0[0], u0[1]), pack_h(u0[2], u0[3])};
  *(u32x2*)(up + L_UC) = u32x2{pack_h(u1[0], u1[1]), pack_h(u1[2], u1[3])};
  base0 = u0 * c.dsk0 + x0; base1 = u1 * c.dsk1 + x1;
}
struct KB { const char* p[4]; };
template <int MM, int LAM> DEVI u32x4 conv_k1(const KB& kb) {
  const u32* kp = (const u32*)(kb.p[(7 - LAM) >> 1] + 64 * (3 - MM) + 512 * ((7 - LAM) & 1));
  return u32x4{kp[0], kp[1], kp[2], kp[3]};
}
template <int MM, int TB> DEVI u32x4 conv_u1(const char* ub) { return *(const u32x4*)(ub + 64 * (3 - MM) + 512 * TB); }
template <int LO> DEVI void conv_ldk(const KB& kb, u32x4 (&af)[4]) {
  sfor<0, 4>([&](auto L) { af[L.v] = conv_k1<0, LO + L.v>(kb); });
}
constexpr int conv_rank(int DT, int LO, int gr, int lt) {
  int k = 0;
  for (int x = 0; x < lt; ++x) { const int tb = DT + (x & 3) - (LO + 4 * gr + (x >> 2)); k += (tb >= 0 && tb < 4); }
  return k;
}
template <int DT, int TB0, bool ZERO, class F>
DEVI void conv_mma(const char* ub, const KB& kb, f32x4 (&acc)[4], const u32x4 (&af0)[4], F&& hook) {
  constexpr int NG = DT ? 2 : 1, LO = DT ? 1 : 0;
  u32x4 af[2][4], bf[2][4];
  sfor<0, 4>([&](auto L) { af[0][L.v] = af0[L.v]; });
  sfor<0, 4>([&](auto T) { constexpr int tb = DT ? 3 - T.v : T.v; bf[0][tb] = conv_u1<0, TB0 + tb>(ub); });
  SCHED();
  sfor<0, 4 * NG>([&](auto ST) {
    constexpr int st = ST.v, mm = st / NG, gr = st % NG;
    constexpr bool more = st + 1 < 4 * NG;
    constexpr int mn = (st + 1) / NG, gn = (st + 1) % NG;
    constexpr bool nu = more && gr == NG - 1;
    sfor<0, 16>([&](auto LT) {
      constexpr int l = LT.v >> 2, T = LT.v & 3, tb = DT + T - (LO + 4 * gr + l);
      if constexpr (tb >= 0 && tb < 4) {
        constexpr int k = conv_rank(DT, LO, gr, LT.v);
        constexpr bool first = ZERO && st == 0 && (DT ? (l == T) : (l == 0));
        mma_hh<first>(acc[T], af[st & 1][l], bf[mm & 1][tb]);
        sfor<(gr == 1 ? 2 * k : k), (gr == 1 ? 2 * k + 2 : k + 1)>([&](auto J) {
          constexpr int j = J.v;
          if constexpr (nu && j < 8) {
            if constexpr (DT != 0) {
              if constexpr (j & 1) bf[mn & 1][3 - (j >> 1)] = conv_u1<mn, TB0 + 3 - (j >> 1)>(ub);
              else af[(st + 1) & 1][j >> 1] = conv_k1<mn, LO + (j >> 1)>(kb);
            } else {
              if constexpr (j >= 1 && j <= 4) bf[mn & 1][j - 1] = conv_u1<mn, TB0 + j - 1>(ub);
              else af[(st + 1) & 1][j ? j - 4 : 0] = conv_k1<mn, LO + (j ? j - 4 : 0)>(kb);
            }
          } else if constexpr (more && !nu && j < 3) af[(st + 1) & 1][j] = conv_k1<mn, LO + 4 * gn + j>(kb);
        });
        SCHED();
        hook(IC<mm * (DT ? 16 : 10) + gr * 10 + k>{});
      }
    });
  });
}
DEVI void conv_part(const Ctx& c, int lane, const f32x4 (&acc)[4]) {
  char* red = c.smem + L_RED;
#pragma unroll
  for (int T = 0; T < 4; ++T) *(f32x4*)(red + (c.w * 4 + T) * 1024 + (lane & 15) * 64 + (lane >> 4) * 16) = acc[T];
}
DEVI void conv_out(const Ctx& c, int lane, const f32x4& base0, const f32x4& base1, int xoff, int h) {
  const int sx = xoff + (16 * h + 4 * c.w) * 65536 + c.b * 256;
  const int vx = (lane >> 4) * 65536 + (lane & 15) * 8;
  const char* p = c.smem + L_RED + c.w * 1024 + lane * 16;
  const f32x4 a0 = *(const f32x4*)p, b0 = *(const f32x4*)(p + 4096), a1 = *(const f32x4*)(p + 8192), b1 = *(const f32x4*)(p + 12288);
  SCHED();
  const f32x4 y0 = (a0 + b0) * c.ks0 + base0, y1 = (a1 + b1) * c.ks1 + base1;
  st64(c.rs, vx, sx, u32x2{pack_bf(y0[0], y0[1]), pack_bf(y0[2], y0[3])});
  st64(c.rs, vx + 128, sx, u32x2{pack_bf(y1[0], y1[1]), pack_bf(y1[2], y1[3])});
}

template <int PC> DEVI void g1_dma1(const Ctx& c, int ve, int vo, int ldsx, int soff) {
  dma1k<(PC & 3) * 1024>(c.rs, lds_addr(c.smem) + ldsx + (4 * (PC >> 2) + c.w) * 4096, (PC & 1) ? vo : ve, soff + (4 * (PC >> 2) + c.w) * 4096);
}
template <bool ALL> DEVI void sig_bar(const Ctx& c, int lane, int line) {
  const int v = (c.w == 0 && lane < (ALL ? 8 : 1)) ? (ALL ? lane * 2048 : 0) : 0x40000000;
  asm volatile("s_nop 4\n\tbuffer_atomic_add %0, %1, %2, %3 offen" :: "v"(1u), "v"(v), "s"(c.rs),
               "s"(WS_CT + (line + (ALL ? (c.b >> 3) & 7 : 0)) * 256) : "memory");
}
template <class F, class G>
DEVI void g1_mma(const char* const (&xb)[4], const u32x4 (&w1f)[2][16], f32x4 (&acc)[2][4], f32x4& sa, F&& mid, G&& hook) {
  u32x2 xf[2][4][2];
#pragma unroll
  for (int i = 0; i < 4; ++i) { xf[0][i][0] = tr_read(xb[i]); xf[0][i][1] = tr_read(xb[i] + 512); }
  SCHED();
  sfor<0, 16>([&](auto KS) {
    constexpr int ks = KS.v;
    mid(KS);
    sfor<0, 4>([&](auto I) {
      constexpr int i = I.v;
      const u32x4 x = cat2(xf[ks & 1][i][0], xf[ks & 1][i][1]);
      mma_vv<ks == 0>(acc[0][i], w1f[0][ks], x);
      if constexpr (ks < 15) xf[(ks + 1) & 1][i][0] = tr_read(xb[i] + (ks + 1) * 4096);
      SCHED();
      if constexpr (ks < 8) mma_av<ks == 0>(acc[1][i], w1f[1][ks], x); else mma_vv<false>(acc[1][i], w1f[1][ks], x);
      if constexpr (ks < 15) xf[(ks + 1) & 1][i][1] = tr_read(xb[i] + (ks + 1) * 4096 + 512);
      SCHED();
      if constexpr (i == 0) { mma_vv<ks == 0>(sa, x, x); hook(IC<2 * ks>{}); SCHED(); }
      if constexpr (i == 2) { hook(IC<2 * ks + 1>{}); SCHED(); }
    });
  });
  MFMA_DONE();
}
DEVI void st_h(const Ctx& c, int v, int so0, int so1, u32x4 a, u32x4 b) {
  const u32x2 al = {a[0], a[1]}, bl = {b[0], b[1]};
  asm volatile("s_cmp_eq_u32 %0, 3\n\ts_nop 3\n\ts_cbranch_scc1 1f\n\t"
               "buffer_store_dwordx4 %5, %1, %2, %3 offen sc1\n\tbuffer_store_dwordx4 %6, %1, %2, %4 offen sc1\n\ts_branch 2f\n"
               "1:\n\tbuffer_store_dwordx2 %7, %1, %2, %3 offen sc1\n\tbuffer_store_dwordx2 %8, %1, %2, %4 offen sc1\n"
               "2:\n\ts_nop 1"
               :: "s"(c.w), "v"(v), "s"(c.rs), "s"(so0), "s"(so1), "v"(a), "v"(b), "v"(al), "v"(bl) : "memory", "scc");
}
template <class F>
DEVI void g1_epi(const Ctx& c, int lane, f32x4 (&acc)[2][4], const f32x4& sa, int hoff, F&& hook) {
  const int g = lane >> 4, cc = lane & 15, q = cc >> 2, p = lane & 3, w = c.w;
  float* sums = (float*)(c.smem + L_MISC);
  {
    const float d = sel4(p, sa[0], sa[1], sa[2], sa[3]);
    sums[q == g ? (w * 16 + cc) * 2 + 1 : 128 + lane] = d;
    const float r = sel4(g, acc[1][1][0], acc[1][2][0], acc[1][3][0], acc[1][0][0]);
    sums[w == 3 ? lane * 2 : 192 + lane] = r;
  }
  f32x4 s1v[2], b1v[2];
#pragma unroll
  for (int j = 0; j < 2; ++j) {
    s1v[j] = *(const f32x4*)(c.smem + L_CST + w * 256 + j * 128 + g * 16);
    b1v[j] = *(const f32x4*)(c.smem + L_CST + w * 256 + j * 128 + 64 + g * 16);
  }
  const int vh = w < 3 ? lane * 16 : lane * 8 + c.eo * 512;
  __syncthreads();
  f32x2 k[4];
#pragma unroll
  for (int i = 0; i < 4; ++i) k[i] = *(const f32x2*)(sums + (((i + w) & 3) * 16 + cc) * 2);
#pragma unroll
  for (int i = 0; i < 4; ++i) {
    const float m = k[i][0] * (1.0f / DM);
    const float ve = fmaxf(fmaf(-m, m, fmaf(k[i][1], 1.0f / DM, LN_EPS)), LN_EPS);
    const float r = __builtin_amdgcn_rsqf(fmaf(ve, 1.0f + LN_EPS, -LN_EPS));
    k[i] = f32x2{r, -m * r};
  }
  SCHED();
  hook(IC<-1>{});
  SCHED();
  sfor<0, 2>([&](auto IH) {
    constexpr int ih = IH.v;
    f32x4 x[4], e[4];
#define STAGE(n) do { SCHED(); hook(IC<10 * ih + n>{}); SCHED(); } while (0)
#pragma unroll
    for (int n = 0; n < 4; ++n) x[n] = k[2 * ih + (n >> 1)][1] * s1v[n & 1] + b1v[n & 1];
#pragma unroll
    for (int n = 0; n < 4; ++n) x[n] = acc[n & 1][2 * ih + (n >> 1)] * k[2 * ih + (n >> 1)][0] + x[n];
    STAGE(0);
#pragma unroll
    for (int n = 0; n < 4; ++n) e[n] = x[n] * x[n];
#pragma unroll
    for (int n = 0; n < 4; ++n) e[n] = e[n] * (-0.0713548162726f * 1.44269504088896f) + (-1.59576912160573f * 1.44269504088896f);
    STAGE(1);
#pragma unroll
    for (int n = 0; n < 4; ++n) e[n] = e[n] * x[n];
    STAGE(2);
#pragma unroll
    for (int r = 0; r < 4; ++r) { e[0][r] = __builtin_amdgcn_exp2f(e[0][r]); e[1][r] = __builtin_amdgcn_exp2f(e[1][r]); }
    STAGE(3);
#pragma unroll
    for (int r = 0; r < 4; ++r) { e[2][r] = __builtin_amdgcn_exp2f(e[2][r]); e[3][r] = __builtin_amdgcn_exp2f(e[3][r]); }
    STAGE(4);
#pragma unroll
    for (int n = 0; n < 4; ++n) e[n] = e[n] + 1.0f;
    STAGE(5);
#pragma unroll
    for (int r = 0; r < 4; ++r) { e[0][r] = rcp(e[0][r]); e[1][r] = rcp(e[1][r]); }
    STAGE(6);
#pragma unroll
    for (int r = 0; r < 4; ++r) { e[2][r] = rcp(e[2][r]); e[3][r] = rcp(e[3][r]); }
    STAGE(7);
#pragma unroll
    for (int n = 0; n < 4; ++n) x[n] = x[n] * e[n];
    STAGE(8);
    u32x4 hv[2];
#pragma unroll
    for (int n = 0; n < 4; ++n) { hv[n >> 1][2 * (n & 1)] = pack_bf(x[n][0], x[n][1]); hv[n >> 1][2 * (n & 1) + 1] = pack_bf(x[n][2], x[n][3]); }
    STAGE(9);
    const int so0 = hoff + ((2 * ih + w) & 3) * 56 * 1024, so1 = hoff + ((2 * ih + 1 + w) & 3) * 56 * 1024;
    st_h(c, vh, so0, so1, hv[0], hv[1]);
    SCHED();
#undef STAGE
  });
}

template <int N> DEVI void ld_w1(const Ctx& c, int v, u32x4 (&w1f)[2][16]) {
  constexpr int ks = N >> 1, j = N & 1;
  const int so = (j ? c.f1o + (ks >> 2) * c.f1s : WS_W1P + c.ff0 * 16384 + (ks >> 2) * 4096);
  if constexpr (j && ks < 8) w1f[j][ks] = ld128a<(ks & 3) * 1024>(c.rs, v, so);
  else w1f[j][ks] = ld128v<(ks & 3) * 1024>(c.rs, v, so);
}
template <int N> DEVI void ld_w2(const Ctx& c, int v, u32x4 (&w2f)[14][4]) {
  constexpr int s = N >> 2, j = N & 3;
  w2f[s][j] = ld128a<(s & 3) * 1024>(c.rs, v, WS_W2P + ((c.ct_ * 4 + ((c.w + j) & 3)) * 56 + 14 * c.w + (s & ~3)) * 1024);
}
template <int N> DEVI void g2_ldh1(const Ctx& c, int v16, u32x4 (&hf)[14][2], int ho) {
  constexpr int s = N % 14, i = N / 14;
  if constexpr (s % 7 == 6)
    hf[s][i] = cat2(ld64(c.rs, (v16 >> 1) + (s & 3) * 1024, ho + i * 56 * 1024 + (s >> 2) * 4096),
                    ld64(c.rs, (v16 >> 1) + (s & 3) * 1024 + 512, ho + i * 56 * 1024 + (s >> 2) * 4096));
  else hf[s][i] = ld128(c.rs, v16 + (s & 3) * 1024, ho + i * 56 * 1024 + (s >> 2) * 4096);
}
template <int PRE, int RATE, class F>
DEVI void g2_mma(const Ctx& c, int lane, u32x4 (&hf)[14][2], const u32x4 (&w2f)[14][4], f32x4 (&acc)[2][4], int ho, int h, F&& mid) {
  const int v16 = lane * 16;
  char* red = c.smem + L_XA + h * 32768;
  sfor<0, 28>([&](auto N) {
    constexpr int i = N.v / 14, s = N.v % 14;
    sfor<0, 4>([&](auto J) {
      constexpr int j = J.v;
      mma_va<s == 0>(acc[i][j], hf[s][i], w2f[s][j]);
      constexpr int per = 4 / RATE, n = PRE + RATE * N.v + j / per;
      if constexpr (j % per == per - 1 && n < 28) { g2_ldh1<n>(c, v16, hf, ho); SCHED(); }
      if constexpr (N.v == 17 && j > 0) { *(f32x4*)(red + ((((c.w + j) & 3) * 3 + j - 1) * 2) * 1024 + v16) = acc[0][j]; SCHED(); }
    });
    SCHED();
    mid(N);
  });
  MFMA_DONE();
}
template <bool LAST, class F>
DEVI void g2_epi(const Ctx& c, int lane, const f32x4 (&acc)[2][4], const u32x2 (&xr)[2], int h, int xdst, F&& mid) {
  const int g = lane >> 4, cc = lane & 15, q = cc >> 2, p = lane & 3, w = c.w, t = w * 64 + lane;
  char* red = c.smem + L_XA + h * 32768;
#pragma unroll
  for (int jj = 1; jj < 4; ++jj) *(f32x4*)(red + ((((w + jj) & 3) * 3 + jj - 1) * 2 + 1) * 1024 + lane * 16) = acc[1][jj];
  __syncthreads();
  char* xt = red + 24576;
  f32x4 pr[2][3];
#pragma unroll
  for (int i = 0; i < 2; ++i)
#pragma unroll
    for (int rk = 0; rk < 3; ++rk) pr[i][rk] = *(const f32x4*)(red + ((w * 3 + rk) * 2 + i) * 1024 + lane * 16);
  mid(0);
#pragma unroll
  for (int i = 0; i < 2; ++i) {
    const f32x4 xv = {lo_bf(xr[i][0]), hi_bf(xr[i][0]), lo_bf(xr[i][1]), hi_bf(xr[i][1])};
    const f32x4 o = ((acc[i][0] + (xv + c.bb2)) + pr[i][0]) + (pr[i][1] + pr[i][2]);
    const u32x2 r = {pack_bf(o[0], o[1]), pack_bf(o[2], o[3])};
    *(u32x2*)(xt + (w * 16 + cc) * 64 + ((i ^ (cc >> 3)) * 32) + g * 8) = r;
  }
  __syncthreads();
  const char* xq = xt + ((w >> 1) * 32 + 8 * g + q) * 64 + (((w & 1) ^ (g & 1)) * 32) + 8 * p;
  const u32x4 f0 = cat2(tr_read(xq), tr_read(xq + 256));
  const int tok = (16 * h + c.P) * 64 + c.sub * 32 + (w & 1) * 16 + cc;
  if (LAST) {
    *(u32x4*)(c.out + (size_t)tok * DM + c.ct_ * 64 + (w >> 1) * 32 + 8 * g) = f0;
  } else {
    const int row = t >> 2;
    const u32x4 v = *(const u32x4*)(xt + row * 64 + (((t & 3) ^ ((row >> 2) & 2)) * 16));
    st128(c.rs, c.ct_ * 4096 + t * 16, xdst, v);
    const char* xo = xq + ((w & 2) ? -2048 : 2048);
    const u32x4 f1 = cat2(tr_read(xo), tr_read(xo + 256));
    float s = 0.f, ss = 0.f, s2 = 0.f, ss2 = 0.f;
#pragma unroll
    for (int j = 0; j < 4; ++j) {
      s = dot2bf(f0[j], 0x3f803f80u, s); ss = dot2bf(f0[j], f0[j], ss);
      s2 = dot2bf(f1[j], 0x3f803f80u, s2); ss2 = dot2bf(f1[j], f1[j], ss2);
    }
    s = sum_x32(sum_x16(s + s2)); ss = sum_x32(sum_x16(ss + ss2));
    asm volatile("s_nop 4\n\tbuffer_atomic_add_x2 %0, %1, %2, %3 offen" :: "v"(f2fx((g & 1) ? ss : s)),
                 "v"(t < 128 && lane < 32 ? st_pos(tok) + g * 8 : 0x40000000), "s"(c.rs), "s"(WS_ST + 65536) : "memory");
  }
}

template <int LAYER>
DEVI void layer_body(const Ctx& c, u32x4 (&w1f)[2][16], u32x4 (&w2f)[14][4], CIn& inA, CIn& inB) {
  constexpr int xin = WS_X + LAYER * 4 * MB, xmid = xin + 2 * MB, xnext = xin + 4 * MB;
  constexpr int hbuf = WS_H + LAYER * 8 * MB, stin = WS_ST + LAYER * 65536;
  constexpr u32 ep = LAYER + 1;
  const int w = c.w;
  char* smem = c.smem;
  auto nop = [](auto) {};
  {
    const int lane = fresh_lane(c);
    const int g = lane >> 4, cc = lane & 15, mu0 = (w & 1) * 4, v16 = lane * 16;
    const char* ub = smem + L_U + (w >> 1) * L_UC + 512 + 2 * (16 * cc - 16 + 8 * g) - 64 * (mu0 + 3);
    KB kb;
#pragma unroll
    for (int j = 0; j < 4; ++j) {
      u32 a = lds_addr(smem) + (w >> 1) * 2 * L_KC + ((cc & 1) ? 0 : L_KC) + 2 * (2031 - cc + 8 * g - ((cc & 1) ? 0 : 1)) - 64 * (mu0 + 3 + 56) + 1024 * j;
      asm volatile("" : "+v"(a));
      kb.p[j] = (const char*)(const LAS char*)(size_t)a;
    }
    f32x4 base0, base1;
    u32x4 a4[4];
    conv_ldk<0>(kb, a4);
    if (LAYER) { SCHED(); conv_landed(inA); }
    conv_ln(c, lane, inA, 0, base0, base1);
    if (!LAYER) poll0(c, lane, WS_F1);
    __syncthreads();
    f32x4 acc[4];
    u32 o = 0;
    conv_mma<0, 0, true>(ub, kb, acc, a4, [&](auto N) {
      constexpr int n = N.v;
      if constexpr (LAYER && n == 19) { WAIT_VM(0); o = sig_begin(c, lane); SCHED(); }
      if constexpr (LAYER && n == 30) { sig_end<true, true>(c, lane, o, CT_X + 64); SCHED(); }
    });
    MFMA_DONE();
    conv_part(c, lane, acc);
    conv_ldk<1>(kb, a4);
    __syncthreads();
    float s1p[8];
    if (!LAYER) {
      if (!got0<0>(c.tag)) wait0(c, lane, WS_F1);
      asm volatile("" :: "v"(inB.x1));
      const int so1 = (min(c.ff0 * 16 + (lane & 15) + (lane >> 5) * 16, FF - 1) + (lane & 16) * (FF / 2)) * 4;
#pragma unroll
      for (int j = 0; j < 8; ++j)
        asm volatile("s_nop 4\n\tbuffer_load_dword %0, %1, %2, %3 offen sc1" : "=v"(s1p[j]) : "v"(so1), "s"(c.rs), "s"(WS_S1 + j * FF * 4) : "memory");
    }
    conv_out(c, lane, base0, base1, xmid, 0);
    f32x4 acb[4];
    conv_mma<4, 0, true>(ub, kb, acb, a4, [&](auto N) {
      constexpr int n = N.v;
      if constexpr (!LAYER && n >= 48 && (n & 1)) { ld_w1<(n - 48) / 2>(c, v16, w1f); SCHED(); }
      if constexpr (n == 47) {
        WAIT_VM(0);
        o = sig_begin(c, lane);
        if (LAYER) poll<true>(c, lane, CT_X + 64);
        SCHED();
      }
      if constexpr (n == 58) { sig_end<true, true>(c, lane, o, CT_C); SCHED(); }
    });
    if (!LAYER) {
#pragma unroll
      for (int j = 0; j < 8; ++j) asm volatile("" : "+v"(s1p[j]));
      const float sv = sum_x16(((s1p[0] + s1p[1]) + (s1p[2] + s1p[3])) + ((s1p[4] + s1p[5]) + (s1p[6] + s1p[7])));
      ((float*)(smem + L_CST))[w * 64 + lane] = (lane & 16) ? c.cs1 : sv;
    }
    if (LAYER) {
      if (!got<1>(32u)) wait_ge<true>(c, lane, CT_X + 64, 32u);
      conv_load(c, lane, inB, xin, stin, 1);
    }
    conv_ldk<0>(kb, a4);
    if (!LAYER) sfor<8, 12>([&](auto N) { ld_w1<N.v>(c, v16, w1f); });
    conv_ln(c, lane, inB, 1, base0, base1);
    if (!LAYER) sfor<12, 14>([&](auto N) { ld_w1<N.v>(c, v16, w1f); });
    __syncthreads();
    conv_mma<0, 4, false>(ub, kb, acb, a4, [&](auto N) {
      constexpr int n = N.v;
      if constexpr (!LAYER && n % 3 == 1 && n < 36) { ld_w1<14 + n / 3>(c, v16, w1f); SCHED(); }
      if constexpr (n == 30) { poll<true>(c, lane, CT_C); SCHED(); }
    });
    MFMA_DONE();
    conv_part(c, lane, acb);
    __syncthreads();
    conv_out(c, lane, base0, base1, xmid, 1);
    if (!got<(LAYER ? 2 : 4)>(32u * ep)) wait_ge<true>(c, lane, CT_C, 32u * ep);
    const int row = lane >> 3, sw = ((row >> 1) & 1) << 1;
    const int ve = row * 128 + ((lane & 7) ^ sw) * 16, vo = row * 128 + ((lane & 7) ^ (sw | 4)) * 16;
    if (!LAYER && c.b == 0 && w == 0) __hip_atomic_store((gu32*)(c.ws + WS_GEN), c.tag, RLX_AGENT);
    sfor<0, 8>([&](auto N) { g1_dma1<N.v>(c, ve, vo, L_XA, xmid + c.P * 65536); });
    WAIT_VM(8);
    o = sig_begin(c, lane);
    sig_end<true>(c, lane, o, CT_C + 64);
  }
  u32x4 hf[14][2];
  u32x2 xr[2];
  {
    const int lane = fresh_lane(c);
    const int g = lane >> 4, cc = lane & 15, q = cc >> 2, p = lane & 3, v16 = lane * 16;
    const int sx = ((q >> 1) & 1) | ((g & 1) << 1);
    const char* xb[4];
#pragma unroll
    for (int i = 0; i < 4; ++i) xb[i] = smem + L_XA + (8 * g + q) * 128 + ((((i + w) & 3) ^ sx) * 32) + 8 * p;
    const int row = lane >> 3, sw = ((row >> 1) & 1) << 1;
    const int ve = row * 128 + ((lane & 7) ^ sw) * 16, vo = row * 128 + ((lane & 7) ^ (sw | 4)) * 16;
    sfor<8, 12>([&](auto N) { g1_dma1<N.v>(c, ve, vo, L_XA, xmid + c.P * 65536); });
    WAIT_VM_BAR(9);
    f32x4 acc[2][4], sa;
    g1_mma(xb, w1f, acc, sa, [&](auto KS) {
      constexpr int ks = KS.v;
      if constexpr (ks == 3) { if (LAYER) WAIT_VM_BAR(8); else WAIT_VM_BAR(10); SCHED(); }
      if constexpr (ks == 7) { if (LAYER) WAIT_VM_BAR(0); else WAIT_VM_BAR(10); SCHED(); }
      if constexpr (ks == 13) { if (!LAYER) WAIT_VM(16); poll<true>(c, lane, CT_C + 64); SCHED(); }
    }, [&](auto N) {
      constexpr int n = N.v;
      if constexpr (n < 4) g1_dma1<12 + n>(c, ve, vo, L_XA, xmid + c.P * 65536);
      if constexpr (!LAYER && n >= 4 && n < 10) ld_w1<22 + n>(c, v16, w1f);
      if constexpr (!LAYER && n >= 10) ld_w2<n - 10>(c, v16, w2f);
    });
    const int xo = xmid + (16 + c.P) * 65536;
    g1_epi(c, lane, acc, sa, hbuf + (c.P * 4 * 56 + c.hks) * 1024, [&](auto N) {
      constexpr int n = N.v;
      if constexpr (n == -1) { if (!got<(LAYER ? 0 : 6)>(32u * ep)) wait_ge<true>(c, lane, CT_C + 64, 32u * ep); }
      if constexpr (n >= 0 && n < 8) g1_dma1<n>(c, ve, vo, L_XB, xo);
      if constexpr (n >= 10 && n < 18) g1_dma1<n - 2>(c, ve, vo, L_XB, xo);
    });
  }
  {
    const int lane = fresh_lane(c);
    const int g = lane >> 4, cc = lane & 15, q = cc >> 2, p = lane & 3, v16 = lane * 16;
    const int sx = ((q >> 1) & 1) | ((g & 1) << 1);
    const char* xb[4];
#pragma unroll
    for (int i = 0; i < 4; ++i) xb[i] = smem + L_XB + (8 * g + q) * 128 + ((((i + w) & 3) ^ sx) * 32) + 8 * p;
    WAIT_VM_BAR(12);
    f32x4 acc[2][4], sa;
    g1_mma(xb, w1f, acc, sa, [&](auto KS) {
      constexpr int ks = KS.v;
      if constexpr (ks == 5) {
        if (LAYER) WAIT_VM_BAR(0); else WAIT_VM_BAR(10);
        sig_bar<false>(c, lane, CT_H + c.P);
        SCHED();
      }
      if constexpr (ks == 15) {
#pragma unroll
        for (int i = 0; i < 2; ++i) xr[i] = ld64(c.rs, (c.ct_ * 64 + w * 16 + cc) * 128 + i * 32 + g * 8, xmid + c.P * 65536 + c.sub * 64);
        SCHED();
      }
    }, [&](auto N) {
      constexpr int n = N.v;
      if constexpr (!LAYER) ld_w2<22 + n>(c, v16, w2f);
      if constexpr (!LAYER && n >= 30) ld_w2<24 + n>(c, v16, w2f);
    });
    poll<false>(c, lane, CT_H + c.P);
    const int ho = hbuf + ((c.P * 4 + c.sub * 2) * 56 + 14 * w) * 1024;
    g1_epi(c, lane, acc, sa, hbuf + ((16 + c.P) * 4 * 56 + c.hks) * 1024, [&](auto N) {
      constexpr int n = N.v;
      if constexpr (n == -1) { if (!got<0>(16u * ep)) wait_ge<false>(c, lane, CT_H + c.P, 16u * ep); }
      if constexpr (n >= 0) g2_ldh1<n>(c, v16, hf, ho);
    });
  }
  {
    const int lane = fresh_lane(c);
    const int g = lane >> 4, cc = lane & 15;
    const int ho = hbuf + ((c.P * 4 + c.sub * 2) * 56 + 14 * w) * 1024, hoB = ho + 64 * 56 * 1024;
    f32x4 acc[2][4];
    u32 o = 0;
    g2_mma<20, 1>(c, lane, hf, w2f, acc, ho, 0, [&](auto S) {
      constexpr int s = S.v;
      if constexpr (s == 11) { WAIT_VM(10); o = sig_begin(c, lane); SCHED(); }
      if constexpr (s == 13) { sig_end<false>(c, lane, o, CT_H + 16 + c.P); SCHED(); }
    });
    poll<false>(c, lane, CT_H + 16 + c.P);
    g2_epi<LAYER == 1>(c, lane, acc, xr, 0, xnext + c.P * 65536 + c.sub * 32768, nop);
    if (!got<(LAYER ? 1 : 2)>(16u * ep)) wait_ge<false>(c, lane, CT_H + 16 + c.P, 16u * ep);
    sfor<0, 8>([&](auto N) { g2_ldh1<N.v>(c, lane * 16, hf, hoB); });
#pragma unroll
    for (int i = 0; i < 2; ++i) xr[i] = ld64(c.rs, (c.ct_ * 64 + w * 16 + cc) * 128 + i * 32 + g * 8, xmid + (16 + c.P) * 65536 + c.sub * 64);
    if (LAYER == 0) {
      WAIT_VM(11);
      o = sig_begin(c, lane);
      sig_end<true>(c, lane, o, CT_X);
    }
    SCHED();
  }
  {
    const int lane = fresh_lane(c);
    const int hoB = hbuf + (((16 + c.P) * 4 + c.sub * 2) * 56 + 14 * w) * 1024;
    f32x4 acc[2][4];
    g2_mma<8, 2>(c, lane, hf, w2f, acc, hoB, 1, [&](auto S) {
      constexpr int s = S.v;
      if constexpr (LAYER == 0 && s == 27) { poll<true>(c, lane, CT_X); SCHED(); }
    });
    g2_epi<LAYER == 1>(c, lane, acc, xr, 1, xnext + (16 + c.P) * 65536 + c.sub * 32768, [&](int) {
      if (LAYER == 0) {
        if (!got<0>(32u)) wait_ge<true>(c, lane, CT_X, 32u);
        conv_load_u(c, lane, inA, xnext, WS_ST + 65536, 0);
        SCHED();
      }
    });
  }
}

constexpr int KG_RS = 528, KG_X = 16 * KG_RS, KG_CH = 64 * KG_RS + 1296;
__global__ void __launch_bounds__(256, 1) main_kernel(const u16* x, const u16* lar, const u16* aim, const u16* cre,
                                                      const u16* cim, const u16* ldt, const u16* dsk, const u16* w1,
                                                      const u16* b1, const u16* w2, const u16* b2, char* ws, u16* out) {
  extern __shared__ __attribute__((aligned(16))) char smem[];
  const int t = threadIdx.x, lane = t & 63, b = blockIdx.x;
  const int w = __builtin_amdgcn_readfirstlane(t >> 6);
  const u32 junk = warm_code((b >> 3) * 4 + w, lane);
  const int ch = w >> 1, d = 2 * b + ch, n = t & 127;
  const u32 po = (u32)(d * NST + n) * 2, po2 = po ^ 128u;
#define LD16(p, off) (*(const u16*)((const char*)(p) + (u32)(off)))
  const u16 r0 = ldt[d], r1 = LD16(lar, po), r2 = LD16(aim, po), r3 = LD16(cre, po), r4 = LD16(cim, po);
  const u16 r5 = LD16(cre, po2), r6 = LD16(cim, po2);
  const u32 gen = __hip_atomic_load((gu32*)(ws + WS_GEN), RLX_AGENT);
  const bool early = !(b & 64);
  const int pks = (b & 7) + (b >> 7) * 8, pcg = (b >> 3) & 7;
  u32x4 rv[8], rw[2];
  {
    const rsrc_t rr = __builtin_amdgcn_make_buffer_rsrc((void*)(early ? x : w1), 0, early ? SEQ * DM * 2 : DM * FF * 2, 0x00020000);
    const int vo = early ? w * 1024 + lane * 16 : ((b & 63) < 56 ? (t >> 5) * (FF * 2) + (t & 31) * 16 : 0x40000000);
    const int so = early ? (b & 63) * 32768 : pks * (32 * FF * 2) + min(pcg, 6) * 512, st = early ? 4096 : 8 * FF * 2;
#pragma unroll
    for (int j = 0; j < 8; ++j) rv[j] = __builtin_amdgcn_raw_buffer_load_b128(rr, (j < 4 || early) ? vo : 0x40000000, so + (j & 3) * st + (j >> 2) * (early ? 16384 : 0), 0);
  }
  CIn inA, inB;
  Ctx c;
  c.rs = __builtin_amdgcn_make_buffer_rsrc(ws, 0, WS_END, 0x00020000);
  c.ct = (gu32*)(ws + WS_CT);
  c.smem = smem; c.ws = ws; c.out = out;
  c.lane0 = lane; c.w = w; c.b = b;
  c.P = (b & 7) * 2 + ((b >> 3) & 1);
  const int idx = b >> 4;
  const int sg = idx >> 1, w3 = -(int)(w == 3);
  c.eo = idx & 1; c.sub = idx >> 3; c.ct_ = idx & 7;
  c.ff0 = 14 * sg + (((6 * c.eo + 2 * w) & ~w3) | ((12 + c.eo) & w3));
  c.hks = 7 * sg + (((3 * c.eo + w) & ~w3) | (6 & w3));
  c.f1o = w < 3 ? WS_W1P + c.ff0 * 16384 + 16384 : WS_ONE; c.f1s = w < 3 ? 4096 : 0;
  {
    const u32 r7 = *(const u32*)(dsk + 2 * b);
    u32 r8 = LD16(b1, min(c.ff0 * 16 + (lane & 15) + (lane >> 5) * 16, FF - 1) * 2), r9 = LD16(b2, (c.ct_ * 64 + w * 16 + (lane & 15)) * 2);
    SCHED();
    char* kg = smem + L_XA + ch * KG_CH;
    char* stg = smem + L_XA + 2 * KG_CH;
    const int i = lane & 15, g = lane >> 4;
    int ix[9];
#pragma unroll
    for (int e2 = 0; e2 < 9; ++e2) { ix[e2] = 2 * cidx(max(2047 - 8 * t - e2, 0)); asm volatile("" : "+v"(ix[e2])); }
    int wi[2][4];
    {
      const int m8 = -(i >> 3), s1 = -(w & 1);
      const int h1 = ((256 + 128 * (i >> 2) + 2 * (i & 3)) & ~m8) | ((128 * (i >> 1) + 4 * (i & 1)) & m8);
      const int hp = (((i + (i & 8) * 15) & ~s1) | (h1 & s1)) + 32 * g, hq = 1024 + 128 * i + 32 * g;
#pragma unroll
      for (int r = 0; r < 4; ++r) {
        wi[0][r] = 2 * cidx(hp + 8 * r); wi[1][r] = 2 * cidx(hq + 8 * r > 2048 ? 2064 : hq + 8 * r);
        asm volatile("" : "+v"(wi[0][r]), "+v"(wi[1][r]));
      }
    }
    if (t < 16) *(u32x4*)(smem + (t >> 2) * L_KC + 4096 + (t & 3) * 16) = u32x4{0, 0, 0, 0};
    if (t >= 64 && t < 128) *(u32x4*)(smem + L_U + ((t - 64) >> 5) * L_UC + (t & 31) * 16) = u32x4{0, 0, 0, 0};
    if (t == 128) *(u32*)(smem + L_SIG) = 0u;
    if (w >= 2) poll0(c, lane, w == 2 ? WS_F1 : WS_F0);
    SCHED();
    const float dt = rbf(expf(bf2f(r0)));
    const float cr = bf2f(r3), ci = bf2f(r4), cr2 = bf2f(r5), ci2 = bf2f(r6);
    const float S = wave_sum(sqrt_(cr * cr + ci * ci) + sqrt_(cr2 * cr2 + ci2 * ci2)) * (2.002f * dt);
    int e = __builtin_amdgcn_frexp_expf(S);
    e = (S > 0.0f && S < 3.0e38f) ? max(-100, min(100, e)) : 14;
    const float scale = __builtin_amdgcn_ldexpf(2.0f, 14 - e);
    if (n == 0) *(float*)(smem + L_MISC + ch * 4) = __builtin_amdgcn_ldexpf(1.0f, e - 14);
    const float a2 = (-rbf(expf(bf2f(r1))) * dt) * 1.44269504088896f;
    const float br = (bf2f(r2) * dt) * 0.159154943091895f;
    {
      char* yp = kg + n * 4;
      char* xp = kg + KG_X + n * 4;
      const CM e128 = cexp_(a2, -br, 128.f), e8 = cexp_(a2, br, 8.f), e1 = cexp_(a2, -br, 1.f), e2 = cexp_(a2, -br, 2.f);
      const CM e4 = cexp_(a2, -br, 4.f);
      f32x2 f0 = {(cr * dt) * scale, -(ci * dt) * scale};
      f32x2 f = cmul(f0, cexp_(a2, -br, 512.f)), y = {1.f, 0.f};
      f32x2 g1 = cmul(f0, e128), g2 = cmul(g1, e128), g3 = cmul(g2, e128);
#define PUT(base, row, v) *(u32*)(base + (row) * KG_RS) = pack_h(v[0], v[1])
      auto chain = [&](auto K) {
        constexpr int k = K.v;
        PUT(yp, k, y);
        if (k < 15) y = cmul(y, e8);
        if (k < 4) { const f32x2 q4 = cmul(f, e4); PUT(xp, 24 + 2 * k, f); PUT(xp, 25 + 2 * k, q4); }
        else if (k < 13) PUT(xp, 28 + k, f);
        if (k < 12) f = cmul(f, e128);
        if (k < 8) { PUT(xp, k, f0); PUT(xp, 8 + k, g1); }
        if (k < 7) { f0 = cmul(f0, e1); g1 = cmul(g1, e1); }
        if (k >= 8 && k < 12) { PUT(xp, 8 + k, g2); PUT(xp, 12 + k, g3); }
        if (k >= 8 && k < 11) { g2 = cmul(g2, e2); g3 = cmul(g3, e2); }
      };
      c.tag = __builtin_amdgcn_readfirstlane(gen) * 0x9E3779B1u + 0x7F4A7C15u;
      sfor<0, 2>(chain);
      asm volatile("" : "+v"(y), "+v"(f), "+v"(f0), "+v"(g1));
      if (b < 64) xpose(b, rv, c.rs, stg);
      else if (early) row_stats(b & 63, rv, c.rs);
      asm volatile("" : "+v"(y), "+v"(f), "+v"(f0), "+v"(g1));
      sfor<2, 12>(chain);
      asm volatile("" : "+v"(y), "+v"(f));
      if (early) {
        WAIT_VM(0);
        __syncthreads();
        if (w == 0 && lane < 8)
          __hip_atomic_store((gu32*)(ws + WS_F0 + lane * 2048 + (b & 7) * 256 + ((b >> 3) & 7) * 8 + (b >> 7) * 4), c.tag, RLX_AGENT);
      }
      asm volatile("" : "+v"(y), "+v"(f));
      {
    const rsrc_t r2w = __builtin_amdgcn_make_buffer_rsrc((void*)w2, 0, DM * FF * 2, 0x00020000);
#pragma unroll
    for (int j = 0; j < 2; ++j) rw[j] = __builtin_amdgcn_raw_buffer_load_b128(r2w, (t >> 4) * 1024 + (t & 15) * 16 + j * 16384, min(b >> 2, 55) * 32768 + (b & 3) * 256, 0);
      }
      SCHED();
      sfor<12, 16>(chain);
      if (!early && pcg < 7) pack_w1(pks, pcg, rv, c.rs, stg);
#undef PUT
    }
    __syncthreads();
    {
      const char* ya = kg + i * KG_RS + g * 16;
      const char* xa = kg + KG_X + (16 * (w & 1) + i) * KG_RS + g * 16;
      const char* xc = kg + KG_X + (32 + i) * KG_RS + g * 16;
      u32x4 fa[8], fb[8], fc[8];
#pragma unroll
      for (int ks = 0; ks < 8; ++ks) { fa[ks] = *(const u32x4*)(ya + ks * 64); fb[ks] = *(const u32x4*)(xa + ks * 64); }
#pragma unroll
      for (int ks = 0; ks < 8; ++ks) fc[ks] = *(const u32x4*)(xc + ks * 64);
      f32x4 ac0, ac1;
      SCHED();
      sfor<0, 8>([&](auto KS) { mma_hh<KS.v == 0>(ac0, fa[KS.v], fb[KS.v]); });
      sfor<0, 8>([&](auto KS) { mma_hh<KS.v == 0>(ac1, fa[KS.v], fc[KS.v]); });
      MFMA_DONE();
#pragma unroll
      for (int r = 0; r < 4; ++r) {
        *(u16*)(kg + 64 * KG_RS + wi[0][r]) = __builtin_bit_cast(u16, (_Float16)ac0[r]);
        *(u16*)(kg + 64 * KG_RS + wi[1][r]) = __builtin_bit_cast(u16, (_Float16)ac1[r]);
      }
    }
    if (!early) WAIT_VM(0);
    __syncthreads();
    if (w == 1 && !early && lane < 8)
      __hip_atomic_store((gu32*)(ws + WS_F1 + lane * 2048 + (b & 7) * 256 + ((b >> 3) & 7) * 8 + (b >> 7) * 4), c.tag, RLX_AGENT);
    if (w == 0) poll0(c, lane, WS_F0);
#pragma unroll
    for (int c2 = 0; c2 < 2; ++c2) {
      u32 v[9];
#pragma unroll
      for (int e2 = 0; e2 < 9; ++e2) v[e2] = *(const u16*)(smem + L_XA + c2 * KG_CH + 64 * KG_RS + ix[e2]);
      if (t == 255) v[8] = 0u;
      u32x4 oe, oo;
#pragma unroll
      for (int j = 0; j < 4; ++j) { oe[j] = v[2 * j] | (v[2 * j + 1] << 16); oo[j] = v[2 * j + 1] | (v[2 * j + 2] << 16); }
      *(u32x4*)(smem + c2 * 2 * L_KC + t * 16) = oe;
      *(u32x4*)(smem + c2 * 2 * L_KC + L_KC + t * 16) = oo;
    }
    const f32x2 ksc = *(const f32x2*)(smem + L_MISC);
    c.ks0 = __uint_as_float(__builtin_amdgcn_readfirstlane(__float_as_uint(ksc[0])));
    c.ks1 = __uint_as_float(__builtin_amdgcn_readfirstlane(__float_as_uint(ksc[1])));
    const u32 ds = __builtin_amdgcn_readfirstlane(r7);
    c.dsk0 = lo_bf(ds); c.dsk1 = hi_bf(ds);
    c.cs1 = lo_bf(r8); c.bb2 = lo_bf(r9);
    asm volatile("" :: "v"(junk));
    pack_w2_put(rw, smem + L_XB + 55552);
    if (w == 0 && !got0<0>(c.tag)) wait0(c, lane, WS_F0);
    __syncthreads();
    conv_load(c, lane, inA, WS_X, WS_ST, 0);
    conv_load(c, lane, inB, WS_X, WS_ST, 1);
    pack_w2_out(b, c.rs, smem + L_XB + 55552);
  }
  u32x4 w1f[2][16], w2f[14][4];
  layer_body<0>(c, w1f, w2f, inA, inB);
  layer_body<1>(c, w1f, w2f, inA, inB);
}

extern "C" __global__ void code_end_marker() {}

extern "C" void kernel_launch(void* const* d_in, const int* in_sizes, int n_in, void* d_out, int out_size,
                              void* d_ws, size_t ws_size, hipStream_t stream) {
  (void)in_sizes; (void)n_in; (void)out_size; (void)ws_size;
  char* ws = (char*)d_ws + ((ws_size - (size_t)WS_END) & ~(size_t)(2 * MB - 1));
  (void)hipFuncSetAttribute((const void*)main_kernel, hipFuncAttributeMaxDynamicSharedMemorySize, MAIN_LDS);
  main_kernel<<<256, 256, MAIN_LDS, stream>>>((const u16*)d_in[0], (const u16*)d_in[1], (const u16*)d_in[2],
                                              (const u16*)d_in[3], (const u16*)d_in[4], (const u16*)d_in[5],
                                              (const u16*)d_in[6], (const u16*)d_in[8], (const u16*)d_in[9],
                                              (const u16*)d_in[10], (const u16*)d_in[11], ws, (u16*)d_out);
}
```
